```python
import torch

def _softmax(x, axis=-1):
    return torch.softmax(x, dim=axis)

def _gelu(x):
    return torch.nn.functional.gelu(x, approximate='tanh')

def _silu(x):
    return torch.nn.functional.silu(x)

def _sigmoid(x):
    return torch.sigmoid(x)

def _layernorm(x, axis=-1, eps=1e-05):
    m = x.mean(dim=axis, keepdim=True)
    v = x.var(dim=axis, keepdim=True, unbiased=False)
    return (x - m) / torch.sqrt(v + eps)

def _scatter_add(tgt, idx, src, axis=0):
    return tgt.clone().index_add_(axis, idx, src)

def _segment_sum(src, seg, n):
    out = torch.zeros((n,) + src.shape[1:], dtype=src.dtype)
    return out.index_add_(0, seg, src)

def _pad(x, pw):
    flat = tuple((v for pair in reversed(pw) for v in pair))
    return torch.nn.functional.pad(x, flat)
kernel_name = 'adaLN_cond__gqa_block_cached__glu_gated_reglu__prompt'

def setup_inputs():

    _w = lambda a: (lambda t: t.to(torch.bfloat16) if t.is_floating_point() else t)(torch.as_tensor(a))

    def _A_setup():
        B, Sq, D = (1, 2048, 640)
        Hq, Hkv, Sk, Dh = (32, 32, 2048, 128)
        assert Hq % Hkv == 0, (Hq, Hkv)
        return {'x': _w((lambda *s: torch.randn(*s, dtype=torch.bfloat16) * 0.05)(B, Sq, D)), 'k_cache': _w((lambda *s: torch.randn(*s, dtype=torch.bfloat16) * 1.6)(B, Hkv, Sk, Dh)), 'v_cache': _w((lambda *s: torch.randn(*s, dtype=torch.bfloat16) * 0.8)(B, Hkv, Sk, Dh)), 'wq': _w((lambda *s: torch.randn(*s, dtype=torch.bfloat16) * 1.6)(D, Hq * Dh) * D ** (-0.5)), 'wo': _w((lambda *s: torch.randn(*s, dtype=torch.bfloat16) * 0.8)(Hq * Dh, D) * (Hq * Dh) ** (-0.5))}
    _A_in = _A_setup()

    def _B_setup():
        B, S, D, F = (1, 2048, 640, 1728)
        return {'x': _w((lambda *s: torch.randn(*s, dtype=torch.bfloat16) * 0.05)(B, S, D)), 'W_gate': _w((lambda *s: torch.randn(*s, dtype=torch.bfloat16) * 0.4)(D, F) * (2.0 / D) ** 0.5), 'W_up': _w((lambda *s: torch.randn(*s, dtype=torch.bfloat16) * 0.4)(D, F) * (2.0 / D) ** 0.5), 'W_down': _w((lambda *s: torch.randn(*s, dtype=torch.bfloat16) * 0.4)(F, D) * (1.0 / F) ** 0.5)}
    _B_in = _B_setup()
    return {'A_x': _A_in['x'], 'A_k_cache': _A_in['k_cache'], 'A_v_cache': _A_in['v_cache'], 'A_wq': _A_in['wq'], 'A_wo': _A_in['wo'], 'B_x': _B_in['x'], 'B_W_gate': _B_in['W_gate'], 'B_W_up': _B_in['W_up'], 'B_W_down': _B_in['W_down'], 'c': _w((lambda *s: torch.randn(*s, dtype=torch.bfloat16) * 0.2)(1, 640) * 8.0), 'w_mod': _w((lambda *s: torch.randn(*s, dtype=torch.bfloat16) * 0.2)(640, 6 * 640) * 640 ** (-0.5) * 8.0)}

def reference(A_x, A_k_cache, A_v_cache, A_wq, A_wo, B_x, B_W_gate, B_W_up, B_W_down, c, w_mod):

    def _A_ref(x, k_cache, v_cache, wq, wo):
        B, Sq, D = x.shape
        Hq, Dh = (32, 128)
        Hkv = k_cache.shape[1]
        rep = Hq // Hkv
        q = (lambda x, *d: x.permute(*d))((x @ wq).reshape(B, Sq, Hq, Dh), 0, 2, 1, 3)
        k = (lambda x, n, axis: torch.repeat_interleave(x, n, dim=axis))(k_cache, rep, axis=1)
        v = (lambda x, n, axis: torch.repeat_interleave(x, n, dim=axis))(v_cache, rep, axis=1)
        scores = torch.einsum('bhqd,bhkd->bhqk', q, k) * Dh ** (-0.5)
        p = _softmax(scores, axis=-1)
        o = torch.einsum('bhqk,bhkd->bhqd', p, v)
        return (lambda x, *d: x.permute(*d))(o, 0, 2, 1, 3).reshape(B, Sq, Hq * Dh) @ wo

    def _B_ref(x, W_gate, W_up, W_down):
        gate = torch.einsum('bsd,df->bsf', x, W_gate)
        up = torch.einsum('bsd,df->bsf', x, W_up)
        gate = gate * (lambda x, dt: x.to(dt))(gate > 0, gate.dtype)
        h = gate * up
        return torch.einsum('bsf,fd->bsd', h, W_down)
    x = A_x
    mod = (_silu(c) @ w_mod).reshape(c.shape[0], 6, 1, -1)
    sa, ca, ga, sb, cb, gb = (mod[:, 0].clone(), mod[:, 1].clone(), mod[:, 2].clone(), mod[:, 3].clone(), mod[:, 4].clone(), mod[:, 5].clone())
    h = _layernorm(x, axis=-1) * (1 + ca) + sa
    x = x + ga * _A_ref(h, A_k_cache, A_v_cache, A_wq, A_wo)
    h = _layernorm(x, axis=-1) * (1 + cb) + sb
    x = x + gb * _B_ref(h, B_W_gate, B_W_up, B_W_down)
    return x
```

```cpp
#include <hip/hip_runtime.h>
#include <hip/hip_bf16.h>
typedef unsigned short u16;
__device__ __forceinline__ float bf2f(u16 x){ return __uint_as_float(((unsigned)x)<<16); }
__device__ __forceinline__ u16 f2bf(float f){ unsigned u=__float_as_uint(f); u += 0x7fff + ((u>>16)&1); return (u16)(u>>16); }
constexpr int S=2048, D=640, H=32, DH=128, F=1728;

__global__ void k_mod(const u16* c, const u16* wmod, float* mod){
  int n = blockIdx.x*256+threadIdx.x; if(n>=6*D) return;
  float a=0; for(int k=0;k<D;k++){ float cv=bf2f(c[k]); float s=cv/(1.f+__expf(-cv)); a+= s*bf2f(wmod[k*6*D+n]); }
  mod[n]=a;
}
__global__ void k_ln(const u16* x, const float* sh, const float* sc, u16* h){
  int r=blockIdx.x; __shared__ float red[256];
  float v[3]; float s=0; for(int i=0;i<3;i++){ int k=threadIdx.x+256*i; v[i]= k<D? bf2f(x[r*D+k]):0.f; s+=v[i]; }
  red[threadIdx.x]=s; __syncthreads(); for(int o=128;o>0;o>>=1){ if(threadIdx.x<o) red[threadIdx.x]+=red[threadIdx.x+o]; __syncthreads(); }
  float m=red[0]/D; __syncthreads();
  s=0; for(int i=0;i<3;i++){ int k=threadIdx.x+256*i; if(k<D){ float d=v[i]-m; s+=d*d; } }
  red[threadIdx.x]=s; __syncthreads(); for(int o=128;o>0;o>>=1){ if(threadIdx.x<o) red[threadIdx.x]+=red[threadIdx.x+o]; __syncthreads(); }
  float rs=rsqrtf(red[0]/D+1e-5f);
  for(int i=0;i<3;i++){ int k=threadIdx.x+256*i; if(k<D) h[r*D+k]=f2bf((v[i]-m)*rs*(1.f+sc[k])+sh[k]); }
}
template<int MODE>
__global__ void k_gemm(const u16* A, const u16* B, const u16* B2, u16* C, const u16* res, const float* g, int M,int N,int K){
  __shared__ float As[16][17], Bs[16][17], Bs2[16][17];
  int tx=threadIdx.x&15, ty=threadIdx.x>>4; int row=blockIdx.y*16+ty, col=blockIdx.x*16+tx;
  float a=0,a2=0;
  for(int k0=0;k0<K;k0+=16){
    As[ty][tx]=bf2f(A[row*K+k0+tx]); Bs[ty][tx]=bf2f(B[(k0+ty)*N+col]); if(MODE==2) Bs2[ty][tx]=bf2f(B2[(k0+ty)*N+col]);
    __syncthreads();
    for(int k=0;k<16;k++){ a+=As[ty][k]*Bs[k][tx]; if(MODE==2) a2+=As[ty][k]*Bs2[k][tx]; }
    __syncthreads();
  }
  if(MODE==0) C[row*N+col]=f2bf(a);
  if(MODE==1) C[row*N+col]=f2bf(bf2f(res[row*N+col])+g[col]*a);
  if(MODE==2) C[row*N+col]=f2bf(fmaxf(a,0.f)*a2);
}
__global__ __launch_bounds__(256) void k_attn(const u16* q, const u16* kc, const u16* vc, u16* o){
  int h=blockIdx.y, r=blockIdx.x; __shared__ float qs[DH], p[S], red[256];
  if(threadIdx.x<DH) qs[threadIdx.x]=bf2f(q[r*H*DH+h*DH+threadIdx.x]); __syncthreads();
  float mx=-1e30f;
  for(int k=threadIdx.x;k<S;k+=256){ const u16* kp=kc+((size_t)h*S+k)*DH; float a=0; _Pragma("unroll 2") for(int d=0;d<DH;d+=8){ uint4 w=*(const uint4*)(kp+d); a+=qs[d]*__uint_as_float(w.x<<16)+qs[d+1]*__uint_as_float(w.x&0xffff0000u)+qs[d+2]*__uint_as_float(w.y<<16)+qs[d+3]*__uint_as_float(w.y&0xffff0000u)+qs[d+4]*__uint_as_float(w.z<<16)+qs[d+5]*__uint_as_float(w.z&0xffff0000u)+qs[d+6]*__uint_as_float(w.w<<16)+qs[d+7]*__uint_as_float(w.w&0xffff0000u); } a*=0.08838834764f; p[k]=a; mx=fmaxf(mx,a);}
  red[threadIdx.x]=mx; __syncthreads(); for(int o2=128;o2>0;o2>>=1){ if(threadIdx.x<o2) red[threadIdx.x]=fmaxf(red[threadIdx.x],red[threadIdx.x+o2]); __syncthreads(); }
  mx=red[0]; __syncthreads(); float s=0;
  for(int k=threadIdx.x;k<S;k+=256){ float e=__expf(p[k]-mx); p[k]=e; s+=e; }
  red[threadIdx.x]=s; __syncthreads(); for(int o2=128;o2>0;o2>>=1){ if(threadIdx.x<o2) red[threadIdx.x]+=red[threadIdx.x+o2]; __syncthreads(); }
  float il=1.f/red[0]; __syncthreads();
  int d=threadIdx.x&127, half=threadIdx.x>>7; float a=0;
  for(int k=half*1024;k<half*1024+1024;k++) a+=p[k]*bf2f(vc[((size_t)h*S+k)*DH+d]);
  red[threadIdx.x]=a; __syncthreads();
  if(threadIdx.x<128) o[r*H*DH+h*DH+d]=f2bf((red[d]+red[d+128])*il);
}
extern "C" void kernel_launch(void* const* d_in, const int* in_sizes, int n_in, void* d_out, int out_size, void* d_ws, size_t ws_size, hipStream_t stream){
  const u16* x=(const u16*)d_in[0]; const u16* kc=(const u16*)d_in[1]; const u16* vc=(const u16*)d_in[2]; const u16* wq=(const u16*)d_in[3]; const u16* wo=(const u16*)d_in[4];
  const u16* wg=(const u16*)d_in[6]; const u16* wu=(const u16*)d_in[7]; const u16* wd=(const u16*)d_in[8]; const u16* c=(const u16*)d_in[9]; const u16* wmod=(const u16*)d_in[10];
  char* ws=(char*)d_ws; float* mod=(float*)ws; u16* h=(u16*)(ws+(1<<20)); u16* q=(u16*)(ws+(8<<20)); u16* o=(u16*)(ws+(32<<20)); u16* x1=(u16*)(ws+(64<<20)); u16* hm=(u16*)(ws+(80<<20));
  k_mod<<<15,256,0,stream>>>(c,wmod,mod);
  k_ln<<<S,256,0,stream>>>(x,mod,mod+D,h);
  k_gemm<0><<<dim3(H*DH/16,S/16),256,0,stream>>>(h,wq,nullptr,q,nullptr,nullptr,S,H*DH,D);
  k_attn<<<dim3(S,H),256,0,stream>>>(q,kc,vc,o);
  k_gemm<1><<<dim3(D/16,S/16),256,0,stream>>>(o,wo,nullptr,x1,x,mod+2*D,S,D,H*DH);
  k_ln<<<S,256,0,stream>>>(x1,mod+3*D,mod+4*D,h);
  k_gemm<2><<<dim3(F/16,S/16),256,0,stream>>>(h,wg,wu,hm,nullptr,nullptr,S,F,D);
  k_gemm<1><<<dim3(D/16,S/16),256,0,stream>>>(hm,wd,nullptr,(u16*)d_out,x1,mod+5*D,S,D,F);
}
```

```cpp
#include <hip/hip_runtime.h>
#include <hip/hip_bf16.h>

typedef unsigned short u16;
typedef unsigned int u32;

__global__ __launch_bounds__(256, 1) __attribute__((aligned(4096))) void k_main(const u16* x, const u16* c, const u16* wmod, char* ws, const u16* kc, const u16* vc, const u16* wq,
                                                                               u16* out, const u16* wo, const u16* wg, const u16* wu, const u16* wd) {
  __shared__ __attribute__((aligned(16))) char lds[163840];
  const u32 b = blockIdx.x, w = __builtin_amdgcn_readfirstlane(threadIdx.x >> 6);
  const void* ka = (const void*)__builtin_amdgcn_kernarg_segment_ptr();
  unsigned long long p0, p1, p2, p3;
  u32 t0, t1, t2, t3, t4, t5, t6, t7, t8, t9, t10, t11;
  asm volatile(
".p2align 4\n"
"L_body_%=:\n"
"s_load_dwordx2 %1, %16, 0x0\n"
"s_load_dwordx2 %2, %16, 0x8\n"
"s_load_dwordx2 %3, %16, 0x10\n"
"s_load_dwordx2 %0, %16, 0x18\n"
"s_and_b32 s64, %18, 7\n"
"s_lshl_b32 %4, s64, 2\n"
"s_bfe_u32 s65, %18, 0x20003\n"
"s_add_u32 %4, %4, s65\n"
"s_lshr_b32 %5, %18, 5\n"
"s_add_u32 s64, s64, 7\n"
"s_and_b32 s64, s64, 7\n"
"s_lshl_b32 s64, s64, 5\n"
"s_lshr_b32 %6, %18, 3\n"
"s_add_u32 %6, %6, s64\n"
"v_mbcnt_lo_u32_b32 v0, -1, 0\n"
"v_mbcnt_hi_u32_b32 v0, -1, v0\n"
"v_lshlrev_b32 v228, 4, v0\n"
"s_xor_b32 s66, %18, 248\n"
"s_lshl_b32 s66, s66, 2\n"
"s_xor_b32 s64, %17, 3\n"
"s_or_b32 s66, s66, s64\n"
"v_mov_b32 v8, 0\n"
"v_mov_b32 v9, 0\n"
"v_mov_b32 v10, 0\n"
"v_mov_b32 v11, 0\n"
"s_lshl_b32 s64, %5, 5\n"
"s_add_u32 s61, s64, %4\n"
"s_lshl_b32 s64, s61, 2\n"
"s_add_u32 s64, s64, %17\n"
"s_mul_i32 s64, s64, 2560\n"
"v_lshrrev_b32 v3, 5, v0\n"
"v_and_b32 v4, 31, v0\n"
"v_mul_u32_u24 v5, 1280, v3\n"
"v_lshl_add_u32 v5, v4, 3, v5\n"
"v_add_u32 v5, s64, v5\n"
"s_lshl_b32 s64, %17, 5\n"
"v_lshrrev_b32 v1, 1, v0\n"
"v_add_u32 v1, s64, v1\n"
"v_lshlrev_b32 v2, 1, v1\n"
"s_waitcnt lgkmcnt(0)\n"
"s_cmp_lg_u32 %17, 0\n"
"s_cbranch_scc1 L_f0a_%=\n"
"s_lshl_b32 s64, %18, 7\n"
"s_add_u32 s64, s64, 524288\n"
"v_lshl_add_u32 v13, v0, 15, s64\n"
"s_lshr_b32 s64, %18, 2\n"
"s_mul_i32 s64, s64, 12\n"
"s_add_u32 s64, s64, %18\n"
"s_lshl_b32 s64, s64, 2\n"
"s_add_u32 s64, s64, 630784\n"
"v_writelane_b32 v13, s64, 3\n"
"s_mov_b64 exec, 15\n"
"global_store_dword v13, v8, %0 sc1\n"
"s_mov_b64 exec, -1\n"
"L_f0a_%=:\n"
"s_cmp_lg_u32 s66, 0\n"
"s_cbranch_scc1 L_f0_%=\n"
"v_lshlrev_b32 v6, 7, v0\n"
"v_add_u32 v6, 622592, v6\n"
"global_store_dword v6, v8, %0 sc1\n"
"v_add_u32 v6, 4096, v228\n"
"global_store_dwordx4 v6, v[8:11], %0 offset:-4096 sc1\n"
"global_store_dwordx4 v6, v[8:11], %0 offset:-3072 sc1\n"
"global_store_dwordx4 v6, v[8:11], %0 offset:-2048 sc1\n"
"global_store_dwordx4 v6, v[8:11], %0 offset:-1024 sc1\n"
"global_store_dwordx4 v6, v[8:11], %0 offset:0 sc1\n"
"v_lshlrev_b32 v12, 6, v0\n"
"v_add_u32 v12, 630784, v12\n"
"L_f0_%=:\n"
"global_load_dwordx2 v[16:17], v5, %1 offset:0\n"
"global_load_dwordx2 v[18:19], v5, %1 offset:256\n"
"global_load_dwordx2 v[20:21], v5, %1 offset:512\n"
"global_load_dwordx2 v[22:23], v5, %1 offset:768\n"
"global_load_dwordx2 v[24:25], v5, %1 offset:1024\n"
"s_and_b32 s62, %6, 3\n"
"s_mov_b32 s63, s62\n"
"v_lshl_add_u32 v6, s63, 8, v2\n"
"global_load_ushort v46, v6, %2\n"
"s_add_u32 s63, s63, 1\n"
"s_cmp_eq_u32 s63, 5\n"
"s_cselect_b32 s63, 0, s63\n"
"v_lshl_add_u32 v6, s63, 8, v2\n"
"global_load_ushort v48, v6, %2\n"
"s_add_u32 s63, s63, 1\n"
"s_cmp_eq_u32 s63, 5\n"
"s_cselect_b32 s63, 0, s63\n"
"v_lshl_add_u32 v6, s63, 8, v2\n"
"global_load_ushort v50, v6, %2\n"
"s_add_u32 s63, s63, 1\n"
"s_cmp_eq_u32 s63, 5\n"
"s_cselect_b32 s63, 0, s63\n"
"v_lshl_add_u32 v6, s63, 8, v2\n"
"global_load_ushort v52, v6, %2\n"
"s_add_u32 s63, s63, 1\n"
"s_cmp_eq_u32 s63, 5\n"
"s_cselect_b32 s63, 0, s63\n"
"v_lshl_add_u32 v6, s63, 8, v2\n"
"global_load_ushort v54, v6, %2\n"
"s_cmp_lt_u32 %6, 240\n"
"s_cselect_b32 s65, 7680, 0\n"
"s_cselect_b32 s64, %6, 0\n"
"s_lshl_b32 s64, s64, 5\n"
"v_mul_u32_u24 v1, s65, v1\n"
"v_and_b32 v2, 1, v0\n"
"v_lshl_add_u32 v1, v2, 4, v1\n"
"v_add_u32 v1, s64, v1\n"
"s_barrier\n"
"s_mul_i32 s63, s62, 983040\n"
"v_add_u32 v6, s63, v1\n"
"global_load_dwordx4 v[56:59], v6, %3\n"
"s_add_u32 s62, s62, 1\n"
"s_cmp_eq_u32 s62, 5\n"
"s_cselect_b32 s62, 0, s62\n"
"s_mul_i32 s63, s62, 983040\n"
"v_add_u32 v6, s63, v1\n"
"global_load_dwordx4 v[60:63], v6, %3\n"
"s_add_u32 s62, s62, 1\n"
"s_cmp_eq_u32 s62, 5\n"
"s_cselect_b32 s62, 0, s62\n"
"s_mul_i32 s63, s62, 983040\n"
"v_add_u32 v6, s63, v1\n"
"global_load_dwordx4 v[64:67], v6, %3\n"
"s_add_u32 s62, s62, 1\n"
"s_cmp_eq_u32 s62, 5\n"
"s_cselect_b32 s62, 0, s62\n"
"s_mul_i32 s63, s62, 983040\n"
"v_add_u32 v6, s63, v1\n"
"global_load_dwordx4 v[68:71], v6, %3\n"
"s_add_u32 s62, s62, 1\n"
"s_cmp_eq_u32 s62, 5\n"
"s_cselect_b32 s62, 0, s62\n"
"s_mul_i32 s63, s62, 983040\n"
"v_add_u32 v6, s63, v1\n"
"global_load_dwordx4 v[72:75], v6, %3\n"
"s_load_dwordx2 s[36:37], %16, 0x20\n"
"s_load_dwordx2 s[40:41], %16, 0x28\n"
"s_load_dwordx2 s[52:53], %16, 0x30\n"
"s_lshl_b32 s60, %17, 12\n"
"s_mov_b64 s[44:45], %0\n"
"s_and_b32 s45, s45, 0xffff\n"
"s_mov_b32 s46, 67108864\n"
"s_mov_b32 s47, 0x20000\n"
"v_and_b32 v1, 15, v0\n"
"v_lshrrev_b32 v2, 4, v0\n"
"s_lshl_b32 s65, %17, 4\n"
"v_add_u32 v14, s65, v2\n"
"v_lshlrev_b32 v15, 1, v2\n"
"v_xor_b32 v15, v15, v1\n"
"v_lshlrev_b32 v15, 4, v15\n"
"v_lshl_add_u32 v230, v14, 13, v15\n"
"v_xor_b32 v231, 0x80, v230\n"
"s_waitcnt lgkmcnt(0)\n"
"s_lshl_b32 s64, %4, 19\n"
"s_add_u32 s36, s36, s64\n"
"s_addc_u32 s37, s37, 0\n"
"s_add_u32 s40, s40, s64\n"
"s_addc_u32 s41, s41, 0\n"
"s_lshl_b32 s64, %4, 8\n"
"s_add_u32 s52, s52, s64\n"
"s_addc_u32 s53, s53, 0\n"
"s_sub_u32 s54, 5242880, s64\n"
"s_and_b32 s37, s37, 0xffff\n"
"s_mov_b32 s39, 0x20000\n"
"s_and_b32 s41, s41, 0xffff\n"
"s_mov_b32 s43, 0x20000\n"
"s_and_b32 s53, s53, 0xffff\n"
"s_mov_b32 s55, 0x20000\n"
"s_mov_b32 s38, 524288\n"
"s_mov_b32 s42, 524288\n"
"v_and_b32 v7, 3, v0\n"
"v_lshrrev_b32 v5, 3, v4\n"
"v_lshlrev_b32 v5, 9, v5\n"
"v_lshl_add_u32 v5, v7, 7, v5\n"
"v_bfe_u32 v6, v0, 2, 1\n"
"v_lshl_add_u32 v5, v6, 3, v5\n"
"s_lshl_b32 s65, %17, 5\n"
"v_lshl_add_u32 v5, v3, 4, v5\n"
"v_add_u32 v14, s65, v5\n"
"v_bfe_u32 v6, v14, 7, 3\n"
"v_lshlrev_b32 v6, 4, v6\n"
"v_xor_b32 v14, v14, v6\n"
"v_lshlrev_b32 v5, 12, v3\n"
"v_bfe_u32 v6, v0, 3, 2\n"
"v_lshl_add_u32 v5, v6, 8, v5\n"
"v_lshrrev_b32 v6, 3, v0\n"
"v_xor_b32 v6, v6, v0\n"
"v_and_b32 v6, 7, v6\n"
"v_lshl_add_u32 v15, v6, 4, v5\n"
"s_sub_u32 s64, 2, %17\n"
"s_lshl_b32 s65, s64, 10\n"
"v_add_u32 v5, s65, v228\n"
"s_lshl_b32 s64, s64, 13\n"
"s_lshr_b32 s65, s61, 3\n"
"s_mul_i32 s65, s65, 81920\n"
"s_add_u32 s64, s64, s65\n"
"s_bfe_u32 s65, s61, 0x20001\n"
"s_lshl_b32 s65, s65, 10\n"
"s_add_u32 s64, s64, s65\n"
"s_and_b32 s65, s61, 1\n"
"s_lshl_b32 s65, s65, 7\n"
"s_add_u32 s64, s64, s65\n"
"s_add_u32 s64, s64, 1048576\n"
"v_mov_b32 v8, 1\n"
"v_mov_b32 v9, 1\n"
"v_mov_b32 v10, 1\n"
"v_mov_b32 v11, 1\n"
"s_lshl_b32 s65, %17, 8\n"
"v_lshlrev_b32 v94, 6, v2\n"
"v_lshl_add_u32 v94, v1, 5, v94\n"
"v_add_u32 v94, s65, v94\n"
"s_lshl_b32 s65, %5, 9\n"
"s_add_u32 s65, s65, 622592\n"
"v_lshl_add_u32 v96, v0, 7, s65\n"
"s_cmp_lg_u32 s66, 0\n"
"s_cbranch_scc1 L_f4_%=\n"
"s_waitcnt vmcnt(0)\n"
"global_store_dwordx4 v12, v[8:11], %0 sc1\n"
"L_f4_%=:\n"
"s_mov_b64 exec, 0\n"
"s_cmp_eq_u32 s60, 0\n"
"s_cbranch_scc1 L_w1_%=\n"
"s_cmp_eq_u32 s60, 4096\n"
"s_cbranch_scc1 L_w2_%=\n"
"s_cmp_eq_u32 s60, 8192\n"
"s_cbranch_scc1 L_w3_%=\n"
"s_branch L_w4_%=\n"
"L_wd0_%=:\n"
"s_mov_b64 exec, -1\n"
"s_waitcnt vmcnt(10)\n"
"v_lshlrev_b32 v26, 16, v16\n"
"v_and_b32 v27, 0xffff0000, v16\n"
"v_lshlrev_b32 v28, 16, v17\n"
"v_and_b32 v29, 0xffff0000, v17\n"
"v_lshlrev_b32 v30, 16, v18\n"
"v_and_b32 v31, 0xffff0000, v18\n"
"v_lshlrev_b32 v32, 16, v19\n"
"v_and_b32 v33, 0xffff0000, v19\n"
"v_lshlrev_b32 v34, 16, v20\n"
"v_and_b32 v35, 0xffff0000, v20\n"
"v_lshlrev_b32 v36, 16, v21\n"
"v_and_b32 v37, 0xffff0000, v21\n"
"v_lshlrev_b32 v38, 16, v22\n"
"v_and_b32 v39, 0xffff0000, v22\n"
"v_lshlrev_b32 v40, 16, v23\n"
"v_and_b32 v41, 0xffff0000, v23\n"
"v_lshlrev_b32 v42, 16, v24\n"
"v_and_b32 v43, 0xffff0000, v24\n"
"v_lshlrev_b32 v44, 16, v25\n"
"v_and_b32 v45, 0xffff0000, v25\n"
"v_pk_add_f32 v[16:17], v[26:27], v[28:29]\n"
"v_pk_add_f32 v[18:19], v[30:31], v[32:33]\n"
"v_pk_add_f32 v[16:17], v[16:17], v[34:35]\n"
"v_pk_add_f32 v[18:19], v[18:19], v[36:37]\n"
"v_pk_add_f32 v[16:17], v[16:17], v[38:39]\n"
"v_pk_add_f32 v[18:19], v[18:19], v[40:41]\n"
"v_pk_add_f32 v[16:17], v[16:17], v[42:43]\n"
"v_pk_add_f32 v[18:19], v[18:19], v[44:45]\n"
"v_pk_add_f32 v[16:17], v[16:17], v[18:19]\n"
"v_add_f32 v16, v16, v17\n"
"s_nop 1\n"
"v_add_f32_dpp v16, v16, v16 row_ror:8 row_mask:0xf bank_mask:0xf\n"
"s_nop 1\n"
"v_add_f32_dpp v16, v16, v16 row_ror:4 row_mask:0xf bank_mask:0xf\n"
"s_nop 1\n"
"v_add_f32_dpp v16, v16, v16 row_ror:2 row_mask:0xf bank_mask:0xf\n"
"s_nop 1\n"
"v_add_f32_dpp v16, v16, v16 row_ror:1 row_mask:0xf bank_mask:0xf\n"
"v_mov_b32 v17, v16\n"
"s_nop 1\n"
"v_permlane16_swap_b32 v16, v17\n"
"v_add_f32 v16, v16, v17\n"
"v_mul_f32 v16, 0x3acccccd, v16\n"
"v_pk_add_f32 v[26:27], v[26:27], v[16:17] op_sel_hi:[1,0] neg_lo:[0,1] neg_hi:[0,1]\n"
"v_pk_add_f32 v[28:29], v[28:29], v[16:17] op_sel_hi:[1,0] neg_lo:[0,1] neg_hi:[0,1]\n"
"v_pk_add_f32 v[30:31], v[30:31], v[16:17] op_sel_hi:[1,0] neg_lo:[0,1] neg_hi:[0,1]\n"
"v_pk_add_f32 v[32:33], v[32:33], v[16:17] op_sel_hi:[1,0] neg_lo:[0,1] neg_hi:[0,1]\n"
"v_pk_add_f32 v[34:35], v[34:35], v[16:17] op_sel_hi:[1,0] neg_lo:[0,1] neg_hi:[0,1]\n"
"v_pk_add_f32 v[36:37], v[36:37], v[16:17] op_sel_hi:[1,0] neg_lo:[0,1] neg_hi:[0,1]\n"
"v_pk_add_f32 v[38:39], v[38:39], v[16:17] op_sel_hi:[1,0] neg_lo:[0,1] neg_hi:[0,1]\n"
"v_pk_add_f32 v[40:41], v[40:41], v[16:17] op_sel_hi:[1,0] neg_lo:[0,1] neg_hi:[0,1]\n"
"v_pk_add_f32 v[42:43], v[42:43], v[16:17] op_sel_hi:[1,0] neg_lo:[0,1] neg_hi:[0,1]\n"
"v_pk_add_f32 v[44:45], v[44:45], v[16:17] op_sel_hi:[1,0] neg_lo:[0,1] neg_hi:[0,1]\n"
"v_pk_mul_f32 v[18:19], v[26:27], v[26:27]\n"
"v_pk_mul_f32 v[20:21], v[28:29], v[28:29]\n"
"v_pk_fma_f32 v[18:19], v[30:31], v[30:31], v[18:19]\n"
"v_pk_fma_f32 v[20:21], v[32:33], v[32:33], v[20:21]\n"
"v_pk_fma_f32 v[18:19], v[34:35], v[34:35], v[18:19]\n"
"v_pk_fma_f32 v[20:21], v[36:37], v[36:37], v[20:21]\n"
"v_pk_fma_f32 v[18:19], v[38:39], v[38:39], v[18:19]\n"
"v_pk_fma_f32 v[20:21], v[40:41], v[40:41], v[20:21]\n"
"v_pk_fma_f32 v[18:19], v[42:43], v[42:43], v[18:19]\n"
"v_pk_fma_f32 v[20:21], v[44:45], v[44:45], v[20:21]\n"
"v_pk_add_f32 v[18:19], v[18:19], v[20:21]\n"
"v_add_f32 v18, v18, v19\n"
"s_nop 1\n"
"v_add_f32_dpp v18, v18, v18 row_ror:8 row_mask:0xf bank_mask:0xf\n"
"s_nop 1\n"
"v_add_f32_dpp v18, v18, v18 row_ror:4 row_mask:0xf bank_mask:0xf\n"
"s_nop 1\n"
"v_add_f32_dpp v18, v18, v18 row_ror:2 row_mask:0xf bank_mask:0xf\n"
"s_nop 1\n"
"v_add_f32_dpp v18, v18, v18 row_ror:1 row_mask:0xf bank_mask:0xf\n"
"v_mov_b32 v19, v18\n"
"s_nop 1\n"
"v_permlane16_swap_b32 v18, v19\n"
"v_add_f32 v18, v18, v19\n"
"v_mov_b32 v16, 0x3727c5ac\n"
"v_fmac_f32 v16, 0x3acccccd, v18\n"
"v_rsq_f32 v16, v16\n"
"s_nop 0\n"
"v_pk_mul_f32 v[26:27], v[26:27], v[16:17] op_sel_hi:[1,0]\n"
"v_pk_mul_f32 v[28:29], v[28:29], v[16:17] op_sel_hi:[1,0]\n"
"v_pk_mul_f32 v[30:31], v[30:31], v[16:17] op_sel_hi:[1,0]\n"
"v_pk_mul_f32 v[32:33], v[32:33], v[16:17] op_sel_hi:[1,0]\n"
"v_pk_mul_f32 v[34:35], v[34:35], v[16:17] op_sel_hi:[1,0]\n"
"v_pk_mul_f32 v[36:37], v[36:37], v[16:17] op_sel_hi:[1,0]\n"
"v_pk_mul_f32 v[38:39], v[38:39], v[16:17] op_sel_hi:[1,0]\n"
"v_pk_mul_f32 v[40:41], v[40:41], v[16:17] op_sel_hi:[1,0]\n"
"v_pk_mul_f32 v[42:43], v[42:43], v[16:17] op_sel_hi:[1,0]\n"
"v_pk_mul_f32 v[44:45], v[44:45], v[16:17] op_sel_hi:[1,0]\n"
"v_cvt_pk_f16_f32 v16, v26, v27\n"
"v_cvt_pk_f16_f32 v17, v28, v29\n"
"v_cvt_pk_f16_f32 v18, v30, v31\n"
"v_cvt_pk_f16_f32 v19, v32, v33\n"
"v_cvt_pk_f16_f32 v20, v34, v35\n"
"v_cvt_pk_f16_f32 v21, v36, v37\n"
"v_cvt_pk_f16_f32 v22, v38, v39\n"
"v_cvt_pk_f16_f32 v23, v40, v41\n"
"v_cvt_pk_f16_f32 v24, v42, v43\n"
"v_cvt_pk_f16_f32 v25, v44, v45\n"
"ds_write_b64 v14, v[16:17] offset:0\n"
"ds_write_b64 v14, v[18:19] offset:2048\n"
"ds_write_b64 v14, v[20:21] offset:4096\n"
"ds_write_b64 v14, v[22:23] offset:6144\n"
"ds_write_b64 v14, v[24:25] offset:8192\n"
"s_cmp_lg_u32 s66, 0\n"
"s_cbranch_scc1 L_nb0_%=\n"
"global_store_dwordx4 v12, v[8:11], %0 sc1\n"
"L_nb0_%=:\n"
"s_waitcnt lgkmcnt(0)\n"
"s_barrier\n"
"s_cmp_eq_u32 %17, 3\n"
"s_cbranch_scc1 L_t3a_%=\n"
"ds_read_b128 v[20:23], v5 offset:0\n"
"ds_read_b128 v[24:27], v5 offset:3072\n"
"ds_read_b128 v[28:31], v5 offset:6144\n"
"ds_read_b128 v[32:35], v5 offset:9216\n"
"s_waitcnt vmcnt(0) lgkmcnt(3)\n"
"buffer_store_dwordx4 v[20:23], v15, s[44:47], s64 offen sc1\n"
"s_add_u32 s64, s64, 24576\n"
"s_waitcnt lgkmcnt(2)\n"
"buffer_store_dwordx4 v[24:27], v15, s[44:47], s64 offen sc1\n"
"s_add_u32 s64, s64, 24576\n"
"s_waitcnt lgkmcnt(1)\n"
"buffer_store_dwordx4 v[28:31], v15, s[44:47], s64 offen sc1\n"
"s_add_u32 s64, s64, 24576\n"
"s_waitcnt lgkmcnt(0)\n"
"s_cmp_eq_u32 %17, 2\n"
"s_cselect_b32 s64, s64, 134217728\n"
"buffer_store_dwordx4 v[32:35], v15, s[44:47], s64 offen sc1\n"
"s_add_u32 s64, s64, 24576\n"
"s_branch L_t3c_%=\n"
"L_t3a_%=:\n"
"v_and_b32 v92, 12, v1\n"
"s_mul_i32 s62, %6, 205\n"
"s_lshr_b32 s62, s62, 13\n"
"s_mul_i32 s63, s62, 40\n"
"s_sub_u32 s63, %6, s63\n"
"s_lshr_b32 s67, 0x12, s62\n"
"s_and_b32 s67, s67, 1\n"
"s_cselect_b32 s67, 1.0, 0\n"
"s_cmp_lt_u32 s62, 2\n"
"s_cselect_b32 s35, 0x3e0293ee, 1.0\n"
"s_cselect_b32 s34, 1, 0\n"
"v_mad_u32_u24 v93, v92, s34, v1\n"
"v_lshlrev_b32 v93, 2, v93\n"
"s_and_b32 s56, s63, s34\n"
"s_mul_i32 s56, s56, 48\n"
"s_lshl_b32 s57, s63, 6\n"
"s_sub_u32 s56, s57, s56\n"
"s_lshl_b32 s57, s62, 3\n"
"s_mov_b32 s68, 0xff20000a\n"
"s_mov_b32 s69, 0xffff3040\n"
"s_lshr_b64 s[68:69], s[68:69], s57\n"
"s_and_b32 s68, s68, 0xff\n"
"s_cmp_eq_u32 s68, 0xff\n"
"s_cselect_b32 s68, 524288, s68\n"
"s_lshl_b32 s68, s68, 8\n"
"s_add_u32 s56, s56, s68\n"
"s_mul_i32 s57, s63, 52\n"
"s_lshr_b32 s57, s57, 8\n"
"s_mul_i32 s58, s57, 5\n"
"s_sub_u32 s58, s63, s58\n"
"s_lshl_b32 s63, s57, 6\n"
"s_lshr_b32 s65, s58, 1\n"
"s_lshl_b32 s65, s65, 5\n"
"s_add_u32 s63, s63, s65\n"
"s_and_b32 s65, s58, 1\n"
"s_lshl_b32 s65, s65, 2\n"
"s_add_u32 s63, s63, s65\n"
"s_lshl_b32 s65, s57, 4\n"
"s_add_u32 s65, s65, 512\n"
"s_cmp_lt_u32 s58, 4\n"
"s_cselect_b32 s63, s63, s65\n"
"s_cselect_b32 s65, 1, 0\n"
"v_mad_u32_u24 v92, v92, s65, v1\n"
"v_lshlrev_b32 v92, 1, v92\n"
"s_lshl_b32 s63, s63, 1\n"
"s_cmp_eq_u32 s62, 3\n"
"s_cselect_b32 s57, 21760, 0\n"
"s_cmp_eq_u32 s62, 4\n"
"s_cselect_b32 s57, 20480, s57\n"
"s_cmp_eq_u32 s57, 0\n"
"s_cselect_b32 s57, 134217728, s57\n"
"s_add_u32 s57, s57, s63\n"
"v_lshlrev_b32 v95, 2, v1\n"
"v_lshl_add_u32 v95, v2, 8, v95\n"
"s_cmp_ge_u32 %6, 240\n"
"s_cbranch_scc1 L_f7_%=\n"
"s_waitcnt vmcnt(0)\n"
"L_t3c_%=:\n"
"s_cmp_ge_u32 %6, 240\n"
"s_cbranch_scc1 L_f1a_%=\n"
"v_lshlrev_b32 v46, 16, v46\n"
"v_lshlrev_b32 v48, 16, v48\n"
"v_lshlrev_b32 v50, 16, v50\n"
"v_lshlrev_b32 v52, 16, v52\n"
"v_lshlrev_b32 v54, 16, v54\n"
"v_mul_f32 v47, 0xbfb8aa3b, v46\n"
"v_mul_f32 v49, 0xbfb8aa3b, v48\n"
"v_mul_f32 v51, 0xbfb8aa3b, v50\n"
"v_mul_f32 v53, 0xbfb8aa3b, v52\n"
"v_mul_f32 v55, 0xbfb8aa3b, v54\n"
"v_exp_f32 v47, v47\n"
"v_exp_f32 v49, v49\n"
"v_exp_f32 v51, v51\n"
"v_exp_f32 v53, v53\n"
"v_exp_f32 v55, v55\n"
"v_add_f32 v47, 1.0, v47\n"
"v_add_f32 v49, 1.0, v49\n"
"v_add_f32 v51, 1.0, v51\n"
"v_add_f32 v53, 1.0, v53\n"
"v_add_f32 v55, 1.0, v55\n"
"v_rcp_f32 v47, v47\n"
"v_rcp_f32 v49, v49\n"
"v_rcp_f32 v51, v51\n"
"v_rcp_f32 v53, v53\n"
"v_rcp_f32 v55, v55\n"
"v_mul_f32 v46, v46, v47\n"
"v_mul_f32 v48, v48, v49\n"
"v_mul_f32 v50, v50, v51\n"
"v_mul_f32 v52, v52, v53\n"
"v_mul_f32 v54, v54, v55\n"
"v_cvt_pk_bf16_f32 v46, v46, v46\n"
"v_cvt_pk_bf16_f32 v48, v48, v48\n"
"v_cvt_pk_bf16_f32 v50, v50, v50\n"
"v_cvt_pk_bf16_f32 v52, v52, v52\n"
"v_cvt_pk_bf16_f32 v54, v54, v54\n"
"v_lshlrev_b32 v46, 16, v46\n"
"v_lshlrev_b32 v48, 16, v48\n"
"v_lshlrev_b32 v50, 16, v50\n"
"v_lshlrev_b32 v52, 16, v52\n"
"v_lshlrev_b32 v54, 16, v54\n"
"L_f1a_%=:\n"
"s_cmp_lg_u32 %17, 0\n"
"s_cbranch_scc1 L_f1_%=\n"
"s_cmp_eq_u32 s66, 3\n"
"s_cbranch_scc1 L_f1_%=\n"
"v_mov_b32 v103, 0\n"
"v_mov_b32 v101, 1\n"
"s_mov_b64 exec, 8\n"
"global_atomic_add v101, v13, v103, %0 sc0\n"
"s_mov_b64 exec, -1\n"
"L_f1_%=:\n"
"s_cmp_ge_u32 %6, 240\n"
"s_cbranch_scc1 L_f7_%=\n"
"v_lshlrev_b32 v76, 16, v56\n"
"v_and_b32 v77, 0xffff0000, v56\n"
"v_lshlrev_b32 v78, 16, v57\n"
"v_and_b32 v79, 0xffff0000, v57\n"
"v_lshlrev_b32 v80, 16, v58\n"
"v_and_b32 v81, 0xffff0000, v58\n"
"v_lshlrev_b32 v82, 16, v59\n"
"v_and_b32 v83, 0xffff0000, v59\n"
"v_pk_mul_f32 v[84:85], v[76:77], v[46:47] op_sel_hi:[1,0]\n"
"v_pk_mul_f32 v[86:87], v[78:79], v[46:47] op_sel_hi:[1,0]\n"
"v_pk_mul_f32 v[88:89], v[80:81], v[46:47] op_sel_hi:[1,0]\n"
"v_pk_mul_f32 v[90:91], v[82:83], v[46:47] op_sel_hi:[1,0]\n"
"v_lshlrev_b32 v76, 16, v60\n"
"v_and_b32 v77, 0xffff0000, v60\n"
"v_lshlrev_b32 v78, 16, v61\n"
"v_and_b32 v79, 0xffff0000, v61\n"
"v_lshlrev_b32 v80, 16, v62\n"
"v_and_b32 v81, 0xffff0000, v62\n"
"v_lshlrev_b32 v82, 16, v63\n"
"v_and_b32 v83, 0xffff0000, v63\n"
"v_pk_fma_f32 v[84:85], v[76:77], v[48:49], v[84:85] op_sel_hi:[1,0,1]\n"
"v_pk_fma_f32 v[86:87], v[78:79], v[48:49], v[86:87] op_sel_hi:[1,0,1]\n"
"v_pk_fma_f32 v[88:89], v[80:81], v[48:49], v[88:89] op_sel_hi:[1,0,1]\n"
"v_pk_fma_f32 v[90:91], v[82:83], v[48:49], v[90:91] op_sel_hi:[1,0,1]\n"
"v_lshlrev_b32 v76, 16, v64\n"
"v_and_b32 v77, 0xffff0000, v64\n"
"v_lshlrev_b32 v78, 16, v65\n"
"v_and_b32 v79, 0xffff0000, v65\n"
"v_lshlrev_b32 v80, 16, v66\n"
"v_and_b32 v81, 0xffff0000, v66\n"
"v_lshlrev_b32 v82, 16, v67\n"
"v_and_b32 v83, 0xffff0000, v67\n"
"v_pk_fma_f32 v[84:85], v[76:77], v[50:51], v[84:85] op_sel_hi:[1,0,1]\n"
"v_pk_fma_f32 v[86:87], v[78:79], v[50:51], v[86:87] op_sel_hi:[1,0,1]\n"
"v_pk_fma_f32 v[88:89], v[80:81], v[50:51], v[88:89] op_sel_hi:[1,0,1]\n"
"v_pk_fma_f32 v[90:91], v[82:83], v[50:51], v[90:91] op_sel_hi:[1,0,1]\n"
"v_lshlrev_b32 v76, 16, v68\n"
"v_and_b32 v77, 0xffff0000, v68\n"
"v_lshlrev_b32 v78, 16, v69\n"
"v_and_b32 v79, 0xffff0000, v69\n"
"v_lshlrev_b32 v80, 16, v70\n"
"v_and_b32 v81, 0xffff0000, v70\n"
"v_lshlrev_b32 v82, 16, v71\n"
"v_and_b32 v83, 0xffff0000, v71\n"
"v_pk_fma_f32 v[84:85], v[76:77], v[52:53], v[84:85] op_sel_hi:[1,0,1]\n"
"v_pk_fma_f32 v[86:87], v[78:79], v[52:53], v[86:87] op_sel_hi:[1,0,1]\n"
"v_pk_fma_f32 v[88:89], v[80:81], v[52:53], v[88:89] op_sel_hi:[1,0,1]\n"
"v_pk_fma_f32 v[90:91], v[82:83], v[52:53], v[90:91] op_sel_hi:[1,0,1]\n"
"v_lshlrev_b32 v76, 16, v72\n"
"v_and_b32 v77, 0xffff0000, v72\n"
"v_lshlrev_b32 v78, 16, v73\n"
"v_and_b32 v79, 0xffff0000, v73\n"
"v_lshlrev_b32 v80, 16, v74\n"
"v_and_b32 v81, 0xffff0000, v74\n"
"v_lshlrev_b32 v82, 16, v75\n"
"v_and_b32 v83, 0xffff0000, v75\n"
"v_pk_fma_f32 v[84:85], v[76:77], v[54:55], v[84:85] op_sel_hi:[1,0,1]\n"
"v_pk_fma_f32 v[86:87], v[78:79], v[54:55], v[86:87] op_sel_hi:[1,0,1]\n"
"v_pk_fma_f32 v[88:89], v[80:81], v[54:55], v[88:89] op_sel_hi:[1,0,1]\n"
"v_pk_fma_f32 v[90:91], v[82:83], v[54:55], v[90:91] op_sel_hi:[1,0,1]\n"
"s_nop 0\n"
"v_add_f32_dpp v84, v84, v84 row_ror:8 row_mask:0xf bank_mask:0xf\n"
"v_add_f32_dpp v85, v85, v85 row_ror:8 row_mask:0xf bank_mask:0xf\n"
"v_add_f32_dpp v86, v86, v86 row_ror:8 row_mask:0xf bank_mask:0xf\n"
"v_add_f32_dpp v87, v87, v87 row_ror:8 row_mask:0xf bank_mask:0xf\n"
"v_add_f32_dpp v88, v88, v88 row_ror:8 row_mask:0xf bank_mask:0xf\n"
"v_add_f32_dpp v89, v89, v89 row_ror:8 row_mask:0xf bank_mask:0xf\n"
"v_add_f32_dpp v90, v90, v90 row_ror:8 row_mask:0xf bank_mask:0xf\n"
"v_add_f32_dpp v91, v91, v91 row_ror:8 row_mask:0xf bank_mask:0xf\n"
"v_add_f32_dpp v84, v84, v84 row_ror:4 row_mask:0xf bank_mask:0xf\n"
"v_add_f32_dpp v85, v85, v85 row_ror:4 row_mask:0xf bank_mask:0xf\n"
"v_add_f32_dpp v86, v86, v86 row_ror:4 row_mask:0xf bank_mask:0xf\n"
"v_add_f32_dpp v87, v87, v87 row_ror:4 row_mask:0xf bank_mask:0xf\n"
"v_add_f32_dpp v88, v88, v88 row_ror:4 row_mask:0xf bank_mask:0xf\n"
"v_add_f32_dpp v89, v89, v89 row_ror:4 row_mask:0xf bank_mask:0xf\n"
"v_add_f32_dpp v90, v90, v90 row_ror:4 row_mask:0xf bank_mask:0xf\n"
"v_add_f32_dpp v91, v91, v91 row_ror:4 row_mask:0xf bank_mask:0xf\n"
"v_add_f32_dpp v84, v84, v84 row_ror:2 row_mask:0xf bank_mask:0xf\n"
"v_add_f32_dpp v85, v85, v85 row_ror:2 row_mask:0xf bank_mask:0xf\n"
"v_add_f32_dpp v86, v86, v86 row_ror:2 row_mask:0xf bank_mask:0xf\n"
"v_add_f32_dpp v87, v87, v87 row_ror:2 row_mask:0xf bank_mask:0xf\n"
"v_add_f32_dpp v88, v88, v88 row_ror:2 row_mask:0xf bank_mask:0xf\n"
"v_add_f32_dpp v89, v89, v89 row_ror:2 row_mask:0xf bank_mask:0xf\n"
"v_add_f32_dpp v90, v90, v90 row_ror:2 row_mask:0xf bank_mask:0xf\n"
"v_add_f32_dpp v91, v91, v91 row_ror:2 row_mask:0xf bank_mask:0xf\n"
"s_mov_b32 s64, 0x30003\n"
"s_mov_b32 exec_lo, s64\n"
"s_mov_b32 exec_hi, s64\n"
"ds_write_b128 v94, v[84:87] offset:12288\n"
"ds_write_b128 v94, v[88:91] offset:12304\n"
"s_mov_b64 exec, -1\n"
"L_f7_%=:\n"
"s_cmp_lg_u32 s66, 0\n"
"s_cbranch_scc1 L_nb1_%=\n"
"global_store_dwordx4 v12, v[8:11], %0 sc1\n"
"L_nb1_%=:\n"
"s_cmp_lg_u32 %17, 3\n"
"s_cbranch_scc1 L_t3b_%=\n"
"s_lshl_b32 s64, %5, 13\n"
"v_and_b32 v103, 1, v0\n"
"v_lshrrev_b32 v102, 1, v0\n"
"v_lshlrev_b32 v102, 16, v102\n"
"v_lshl_add_u32 v102, v103, 7, v102\n"
"buffer_load_dword v108, v102, s[52:55], s64 offen\n"
"s_add_u32 s64, s64, 2097152\n"
"s_branch L_f3_%=\n"
"L_t3b_%=:\n"
"s_waitcnt vmcnt(0)\n"
"s_cmp_lg_u32 %17, 0\n"
"s_cbranch_scc1 L_f3_%=\n"
"s_cmp_eq_u32 s66, 3\n"
"s_cbranch_scc1 L_f3_%=\n"
"s_mov_b32 s65, 200000\n"
"L_al_%=:\n"
"s_waitcnt vmcnt(0)\n"
"v_cmp_eq_u32 vcc, 0, v101\n"
"s_cbranch_vccz L_f3_%=\n"
"s_sleep 2\n"
"s_mov_b64 exec, 8\n"
"global_atomic_add v101, v13, v103, %0 sc0\n"
"s_mov_b64 exec, -1\n"
"s_sub_u32 s65, s65, 1\n"
"s_cbranch_scc0 L_al_%=\n"
"s_waitcnt vmcnt(0)\n"
"L_f3_%=:\n"
"s_waitcnt lgkmcnt(0)\n"
"s_barrier\n"
"s_cmp_lg_u32 %17, 0\n"
"s_cbranch_scc1 L_f6_%=\n"
"s_mov_b64 exec, 15\n"
"global_atomic_add v96, v8, %0\n"
"s_mov_b64 exec, -1\n"
"L_f6_%=:\n"
"s_cmp_lg_u32 %17, 3\n"
"s_cbranch_scc1 L_f5_%=\n"
"ds_read_b32 v40, v95 offset:12288\n"
"ds_read_b32 v41, v95 offset:12352\n"
"ds_read_b32 v42, v95 offset:12416\n"
"ds_read_b32 v43, v95 offset:12480\n"
"s_waitcnt lgkmcnt(0)\n"
"v_add_f32 v40, v40, v41\n"
"v_add_f32 v42, v42, v43\n"
"v_add_f32 v40, v40, v42\n"
"v_mov_b32 v41, v40\n"
"s_nop 1\n"
"v_permlane16_swap_b32 v40, v41\n"
"v_add_f32 v40, v40, v41\n"
"v_mov_b32 v41, v40\n"
"s_nop 1\n"
"v_permlane32_swap_b32 v40, v41\n"
"v_add_f32 v40, v40, v41\n"
"v_cvt_pk_bf16_f32 v40, v40, v40\n"
"v_lshlrev_b32 v40, 16, v40\n"
"v_add_f32 v40, s67, v40\n"
"v_cvt_pk_bf16_f32 v41, v40, v40\n"
"v_lshlrev_b32 v40, 16, v41\n"
"v_mul_f32 v40, s35, v40\n"
"v_or_b32 v40, s34, v40\n"
"s_mov_b64 exec, 0xffff\n"
"buffer_store_dword v40, v93, s[44:47], s56 offen sc1\n"
"buffer_store_short v41, v92, s[44:47], s57 offen sc1\n"
"s_mov_b64 exec, -1\n"
"L_f5_%=:\n"
"s_add_u32 m0, s60, 98304\n"
"s_mov_b32 s64, 0\n"
"buffer_load_dwordx4 v230, s[52:55], s64 offen offset:0 lds\n"
"s_mov_b32 s64, 31744\n"
"buffer_load_dwordx4 v231, s[52:55], s64 offen offset:1024 lds\n"
"s_mov_b32 s64, 63488\n"
"buffer_load_dwordx4 v230, s[52:55], s64 offen offset:2048 lds\n"
"s_mov_b32 s64, 95232\n"
"buffer_load_dwordx4 v231, s[52:55], s64 offen offset:3072 lds\n"
"s_add_u32 m0, s60, 114688\n"
"s_mov_b32 s64, 524288\n"
"buffer_load_dwordx4 v230, s[52:55], s64 offen offset:0 lds\n"
"s_mov_b32 s64, 556032\n"
"buffer_load_dwordx4 v231, s[52:55], s64 offen offset:1024 lds\n"
"s_mov_b32 s64, 587776\n"
"buffer_load_dwordx4 v230, s[52:55], s64 offen offset:2048 lds\n"
"s_mov_b32 s64, 619520\n"
"buffer_load_dwordx4 v231, s[52:55], s64 offen offset:3072 lds\n"
"v_mov_b32 v104, 0\n"
"v_mov_b32 v105, 0\n"
"v_mov_b32 v106, 0\n"
"v_mov_b32 v107, 0\n"
"v_and_b32 v1, 15, v0\n"
"v_lshrrev_b32 v2, 4, v0\n"
"s_lshl_b32 s65, %17, 4\n"
"v_add_u32 v14, s65, v2\n"
"v_lshlrev_b32 v15, 1, v2\n"
"v_xor_b32 v15, v15, v1\n"
"v_lshlrev_b32 v15, 4, v15\n"
"v_lshl_add_u32 v248, v14, 8, v15\n"
"v_xor_b32 v249, 0x80, v248\n"
"v_xor_b32 v5, v1, v2\n"
"v_lshlrev_b32 v5, 4, v5\n"
"v_lshl_add_u32 v244, v14, 8, v5\n"
"v_xor_b32 v245, 64, v244\n"
"v_xor_b32 v246, 128, v244\n"
"v_xor_b32 v247, 192, v244\n"
"v_and_b32 v5, 1, v2\n"
"v_lshlrev_b32 v5, 1, v5\n"
"v_lshrrev_b32 v6, 1, v2\n"
"v_or_b32 v5, v5, v6\n"
"v_xor_b32 v5, v5, v1\n"
"v_lshlrev_b32 v5, 4, v5\n"
"v_lshl_add_u32 v232, v1, 8, v5\n"
"v_xor_b32 v233, 64, v232\n"
"v_xor_b32 v234, 128, v232\n"
"v_xor_b32 v235, 192, v232\n"
"v_lshrrev_b32 v5, 2, v0\n"
"v_and_b32 v6, 7, v5\n"
"v_lshlrev_b32 v6, 5, v6\n"
"v_lshl_add_u32 v6, v5, 8, v6\n"
"v_and_b32 v7, 3, v0\n"
"v_lshl_add_u32 v6, v7, 3, v6\n"
"v_add_u32 v236, 65536, v6\n"
"v_xor_b32 v237, 32, v236\n"
"v_xor_b32 v238, 64, v236\n"
"v_xor_b32 v239, 96, v236\n"
"v_xor_b32 v240, 128, v236\n"
"v_xor_b32 v241, 160, v236\n"
"v_xor_b32 v242, 192, v236\n"
"v_xor_b32 v243, 224, v236\n"
"v_lshlrev_b32 v5, 5, v2\n"
"v_add_u32 v229, 131072, v5\n"
"v_and_b32 v5, 1, v0\n"
"v_mov_b32 v6, 0xaaaaaaaa\n"
"v_mul_lo_u32 v5, v5, v6\n"
"v_or_b32 v250, 0x44444444, v5\n"
"v_mov_b32 v6, 0x3f803f80\n"
"v_accvgpr_write_b32 a208, v6\n"
"v_accvgpr_write_b32 a209, v6\n"
"v_accvgpr_write_b32 a210, v6\n"
"v_accvgpr_write_b32 a211, v6\n"
"v_mfma_f32_32x32x16_bf16 a[0:15], v[104:107], v[104:107], 0\n"
"L_w1_%=:\n"
"s_cbranch_execz L_wd0_%=\n"
"v_mfma_f32_32x32x16_bf16 a[16:31], v[104:107], v[104:107], 0\n"
"v_mfma_f32_32x32x16_bf16 a[32:47], v[104:107], v[104:107], 0\n"
"v_mfma_f32_32x32x16_bf16 a[48:63], v[104:107], v[104:107], 0\n"
"v_mfma_f32_32x32x16_bf16 a[64:79], v[104:107], v[104:107], 0\n"
"v_mfma_f32_32x32x16_bf16 a[80:95], v[104:107], v[104:107], 0\n"
"v_mfma_f32_32x32x16_bf16 a[96:111], v[104:107], v[104:107], 0\n"
"v_mfma_f32_32x32x16_bf16 a[112:127], v[104:107], v[104:107], 0\n"
"v_mfma_f32_32x32x16_bf16 a[192:207], v[104:107], v[104:107], 0\n"
"s_mov_b32 s67, 0x42200000\n"
"s_mov_b32 s35, 1\n"
"s_lshl_b32 s64, %17, 10\n"
"v_add_u32 v97, s64, v228\n"
"v_add_u32 v98, 131072, v97\n"
"v_add_u32 v99, 4096, v228\n"
"v_add_u32 v100, 131072, v99\n"
"s_cmp_eq_u32 s66, 0\n"
"s_cbranch_scc1 L_g0_%=\n"
"s_cmp_eq_u32 %17, 1\n"
"s_cbranch_scc1 L_g1_%=\n"
"s_cmp_ge_u32 %17, 2\n"
"s_cbranch_scc1 L_g2_%=\n"
"L_g0_%=:\n"
"v_add_u32 v6, %5, v0\n"
"v_and_b32 v6, 7, v6\n"
"s_and_b32 s64, %4, 3\n"
"s_lshl_b32 s64, s64, 7\n"
"s_add_u32 s64, s64, 622592\n"
"v_lshl_add_u32 v6, v6, 9, s64\n"
"v_mov_b32 v16, 0\n"
"s_cmp_eq_u32 s66, 0\n"
"s_cselect_b32 s64, 0xff, 1\n"
"s_mov_b32 s65, 200000\n"
"L_gl_%=:\n"
"s_mov_b64 exec, -1\n"
"s_and_b32 s68, s65, 1\n"
"s_cmp_lg_u32 s68, 1\n"
"s_cbranch_scc1 L_gb_%=\n"
"s_cmp_lg_u32 s66, 0\n"
"s_cbranch_scc1 L_nb2_%=\n"
"global_store_dwordx4 v12, v[8:11], %0 sc1\n"
"L_nb2_%=:\n"
"L_gb_%=:\n"
"s_mov_b32 exec_lo, s64\n"
"s_mov_b32 exec_hi, 0\n"
"global_atomic_add v7, v6, v16, %0 sc0\n"
"s_waitcnt vmcnt(0)\n"
"v_cmp_gt_u32 vcc, 32, v7\n"
"s_cbranch_vccz L_gd_%=\n"
"s_sub_u32 s65, s65, 1\n"
"s_cbranch_scc0 L_gl_%=\n"
"L_gd_%=:\n"
"s_mov_b64 exec, -1\n"
"s_branch L_g9_%=\n"
"L_g1_%=:\n"
"s_load_dwordx4 s[48:51], %16, 0x40\n"
"s_load_dwordx4 s[56:59], %16, 0x50\n"
"s_lshl_b32 s64, %6, 2\n"
"v_add_u32 v20, s64, v2\n"
"v_and_b32 v21, 7, v20\n"
"v_lshrrev_b32 v22, 3, v20\n"
"v_mul_u32_u24 v30, 40960, v22\n"
"v_lshl_add_u32 v30, v1, 5, v30\n"
"v_mov_b32 v23, v22\n"
"v_mov_b32 v24, v1\n"
"v_mov_b32 v25, 32\n"
"v_mov_b32 v26, 25165824\n"
"v_mul_u32_u24 v32, 205, v22\n"
"v_lshrrev_b32 v32, 10, v32\n"
"v_mul_u32_u24 v33, 5, v32\n"
"v_sub_u32 v33, v22, v33\n"
"v_subrev_u32 v34, 10, v1\n"
"v_mad_u32_u24 v35, v32, 3, v34\n"
"v_min_u32 v36, 12, v32\n"
"v_mad_u32_u24 v36, v32, 6, v36\n"
"v_lshlrev_b32 v36, 4, v36\n"
"v_lshl_add_u32 v36, v34, 5, v36\n"
"v_subrev_u32 v37, 48, v35\n"
"v_mul_u32_u24 v37, 224, v37\n"
"v_add_u32 v37, 96, v37\n"
"v_cmp_lt_u32 vcc, 15, v32\n"
"v_mov_b32 v38, 112\n"
"s_nop 0\n"
"v_cndmask_b32 v36, v36, v37, vcc\n"
"v_cndmask_b32 v38, 0, v38, vcc\n"
"v_mul_u32_u24 v36, 1280, v36\n"
"v_lshl_add_u32 v36, v33, 5, v36\n"
"v_cmp_lt_u32 vcc, 9, v1\n"
"v_mov_b32 v39, 14\n"
"v_mov_b32 v40, 37748736\n"
"v_cndmask_b32 v30, v30, v36, vcc\n"
"v_cndmask_b32 v23, v23, v35, vcc\n"
"v_cndmask_b32 v24, v24, v33, vcc\n"
"v_cndmask_b32 v25, v25, v39, vcc\n"
"v_cndmask_b32 v26, v26, v40, vcc\n"
"v_cndmask_b32 v27, 0, v38, vcc\n"
"s_waitcnt lgkmcnt(0)\n"
"v_mov_b32 v28, s48\n"
"v_mov_b32 v29, s49\n"
"v_mov_b32 v39, s58\n"
"v_mov_b32 v40, s59\n"
"v_cndmask_b32 v28, v28, v39, vcc\n"
"v_cndmask_b32 v29, v29, v40, vcc\n"
"v_mul_u32_u24 v31, 160, v21\n"
"v_add_u32 v30, v30, v31\n"
"v_and_b32 v31, 3, v23\n"
"v_lshl_add_u32 v31, v21, 2, v31\n"
"v_lshrrev_b32 v23, 2, v23\n"
"v_mad_u32_u24 v31, v31, v25, v23\n"
"v_mad_u32_u24 v31, v31, 5, v24\n"
"v_lshl_add_u32 v31, v31, 10, v26\n"
"v_mov_b32 v25, 1280\n"
"v_lshrrev_b32 v32, 2, v20\n"
"v_and_b32 v33, 3, v20\n"
"v_subrev_u32 v34, 5, v1\n"
"v_lshrrev_b32 v35, 1, v34\n"
"v_mul_u32_u24 v35, 80, v35\n"
"v_and_b32 v36, 1, v34\n"
"v_lshl_add_u32 v35, v36, 5, v35\n"
"v_lshl_add_u32 v36, v33, 2, v34\n"
"v_cmp_eq_u32 vcc, 4, v34\n"
"v_add_u32 v37, 16, v33\n"
"v_mov_b32 v38, 80\n"
"v_mov_b32 v39, 64\n"
"v_cndmask_b32 v35, v35, v39, vcc\n"
"v_cndmask_b32 v36, v36, v37, vcc\n"
"v_cndmask_b32 v38, 0, v38, vcc\n"
"v_mul_u32_u24 v37, 160, v33\n"
"v_add_u32 v35, v35, v37\n"
"v_mul_u32_u24 v35, 3456, v35\n"
"v_lshrrev_b32 v37, 1, v32\n"
"v_lshl_add_u32 v35, v37, 5, v35\n"
"v_mad_u32_u24 v36, v32, 21, v36\n"
"v_lshlrev_b32 v36, 10, v36\n"
"v_add_u32 v36, 31457280, v36\n"
"v_and_b32 v37, 1, v32\n"
"v_cmp_eq_u32 vcc, 1, v37\n"
"v_mov_b32 v39, s50\n"
"v_mov_b32 v40, s51\n"
"v_mov_b32 v41, s56\n"
"v_mov_b32 v42, s57\n"
"v_cndmask_b32 v39, v39, v41, vcc\n"
"v_cndmask_b32 v40, v40, v42, vcc\n"
"v_cmp_gt_u32 vcc, 5, v34\n"
"v_mov_b32 v41, 3456\n"
"v_mov_b32 v42, 864\n"
"v_mov_b32 v43, 1024\n"
"v_cndmask_b32 v30, v30, v35, vcc\n"
"v_cndmask_b32 v31, v31, v36, vcc\n"
"v_cndmask_b32 v27, v27, v38, vcc\n"
"v_cndmask_b32 v28, v28, v39, vcc\n"
"v_cndmask_b32 v29, v29, v40, vcc\n"
"v_cndmask_b32 v25, v25, v41, vcc\n"
"v_cndmask_b32 v43, v43, v42, vcc\n"
"v_cmp_lt_u32 vcc, 9, v1\n"
"v_mov_b32 v42, 720\n"
"s_nop 0\n"
"v_cndmask_b32 v43, v43, v42, vcc\n"
"v_cmp_gt_u32 vcc, 13, v1\n"
"s_nop 1\n"
"v_cndmask_b32 v43, 0, v43, vcc\n"
"v_cmp_lt_u32 vcc, v20, v43\n"
"v_mov_b32 v42, 134217728\n"
"s_nop 0\n"
"v_cndmask_b32 v30, 0, v30, vcc\n"
"v_cndmask_b32 v50, v42, v31, vcc\n"
"v_cndmask_b32 v48, 0, v25, vcc\n"
"v_add_co_u32 v44, vcc, v28, v30\n"
"s_nop 1\n"
"v_addc_co_u32 v45, vcc, 0, v29, vcc\n"
"v_and_b32 v45, 0xffff, v45\n"
"v_cmp_ne_u32 vcc, 0, v27\n"
"v_max_u32 v47, 8, v27\n"
"v_mul_u32_u24 v47, v47, v48\n"
"v_cndmask_b32_e64 v46, 4, 8, vcc\n"
"v_cndmask_b32_e64 v49, 16, 4, vcc\n"
"v_mul_u32_u24 v46, v46, v48\n"
"v_mul_u32_u24 v49, v49, v48\n"
"s_lshl_b32 s64, %18, 11\n"
"s_add_u32 s64, s64, 23068672\n"
"v_lshl_add_u32 v20, v0, 5, s64\n"
"v_cmp_gt_u32 vcc, 15, v1\n"
"v_add_u32 v21, 32, v20\n"
"s_nop 0\n"
"v_cndmask_b32 v51, v20, v21, vcc\n"
"global_store_dwordx4 v20, v[44:47], %0\n"
"global_store_dwordx4 v20, v[48:51], %0 offset:16\n"
"s_waitcnt vmcnt(0)\n"
"s_branch L_g9_%=\n"
"L_g2_%=:\n"
"s_mov_b64 exec, 0\n"
"s_cmp_eq_u32 s60, 8192\n"
"s_cbranch_scc1 L_w5_%=\n"
"s_branch L_w6_%=\n"
"L_wd1_%=:\n"
"s_mov_b64 exec, -1\n"
"s_mov_b64 exec, 0\n"
"s_cmp_eq_u32 s60, 8192\n"
"s_cbranch_scc1 L_w7_%=\n"
"s_branch L_w8_%=\n"
"L_wd2_%=:\n"
"s_mov_b64 exec, -1\n"
"L_g9_%=:\n"
"s_mov_b32 s66, 0\n"
"s_lshl_b32 %10, %5, 2\n"
"s_add_u32 %10, %10, %17\n"
"s_mul_i32 %7, %10, 81920\n"
"s_add_u32 %7, %7, 1048576\n"
"s_lshl_b32 s59, %18, 2\n"
"s_add_u32 s59, s59, %17\n"
"s_lshl_b32 s59, s59, 9\n"
"s_add_u32 s59, s59, 23068672\n"
"s_barrier\n"
"global_load_dwordx4 v[20:23], v97, %0 sc1\n"
"global_load_dwordx4 v[24:27], v99, %0 sc1\n"
"s_add_u32 s65, %7, 0\n"
"buffer_load_dwordx4 v[128:131], v228, s[44:47], s65 offen offset:0 sc1\n"
"buffer_load_dwordx4 v[132:135], v228, s[44:47], s65 offen offset:1024 sc1\n"
"buffer_load_dwordx4 v[136:139], v228, s[44:47], s65 offen offset:2048 sc1\n"
"buffer_load_dwordx4 v[140:143], v228, s[44:47], s65 offen offset:3072 sc1\n"
"s_add_u32 s65, %7, 4096\n"
"buffer_load_dwordx4 v[144:147], v228, s[44:47], s65 offen offset:0 sc1\n"
"buffer_load_dwordx4 v[148:151], v228, s[44:47], s65 offen offset:1024 sc1\n"
"buffer_load_dwordx4 v[152:155], v228, s[44:47], s65 offen offset:2048 sc1\n"
"buffer_load_dwordx4 v[156:159], v228, s[44:47], s65 offen offset:3072 sc1\n"
"s_lshl_b32 %8, %10, 19\n"
"s_lshl_b32 s64, %4, 14\n"
"s_add_u32 %8, %8, s64\n"
"s_add_u32 %8, %8, 4194304\n"
"s_and_b32 %10, %10, -2\n"
"s_lshr_b32 s64, %4, 4\n"
"s_or_b32 %10, %10, s64\n"
"s_bfe_u32 %13, %18, 131073\n"
"s_lshl_b32 %13, %13, 2\n"
"s_lshr_b32 s64, %18, 6\n"
"s_add_u32 %13, %13, s64\n"
"s_and_b32 s64, %18, 1\n"
"s_lshl_b32 %11, %13, 1\n"
"s_add_u32 %11, %11, s64\n"
"s_bfe_u32 %12, %18, 0x30003\n"
"s_lshl_b32 %14, s64, 3\n"
"s_add_u32 %14, %14, %12\n"
"s_mul_i32 %15, %14, 6\n"
"s_min_u32 s64, %14, 12\n"
"s_add_u32 %15, %15, s64\n"
"s_lshl_b32 %9, %11, 3\n"
"s_add_u32 %9, %9, %12\n"
"s_lshl_b32 %9, %9, 2\n"
"s_add_u32 %9, %9, %17\n"
"s_mul_i32 %9, %9, 2560\n"
"s_add_u32 %9, %9, 58720256\n"
"s_lshl_b32 s48, %5, 2\n"
"s_add_u32 s48, s48, %17\n"
"s_lshl_b32 s49, s48, 7\n"
"s_lshl_b32 s48, s48, 13\n"
"s_add_u32 s48, s48, 2097152\n"
"v_and_b32 v5, 1, v0\n"
"v_lshrrev_b32 v6, 1, v0\n"
"v_lshlrev_b32 v6, 18, v6\n"
"v_lshl_add_u32 v252, v5, 7, v6\n"
"v_min_u32 v253, 15, v0\n"
"v_lshlrev_b32 v253, 12, v253\n"
"v_min_u32 v254, 7, v0\n"
"v_lshlrev_b32 v254, 12, v254\n"
"s_waitcnt vmcnt(8)\n"
"v_and_b32 v7, v20, v21\n"
"v_bitop3_b32 v7, v7, v22, v23 bitop3:0x80\n"
"v_bitop3_b32 v7, v7, v24, v25 bitop3:0x80\n"
"v_bitop3_b32 v7, v7, v26, v27 bitop3:0x80\n"
"v_and_b32 v7, 1, v7\n"
"v_cmp_eq_u32 vcc, 0, v7\n"
"s_cbranch_vccz L_sd_%=\n"
"s_mov_b32 s64, 200000\n"
"L_sl_%=:\n"
"s_sleep 2\n"
"global_load_dwordx4 v[20:23], v97, %0 sc1\n"
"global_load_dwordx4 v[24:27], v99, %0 sc1\n"
"s_waitcnt vmcnt(0)\n"
"v_and_b32 v7, v20, v21\n"
"v_bitop3_b32 v7, v7, v22, v23 bitop3:0x80\n"
"v_bitop3_b32 v7, v7, v24, v25 bitop3:0x80\n"
"v_bitop3_b32 v7, v7, v26, v27 bitop3:0x80\n"
"v_and_b32 v7, 1, v7\n"
"v_cmp_eq_u32 vcc, 0, v7\n"
"s_cbranch_vccz L_sd_%=\n"
"s_sub_u32 s64, s64, 1\n"
"s_cbranch_scc0 L_sl_%=\n"
"L_sd_%=:\n"
"ds_write_b128 v98, v[20:23]\n"
"ds_write_b128 v100, v[24:27]\n"
"s_waitcnt lgkmcnt(0)\n"
"s_barrier\n"
"ds_read_b128 v[208:211], v229 offset:0\n"
"ds_read_b128 v[216:219], v229 offset:2560\n"
"ds_read_b128 v[212:215], v229 offset:16\n"
"ds_read_b128 v[220:223], v229 offset:2576\n"
"ds_read_b64_tr_b16 a[128:129], v236 offset:32768\n"
"ds_read_b64_tr_b16 a[130:131], v236 offset:36864\n"
"ds_read_b64_tr_b16 a[132:133], v237 offset:32768\n"
"ds_read_b64_tr_b16 a[134:135], v237 offset:36864\n"
"ds_read_b64_tr_b16 a[136:137], v238 offset:32768\n"
"ds_read_b64_tr_b16 a[138:139], v238 offset:36864\n"
"ds_read_b64_tr_b16 a[140:141], v239 offset:32768\n"
"ds_read_b64_tr_b16 a[142:143], v239 offset:36864\n"
"ds_read_b64_tr_b16 a[144:145], v240 offset:32768\n"
"ds_read_b64_tr_b16 a[146:147], v240 offset:36864\n"
"ds_read_b64_tr_b16 a[148:149], v241 offset:32768\n"
"ds_read_b64_tr_b16 a[150:151], v241 offset:36864\n"
"ds_read_b64_tr_b16 a[152:153], v242 offset:32768\n"
"ds_read_b64_tr_b16 a[154:155], v242 offset:36864\n"
"ds_read_b64_tr_b16 a[156:157], v243 offset:32768\n"
"ds_read_b64_tr_b16 a[158:159], v243 offset:36864\n"
"s_add_u32 s65, %7, 8192\n"
"buffer_load_dwordx4 v[160:163], v228, s[44:47], s65 offen offset:0 sc1\n"
"buffer_load_dwordx4 v[164:167], v228, s[44:47], s65 offen offset:1024 sc1\n"
"s_waitcnt vmcnt(6) lgkmcnt(0)\n"
"v_fma_mix_f32 v224, v128, v208, v216 op_sel:[0,0,0] op_sel_hi:[1,0,0]\n"
"v_fma_mix_f32 v225, v128, v209, v217 op_sel:[1,0,0] op_sel_hi:[1,0,0]\n"
"v_fma_mix_f32 v226, v132, v208, v216 op_sel:[0,0,0] op_sel_hi:[1,0,0]\n"
"v_cvt_pk_bf16_f32 v128, v224, v225\n"
"v_fma_mix_f32 v227, v132, v209, v217 op_sel:[1,0,0] op_sel_hi:[1,0,0]\n"
"v_fma_mix_f32 v224, v136, v208, v216 op_sel:[0,0,0] op_sel_hi:[1,0,0]\n"
"v_cvt_pk_bf16_f32 v132, v226, v227\n"
"v_fma_mix_f32 v225, v136, v209, v217 op_sel:[1,0,0] op_sel_hi:[1,0,0]\n"
"buffer_load_dwordx4 v[168:171], v228, s[44:47], s65 offen offset:2048 sc1\n"
"v_fma_mix_f32 v226, v140, v208, v216 op_sel:[0,0,0] op_sel_hi:[1,0,0]\n"
"v_cvt_pk_bf16_f32 v136, v224, v225\n"
"v_fma_mix_f32 v227, v140, v209, v217 op_sel:[1,0,0] op_sel_hi:[1,0,0]\n"
"v_fma_mix_f32 v224, v129, v210, v218 op_sel:[0,0,0] op_sel_hi:[1,0,0]\n"
"v_cvt_pk_bf16_f32 v140, v226, v227\n"
"v_fma_mix_f32 v225, v129, v211, v219 op_sel:[1,0,0] op_sel_hi:[1,0,0]\n"
"v_fma_mix_f32 v226, v133, v210, v218 op_sel:[0,0,0] op_sel_hi:[1,0,0]\n"
"v_cvt_pk_bf16_f32 v129, v224, v225\n"
"buffer_load_dwordx4 v[172:175], v228, s[44:47], s65 offen offset:3072 sc1\n"
"v_fma_mix_f32 v227, v133, v211, v219 op_sel:[1,0,0] op_sel_hi:[1,0,0]\n"
"v_fma_mix_f32 v224, v137, v210, v218 op_sel:[0,0,0] op_sel_hi:[1,0,0]\n"
"v_cvt_pk_bf16_f32 v133, v226, v227\n"
"v_fma_mix_f32 v225, v137, v211, v219 op_sel:[1,0,0] op_sel_hi:[1,0,0]\n"
"v_fma_mix_f32 v226, v141, v210, v218 op_sel:[0,0,0] op_sel_hi:[1,0,0]\n"
"v_cvt_pk_bf16_f32 v137, v224, v225\n"
"v_fma_mix_f32 v227, v141, v211, v219 op_sel:[1,0,0] op_sel_hi:[1,0,0]\n"
"v_fma_mix_f32 v224, v130, v212, v220 op_sel:[0,0,0] op_sel_hi:[1,0,0]\n"
"ds_read_b128 v[208:211], v229 offset:128\n"
"ds_read_b128 v[216:219], v229 offset:2688\n"
"s_add_u32 s65, %7, 12288\n"
"buffer_load_dwordx4 v[176:179], v228, s[44:47], s65 offen offset:0 sc1\n"
"v_cvt_pk_bf16_f32 v141, v226, v227\n"
"v_fma_mix_f32 v225, v130, v213, v221 op_sel:[1,0,0] op_sel_hi:[1,0,0]\n"
"v_fma_mix_f32 v226, v134, v212, v220 op_sel:[0,0,0] op_sel_hi:[1,0,0]\n"
"v_cvt_pk_bf16_f32 v130, v224, v225\n"
"v_fma_mix_f32 v227, v134, v213, v221 op_sel:[1,0,0] op_sel_hi:[1,0,0]\n"
"v_fma_mix_f32 v224, v138, v212, v220 op_sel:[0,0,0] op_sel_hi:[1,0,0]\n"
"v_cvt_pk_bf16_f32 v134, v226, v227\n"
"v_fma_mix_f32 v225, v138, v213, v221 op_sel:[1,0,0] op_sel_hi:[1,0,0]\n"
"buffer_load_dwordx4 v[180:183], v228, s[44:47], s65 offen offset:1024 sc1\n"
"v_fma_mix_f32 v226, v142, v212, v220 op_sel:[0,0,0] op_sel_hi:[1,0,0]\n"
"v_cvt_pk_bf16_f32 v138, v224, v225\n"
"v_fma_mix_f32 v227, v142, v213, v221 op_sel:[1,0,0] op_sel_hi:[1,0,0]\n"
"v_fma_mix_f32 v224, v131, v214, v222 op_sel:[0,0,0] op_sel_hi:[1,0,0]\n"
"v_cvt_pk_bf16_f32 v142, v226, v227\n"
"v_fma_mix_f32 v225, v131, v215, v223 op_sel:[1,0,0] op_sel_hi:[1,0,0]\n"
"v_fma_mix_f32 v226, v135, v214, v222 op_sel:[0,0,0] op_sel_hi:[1,0,0]\n"
"v_cvt_pk_bf16_f32 v131, v224, v225\n"
"buffer_load_dwordx4 v[184:187], v228, s[44:47], s65 offen offset:2048 sc1\n"
"v_fma_mix_f32 v227, v135, v215, v223 op_sel:[1,0,0] op_sel_hi:[1,0,0]\n"
"v_fma_mix_f32 v224, v139, v214, v222 op_sel:[0,0,0] op_sel_hi:[1,0,0]\n"
"v_cvt_pk_bf16_f32 v135, v226, v227\n"
"v_fma_mix_f32 v225, v139, v215, v223 op_sel:[1,0,0] op_sel_hi:[1,0,0]\n"
"v_fma_mix_f32 v226, v143, v214, v222 op_sel:[0,0,0] op_sel_hi:[1,0,0]\n"
"v_cvt_pk_bf16_f32 v139, v224, v225\n"
"v_fma_mix_f32 v227, v143, v215, v223 op_sel:[1,0,0] op_sel_hi:[1,0,0]\n"
"v_cvt_pk_bf16_f32 v143, v226, v227\n"
"buffer_load_dwordx4 v[188:191], v228, s[44:47], s65 offen offset:3072 sc1\n"
"s_waitcnt vmcnt(8) lgkmcnt(0)\n"
"v_mfma_f32_16x16x32_bf16 v[0:3], a[128:131], v[128:131], 0\n"
"ds_read_b128 v[212:215], v229 offset:144\n"
"v_fma_mix_f32 v224, v144, v208, v216 op_sel:[0,0,0] op_sel_hi:[1,0,0]\n"
"v_mfma_f32_16x16x32_bf16 v[32:35], a[128:131], v[132:135], 0\n"
"s_add_u32 m0, s60, 65536\n"
"s_mov_b32 s64, 1048576\n"
"ds_read_b128 v[220:223], v229 offset:2704\n"
"v_fma_mix_f32 v225, v144, v209, v217 op_sel:[1,0,0] op_sel_hi:[1,0,0]\n"
"v_mfma_f32_16x16x32_bf16 v[64:67], a[128:131], v[136:139], 0\n"
"buffer_load_dwordx4 v230, s[52:55], s64 offen offset:0 lds\n"
"v_fma_mix_f32 v226, v148, v208, v216 op_sel:[0,0,0] op_sel_hi:[1,0,0]\n"
"v_cvt_pk_bf16_f32 v144, v224, v225\n"
"v_mfma_f32_16x16x32_bf16 v[96:99], a[128:131], v[140:143], 0\n"
"ds_read_b64_tr_b16 a[160:161], v236 offset:40960\n"
"v_fma_mix_f32 v227, v148, v209, v217 op_sel:[1,0,0] op_sel_hi:[1,0,0]\n"
"v_mfma_f32_16x16x32_bf16 v[100:103], a[132:135], v[140:143], 0\n"
"ds_read_b64_tr_b16 a[162:163], v236 offset:45056\n"
"v_fma_mix_f32 v224, v152, v208, v216 op_sel:[0,0,0] op_sel_hi:[1,0,0]\n"
"v_cvt_pk_bf16_f32 v148, v226, v227\n"
"v_mfma_f32_16x16x32_bf16 v[68:71], a[132:135], v[136:139], 0\n"
"s_mov_b32 s64, 1080320\n"
"ds_read_b64_tr_b16 a[164:165], v237 offset:40960\n"
"v_fma_mix_f32 v225, v152, v209, v217 op_sel:[1,0,0] op_sel_hi:[1,0,0]\n"
"v_mfma_f32_16x16x32_bf16 v[36:39], a[132:135], v[132:135], 0\n"
"buffer_load_dwordx4 v231, s[52:55], s64 offen offset:1024 lds\n"
"v_fma_mix_f32 v226, v156, v208, v216 op_sel:[0,0,0] op_sel_hi:[1,0,0]\n"
"v_mfma_f32_16x16x32_bf16 v[4:7], a[132:135], v[128:131], 0\n"
"ds_read_b64_tr_b16 a[166:167], v237 offset:45056\n"
"v_cvt_pk_bf16_f32 v152, v224, v225\n"
"v_fma_mix_f32 v227, v156, v209, v217 op_sel:[1,0,0] op_sel_hi:[1,0,0]\n"
"v_mfma_f32_16x16x32_bf16 v[8:11], a[136:139], v[128:131], 0\n"
"ds_read_b64_tr_b16 a[168:169], v238 offset:40960\n"
"v_fma_mix_f32 v224, v145, v210, v218 op_sel:[0,0,0] op_sel_hi:[1,0,0]\n"
"v_cvt_pk_bf16_f32 v156, v226, v227\n"
"v_mfma_f32_16x16x32_bf16 v[40:43], a[136:139], v[132:135], 0\n"
"s_mov_b32 s64, 1112064\n"
"ds_read_b64_tr_b16 a[170:171], v238 offset:45056\n"
"v_mfma_f32_16x16x32_bf16 v[72:75], a[136:139], v[136:139], 0\n"
"buffer_load_dwordx4 v230, s[52:55], s64 offen offset:2048 lds\n"
"v_fma_mix_f32 v225, v145, v211, v219 op_sel:[1,0,0] op_sel_hi:[1,0,0]\n"
"v_fma_mix_f32 v226, v149, v210, v218 op_sel:[0,0,0] op_sel_hi:[1,0,0]\n"
"v_mfma_f32_16x16x32_bf16 v[104:107], a[136:139], v[140:143], 0\n"
"ds_read_b64_tr_b16 a[172:173], v239 offset:40960\n"
"v_cvt_pk_bf16_f32 v145, v224, v225\n"
"v_fma_mix_f32 v227, v149, v211, v219 op_sel:[1,0,0] op_sel_hi:[1,0,0]\n"
"v_mfma_f32_16x16x32_bf16 v[108:111], a[140:143], v[140:143], 0\n"
"ds_read_b64_tr_b16 a[174:175], v239 offset:45056\n"
"v_fma_mix_f32 v224, v153, v210, v218 op_sel:[0,0,0] op_sel_hi:[1,0,0]\n"
"v_mfma_f32_16x16x32_bf16 v[76:79], a[140:143], v[136:139], 0\n"
"s_mov_b32 s64, 1143808\n"
"ds_read_b64_tr_b16 a[176:177], v240 offset:40960\n"
"v_cvt_pk_bf16_f32 v149, v226, v227\n"
"v_mfma_f32_16x16x32_bf16 v[44:47], a[140:143], v[132:135], 0\n"
"buffer_load_dwordx4 v231, s[52:55], s64 offen offset:3072 lds\n"
"v_fma_mix_f32 v225, v153, v211, v219 op_sel:[1,0,0] op_sel_hi:[1,0,0]\n"
"v_fma_mix_f32 v226, v157, v210, v218 op_sel:[0,0,0] op_sel_hi:[1,0,0]\n"
"v_mfma_f32_16x16x32_bf16 v[12:15], a[140:143], v[128:131], 0\n"
"ds_read_b64_tr_b16 a[178:179], v240 offset:45056\n"
"v_cvt_pk_bf16_f32 v153, v224, v225\n"
"v_mfma_f32_16x16x32_bf16 v[16:19], a[144:147], v[128:131], 0\n"
"s_waitcnt lgkmcnt(10)\n"
"ds_read_b64_tr_b16 a[180:181], v241 offset:40960\n"
"v_fma_mix_f32 v227, v157, v211, v219 op_sel:[1,0,0] op_sel_hi:[1,0,0]\n"
"v_fma_mix_f32 v224, v146, v212, v220 op_sel:[0,0,0] op_sel_hi:[1,0,0]\n"
"v_mfma_f32_16x16x32_bf16 v[48:51], a[144:147], v[132:135], 0\n"
"s_add_u32 s65, %7, 16384\n"
"ds_read_b64_tr_b16 a[182:183], v241 offset:45056\n"
"v_cvt_pk_bf16_f32 v157, v226, v227\n"
"v_mfma_f32_16x16x32_bf16 v[80:83], a[144:147], v[136:139], 0\n"
"buffer_load_dwordx4 v[192:195], v228, s[44:47], s65 offen offset:0 sc1\n"
"v_fma_mix_f32 v225, v146, v213, v221 op_sel:[1,0,0] op_sel_hi:[1,0,0]\n"
"v_mfma_f32_16x16x32_bf16 v[112:115], a[144:147], v[140:143], 0\n"
"ds_read_b64_tr_b16 a[184:185], v242 offset:40960\n"
"v_fma_mix_f32 v226, v150, v212, v220 op_sel:[0,0,0] op_sel_hi:[1,0,0]\n"
"v_cvt_pk_bf16_f32 v146, v224, v225\n"
"v_mfma_f32_16x16x32_bf16 v[116:119], a[148:151], v[140:143], 0\n"
"ds_read_b64_tr_b16 a[186:187], v242 offset:45056\n"
"v_fma_mix_f32 v227, v150, v213, v221 op_sel:[1,0,0] op_sel_hi:[1,0,0]\n"
"v_fma_mix_f32 v224, v154, v212, v220 op_sel:[0,0,0] op_sel_hi:[1,0,0]\n"
"v_mfma_f32_16x16x32_bf16 v[84:87], a[148:151], v[136:139], 0\n"
"ds_read_b64_tr_b16 a[188:189], v243 offset:40960\n"
"v_cvt_pk_bf16_f32 v150, v226, v227\n"
"v_mfma_f32_16x16x32_bf16 v[52:55], a[148:151], v[132:135], 0\n"
"buffer_load_dwordx4 v[196:199], v228, s[44:47], s65 offen offset:1024 sc1\n"
"v_fma_mix_f32 v225, v154, v213, v221 op_sel:[1,0,0] op_sel_hi:[1,0,0]\n"
"v_fma_mix_f32 v226, v158, v212, v220 op_sel:[0,0,0] op_sel_hi:[1,0,0]\n"
"v_mfma_f32_16x16x32_bf16 v[20:23], a[148:151], v[128:131], 0\n"
"ds_read_b64_tr_b16 a[190:191], v243 offset:45056\n"
"v_cvt_pk_bf16_f32 v154, v224, v225\n"
"v_mfma_f32_16x16x32_bf16 v[24:27], a[152:155], v[128:131], 0\n"
"ds_read_b128 v[208:211], v229 offset:256\n"
"v_fma_mix_f32 v227, v158, v213, v221 op_sel:[1,0,0] op_sel_hi:[1,0,0]\n"
"v_mfma_f32_16x16x32_bf16 v[56:59], a[152:155], v[132:135], 0\n"
"ds_read_b128 v[216:219], v229 offset:2816\n"
"v_fma_mix_f32 v224, v147, v214, v222 op_sel:[0,0,0] op_sel_hi:[1,0,0]\n"
"v_cvt_pk_bf16_f32 v158, v226, v227\n"
"v_mfma_f32_16x16x32_bf16 v[88:91], a[152:155], v[136:139], 0\n"
"buffer_load_dwordx4 v[200:203], v228, s[44:47], s65 offen offset:2048 sc1\n"
"v_fma_mix_f32 v225, v147, v215, v223 op_sel:[1,0,0] op_sel_hi:[1,0,0]\n"
"v_mfma_f32_16x16x32_bf16 v[120:123], a[152:155], v[140:143], 0\n"
"v_fma_mix_f32 v226, v151, v214, v222 op_sel:[0,0,0] op_sel_hi:[1,0,0]\n"
"v_cvt_pk_bf16_f32 v147, v224, v225\n"
"v_mfma_f32_16x16x32_bf16 v[124:127], a[156:159], v[140:143], 0\n"
"v_fma_mix_f32 v227, v151, v215, v223 op_sel:[1,0,0] op_sel_hi:[1,0,0]\n"
"v_fma_mix_f32 v224, v155, v214, v222 op_sel:[0,0,0] op_sel_hi:[1,0,0]\n"
"v_cvt_pk_bf16_f32 v151, v226, v227\n"
"v_mfma_f32_16x16x32_bf16 v[92:95], a[156:159], v[136:139], 0\n"
"v_fma_mix_f32 v225, v155, v215, v223 op_sel:[1,0,0] op_sel_hi:[1,0,0]\n"
"v_fma_mix_f32 v226, v159, v214, v222 op_sel:[0,0,0] op_sel_hi:[1,0,0]\n"
"v_mfma_f32_16x16x32_bf16 v[60:63], a[156:159], v[132:135], 0\n"
"buffer_load_dwordx4 v[204:207], v228, s[44:47], s65 offen offset:3072 sc1\n"
"v_cvt_pk_bf16_f32 v155, v224, v225\n"
"v_mfma_f32_16x16x32_bf16 v[28:31], a[156:159], v[128:131], 0\n"
"buffer_load_dword v252, v252, s[52:55], s48 offen\n"
"v_fma_mix_f32 v227, v159, v215, v223 op_sel:[1,0,0] op_sel_hi:[1,0,0]\n"
"v_cvt_pk_bf16_f32 v159, v226, v227\n"
"s_waitcnt vmcnt(13) lgkmcnt(0)\n"
"v_mfma_f32_16x16x32_bf16 v[0:3], a[160:163], v[144:147], v[0:3]\n"
"s_barrier\n"
"v_fma_mix_f32 v224, v160, v208, v216 op_sel:[0,0,0] op_sel_hi:[1,0,0]\n"
"v_fma_mix_f32 v225, v160, v209, v217 op_sel:[1,0,0] op_sel_hi:[1,0,0]\n"
"v_mfma_f32_16x16x32_bf16 v[32:35], a[160:163], v[148:151], v[32:35]\n"
"s_add_u32 s65, %7, 20480\n"
"ds_read_b128 v[212:215], v229 offset:272\n"
"v_fma_mix_f32 v226, v164, v208, v216 op_sel:[0,0,0] op_sel_hi:[1,0,0]\n"
"v_mfma_f32_16x16x32_bf16 v[64:67], a[160:163], v[152:155], v[64:67]\n"
"buffer_load_dwordx4 v[128:131], v228, s[44:47], s65 offen offset:0 sc1\n"
"v_cvt_pk_bf16_f32 v160, v224, v225\n"
"v_fma_mix_f32 v227, v164, v209, v217 op_sel:[1,0,0] op_sel_hi:[1,0,0]\n"
"v_mfma_f32_16x16x32_bf16 v[96:99], a[160:163], v[156:159], v[96:99]\n"
"ds_read_b128 v[220:223], v229 offset:2832\n"
"v_fma_mix_f32 v224, v168, v208, v216 op_sel:[0,0,0] op_sel_hi:[1,0,0]\n"
"v_mfma_f32_16x16x32_bf16 v[100:103], a[164:167], v[156:159], v[100:103]\n"
"ds_read_b64_tr_b16 a[128:129], v236 offset:49152\n"
"v_cvt_pk_bf16_f32 v164, v226, v227\n"
"v_fma_mix_f32 v225, v168, v209, v217 op_sel:[1,0,0] op_sel_hi:[1,0,0]\n"
"v_mfma_f32_16x16x32_bf16 v[68:71], a[164:167], v[152:155], v[68:71]\n"
"ds_read_b64_tr_b16 a[130:131], v236 offset:53248\n"
"v_fma_mix_f32 v226, v172, v208, v216 op_sel:[0,0,0] op_sel_hi:[1,0,0]\n"
"v_cvt_pk_bf16_f32 v168, v224, v225\n"
"v_mfma_f32_16x16x32_bf16 v[36:39], a[164:167], v[148:151], v[36:39]\n"
"buffer_load_dwordx4 v[132:135], v228, s[44:47], s65 offen offset:1024 sc1\n"
"v_fma_mix_f32 v227, v172, v209, v217 op_sel:[1,0,0] op_sel_hi:[1,0,0]\n"
"v_mfma_f32_16x16x32_bf16 v[4:7], a[164:167], v[144:147], v[4:7]\n"
"ds_read_b64_tr_b16 a[132:133], v237 offset:49152\n"
"v_fma_mix_f32 v224, v161, v210, v218 op_sel:[0,0,0] op_sel_hi:[1,0,0]\n"
"v_cvt_pk_bf16_f32 v172, v226, v227\n"
"v_mfma_f32_16x16x32_bf16 v[8:11], a[168:171], v[144:147], v[8:11]\n"
"ds_read_b64_tr_b16 a[134:135], v237 offset:53248\n"
"v_fma_mix_f32 v225, v161, v211, v219 op_sel:[1,0,0] op_sel_hi:[1,0,0]\n"
"v_fma_mix_f32 v226, v165, v210, v218 op_sel:[0,0,0] op_sel_hi:[1,0,0]\n"
"v_mfma_f32_16x16x32_bf16 v[40:43], a[168:171], v[148:151], v[40:43]\n"
"ds_read_b64_tr_b16 a[136:137], v238 offset:49152\n"
"v_cvt_pk_bf16_f32 v161, v224, v225\n"
"v_mfma_f32_16x16x32_bf16 v[72:75], a[168:171], v[152:155], v[72:75]\n"
"buffer_load_dwordx4 v[136:139], v228, s[44:47], s65 offen offset:2048 sc1\n"
"v_fma_mix_f32 v227, v165, v211, v219 op_sel:[1,0,0] op_sel_hi:[1,0,0]\n"
"v_fma_mix_f32 v224, v169, v210, v218 op_sel:[0,0,0] op_sel_hi:[1,0,0]\n"
"v_mfma_f32_16x16x32_bf16 v[104:107], a[168:171], v[156:159], v[104:107]\n"
"ds_read_b64_tr_b16 a[138:139], v238 offset:53248\n"
"v_cvt_pk_bf16_f32 v165, v226, v227\n"
"v_fma_mix_f32 v225, v169, v211, v219 op_sel:[1,0,0] op_sel_hi:[1,0,0]\n"
"v_mfma_f32_16x16x32_bf16 v[108:111], a[172:175], v[156:159], v[108:111]\n"
"ds_read_b64_tr_b16 a[140:141], v239 offset:49152\n"
"v_fma_mix_f32 v226, v173, v210, v218 op_sel:[0,0,0] op_sel_hi:[1,0,0]\n"
"v_mfma_f32_16x16x32_bf16 v[76:79], a[172:175], v[152:155], v[76:79]\n"
"ds_read_b64_tr_b16 a[142:143], v239 offset:53248\n"
"v_cvt_pk_bf16_f32 v169, v224, v225\n"
"v_fma_mix_f32 v227, v173, v211, v219 op_sel:[1,0,0] op_sel_hi:[1,0,0]\n"
"v_mfma_f32_16x16x32_bf16 v[44:47], a[172:175], v[148:151], v[44:47]\n"
"s_waitcnt lgkmcnt(8)\n"
"buffer_load_dwordx4 v[140:143], v228, s[44:47], s65 offen offset:3072 sc1\n"
"v_fma_mix_f32 v224, v162, v212, v220 op_sel:[0,0,0] op_sel_hi:[1,0,0]\n"
"v_mfma_f32_16x16x32_bf16 v[12:15], a[172:175], v[144:147], v[12:15]\n"
"ds_read_b64_tr_b16 a[144:145], v240 offset:49152\n"
"v_cvt_pk_bf16_f32 v173, v226, v227\n"
"v_mfma_f32_16x16x32_bf16 v[16:19], a[176:179], v[144:147], v[16:19]\n"
"ds_read_b64_tr_b16 a[146:147], v240 offset:53248\n"
"v_fma_mix_f32 v225, v162, v213, v221 op_sel:[1,0,0] op_sel_hi:[1,0,0]\n"
"v_fma_mix_f32 v226, v166, v212, v220 op_sel:[0,0,0] op_sel_hi:[1,0,0]\n"
"v_mfma_f32_16x16x32_bf16 v[48:51], a[176:179], v[148:151], v[48:51]\n"
"s_add_u32 m0, s60, 81920\n"
"s_mov_b32 s64, 1572864\n"
"ds_read_b64_tr_b16 a[148:149], v241 offset:49152\n"
"v_mfma_f32_16x16x32_bf16 v[80:83], a[176:179], v[152:155], v[80:83]\n"
"buffer_load_dwordx4 v230, s[52:55], s64 offen offset:0 lds\n"
"v_cvt_pk_bf16_f32 v162, v224, v225\n"
"v_mfma_f32_16x16x32_bf16 v[112:115], a[176:179], v[156:159], v[112:115]\n"
"ds_read_b64_tr_b16 a[150:151], v241 offset:53248\n"
"v_fma_mix_f32 v227, v166, v213, v221 op_sel:[1,0,0] op_sel_hi:[1,0,0]\n"
"v_fma_mix_f32 v224, v170, v212, v220 op_sel:[0,0,0] op_sel_hi:[1,0,0]\n"
"v_mfma_f32_16x16x32_bf16 v[116:119], a[180:183], v[156:159], v[116:119]\n"
"ds_read_b64_tr_b16 a[152:153], v242 offset:49152\n"
"v_cvt_pk_bf16_f32 v166, v226, v227\n"
"v_mfma_f32_16x16x32_bf16 v[84:87], a[180:183], v[152:155], v[84:87]\n"
"s_mov_b32 s64, 1604608\n"
"ds_read_b64_tr_b16 a[154:155], v242 offset:53248\n"
"v_mfma_f32_16x16x32_bf16 v[52:55], a[180:183], v[148:151], v[52:55]\n"
"buffer_load_dwordx4 v231, s[52:55], s64 offen offset:1024 lds\n"
"v_fma_mix_f32 v225, v170, v213, v221 op_sel:[1,0,0] op_sel_hi:[1,0,0]\n"
"v_fma_mix_f32 v226, v174, v212, v220 op_sel:[0,0,0] op_sel_hi:[1,0,0]\n"
"v_mfma_f32_16x16x32_bf16 v[20:23], a[180:183], v[144:147], v[20:23]\n"
"ds_read_b64_tr_b16 a[156:157], v243 offset:49152\n"
"v_cvt_pk_bf16_f32 v170, v224, v225\n"
"v_mfma_f32_16x16x32_bf16 v[24:27], a[184:187], v[144:147], v[24:27]\n"
"ds_read_b64_tr_b16 a[158:159], v243 offset:53248\n"
"v_fma_mix_f32 v227, v174, v213, v221 op_sel:[1,0,0] op_sel_hi:[1,0,0]\n"
"v_mfma_f32_16x16x32_bf16 v[56:59], a[184:187], v[148:151], v[56:59]\n"
"s_mov_b32 s64, 1636352\n"
"ds_read_b128 v[208:211], v229 offset:384\n"
"v_fma_mix_f32 v224, v163, v214, v222 op_sel:[0,0,0] op_sel_hi:[1,0,0]\n"
"v_cvt_pk_bf16_f32 v174, v226, v227\n"
"v_mfma_f32_16x16x32_bf16 v[88:91], a[184:187], v[152:155], v[88:91]\n"
"buffer_load_dwordx4 v230, s[52:55], s64 offen offset:2048 lds\n"
"v_fma_mix_f32 v225, v163, v215, v223 op_sel:[1,0,0] op_sel_hi:[1,0,0]\n"
"v_mfma_f32_16x16x32_bf16 v[120:123], a[184:187], v[156:159], v[120:123]\n"
"ds_read_b128 v[216:219], v229 offset:2944\n"
"v_fma_mix_f32 v226, v167, v214, v222 op_sel:[0,0,0] op_sel_hi:[1,0,0]\n"
"v_mfma_f32_16x16x32_bf16 v[124:127], a[188:191], v[156:159], v[124:127]\n"
"v_cvt_pk_bf16_f32 v163, v224, v225\n"
"v_fma_mix_f32 v227, v167, v215, v223 op_sel:[1,0,0] op_sel_hi:[1,0,0]\n"
"v_fma_mix_f32 v224, v171, v214, v222 op_sel:[0,0,0] op_sel_hi:[1,0,0]\n"
"v_mfma_f32_16x16x32_bf16 v[92:95], a[188:191], v[152:155], v[92:95]\n"
"s_mov_b32 s64, 1668096\n"
"v_cvt_pk_bf16_f32 v167, v226, v227\n"
"v_fma_mix_f32 v225, v171, v215, v223 op_sel:[1,0,0] op_sel_hi:[1,0,0]\n"
"v_mfma_f32_16x16x32_bf16 v[60:63], a[188:191], v[148:151], v[60:63]\n"
"buffer_load_dwordx4 v231, s[52:55], s64 offen offset:3072 lds\n"
"v_fma_mix_f32 v226, v175, v214, v222 op_sel:[0,0,0] op_sel_hi:[1,0,0]\n"
"v_mfma_f32_16x16x32_bf16 v[28:31], a[188:191], v[144:147], v[28:31]\n"
"v_cvt_pk_bf16_f32 v171, v224, v225\n"
"v_fma_mix_f32 v227, v175, v215, v223 op_sel:[1,0,0] op_sel_hi:[1,0,0]\n"
"v_cvt_pk_bf16_f32 v175, v226, v227\n"
"s_waitcnt vmcnt(17) lgkmcnt(0)\n"
"v_mfma_f32_16x16x32_bf16 v[0:3], a[128:131], v[160:163], v[0:3]\n"
"ds_read_b128 v[212:215], v229 offset:400\n"
"v_fma_mix_f32 v224, v176, v208, v216 op_sel:[0,0,0] op_sel_hi:[1,0,0]\n"
"v_mfma_f32_16x16x32_bf16 v[32:35], a[128:131], v[164:167], v[32:35]\n"
"s_add_u32 s65, %7, 24576\n"
"ds_read_b128 v[220:223], v229 offset:2960\n"
"v_fma_mix_f32 v225, v176, v209, v217 op_sel:[1,0,0] op_sel_hi:[1,0,0]\n"
"v_mfma_f32_16x16x32_bf16 v[64:67], a[128:131], v[168:171], v[64:67]\n"
"buffer_load_dwordx4 v[144:147], v228, s[44:47], s65 offen offset:0 sc1\n"
"v_fma_mix_f32 v226, v180, v208, v216 op_sel:[0,0,0] op_sel_hi:[1,0,0]\n"
"v_cvt_pk_bf16_f32 v176, v224, v225\n"
"v_mfma_f32_16x16x32_bf16 v[96:99], a[128:131], v[172:175], v[96:99]\n"
"L_w2_%=:\n"
"s_cbranch_execz L_wd0_%=\n"
"ds_read_b64_tr_b16 a[160:161], v236 offset:57344\n"
"v_fma_mix_f32 v227, v180, v209, v217 op_sel:[1,0,0] op_sel_hi:[1,0,0]\n"
"v_mfma_f32_16x16x32_bf16 v[100:103], a[132:135], v[172:175], v[100:103]\n"
"ds_read_b64_tr_b16 a[162:163], v236 offset:61440\n"
"v_fma_mix_f32 v224, v184, v208, v216 op_sel:[0,0,0] op_sel_hi:[1,0,0]\n"
"v_cvt_pk_bf16_f32 v180, v226, v227\n"
"v_mfma_f32_16x16x32_bf16 v[68:71], a[132:135], v[168:171], v[68:71]\n"
"ds_read_b64_tr_b16 a[164:165], v237 offset:57344\n"
"v_fma_mix_f32 v225, v184, v209, v217 op_sel:[1,0,0] op_sel_hi:[1,0,0]\n"
"v_fma_mix_f32 v226, v188, v208, v216 op_sel:[0,0,0] op_sel_hi:[1,0,0]\n"
"v_mfma_f32_16x16x32_bf16 v[36:39], a[132:135], v[164:167], v[36:39]\n"
"buffer_load_dwordx4 v[148:151], v228, s[44:47], s65 offen offset:1024 sc1\n"
"v_cvt_pk_bf16_f32 v184, v224, v225\n"
"v_mfma_f32_16x16x32_bf16 v[4:7], a[132:135], v[160:163], v[4:7]\n"
"ds_read_b64_tr_b16 a[166:167], v237 offset:61440\n"
"v_fma_mix_f32 v227, v188, v209, v217 op_sel:[1,0,0] op_sel_hi:[1,0,0]\n"
"v_fma_mix_f32 v224, v177, v210, v218 op_sel:[0,0,0] op_sel_hi:[1,0,0]\n"
"v_mfma_f32_16x16x32_bf16 v[8:11], a[136:139], v[160:163], v[8:11]\n"
"ds_read_b64_tr_b16 a[168:169], v238 offset:57344\n"
"v_cvt_pk_bf16_f32 v188, v226, v227\n"
"v_fma_mix_f32 v225, v177, v211, v219 op_sel:[1,0,0] op_sel_hi:[1,0,0]\n"
"v_mfma_f32_16x16x32_bf16 v[40:43], a[136:139], v[164:167], v[40:43]\n"
"ds_read_b64_tr_b16 a[170:171], v238 offset:61440\n"
"v_fma_mix_f32 v226, v181, v210, v218 op_sel:[0,0,0] op_sel_hi:[1,0,0]\n"
"v_mfma_f32_16x16x32_bf16 v[72:75], a[136:139], v[168:171], v[72:75]\n"
"buffer_load_dwordx4 v[152:155], v228, s[44:47], s65 offen offset:2048 sc1\n"
"v_cvt_pk_bf16_f32 v177, v224, v225\n"
"v_fma_mix_f32 v227, v181, v211, v219 op_sel:[1,0,0] op_sel_hi:[1,0,0]\n"
"v_mfma_f32_16x16x32_bf16 v[104:107], a[136:139], v[172:175], v[104:107]\n"
"ds_read_b64_tr_b16 a[172:173], v239 offset:57344\n"
"v_fma_mix_f32 v224, v185, v210, v218 op_sel:[0,0,0] op_sel_hi:[1,0,0]\n"
"v_cvt_pk_bf16_f32 v181, v226, v227\n"
"v_mfma_f32_16x16x32_bf16 v[108:111], a[140:143], v[172:175], v[108:111]\n"
"ds_read_b64_tr_b16 a[174:175], v239 offset:61440\n"
"v_fma_mix_f32 v225, v185, v211, v219 op_sel:[1,0,0] op_sel_hi:[1,0,0]\n"
"v_mfma_f32_16x16x32_bf16 v[76:79], a[140:143], v[168:171], v[76:79]\n"
"ds_read_b64_tr_b16 a[176:177], v240 offset:57344\n"
"v_fma_mix_f32 v226, v189, v210, v218 op_sel:[0,0,0] op_sel_hi:[1,0,0]\n"
"v_cvt_pk_bf16_f32 v185, v224, v225\n"
"v_mfma_f32_16x16x32_bf16 v[44:47], a[140:143], v[164:167], v[44:47]\n"
"buffer_load_dwordx4 v[156:159], v228, s[44:47], s65 offen offset:3072 sc1\n"
"v_fma_mix_f32 v227, v189, v211, v219 op_sel:[1,0,0] op_sel_hi:[1,0,0]\n"
"v_mfma_f32_16x16x32_bf16 v[12:15], a[140:143], v[160:163], v[12:15]\n"
"s_waitcnt lgkmcnt(9)\n"
"ds_read_b64_tr_b16 a[178:179], v240 offset:61440\n"
"v_fma_mix_f32 v224, v178, v212, v220 op_sel:[0,0,0] op_sel_hi:[1,0,0]\n"
"v_mfma_f32_16x16x32_bf16 v[16:19], a[144:147], v[160:163], v[16:19]\n"
"ds_read_b64_tr_b16 a[180:181], v241 offset:57344\n"
"v_cvt_pk_bf16_f32 v189, v226, v227\n"
"v_fma_mix_f32 v225, v178, v213, v221 op_sel:[1,0,0] op_sel_hi:[1,0,0]\n"
"v_mfma_f32_16x16x32_bf16 v[48:51], a[144:147], v[164:167], v[48:51]\n"
"s_add_u32 m0, s60, 98304\n"
"s_mov_b32 s64, 2097152\n"
"ds_read_b64_tr_b16 a[182:183], v241 offset:61440\n"
"v_mfma_f32_16x16x32_bf16 v[80:83], a[144:147], v[168:171], v[80:83]\n"
"buffer_load_dwordx4 v230, s[52:55], s64 offen offset:0 lds\n"
"v_fma_mix_f32 v226, v182, v212, v220 op_sel:[0,0,0] op_sel_hi:[1,0,0]\n"
"v_mfma_f32_16x16x32_bf16 v[112:115], a[144:147], v[172:175], v[112:115]\n"
"ds_read_b64_tr_b16 a[184:185], v242 offset:57344\n"
"v_cvt_pk_bf16_f32 v178, v224, v225\n"
"v_fma_mix_f32 v227, v182, v213, v221 op_sel:[1,0,0] op_sel_hi:[1,0,0]\n"
"v_mfma_f32_16x16x32_bf16 v[116:119], a[148:151], v[172:175], v[116:119]\n"
"ds_read_b64_tr_b16 a[186:187], v242 offset:61440\n"
"v_fma_mix_f32 v224, v186, v212, v220 op_sel:[0,0,0] op_sel_hi:[1,0,0]\n"
"v_mfma_f32_16x16x32_bf16 v[84:87], a[148:151], v[168:171], v[84:87]\n"
"s_mov_b32 s64, 2128896\n"
"ds_read_b64_tr_b16 a[188:189], v243 offset:57344\n"
"v_mfma_f32_16x16x32_bf16 v[52:55], a[148:151], v[164:167], v[52:55]\n"
"buffer_load_dwordx4 v231, s[52:55], s64 offen offset:1024 lds\n"
"v_cvt_pk_bf16_f32 v182, v226, v227\n"
"v_fma_mix_f32 v225, v186, v213, v221 op_sel:[1,0,0] op_sel_hi:[1,0,0]\n"
"v_mfma_f32_16x16x32_bf16 v[20:23], a[148:151], v[160:163], v[20:23]\n"
"ds_read_b64_tr_b16 a[190:191], v243 offset:61440\n"
"v_fma_mix_f32 v226, v190, v212, v220 op_sel:[0,0,0] op_sel_hi:[1,0,0]\n"
"v_mfma_f32_16x16x32_bf16 v[24:27], a[152:155], v[160:163], v[24:27]\n"
"ds_read_b128 v[208:211], v229 offset:512\n"
"v_cvt_pk_bf16_f32 v186, v224, v225\n"
"v_mfma_f32_16x16x32_bf16 v[56:59], a[152:155], v[164:167], v[56:59]\n"
"s_mov_b32 s64, 2160640\n"
"ds_read_b128 v[216:219], v229 offset:3072\n"
"v_fma_mix_f32 v227, v190, v213, v221 op_sel:[1,0,0] op_sel_hi:[1,0,0]\n"
"v_fma_mix_f32 v224, v179, v214, v222 op_sel:[0,0,0] op_sel_hi:[1,0,0]\n"
"v_mfma_f32_16x16x32_bf16 v[88:91], a[152:155], v[168:171], v[88:91]\n"
"buffer_load_dwordx4 v230, s[52:55], s64 offen offset:2048 lds\n"
"v_cvt_pk_bf16_f32 v190, v226, v227\n"
"v_mfma_f32_16x16x32_bf16 v[120:123], a[152:155], v[172:175], v[120:123]\n"
"v_fma_mix_f32 v225, v179, v215, v223 op_sel:[1,0,0] op_sel_hi:[1,0,0]\n"
"v_fma_mix_f32 v226, v183, v214, v222 op_sel:[0,0,0] op_sel_hi:[1,0,0]\n"
"v_mfma_f32_16x16x32_bf16 v[124:127], a[156:159], v[172:175], v[124:127]\n"
"v_cvt_pk_bf16_f32 v179, v224, v225\n"
"v_fma_mix_f32 v227, v183, v215, v223 op_sel:[1,0,0] op_sel_hi:[1,0,0]\n"
"v_fma_mix_f32 v224, v187, v214, v222 op_sel:[0,0,0] op_sel_hi:[1,0,0]\n"
"v_mfma_f32_16x16x32_bf16 v[92:95], a[156:159], v[168:171], v[92:95]\n"
"s_mov_b32 s64, 2192384\n"
"v_cvt_pk_bf16_f32 v183, v226, v227\n"
"v_fma_mix_f32 v225, v187, v215, v223 op_sel:[1,0,0] op_sel_hi:[1,0,0]\n"
"v_mfma_f32_16x16x32_bf16 v[60:63], a[156:159], v[164:167], v[60:63]\n"
"buffer_load_dwordx4 v231, s[52:55], s64 offen offset:3072 lds\n"
"v_fma_mix_f32 v226, v191, v214, v222 op_sel:[0,0,0] op_sel_hi:[1,0,0]\n"
"v_mfma_f32_16x16x32_bf16 v[28:31], a[156:159], v[160:163], v[28:31]\n"
"v_cvt_pk_bf16_f32 v187, v224, v225\n"
"v_fma_mix_f32 v227, v191, v215, v223 op_sel:[1,0,0] op_sel_hi:[1,0,0]\n"
"v_cvt_pk_bf16_f32 v191, v226, v227\n"
"s_waitcnt vmcnt(17) lgkmcnt(0)\n"
"v_mfma_f32_16x16x32_bf16 v[0:3], a[160:163], v[176:179], v[0:3]\n"
"s_barrier\n"
"v_fma_mix_f32 v224, v192, v208, v216 op_sel:[0,0,0] op_sel_hi:[1,0,0]\n"
"v_fma_mix_f32 v225, v192, v209, v217 op_sel:[1,0,0] op_sel_hi:[1,0,0]\n"
"v_mfma_f32_16x16x32_bf16 v[32:35], a[160:163], v[180:183], v[32:35]\n"
"s_add_u32 s65, %7, 28672\n"
"ds_read_b128 v[212:215], v229 offset:528\n"
"v_fma_mix_f32 v226, v196, v208, v216 op_sel:[0,0,0] op_sel_hi:[1,0,0]\n"
"v_mfma_f32_16x16x32_bf16 v[64:67], a[160:163], v[184:187], v[64:67]\n"
"buffer_load_dwordx4 v[160:163], v228, s[44:47], s65 offen offset:0 sc1\n"
"v_cvt_pk_bf16_f32 v192, v224, v225\n"
"v_fma_mix_f32 v227, v196, v209, v217 op_sel:[1,0,0] op_sel_hi:[1,0,0]\n"
"v_mfma_f32_16x16x32_bf16 v[96:99], a[160:163], v[188:191], v[96:99]\n"
"ds_read_b128 v[220:223], v229 offset:3088\n"
"v_fma_mix_f32 v224, v200, v208, v216 op_sel:[0,0,0] op_sel_hi:[1,0,0]\n"
"v_mfma_f32_16x16x32_bf16 v[100:103], a[164:167], v[188:191], v[100:103]\n"
"ds_read_b64_tr_b16 a[128:129], v236 offset:0\n"
"v_cvt_pk_bf16_f32 v196, v226, v227\n"
"v_fma_mix_f32 v225, v200, v209, v217 op_sel:[1,0,0] op_sel_hi:[1,0,0]\n"
"v_mfma_f32_16x16x32_bf16 v[68:71], a[164:167], v[184:187], v[68:71]\n"
"ds_read_b64_tr_b16 a[130:131], v236 offset:4096\n"
"v_fma_mix_f32 v226, v204, v208, v216 op_sel:[0,0,0] op_sel_hi:[1,0,0]\n"
"v_cvt_pk_bf16_f32 v200, v224, v225\n"
"v_mfma_f32_16x16x32_bf16 v[36:39], a[164:167], v[180:183], v[36:39]\n"
"buffer_load_dwordx4 v[164:167], v228, s[44:47], s65 offen offset:1024 sc1\n"
"v_fma_mix_f32 v227, v204, v209, v217 op_sel:[1,0,0] op_sel_hi:[1,0,0]\n"
"v_mfma_f32_16x16x32_bf16 v[4:7], a[164:167], v[176:179], v[4:7]\n"
"ds_read_b64_tr_b16 a[132:133], v237 offset:0\n"
"v_fma_mix_f32 v224, v193, v210, v218 op_sel:[0,0,0] op_sel_hi:[1,0,0]\n"
"v_cvt_pk_bf16_f32 v204, v226, v227\n"
"v_mfma_f32_16x16x32_bf16 v[8:11], a[168:171], v[176:179], v[8:11]\n"
"ds_read_b64_tr_b16 a[134:135], v237 offset:4096\n"
"v_fma_mix_f32 v225, v193, v211, v219 op_sel:[1,0,0] op_sel_hi:[1,0,0]\n"
"v_fma_mix_f32 v226, v197, v210, v218 op_sel:[0,0,0] op_sel_hi:[1,0,0]\n"
"v_mfma_f32_16x16x32_bf16 v[40:43], a[168:171], v[180:183], v[40:43]\n"
"ds_read_b64_tr_b16 a[136:137], v238 offset:0\n"
"v_cvt_pk_bf16_f32 v193, v224, v225\n"
"v_mfma_f32_16x16x32_bf16 v[72:75], a[168:171], v[184:187], v[72:75]\n"
"buffer_load_dwordx4 v[168:171], v228, s[44:47], s65 offen offset:2048 sc1\n"
"v_fma_mix_f32 v227, v197, v211, v219 op_sel:[1,0,0] op_sel_hi:[1,0,0]\n"
"v_fma_mix_f32 v224, v201, v210, v218 op_sel:[0,0,0] op_sel_hi:[1,0,0]\n"
"v_mfma_f32_16x16x32_bf16 v[104:107], a[168:171], v[188:191], v[104:107]\n"
"ds_read_b64_tr_b16 a[138:139], v238 offset:4096\n"
"v_cvt_pk_bf16_f32 v197, v226, v227\n"
"v_fma_mix_f32 v225, v201, v211, v219 op_sel:[1,0,0] op_sel_hi:[1,0,0]\n"
"v_mfma_f32_16x16x32_bf16 v[108:111], a[172:175], v[188:191], v[108:111]\n"
"ds_read_b64_tr_b16 a[140:141], v239 offset:0\n"
"v_fma_mix_f32 v226, v205, v210, v218 op_sel:[0,0,0] op_sel_hi:[1,0,0]\n"
"v_mfma_f32_16x16x32_bf16 v[76:79], a[172:175], v[184:187], v[76:79]\n"
"ds_read_b64_tr_b16 a[142:143], v239 offset:4096\n"
"v_cvt_pk_bf16_f32 v201, v224, v225\n"
"v_fma_mix_f32 v227, v205, v211, v219 op_sel:[1,0,0] op_sel_hi:[1,0,0]\n"
"v_mfma_f32_16x16x32_bf16 v[44:47], a[172:175], v[180:183], v[44:47]\n"
"s_waitcnt lgkmcnt(8)\n"
"buffer_load_dwordx4 v[172:175], v228, s[44:47], s65 offen offset:3072 sc1\n"
"v_fma_mix_f32 v224, v194, v212, v220 op_sel:[0,0,0] op_sel_hi:[1,0,0]\n"
"v_mfma_f32_16x16x32_bf16 v[12:15], a[172:175], v[176:179], v[12:15]\n"
"ds_read_b64_tr_b16 a[144:145], v240 offset:0\n"
"v_cvt_pk_bf16_f32 v205, v226, v227\n"
"v_mfma_f32_16x16x32_bf16 v[16:19], a[176:179], v[176:179], v[16:19]\n"
"ds_read_b64_tr_b16 a[146:147], v240 offset:4096\n"
"v_fma_mix_f32 v225, v194, v213, v221 op_sel:[1,0,0] op_sel_hi:[1,0,0]\n"
"v_fma_mix_f32 v226, v198, v212, v220 op_sel:[0,0,0] op_sel_hi:[1,0,0]\n"
"v_mfma_f32_16x16x32_bf16 v[48:51], a[176:179], v[180:183], v[48:51]\n"
"s_add_u32 m0, s60, 114688\n"
"s_mov_b32 s64, 2621440\n"
"ds_read_b64_tr_b16 a[148:149], v241 offset:0\n"
"v_mfma_f32_16x16x32_bf16 v[80:83], a[176:179], v[184:187], v[80:83]\n"
"buffer_load_dwordx4 v230, s[52:55], s64 offen offset:0 lds\n"
"v_cvt_pk_bf16_f32 v194, v224, v225\n"
"v_mfma_f32_16x16x32_bf16 v[112:115], a[176:179], v[188:191], v[112:115]\n"
"ds_read_b64_tr_b16 a[150:151], v241 offset:4096\n"
"v_fma_mix_f32 v227, v198, v213, v221 op_sel:[1,0,0] op_sel_hi:[1,0,0]\n"
"v_fma_mix_f32 v224, v202, v212, v220 op_sel:[0,0,0] op_sel_hi:[1,0,0]\n"
"v_mfma_f32_16x16x32_bf16 v[116:119], a[180:183], v[188:191], v[116:119]\n"
"ds_read_b64_tr_b16 a[152:153], v242 offset:0\n"
"v_cvt_pk_bf16_f32 v198, v226, v227\n"
"v_mfma_f32_16x16x32_bf16 v[84:87], a[180:183], v[184:187], v[84:87]\n"
"s_mov_b32 s64, 2653184\n"
"ds_read_b64_tr_b16 a[154:155], v242 offset:4096\n"
"v_mfma_f32_16x16x32_bf16 v[52:55], a[180:183], v[180:183], v[52:55]\n"
"buffer_load_dwordx4 v231, s[52:55], s64 offen offset:1024 lds\n"
"v_fma_mix_f32 v225, v202, v213, v221 op_sel:[1,0,0] op_sel_hi:[1,0,0]\n"
"v_fma_mix_f32 v226, v206, v212, v220 op_sel:[0,0,0] op_sel_hi:[1,0,0]\n"
"v_mfma_f32_16x16x32_bf16 v[20:23], a[180:183], v[176:179], v[20:23]\n"
"ds_read_b64_tr_b16 a[156:157], v243 offset:0\n"
"v_cvt_pk_bf16_f32 v202, v224, v225\n"
"v_mfma_f32_16x16x32_bf16 v[24:27], a[184:187], v[176:179], v[24:27]\n"
"ds_read_b64_tr_b16 a[158:159], v243 offset:4096\n"
"v_fma_mix_f32 v227, v206, v213, v221 op_sel:[1,0,0] op_sel_hi:[1,0,0]\n"
"v_mfma_f32_16x16x32_bf16 v[56:59], a[184:187], v[180:183], v[56:59]\n"
"s_mov_b32 s64, 2684928\n"
"ds_read_b128 v[208:211], v229 offset:640\n"
"v_fma_mix_f32 v224, v195, v214, v222 op_sel:[0,0,0] op_sel_hi:[1,0,0]\n"
"v_cvt_pk_bf16_f32 v206, v226, v227\n"
"v_mfma_f32_16x16x32_bf16 v[88:91], a[184:187], v[184:187], v[88:91]\n"
"buffer_load_dwordx4 v230, s[52:55], s64 offen offset:2048 lds\n"
"v_fma_mix_f32 v225, v195, v215, v223 op_sel:[1,0,0] op_sel_hi:[1,0,0]\n"
"v_mfma_f32_16x16x32_bf16 v[120:123], a[184:187], v[188:191], v[120:123]\n"
"ds_read_b128 v[216:219], v229 offset:3200\n"
"v_fma_mix_f32 v226, v199, v214, v222 op_sel:[0,0,0] op_sel_hi:[1,0,0]\n"
"v_mfma_f32_16x16x32_bf16 v[124:127], a[188:191], v[188:191], v[124:127]\n"
"v_cvt_pk_bf16_f32 v195, v224, v225\n"
"v_fma_mix_f32 v227, v199, v215, v223 op_sel:[1,0,0] op_sel_hi:[1,0,0]\n"
"v_fma_mix_f32 v224, v203, v214, v222 op_sel:[0,0,0] op_sel_hi:[1,0,0]\n"
"v_mfma_f32_16x16x32_bf16 v[92:95], a[188:191], v[184:187], v[92:95]\n"
"s_mov_b32 s64, 2716672\n"
"v_cvt_pk_bf16_f32 v199, v226, v227\n"
"v_fma_mix_f32 v225, v203, v215, v223 op_sel:[1,0,0] op_sel_hi:[1,0,0]\n"
"v_mfma_f32_16x16x32_bf16 v[60:63], a[188:191], v[180:183], v[60:63]\n"
"buffer_load_dwordx4 v231, s[52:55], s64 offen offset:3072 lds\n"
"v_fma_mix_f32 v226, v207, v214, v222 op_sel:[0,0,0] op_sel_hi:[1,0,0]\n"
"v_mfma_f32_16x16x32_bf16 v[28:31], a[188:191], v[176:179], v[28:31]\n"
"v_cvt_pk_bf16_f32 v203, v224, v225\n"
"v_fma_mix_f32 v227, v207, v215, v223 op_sel:[1,0,0] op_sel_hi:[1,0,0]\n"
"v_cvt_pk_bf16_f32 v207, v226, v227\n"
"s_waitcnt vmcnt(20) lgkmcnt(0)\n"
"v_mfma_f32_16x16x32_bf16 v[0:3], a[128:131], v[192:195], v[0:3]\n"
"ds_read_b128 v[212:215], v229 offset:656\n"
"v_fma_mix_f32 v224, v128, v208, v216 op_sel:[0,0,0] op_sel_hi:[1,0,0]\n"
"v_mfma_f32_16x16x32_bf16 v[32:35], a[128:131], v[196:199], v[32:35]\n"
"s_add_u32 s65, %7, 32768\n"
"ds_read_b128 v[220:223], v229 offset:3216\n"
"v_fma_mix_f32 v225, v128, v209, v217 op_sel:[1,0,0] op_sel_hi:[1,0,0]\n"
"v_mfma_f32_16x16x32_bf16 v[64:67], a[128:131], v[200:203], v[64:67]\n"
"buffer_load_dwordx4 v[176:179], v228, s[44:47], s65 offen offset:0 sc1\n"
"v_fma_mix_f32 v226, v132, v208, v216 op_sel:[0,0,0] op_sel_hi:[1,0,0]\n"
"v_mfma_f32_16x16x32_bf16 v[96:99], a[128:131], v[204:207], v[96:99]\n"
"ds_read_b64_tr_b16 a[160:161], v236 offset:8192\n"
"v_cvt_pk_bf16_f32 v128, v224, v225\n"
"v_mfma_f32_16x16x32_bf16 v[100:103], a[132:135], v[204:207], v[100:103]\n"
"ds_read_b64_tr_b16 a[162:163], v236 offset:12288\n"
"v_fma_mix_f32 v227, v132, v209, v217 op_sel:[1,0,0] op_sel_hi:[1,0,0]\n"
"v_fma_mix_f32 v224, v136, v208, v216 op_sel:[0,0,0] op_sel_hi:[1,0,0]\n"
"v_mfma_f32_16x16x32_bf16 v[68:71], a[132:135], v[200:203], v[68:71]\n"
"ds_read_b64_tr_b16 a[164:165], v237 offset:8192\n"
"v_cvt_pk_bf16_f32 v132, v226, v227\n"
"v_mfma_f32_16x16x32_bf16 v[36:39], a[132:135], v[196:199], v[36:39]\n"
"buffer_load_dwordx4 v[180:183], v228, s[44:47], s65 offen offset:1024 sc1\n"
"v_fma_mix_f32 v225, v136, v209, v217 op_sel:[1,0,0] op_sel_hi:[1,0,0]\n"
"v_mfma_f32_16x16x32_bf16 v[4:7], a[132:135], v[192:195], v[4:7]\n"
"ds_read_b64_tr_b16 a[166:167], v237 offset:12288\n"
"v_fma_mix_f32 v226, v140, v208, v216 op_sel:[0,0,0] op_sel_hi:[1,0,0]\n"
"v_cvt_pk_bf16_f32 v136, v224, v225\n"
"v_mfma_f32_16x16x32_bf16 v[8:11], a[136:139], v[192:195], v[8:11]\n"
"ds_read_b64_tr_b16 a[168:169], v238 offset:8192\n"
"v_fma_mix_f32 v227, v140, v209, v217 op_sel:[1,0,0] op_sel_hi:[1,0,0]\n"
"v_mfma_f32_16x16x32_bf16 v[40:43], a[136:139], v[196:199], v[40:43]\n"
"ds_read_b64_tr_b16 a[170:171], v238 offset:12288\n"
"v_fma_mix_f32 v224, v129, v210, v218 op_sel:[0,0,0] op_sel_hi:[1,0,0]\n"
"v_mfma_f32_16x16x32_bf16 v[72:75], a[136:139], v[200:203], v[72:75]\n"
"buffer_load_dwordx4 v[184:187], v228, s[44:47], s65 offen offset:2048 sc1\n"
"v_cvt_pk_bf16_f32 v140, v226, v227\n"
"v_fma_mix_f32 v225, v129, v211, v219 op_sel:[1,0,0] op_sel_hi:[1,0,0]\n"
"v_mfma_f32_16x16x32_bf16 v[104:107], a[136:139], v[204:207], v[104:107]\n"
"ds_read_b64_tr_b16 a[172:173], v239 offset:8192\n"
"v_fma_mix_f32 v226, v133, v210, v218 op_sel:[0,0,0] op_sel_hi:[1,0,0]\n"
"v_mfma_f32_16x16x32_bf16 v[108:111], a[140:143], v[204:207], v[108:111]\n"
"ds_read_b64_tr_b16 a[174:175], v239 offset:12288\n"
"v_cvt_pk_bf16_f32 v129, v224, v225\n"
"v_mfma_f32_16x16x32_bf16 v[76:79], a[140:143], v[200:203], v[76:79]\n"
"ds_read_b64_tr_b16 a[176:177], v240 offset:8192\n"
"v_fma_mix_f32 v227, v133, v211, v219 op_sel:[1,0,0] op_sel_hi:[1,0,0]\n"
"v_fma_mix_f32 v224, v137, v210, v218 op_sel:[0,0,0] op_sel_hi:[1,0,0]\n"
"v_mfma_f32_16x16x32_bf16 v[44:47], a[140:143], v[196:199], v[44:47]\n"
"buffer_load_dwordx4 v[188:191], v228, s[44:47], s65 offen offset:3072 sc1\n"
"v_cvt_pk_bf16_f32 v133, v226, v227\n"
"v_mfma_f32_16x16x32_bf16 v[12:15], a[140:143], v[192:195], v[12:15]\n"
"ds_read_b64_tr_b16 a[178:179], v240 offset:12288\n"
"v_fma_mix_f32 v225, v137, v211, v219 op_sel:[1,0,0] op_sel_hi:[1,0,0]\n"
"v_mfma_f32_16x16x32_bf16 v[16:19], a[144:147], v[192:195], v[16:19]\n"
"ds_read_b64_tr_b16 a[180:181], v241 offset:8192\n"
"v_fma_mix_f32 v226, v141, v210, v218 op_sel:[0,0,0] op_sel_hi:[1,0,0]\n"
"v_cvt_pk_bf16_f32 v137, v224, v225\n"
"v_mfma_f32_16x16x32_bf16 v[48:51], a[144:147], v[196:199], v[48:51]\n"
"ds_read_b64_tr_b16 a[182:183], v241 offset:12288\n"
"v_fma_mix_f32 v227, v141, v211, v219 op_sel:[1,0,0] op_sel_hi:[1,0,0]\n"
"v_mfma_f32_16x16x32_bf16 v[80:83], a[144:147], v[200:203], v[80:83]\n"
"s_waitcnt lgkmcnt(12)\n"
"ds_read_b64_tr_b16 a[184:185], v242 offset:8192\n"
"v_fma_mix_f32 v224, v130, v212, v220 op_sel:[0,0,0] op_sel_hi:[1,0,0]\n"
"v_mfma_f32_16x16x32_bf16 v[112:115], a[144:147], v[204:207], v[112:115]\n"
"ds_read_b64_tr_b16 a[186:187], v242 offset:12288\n"
"v_cvt_pk_bf16_f32 v141, v226, v227\n"
"v_fma_mix_f32 v225, v130, v213, v221 op_sel:[1,0,0] op_sel_hi:[1,0,0]\n"
"v_mfma_f32_16x16x32_bf16 v[116:119], a[148:151], v[204:207], v[116:119]\n"
"ds_read_b64_tr_b16 a[188:189], v243 offset:8192\n"
"v_fma_mix_f32 v226, v134, v212, v220 op_sel:[0,0,0] op_sel_hi:[1,0,0]\n"
"v_mfma_f32_16x16x32_bf16 v[84:87], a[148:151], v[200:203], v[84:87]\n"
"ds_read_b64_tr_b16 a[190:191], v243 offset:12288\n"
"v_cvt_pk_bf16_f32 v130, v224, v225\n"
"v_mfma_f32_16x16x32_bf16 v[52:55], a[148:151], v[196:199], v[52:55]\n"
"v_fma_mix_f32 v227, v134, v213, v221 op_sel:[1,0,0] op_sel_hi:[1,0,0]\n"
"v_fma_mix_f32 v224, v138, v212, v220 op_sel:[0,0,0] op_sel_hi:[1,0,0]\n"
"v_cvt_pk_bf16_f32 v134, v226, v227\n"
"v_mfma_f32_16x16x32_bf16 v[20:23], a[148:151], v[192:195], v[20:23]\n"
"v_fma_mix_f32 v225, v138, v213, v221 op_sel:[1,0,0] op_sel_hi:[1,0,0]\n"
"v_fma_mix_f32 v226, v142, v212, v220 op_sel:[0,0,0] op_sel_hi:[1,0,0]\n"
"v_mfma_f32_16x16x32_bf16 v[24:27], a[152:155], v[192:195], v[24:27]\n"
"ds_read_b128 v[208:211], v229 offset:768\n"
"v_cvt_pk_bf16_f32 v138, v224, v225\n"
"v_mfma_f32_16x16x32_bf16 v[56:59], a[152:155], v[196:199], v[56:59]\n"
"ds_read_b128 v[216:219], v229 offset:3328\n"
"v_fma_mix_f32 v227, v142, v213, v221 op_sel:[1,0,0] op_sel_hi:[1,0,0]\n"
"v_fma_mix_f32 v224, v131, v214, v222 op_sel:[0,0,0] op_sel_hi:[1,0,0]\n"
"v_mfma_f32_16x16x32_bf16 v[88:91], a[152:155], v[200:203], v[88:91]\n"
"v_cvt_pk_bf16_f32 v142, v226, v227\n"
"v_fma_mix_f32 v225, v131, v215, v223 op_sel:[1,0,0] op_sel_hi:[1,0,0]\n"
"v_mfma_f32_16x16x32_bf16 v[120:123], a[152:155], v[204:207], v[120:123]\n"
"v_fma_mix_f32 v226, v135, v214, v222 op_sel:[0,0,0] op_sel_hi:[1,0,0]\n"
"v_cvt_pk_bf16_f32 v131, v224, v225\n"
"v_mfma_f32_16x16x32_bf16 v[124:127], a[156:159], v[204:207], v[124:127]\n"
"v_fma_mix_f32 v227, v135, v215, v223 op_sel:[1,0,0] op_sel_hi:[1,0,0]\n"
"v_fma_mix_f32 v224, v139, v214, v222 op_sel:[0,0,0] op_sel_hi:[1,0,0]\n"
"v_mfma_f32_16x16x32_bf16 v[92:95], a[156:159], v[200:203], v[92:95]\n"
"v_cvt_pk_bf16_f32 v135, v226, v227\n"
"v_fma_mix_f32 v225, v139, v215, v223 op_sel:[1,0,0] op_sel_hi:[1,0,0]\n"
"v_mfma_f32_16x16x32_bf16 v[60:63], a[156:159], v[196:199], v[60:63]\n"
"v_fma_mix_f32 v226, v143, v214, v222 op_sel:[0,0,0] op_sel_hi:[1,0,0]\n"
"v_cvt_pk_bf16_f32 v139, v224, v225\n"
"v_mfma_f32_16x16x32_bf16 v[28:31], a[156:159], v[192:195], v[28:31]\n"
"v_fma_mix_f32 v227, v143, v215, v223 op_sel:[1,0,0] op_sel_hi:[1,0,0]\n"
"v_cvt_pk_bf16_f32 v143, v226, v227\n"
"s_waitcnt vmcnt(16) lgkmcnt(0)\n"
"v_mfma_f32_16x16x32_bf16 v[0:3], a[160:163], v[128:131], v[0:3]\n"
"s_barrier\n"
"v_fma_mix_f32 v224, v144, v208, v216 op_sel:[0,0,0] op_sel_hi:[1,0,0]\n"
"v_fma_mix_f32 v225, v144, v209, v217 op_sel:[1,0,0] op_sel_hi:[1,0,0]\n"
"v_mfma_f32_16x16x32_bf16 v[32:35], a[160:163], v[132:135], v[32:35]\n"
"s_add_u32 s65, %7, 36864\n"
"ds_read_b128 v[212:215], v229 offset:784\n"
"v_fma_mix_f32 v226, v148, v208, v216 op_sel:[0,0,0] op_sel_hi:[1,0,0]\n"
"v_mfma_f32_16x16x32_bf16 v[64:67], a[160:163], v[136:139], v[64:67]\n"
"buffer_load_dwordx4 v[192:195], v228, s[44:47], s65 offen offset:0 sc1\n"
"v_cvt_pk_bf16_f32 v144, v224, v225\n"
"v_fma_mix_f32 v227, v148, v209, v217 op_sel:[1,0,0] op_sel_hi:[1,0,0]\n"
"v_mfma_f32_16x16x32_bf16 v[96:99], a[160:163], v[140:143], v[96:99]\n"
"ds_read_b128 v[220:223], v229 offset:3344\n"
"v_fma_mix_f32 v224, v152, v208, v216 op_sel:[0,0,0] op_sel_hi:[1,0,0]\n"
"v_mfma_f32_16x16x32_bf16 v[100:103], a[164:167], v[140:143], v[100:103]\n"
"ds_read_b64_tr_b16 a[128:129], v236 offset:16384\n"
"v_cvt_pk_bf16_f32 v148, v226, v227\n"
"v_fma_mix_f32 v225, v152, v209, v217 op_sel:[1,0,0] op_sel_hi:[1,0,0]\n"
"v_mfma_f32_16x16x32_bf16 v[68:71], a[164:167], v[136:139], v[68:71]\n"
"ds_read_b64_tr_b16 a[130:131], v236 offset:20480\n"
"v_fma_mix_f32 v226, v156, v208, v216 op_sel:[0,0,0] op_sel_hi:[1,0,0]\n"
"v_cvt_pk_bf16_f32 v152, v224, v225\n"
"v_mfma_f32_16x16x32_bf16 v[36:39], a[164:167], v[132:135], v[36:39]\n"
"buffer_load_dwordx4 v[196:199], v228, s[44:47], s65 offen offset:1024 sc1\n"
"v_fma_mix_f32 v227, v156, v209, v217 op_sel:[1,0,0] op_sel_hi:[1,0,0]\n"
"v_mfma_f32_16x16x32_bf16 v[4:7], a[164:167], v[128:131], v[4:7]\n"
"ds_read_b64_tr_b16 a[132:133], v237 offset:16384\n"
"v_fma_mix_f32 v224, v145, v210, v218 op_sel:[0,0,0] op_sel_hi:[1,0,0]\n"
"v_cvt_pk_bf16_f32 v156, v226, v227\n"
"v_mfma_f32_16x16x32_bf16 v[8:11], a[168:171], v[128:131], v[8:11]\n"
"ds_read_b64_tr_b16 a[134:135], v237 offset:20480\n"
"v_fma_mix_f32 v225, v145, v211, v219 op_sel:[1,0,0] op_sel_hi:[1,0,0]\n"
"v_fma_mix_f32 v226, v149, v210, v218 op_sel:[0,0,0] op_sel_hi:[1,0,0]\n"
"v_mfma_f32_16x16x32_bf16 v[40:43], a[168:171], v[132:135], v[40:43]\n"
"ds_read_b64_tr_b16 a[136:137], v238 offset:16384\n"
"v_cvt_pk_bf16_f32 v145, v224, v225\n"
"v_mfma_f32_16x16x32_bf16 v[72:75], a[168:171], v[136:139], v[72:75]\n"
"buffer_load_dwordx4 v[200:203], v228, s[44:47], s65 offen offset:2048 sc1\n"
"v_fma_mix_f32 v227, v149, v211, v219 op_sel:[1,0,0] op_sel_hi:[1,0,0]\n"
"v_fma_mix_f32 v224, v153, v210, v218 op_sel:[0,0,0] op_sel_hi:[1,0,0]\n"
"v_mfma_f32_16x16x32_bf16 v[104:107], a[168:171], v[140:143], v[104:107]\n"
"ds_read_b64_tr_b16 a[138:139], v238 offset:20480\n"
"v_cvt_pk_bf16_f32 v149, v226, v227\n"
"v_fma_mix_f32 v225, v153, v211, v219 op_sel:[1,0,0] op_sel_hi:[1,0,0]\n"
"v_mfma_f32_16x16x32_bf16 v[108:111], a[172:175], v[140:143], v[108:111]\n"
"ds_read_b64_tr_b16 a[140:141], v239 offset:16384\n"
"v_fma_mix_f32 v226, v157, v210, v218 op_sel:[0,0,0] op_sel_hi:[1,0,0]\n"
"v_mfma_f32_16x16x32_bf16 v[76:79], a[172:175], v[136:139], v[76:79]\n"
"ds_read_b64_tr_b16 a[142:143], v239 offset:20480\n"
"v_cvt_pk_bf16_f32 v153, v224, v225\n"
"v_fma_mix_f32 v227, v157, v211, v219 op_sel:[1,0,0] op_sel_hi:[1,0,0]\n"
"v_mfma_f32_16x16x32_bf16 v[44:47], a[172:175], v[132:135], v[44:47]\n"
"s_waitcnt lgkmcnt(8)\n"
"buffer_load_dwordx4 v[204:207], v228, s[44:47], s65 offen offset:3072 sc1\n"
"v_fma_mix_f32 v224, v146, v212, v220 op_sel:[0,0,0] op_sel_hi:[1,0,0]\n"
"v_mfma_f32_16x16x32_bf16 v[12:15], a[172:175], v[128:131], v[12:15]\n"
"ds_read_b64_tr_b16 a[144:145], v240 offset:16384\n"
"v_cvt_pk_bf16_f32 v157, v226, v227\n"
"v_mfma_f32_16x16x32_bf16 v[16:19], a[176:179], v[128:131], v[16:19]\n"
"ds_read_b64_tr_b16 a[146:147], v240 offset:20480\n"
"v_fma_mix_f32 v225, v146, v213, v221 op_sel:[1,0,0] op_sel_hi:[1,0,0]\n"
"v_fma_mix_f32 v226, v150, v212, v220 op_sel:[0,0,0] op_sel_hi:[1,0,0]\n"
"v_mfma_f32_16x16x32_bf16 v[48:51], a[176:179], v[132:135], v[48:51]\n"
"s_add_u32 m0, s60, 65536\n"
"s_mov_b32 s64, 3145728\n"
"ds_read_b64_tr_b16 a[148:149], v241 offset:16384\n"
"v_mfma_f32_16x16x32_bf16 v[80:83], a[176:179], v[136:139], v[80:83]\n"
"buffer_load_dwordx4 v230, s[52:55], s64 offen offset:0 lds\n"
"v_cvt_pk_bf16_f32 v146, v224, v225\n"
"v_mfma_f32_16x16x32_bf16 v[112:115], a[176:179], v[140:143], v[112:115]\n"
"ds_read_b64_tr_b16 a[150:151], v241 offset:20480\n"
"v_fma_mix_f32 v227, v150, v213, v221 op_sel:[1,0,0] op_sel_hi:[1,0,0]\n"
"v_fma_mix_f32 v224, v154, v212, v220 op_sel:[0,0,0] op_sel_hi:[1,0,0]\n"
"v_mfma_f32_16x16x32_bf16 v[116:119], a[180:183], v[140:143], v[116:119]\n"
"ds_read_b64_tr_b16 a[152:153], v242 offset:16384\n"
"v_cvt_pk_bf16_f32 v150, v226, v227\n"
"v_mfma_f32_16x16x32_bf16 v[84:87], a[180:183], v[136:139], v[84:87]\n"
"s_mov_b32 s64, 3177472\n"
"ds_read_b64_tr_b16 a[154:155], v242 offset:20480\n"
"v_mfma_f32_16x16x32_bf16 v[52:55], a[180:183], v[132:135], v[52:55]\n"
"buffer_load_dwordx4 v231, s[52:55], s64 offen offset:1024 lds\n"
"v_fma_mix_f32 v225, v154, v213, v221 op_sel:[1,0,0] op_sel_hi:[1,0,0]\n"
"v_fma_mix_f32 v226, v158, v212, v220 op_sel:[0,0,0] op_sel_hi:[1,0,0]\n"
"v_mfma_f32_16x16x32_bf16 v[20:23], a[180:183], v[128:131], v[20:23]\n"
"ds_read_b64_tr_b16 a[156:157], v243 offset:16384\n"
"v_cvt_pk_bf16_f32 v154, v224, v225\n"
"v_mfma_f32_16x16x32_bf16 v[24:27], a[184:187], v[128:131], v[24:27]\n"
"ds_read_b64_tr_b16 a[158:159], v243 offset:20480\n"
"v_fma_mix_f32 v227, v158, v213, v221 op_sel:[1,0,0] op_sel_hi:[1,0,0]\n"
"v_mfma_f32_16x16x32_bf16 v[56:59], a[184:187], v[132:135], v[56:59]\n"
"s_mov_b32 s64, 3209216\n"
"ds_read_b128 v[208:211], v229 offset:896\n"
"v_fma_mix_f32 v224, v147, v214, v222 op_sel:[0,0,0] op_sel_hi:[1,0,0]\n"
"v_cvt_pk_bf16_f32 v158, v226, v227\n"
"v_mfma_f32_16x16x32_bf16 v[88:91], a[184:187], v[136:139], v[88:91]\n"
"buffer_load_dwordx4 v230, s[52:55], s64 offen offset:2048 lds\n"
"v_fma_mix_f32 v225, v147, v215, v223 op_sel:[1,0,0] op_sel_hi:[1,0,0]\n"
"v_mfma_f32_16x16x32_bf16 v[120:123], a[184:187], v[140:143], v[120:123]\n"
"ds_read_b128 v[216:219], v229 offset:3456\n"
"v_fma_mix_f32 v226, v151, v214, v222 op_sel:[0,0,0] op_sel_hi:[1,0,0]\n"
"v_mfma_f32_16x16x32_bf16 v[124:127], a[188:191], v[140:143], v[124:127]\n"
"v_cvt_pk_bf16_f32 v147, v224, v225\n"
"v_fma_mix_f32 v227, v151, v215, v223 op_sel:[1,0,0] op_sel_hi:[1,0,0]\n"
"v_fma_mix_f32 v224, v155, v214, v222 op_sel:[0,0,0] op_sel_hi:[1,0,0]\n"
"v_mfma_f32_16x16x32_bf16 v[92:95], a[188:191], v[136:139], v[92:95]\n"
"s_mov_b32 s64, 3240960\n"
"v_cvt_pk_bf16_f32 v151, v226, v227\n"
"v_fma_mix_f32 v225, v155, v215, v223 op_sel:[1,0,0] op_sel_hi:[1,0,0]\n"
"v_mfma_f32_16x16x32_bf16 v[60:63], a[188:191], v[132:135], v[60:63]\n"
"buffer_load_dwordx4 v231, s[52:55], s64 offen offset:3072 lds\n"
"v_fma_mix_f32 v226, v159, v214, v222 op_sel:[0,0,0] op_sel_hi:[1,0,0]\n"
"v_mfma_f32_16x16x32_bf16 v[28:31], a[188:191], v[128:131], v[28:31]\n"
"v_cvt_pk_bf16_f32 v155, v224, v225\n"
"v_fma_mix_f32 v227, v159, v215, v223 op_sel:[1,0,0] op_sel_hi:[1,0,0]\n"
"v_cvt_pk_bf16_f32 v159, v226, v227\n"
"s_waitcnt vmcnt(16) lgkmcnt(0)\n"
"v_mfma_f32_16x16x32_bf16 v[0:3], a[128:131], v[144:147], v[0:3]\n"
"ds_read_b128 v[212:215], v229 offset:912\n"
"v_fma_mix_f32 v224, v160, v208, v216 op_sel:[0,0,0] op_sel_hi:[1,0,0]\n"
"v_mfma_f32_16x16x32_bf16 v[32:35], a[128:131], v[148:151], v[32:35]\n"
"s_add_u32 s65, %7, 40960\n"
"ds_read_b128 v[220:223], v229 offset:3472\n"
"v_fma_mix_f32 v225, v160, v209, v217 op_sel:[1,0,0] op_sel_hi:[1,0,0]\n"
"v_mfma_f32_16x16x32_bf16 v[64:67], a[128:131], v[152:155], v[64:67]\n"
"buffer_load_dwordx4 v[128:131], v228, s[44:47], s65 offen offset:0 sc1\n"
"v_fma_mix_f32 v226, v164, v208, v216 op_sel:[0,0,0] op_sel_hi:[1,0,0]\n"
"v_mfma_f32_16x16x32_bf16 v[96:99], a[128:131], v[156:159], v[96:99]\n"
"ds_read_b64_tr_b16 a[160:161], v236 offset:24576\n"
"v_cvt_pk_bf16_f32 v160, v224, v225\n"
"v_mfma_f32_16x16x32_bf16 v[100:103], a[132:135], v[156:159], v[100:103]\n"
"ds_read_b64_tr_b16 a[162:163], v236 offset:28672\n"
"v_fma_mix_f32 v227, v164, v209, v217 op_sel:[1,0,0] op_sel_hi:[1,0,0]\n"
"v_fma_mix_f32 v224, v168, v208, v216 op_sel:[0,0,0] op_sel_hi:[1,0,0]\n"
"v_mfma_f32_16x16x32_bf16 v[68:71], a[132:135], v[152:155], v[68:71]\n"
"ds_read_b64_tr_b16 a[164:165], v237 offset:24576\n"
"v_cvt_pk_bf16_f32 v164, v226, v227\n"
"v_mfma_f32_16x16x32_bf16 v[36:39], a[132:135], v[148:151], v[36:39]\n"
"buffer_load_dwordx4 v[132:135], v228, s[44:47], s65 offen offset:1024 sc1\n"
"v_fma_mix_f32 v225, v168, v209, v217 op_sel:[1,0,0] op_sel_hi:[1,0,0]\n"
"v_mfma_f32_16x16x32_bf16 v[4:7], a[132:135], v[144:147], v[4:7]\n"
"ds_read_b64_tr_b16 a[166:167], v237 offset:28672\n"
"v_fma_mix_f32 v226, v172, v208, v216 op_sel:[0,0,0] op_sel_hi:[1,0,0]\n"
"v_cvt_pk_bf16_f32 v168, v224, v225\n"
"v_mfma_f32_16x16x32_bf16 v[8:11], a[136:139], v[144:147], v[8:11]\n"
"ds_read_b64_tr_b16 a[168:169], v238 offset:24576\n"
"v_fma_mix_f32 v227, v172, v209, v217 op_sel:[1,0,0] op_sel_hi:[1,0,0]\n"
"v_mfma_f32_16x16x32_bf16 v[40:43], a[136:139], v[148:151], v[40:43]\n"
"ds_read_b64_tr_b16 a[170:171], v238 offset:28672\n"
"v_fma_mix_f32 v224, v161, v210, v218 op_sel:[0,0,0] op_sel_hi:[1,0,0]\n"
"v_mfma_f32_16x16x32_bf16 v[72:75], a[136:139], v[152:155], v[72:75]\n"
"buffer_load_dwordx4 v[136:139], v228, s[44:47], s65 offen offset:2048 sc1\n"
"v_cvt_pk_bf16_f32 v172, v226, v227\n"
"v_fma_mix_f32 v225, v161, v211, v219 op_sel:[1,0,0] op_sel_hi:[1,0,0]\n"
"v_mfma_f32_16x16x32_bf16 v[104:107], a[136:139], v[156:159], v[104:107]\n"
"ds_read_b64_tr_b16 a[172:173], v239 offset:24576\n"
"v_fma_mix_f32 v226, v165, v210, v218 op_sel:[0,0,0] op_sel_hi:[1,0,0]\n"
"v_mfma_f32_16x16x32_bf16 v[108:111], a[140:143], v[156:159], v[108:111]\n"
"ds_read_b64_tr_b16 a[174:175], v239 offset:28672\n"
"v_cvt_pk_bf16_f32 v161, v224, v225\n"
"v_mfma_f32_16x16x32_bf16 v[76:79], a[140:143], v[152:155], v[76:79]\n"
"ds_read_b64_tr_b16 a[176:177], v240 offset:24576\n"
"v_fma_mix_f32 v227, v165, v211, v219 op_sel:[1,0,0] op_sel_hi:[1,0,0]\n"
"v_fma_mix_f32 v224, v169, v210, v218 op_sel:[0,0,0] op_sel_hi:[1,0,0]\n"
"v_mfma_f32_16x16x32_bf16 v[44:47], a[140:143], v[148:151], v[44:47]\n"
"buffer_load_dwordx4 v[140:143], v228, s[44:47], s65 offen offset:3072 sc1\n"
"v_cvt_pk_bf16_f32 v165, v226, v227\n"
"v_mfma_f32_16x16x32_bf16 v[12:15], a[140:143], v[144:147], v[12:15]\n"
"ds_read_b64_tr_b16 a[178:179], v240 offset:28672\n"
"v_fma_mix_f32 v225, v169, v211, v219 op_sel:[1,0,0] op_sel_hi:[1,0,0]\n"
"v_mfma_f32_16x16x32_bf16 v[16:19], a[144:147], v[144:147], v[16:19]\n"
"ds_read_b64_tr_b16 a[180:181], v241 offset:24576\n"
"v_fma_mix_f32 v226, v173, v210, v218 op_sel:[0,0,0] op_sel_hi:[1,0,0]\n"
"v_cvt_pk_bf16_f32 v169, v224, v225\n"
"v_mfma_f32_16x16x32_bf16 v[48:51], a[144:147], v[148:151], v[48:51]\n"
"ds_read_b64_tr_b16 a[182:183], v241 offset:28672\n"
"v_fma_mix_f32 v227, v173, v211, v219 op_sel:[1,0,0] op_sel_hi:[1,0,0]\n"
"v_mfma_f32_16x16x32_bf16 v[80:83], a[144:147], v[152:155], v[80:83]\n"
"s_waitcnt lgkmcnt(12)\n"
"ds_read_b64_tr_b16 a[184:185], v242 offset:24576\n"
"v_fma_mix_f32 v224, v162, v212, v220 op_sel:[0,0,0] op_sel_hi:[1,0,0]\n"
"v_mfma_f32_16x16x32_bf16 v[112:115], a[144:147], v[156:159], v[112:115]\n"
"ds_read_b64_tr_b16 a[186:187], v242 offset:28672\n"
"v_cvt_pk_bf16_f32 v173, v226, v227\n"
"v_fma_mix_f32 v225, v162, v213, v221 op_sel:[1,0,0] op_sel_hi:[1,0,0]\n"
"v_mfma_f32_16x16x32_bf16 v[116:119], a[148:151], v[156:159], v[116:119]\n"
"L_w3_%=:\n"
"s_cbranch_execz L_wd0_%=\n"
"ds_read_b64_tr_b16 a[188:189], v243 offset:24576\n"
"v_fma_mix_f32 v226, v166, v212, v220 op_sel:[0,0,0] op_sel_hi:[1,0,0]\n"
"v_mfma_f32_16x16x32_bf16 v[84:87], a[148:151], v[152:155], v[84:87]\n"
"ds_read_b64_tr_b16 a[190:191], v243 offset:28672\n"
"v_cvt_pk_bf16_f32 v162, v224, v225\n"
"v_mfma_f32_16x16x32_bf16 v[52:55], a[148:151], v[148:151], v[52:55]\n"
"v_fma_mix_f32 v227, v166, v213, v221 op_sel:[1,0,0] op_sel_hi:[1,0,0]\n"
"v_fma_mix_f32 v224, v170, v212, v220 op_sel:[0,0,0] op_sel_hi:[1,0,0]\n"
"v_cvt_pk_bf16_f32 v166, v226, v227\n"
"v_mfma_f32_16x16x32_bf16 v[20:23], a[148:151], v[144:147], v[20:23]\n"
"v_fma_mix_f32 v225, v170, v213, v221 op_sel:[1,0,0] op_sel_hi:[1,0,0]\n"
"v_fma_mix_f32 v226, v174, v212, v220 op_sel:[0,0,0] op_sel_hi:[1,0,0]\n"
"v_mfma_f32_16x16x32_bf16 v[24:27], a[152:155], v[144:147], v[24:27]\n"
"ds_read_b128 v[208:211], v229 offset:1024\n"
"v_cvt_pk_bf16_f32 v170, v224, v225\n"
"v_mfma_f32_16x16x32_bf16 v[56:59], a[152:155], v[148:151], v[56:59]\n"
"ds_read_b128 v[216:219], v229 offset:3584\n"
"v_fma_mix_f32 v227, v174, v213, v221 op_sel:[1,0,0] op_sel_hi:[1,0,0]\n"
"v_fma_mix_f32 v224, v163, v214, v222 op_sel:[0,0,0] op_sel_hi:[1,0,0]\n"
"v_mfma_f32_16x16x32_bf16 v[88:91], a[152:155], v[152:155], v[88:91]\n"
"v_cvt_pk_bf16_f32 v174, v226, v227\n"
"v_fma_mix_f32 v225, v163, v215, v223 op_sel:[1,0,0] op_sel_hi:[1,0,0]\n"
"v_mfma_f32_16x16x32_bf16 v[120:123], a[152:155], v[156:159], v[120:123]\n"
"v_fma_mix_f32 v226, v167, v214, v222 op_sel:[0,0,0] op_sel_hi:[1,0,0]\n"
"v_cvt_pk_bf16_f32 v163, v224, v225\n"
"v_mfma_f32_16x16x32_bf16 v[124:127], a[156:159], v[156:159], v[124:127]\n"
"v_fma_mix_f32 v227, v167, v215, v223 op_sel:[1,0,0] op_sel_hi:[1,0,0]\n"
"v_fma_mix_f32 v224, v171, v214, v222 op_sel:[0,0,0] op_sel_hi:[1,0,0]\n"
"v_mfma_f32_16x16x32_bf16 v[92:95], a[156:159], v[152:155], v[92:95]\n"
"v_cvt_pk_bf16_f32 v167, v226, v227\n"
"v_fma_mix_f32 v225, v171, v215, v223 op_sel:[1,0,0] op_sel_hi:[1,0,0]\n"
"v_mfma_f32_16x16x32_bf16 v[60:63], a[156:159], v[148:151], v[60:63]\n"
"v_fma_mix_f32 v226, v175, v214, v222 op_sel:[0,0,0] op_sel_hi:[1,0,0]\n"
"v_cvt_pk_bf16_f32 v171, v224, v225\n"
"v_mfma_f32_16x16x32_bf16 v[28:31], a[156:159], v[144:147], v[28:31]\n"
"v_fma_mix_f32 v227, v175, v215, v223 op_sel:[1,0,0] op_sel_hi:[1,0,0]\n"
"v_cvt_pk_bf16_f32 v175, v226, v227\n"
"s_waitcnt vmcnt(12) lgkmcnt(0)\n"
"v_mfma_f32_16x16x32_bf16 v[0:3], a[160:163], v[160:163], v[0:3]\n"
"s_barrier\n"
"v_fma_mix_f32 v224, v176, v208, v216 op_sel:[0,0,0] op_sel_hi:[1,0,0]\n"
"v_fma_mix_f32 v225, v176, v209, v217 op_sel:[1,0,0] op_sel_hi:[1,0,0]\n"
"v_mfma_f32_16x16x32_bf16 v[32:35], a[160:163], v[164:167], v[32:35]\n"
"s_add_u32 s65, %7, 45056\n"
"ds_read_b128 v[212:215], v229 offset:1040\n"
"v_fma_mix_f32 v226, v180, v208, v216 op_sel:[0,0,0] op_sel_hi:[1,0,0]\n"
"v_mfma_f32_16x16x32_bf16 v[64:67], a[160:163], v[168:171], v[64:67]\n"
"buffer_load_dwordx4 v[144:147], v228, s[44:47], s65 offen offset:0 sc1\n"
"v_cvt_pk_bf16_f32 v176, v224, v225\n"
"v_fma_mix_f32 v227, v180, v209, v217 op_sel:[1,0,0] op_sel_hi:[1,0,0]\n"
"v_mfma_f32_16x16x32_bf16 v[96:99], a[160:163], v[172:175], v[96:99]\n"
"ds_read_b128 v[220:223], v229 offset:3600\n"
"v_fma_mix_f32 v224, v184, v208, v216 op_sel:[0,0,0] op_sel_hi:[1,0,0]\n"
"v_mfma_f32_16x16x32_bf16 v[100:103], a[164:167], v[172:175], v[100:103]\n"
"ds_read_b64_tr_b16 a[128:129], v236 offset:32768\n"
"v_cvt_pk_bf16_f32 v180, v226, v227\n"
"v_fma_mix_f32 v225, v184, v209, v217 op_sel:[1,0,0] op_sel_hi:[1,0,0]\n"
"v_mfma_f32_16x16x32_bf16 v[68:71], a[164:167], v[168:171], v[68:71]\n"
"ds_read_b64_tr_b16 a[130:131], v236 offset:36864\n"
"v_fma_mix_f32 v226, v188, v208, v216 op_sel:[0,0,0] op_sel_hi:[1,0,0]\n"
"v_cvt_pk_bf16_f32 v184, v224, v225\n"
"v_mfma_f32_16x16x32_bf16 v[36:39], a[164:167], v[164:167], v[36:39]\n"
"buffer_load_dwordx4 v[148:151], v228, s[44:47], s65 offen offset:1024 sc1\n"
"v_fma_mix_f32 v227, v188, v209, v217 op_sel:[1,0,0] op_sel_hi:[1,0,0]\n"
"v_mfma_f32_16x16x32_bf16 v[4:7], a[164:167], v[160:163], v[4:7]\n"
"ds_read_b64_tr_b16 a[132:133], v237 offset:32768\n"
"v_fma_mix_f32 v224, v177, v210, v218 op_sel:[0,0,0] op_sel_hi:[1,0,0]\n"
"v_cvt_pk_bf16_f32 v188, v226, v227\n"
"v_mfma_f32_16x16x32_bf16 v[8:11], a[168:171], v[160:163], v[8:11]\n"
"ds_read_b64_tr_b16 a[134:135], v237 offset:36864\n"
"v_fma_mix_f32 v225, v177, v211, v219 op_sel:[1,0,0] op_sel_hi:[1,0,0]\n"
"v_fma_mix_f32 v226, v181, v210, v218 op_sel:[0,0,0] op_sel_hi:[1,0,0]\n"
"v_mfma_f32_16x16x32_bf16 v[40:43], a[168:171], v[164:167], v[40:43]\n"
"ds_read_b64_tr_b16 a[136:137], v238 offset:32768\n"
"v_cvt_pk_bf16_f32 v177, v224, v225\n"
"v_mfma_f32_16x16x32_bf16 v[72:75], a[168:171], v[168:171], v[72:75]\n"
"buffer_load_dwordx4 v[152:155], v228, s[44:47], s65 offen offset:2048 sc1\n"
"v_fma_mix_f32 v227, v181, v211, v219 op_sel:[1,0,0] op_sel_hi:[1,0,0]\n"
"v_fma_mix_f32 v224, v185, v210, v218 op_sel:[0,0,0] op_sel_hi:[1,0,0]\n"
"v_mfma_f32_16x16x32_bf16 v[104:107], a[168:171], v[172:175], v[104:107]\n"
"ds_read_b64_tr_b16 a[138:139], v238 offset:36864\n"
"v_cvt_pk_bf16_f32 v181, v226, v227\n"
"v_mfma_f32_16x16x32_bf16 v[108:111], a[172:175], v[172:175], v[108:111]\n"
"ds_read_b64_tr_b16 a[140:141], v239 offset:32768\n"
"v_fma_mix_f32 v225, v185, v211, v219 op_sel:[1,0,0] op_sel_hi:[1,0,0]\n"
"v_mfma_f32_16x16x32_bf16 v[76:79], a[172:175], v[168:171], v[76:79]\n"
"ds_read_b64_tr_b16 a[142:143], v239 offset:36864\n"
"v_fma_mix_f32 v226, v189, v210, v218 op_sel:[0,0,0] op_sel_hi:[1,0,0]\n"
"v_cvt_pk_bf16_f32 v185, v224, v225\n"
"v_mfma_f32_16x16x32_bf16 v[44:47], a[172:175], v[164:167], v[44:47]\n"
"buffer_load_dwordx4 v[156:159], v228, s[44:47], s65 offen offset:3072 sc1\n"
"v_fma_mix_f32 v227, v189, v211, v219 op_sel:[1,0,0] op_sel_hi:[1,0,0]\n"
"v_mfma_f32_16x16x32_bf16 v[12:15], a[172:175], v[160:163], v[12:15]\n"
"s_waitcnt lgkmcnt(8)\n"
"ds_read_b64_tr_b16 a[144:145], v240 offset:32768\n"
"v_fma_mix_f32 v224, v178, v212, v220 op_sel:[0,0,0] op_sel_hi:[1,0,0]\n"
"v_mfma_f32_16x16x32_bf16 v[16:19], a[176:179], v[160:163], v[16:19]\n"
"ds_read_b64_tr_b16 a[146:147], v240 offset:36864\n"
"v_cvt_pk_bf16_f32 v189, v226, v227\n"
"v_fma_mix_f32 v225, v178, v213, v221 op_sel:[1,0,0] op_sel_hi:[1,0,0]\n"
"v_mfma_f32_16x16x32_bf16 v[48:51], a[176:179], v[164:167], v[48:51]\n"
"s_add_u32 m0, s60, 81920\n"
"s_mov_b32 s64, 3670016\n"
"ds_read_b64_tr_b16 a[148:149], v241 offset:32768\n"
"v_mfma_f32_16x16x32_bf16 v[80:83], a[176:179], v[168:171], v[80:83]\n"
"buffer_load_dwordx4 v230, s[52:55], s64 offen offset:0 lds\n"
"v_fma_mix_f32 v226, v182, v212, v220 op_sel:[0,0,0] op_sel_hi:[1,0,0]\n"
"v_mfma_f32_16x16x32_bf16 v[112:115], a[176:179], v[172:175], v[112:115]\n"
"ds_read_b64_tr_b16 a[150:151], v241 offset:36864\n"
"v_cvt_pk_bf16_f32 v178, v224, v225\n"
"v_fma_mix_f32 v227, v182, v213, v221 op_sel:[1,0,0] op_sel_hi:[1,0,0]\n"
"v_mfma_f32_16x16x32_bf16 v[116:119], a[180:183], v[172:175], v[116:119]\n"
"ds_read_b64_tr_b16 a[152:153], v242 offset:32768\n"
"v_fma_mix_f32 v224, v186, v212, v220 op_sel:[0,0,0] op_sel_hi:[1,0,0]\n"
"v_mfma_f32_16x16x32_bf16 v[84:87], a[180:183], v[168:171], v[84:87]\n"
"s_mov_b32 s64, 3701760\n"
"ds_read_b64_tr_b16 a[154:155], v242 offset:36864\n"
"v_mfma_f32_16x16x32_bf16 v[52:55], a[180:183], v[164:167], v[52:55]\n"
"buffer_load_dwordx4 v231, s[52:55], s64 offen offset:1024 lds\n"
"v_cvt_pk_bf16_f32 v182, v226, v227\n"
"v_fma_mix_f32 v225, v186, v213, v221 op_sel:[1,0,0] op_sel_hi:[1,0,0]\n"
"v_mfma_f32_16x16x32_bf16 v[20:23], a[180:183], v[160:163], v[20:23]\n"
"ds_read_b64_tr_b16 a[156:157], v243 offset:32768\n"
"v_fma_mix_f32 v226, v190, v212, v220 op_sel:[0,0,0] op_sel_hi:[1,0,0]\n"
"v_mfma_f32_16x16x32_bf16 v[24:27], a[184:187], v[160:163], v[24:27]\n"
"ds_read_b64_tr_b16 a[158:159], v243 offset:36864\n"
"v_cvt_pk_bf16_f32 v186, v224, v225\n"
"v_mfma_f32_16x16x32_bf16 v[56:59], a[184:187], v[164:167], v[56:59]\n"
"s_mov_b32 s64, 3733504\n"
"ds_read_b128 v[208:211], v229 offset:1152\n"
"v_fma_mix_f32 v227, v190, v213, v221 op_sel:[1,0,0] op_sel_hi:[1,0,0]\n"
"v_fma_mix_f32 v224, v179, v214, v222 op_sel:[0,0,0] op_sel_hi:[1,0,0]\n"
"v_mfma_f32_16x16x32_bf16 v[88:91], a[184:187], v[168:171], v[88:91]\n"
"buffer_load_dwordx4 v230, s[52:55], s64 offen offset:2048 lds\n"
"v_cvt_pk_bf16_f32 v190, v226, v227\n"
"v_mfma_f32_16x16x32_bf16 v[120:123], a[184:187], v[172:175], v[120:123]\n"
"ds_read_b128 v[216:219], v229 offset:3712\n"
"v_fma_mix_f32 v225, v179, v215, v223 op_sel:[1,0,0] op_sel_hi:[1,0,0]\n"
"v_mfma_f32_16x16x32_bf16 v[124:127], a[188:191], v[172:175], v[124:127]\n"
"v_fma_mix_f32 v226, v183, v214, v222 op_sel:[0,0,0] op_sel_hi:[1,0,0]\n"
"v_cvt_pk_bf16_f32 v179, v224, v225\n"
"v_fma_mix_f32 v227, v183, v215, v223 op_sel:[1,0,0] op_sel_hi:[1,0,0]\n"
"v_mfma_f32_16x16x32_bf16 v[92:95], a[188:191], v[168:171], v[92:95]\n"
"v_fma_mix_f32 v224, v187, v214, v222 op_sel:[0,0,0] op_sel_hi:[1,0,0]\n"
"v_cvt_pk_bf16_f32 v183, v226, v227\n"
"v_mfma_f32_16x16x32_bf16 v[60:63], a[188:191], v[164:167], v[60:63]\n"
"v_fma_mix_f32 v225, v187, v215, v223 op_sel:[1,0,0] op_sel_hi:[1,0,0]\n"
"v_fma_mix_f32 v226, v191, v214, v222 op_sel:[0,0,0] op_sel_hi:[1,0,0]\n"
"v_mfma_f32_16x16x32_bf16 v[28:31], a[188:191], v[160:163], v[28:31]\n"
"v_cvt_pk_bf16_f32 v187, v224, v225\n"
"v_fma_mix_f32 v227, v191, v215, v223 op_sel:[1,0,0] op_sel_hi:[1,0,0]\n"
"v_cvt_pk_bf16_f32 v191, v226, v227\n"
"s_waitcnt vmcnt(15) lgkmcnt(0)\n"
"v_mfma_f32_16x16x32_bf16 v[0:3], a[128:131], v[176:179], v[0:3]\n"
"ds_read_b128 v[212:215], v229 offset:1168\n"
"v_fma_mix_f32 v224, v192, v208, v216 op_sel:[0,0,0] op_sel_hi:[1,0,0]\n"
"v_mfma_f32_16x16x32_bf16 v[32:35], a[128:131], v[180:183], v[32:35]\n"
"s_add_u32 s65, %7, 49152\n"
"ds_read_b128 v[220:223], v229 offset:3728\n"
"v_fma_mix_f32 v225, v192, v209, v217 op_sel:[1,0,0] op_sel_hi:[1,0,0]\n"
"v_mfma_f32_16x16x32_bf16 v[64:67], a[128:131], v[184:187], v[64:67]\n"
"buffer_load_dwordx4 v[160:163], v228, s[44:47], s65 offen offset:0 sc1\n"
"v_fma_mix_f32 v226, v196, v208, v216 op_sel:[0,0,0] op_sel_hi:[1,0,0]\n"
"v_mfma_f32_16x16x32_bf16 v[96:99], a[128:131], v[188:191], v[96:99]\n"
"ds_read_b64_tr_b16 a[160:161], v236 offset:40960\n"
"v_cvt_pk_bf16_f32 v192, v224, v225\n"
"v_mfma_f32_16x16x32_bf16 v[100:103], a[132:135], v[188:191], v[100:103]\n"
"ds_read_b64_tr_b16 a[162:163], v236 offset:45056\n"
"v_fma_mix_f32 v227, v196, v209, v217 op_sel:[1,0,0] op_sel_hi:[1,0,0]\n"
"v_fma_mix_f32 v224, v200, v208, v216 op_sel:[0,0,0] op_sel_hi:[1,0,0]\n"
"v_mfma_f32_16x16x32_bf16 v[68:71], a[132:135], v[184:187], v[68:71]\n"
"ds_read_b64_tr_b16 a[164:165], v237 offset:40960\n"
"v_cvt_pk_bf16_f32 v196, v226, v227\n"
"v_mfma_f32_16x16x32_bf16 v[36:39], a[132:135], v[180:183], v[36:39]\n"
"buffer_load_dwordx4 v[164:167], v228, s[44:47], s65 offen offset:1024 sc1\n"
"v_fma_mix_f32 v225, v200, v209, v217 op_sel:[1,0,0] op_sel_hi:[1,0,0]\n"
"v_mfma_f32_16x16x32_bf16 v[4:7], a[132:135], v[176:179], v[4:7]\n"
"ds_read_b64_tr_b16 a[166:167], v237 offset:45056\n"
"v_fma_mix_f32 v226, v204, v208, v216 op_sel:[0,0,0] op_sel_hi:[1,0,0]\n"
"v_cvt_pk_bf16_f32 v200, v224, v225\n"
"v_mfma_f32_16x16x32_bf16 v[8:11], a[136:139], v[176:179], v[8:11]\n"
"ds_read_b64_tr_b16 a[168:169], v238 offset:40960\n"
"v_fma_mix_f32 v227, v204, v209, v217 op_sel:[1,0,0] op_sel_hi:[1,0,0]\n"
"v_mfma_f32_16x16x32_bf16 v[40:43], a[136:139], v[180:183], v[40:43]\n"
"ds_read_b64_tr_b16 a[170:171], v238 offset:45056\n"
"v_fma_mix_f32 v224, v193, v210, v218 op_sel:[0,0,0] op_sel_hi:[1,0,0]\n"
"v_mfma_f32_16x16x32_bf16 v[72:75], a[136:139], v[184:187], v[72:75]\n"
"buffer_load_dwordx4 v[168:171], v228, s[44:47], s65 offen offset:2048 sc1\n"
"v_cvt_pk_bf16_f32 v204, v226, v227\n"
"v_fma_mix_f32 v225, v193, v211, v219 op_sel:[1,0,0] op_sel_hi:[1,0,0]\n"
"v_mfma_f32_16x16x32_bf16 v[104:107], a[136:139], v[188:191], v[104:107]\n"
"ds_read_b64_tr_b16 a[172:173], v239 offset:40960\n"
"v_fma_mix_f32 v226, v197, v210, v218 op_sel:[0,0,0] op_sel_hi:[1,0,0]\n"
"v_mfma_f32_16x16x32_bf16 v[108:111], a[140:143], v[188:191], v[108:111]\n"
"ds_read_b64_tr_b16 a[174:175], v239 offset:45056\n"
"v_cvt_pk_bf16_f32 v193, v224, v225\n"
"v_mfma_f32_16x16x32_bf16 v[76:79], a[140:143], v[184:187], v[76:79]\n"
"ds_read_b64_tr_b16 a[176:177], v240 offset:40960\n"
"v_fma_mix_f32 v227, v197, v211, v219 op_sel:[1,0,0] op_sel_hi:[1,0,0]\n"
"v_fma_mix_f32 v224, v201, v210, v218 op_sel:[0,0,0] op_sel_hi:[1,0,0]\n"
"v_mfma_f32_16x16x32_bf16 v[44:47], a[140:143], v[180:183], v[44:47]\n"
"buffer_load_dwordx4 v[172:175], v228, s[44:47], s65 offen offset:3072 sc1\n"
"v_cvt_pk_bf16_f32 v197, v226, v227\n"
"v_mfma_f32_16x16x32_bf16 v[12:15], a[140:143], v[176:179], v[12:15]\n"
"ds_read_b64_tr_b16 a[178:179], v240 offset:45056\n"
"v_fma_mix_f32 v225, v201, v211, v219 op_sel:[1,0,0] op_sel_hi:[1,0,0]\n"
"v_mfma_f32_16x16x32_bf16 v[16:19], a[144:147], v[176:179], v[16:19]\n"
"ds_read_b64_tr_b16 a[180:181], v241 offset:40960\n"
"v_fma_mix_f32 v226, v205, v210, v218 op_sel:[0,0,0] op_sel_hi:[1,0,0]\n"
"v_cvt_pk_bf16_f32 v201, v224, v225\n"
"v_mfma_f32_16x16x32_bf16 v[48:51], a[144:147], v[180:183], v[48:51]\n"
"s_mov_b32 s64, 3765248\n"
"ds_read_b64_tr_b16 a[182:183], v241 offset:45056\n"
"v_mfma_f32_16x16x32_bf16 v[80:83], a[144:147], v[184:187], v[80:83]\n"
"buffer_load_dwordx4 v231, s[52:55], s64 offen offset:3072 lds\n"
"v_fma_mix_f32 v227, v205, v211, v219 op_sel:[1,0,0] op_sel_hi:[1,0,0]\n"
"v_mfma_f32_16x16x32_bf16 v[112:115], a[144:147], v[188:191], v[112:115]\n"
"s_waitcnt lgkmcnt(12)\n"
"ds_read_b64_tr_b16 a[184:185], v242 offset:40960\n"
"v_fma_mix_f32 v224, v194, v212, v220 op_sel:[0,0,0] op_sel_hi:[1,0,0]\n"
"v_cvt_pk_bf16_f32 v205, v226, v227\n"
"v_mfma_f32_16x16x32_bf16 v[116:119], a[148:151], v[188:191], v[116:119]\n"
"ds_read_b64_tr_b16 a[186:187], v242 offset:45056\n"
"v_fma_mix_f32 v225, v194, v213, v221 op_sel:[1,0,0] op_sel_hi:[1,0,0]\n"
"v_mfma_f32_16x16x32_bf16 v[84:87], a[148:151], v[184:187], v[84:87]\n"
"ds_read_b64_tr_b16 a[188:189], v243 offset:40960\n"
"v_fma_mix_f32 v226, v198, v212, v220 op_sel:[0,0,0] op_sel_hi:[1,0,0]\n"
"v_mfma_f32_16x16x32_bf16 v[52:55], a[148:151], v[180:183], v[52:55]\n"
"ds_read_b64_tr_b16 a[190:191], v243 offset:45056\n"
"v_cvt_pk_bf16_f32 v194, v224, v225\n"
"v_fma_mix_f32 v227, v198, v213, v221 op_sel:[1,0,0] op_sel_hi:[1,0,0]\n"
"v_mfma_f32_16x16x32_bf16 v[20:23], a[148:151], v[176:179], v[20:23]\n"
"v_fma_mix_f32 v224, v202, v212, v220 op_sel:[0,0,0] op_sel_hi:[1,0,0]\n"
"v_cvt_pk_bf16_f32 v198, v226, v227\n"
"v_mfma_f32_16x16x32_bf16 v[24:27], a[152:155], v[176:179], v[24:27]\n"
"ds_read_b128 v[208:211], v229 offset:1280\n"
"v_fma_mix_f32 v225, v202, v213, v221 op_sel:[1,0,0] op_sel_hi:[1,0,0]\n"
"v_mfma_f32_16x16x32_bf16 v[56:59], a[152:155], v[180:183], v[56:59]\n"
"ds_read_b128 v[216:219], v229 offset:3840\n"
"v_fma_mix_f32 v226, v206, v212, v220 op_sel:[0,0,0] op_sel_hi:[1,0,0]\n"
"v_cvt_pk_bf16_f32 v202, v224, v225\n"
"v_mfma_f32_16x16x32_bf16 v[88:91], a[152:155], v[184:187], v[88:91]\n"
"v_fma_mix_f32 v227, v206, v213, v221 op_sel:[1,0,0] op_sel_hi:[1,0,0]\n"
"v_fma_mix_f32 v224, v195, v214, v222 op_sel:[0,0,0] op_sel_hi:[1,0,0]\n"
"v_mfma_f32_16x16x32_bf16 v[120:123], a[152:155], v[188:191], v[120:123]\n"
"v_cvt_pk_bf16_f32 v206, v226, v227\n"
"v_fma_mix_f32 v225, v195, v215, v223 op_sel:[1,0,0] op_sel_hi:[1,0,0]\n"
"v_mfma_f32_16x16x32_bf16 v[124:127], a[156:159], v[188:191], v[124:127]\n"
"v_fma_mix_f32 v226, v199, v214, v222 op_sel:[0,0,0] op_sel_hi:[1,0,0]\n"
"v_cvt_pk_bf16_f32 v195, v224, v225\n"
"v_fma_mix_f32 v227, v199, v215, v223 op_sel:[1,0,0] op_sel_hi:[1,0,0]\n"
"v_mfma_f32_16x16x32_bf16 v[92:95], a[156:159], v[184:187], v[92:95]\n"
"v_fma_mix_f32 v224, v203, v214, v222 op_sel:[0,0,0] op_sel_hi:[1,0,0]\n"
"v_cvt_pk_bf16_f32 v199, v226, v227\n"
"v_mfma_f32_16x16x32_bf16 v[60:63], a[156:159], v[180:183], v[60:63]\n"
"v_fma_mix_f32 v225, v203, v215, v223 op_sel:[1,0,0] op_sel_hi:[1,0,0]\n"
"v_fma_mix_f32 v226, v207, v214, v222 op_sel:[0,0,0] op_sel_hi:[1,0,0]\n"
"v_mfma_f32_16x16x32_bf16 v[28:31], a[156:159], v[176:179], v[28:31]\n"
"v_cvt_pk_bf16_f32 v203, v224, v225\n"
"v_fma_mix_f32 v227, v207, v215, v223 op_sel:[1,0,0] op_sel_hi:[1,0,0]\n"
"v_cvt_pk_bf16_f32 v207, v226, v227\n"
"s_waitcnt vmcnt(12) lgkmcnt(0)\n"
"v_mfma_f32_16x16x32_bf16 v[0:3], a[160:163], v[192:195], v[0:3]\n"
"s_barrier\n"
"v_fma_mix_f32 v224, v128, v208, v216 op_sel:[0,0,0] op_sel_hi:[1,0,0]\n"
"v_fma_mix_f32 v225, v128, v209, v217 op_sel:[1,0,0] op_sel_hi:[1,0,0]\n"
"v_mfma_f32_16x16x32_bf16 v[32:35], a[160:163], v[196:199], v[32:35]\n"
"s_add_u32 s65, %7, 53248\n"
"ds_read_b128 v[212:215], v229 offset:1296\n"
"v_fma_mix_f32 v226, v132, v208, v216 op_sel:[0,0,0] op_sel_hi:[1,0,0]\n"
"v_mfma_f32_16x16x32_bf16 v[64:67], a[160:163], v[200:203], v[64:67]\n"
"buffer_load_dwordx4 v[176:179], v228, s[44:47], s65 offen offset:0 sc1\n"
"v_cvt_pk_bf16_f32 v128, v224, v225\n"
"v_fma_mix_f32 v227, v132, v209, v217 op_sel:[1,0,0] op_sel_hi:[1,0,0]\n"
"v_mfma_f32_16x16x32_bf16 v[96:99], a[160:163], v[204:207], v[96:99]\n"
"ds_read_b128 v[220:223], v229 offset:3856\n"
"v_fma_mix_f32 v224, v136, v208, v216 op_sel:[0,0,0] op_sel_hi:[1,0,0]\n"
"v_mfma_f32_16x16x32_bf16 v[100:103], a[164:167], v[204:207], v[100:103]\n"
"ds_read_b64_tr_b16 a[128:129], v236 offset:49152\n"
"v_cvt_pk_bf16_f32 v132, v226, v227\n"
"v_fma_mix_f32 v225, v136, v209, v217 op_sel:[1,0,0] op_sel_hi:[1,0,0]\n"
"v_mfma_f32_16x16x32_bf16 v[68:71], a[164:167], v[200:203], v[68:71]\n"
"ds_read_b64_tr_b16 a[130:131], v236 offset:53248\n"
"v_fma_mix_f32 v226, v140, v208, v216 op_sel:[0,0,0] op_sel_hi:[1,0,0]\n"
"v_cvt_pk_bf16_f32 v136, v224, v225\n"
"v_mfma_f32_16x16x32_bf16 v[36:39], a[164:167], v[196:199], v[36:39]\n"
"buffer_load_dwordx4 v[180:183], v228, s[44:47], s65 offen offset:1024 sc1\n"
"v_fma_mix_f32 v227, v140, v209, v217 op_sel:[1,0,0] op_sel_hi:[1,0,0]\n"
"v_mfma_f32_16x16x32_bf16 v[4:7], a[164:167], v[192:195], v[4:7]\n"
"ds_read_b64_tr_b16 a[132:133], v237 offset:49152\n"
"v_fma_mix_f32 v224, v129, v210, v218 op_sel:[0,0,0] op_sel_hi:[1,0,0]\n"
"v_cvt_pk_bf16_f32 v140, v226, v227\n"
"v_mfma_f32_16x16x32_bf16 v[8:11], a[168:171], v[192:195], v[8:11]\n"
"ds_read_b64_tr_b16 a[134:135], v237 offset:53248\n"
"v_fma_mix_f32 v225, v129, v211, v219 op_sel:[1,0,0] op_sel_hi:[1,0,0]\n"
"v_fma_mix_f32 v226, v133, v210, v218 op_sel:[0,0,0] op_sel_hi:[1,0,0]\n"
"v_mfma_f32_16x16x32_bf16 v[40:43], a[168:171], v[196:199], v[40:43]\n"
"ds_read_b64_tr_b16 a[136:137], v238 offset:49152\n"
"v_cvt_pk_bf16_f32 v129, v224, v225\n"
"v_mfma_f32_16x16x32_bf16 v[72:75], a[168:171], v[200:203], v[72:75]\n"
"buffer_load_dwordx4 v[184:187], v228, s[44:47], s65 offen offset:2048 sc1\n"
"v_fma_mix_f32 v227, v133, v211, v219 op_sel:[1,0,0] op_sel_hi:[1,0,0]\n"
"v_fma_mix_f32 v224, v137, v210, v218 op_sel:[0,0,0] op_sel_hi:[1,0,0]\n"
"v_mfma_f32_16x16x32_bf16 v[104:107], a[168:171], v[204:207], v[104:107]\n"
"ds_read_b64_tr_b16 a[138:139], v238 offset:53248\n"
"v_cvt_pk_bf16_f32 v133, v226, v227\n"
"v_mfma_f32_16x16x32_bf16 v[108:111], a[172:175], v[204:207], v[108:111]\n"
"ds_read_b64_tr_b16 a[140:141], v239 offset:49152\n"
"v_fma_mix_f32 v225, v137, v211, v219 op_sel:[1,0,0] op_sel_hi:[1,0,0]\n"
"v_mfma_f32_16x16x32_bf16 v[76:79], a[172:175], v[200:203], v[76:79]\n"
"ds_read_b64_tr_b16 a[142:143], v239 offset:53248\n"
"v_fma_mix_f32 v226, v141, v210, v218 op_sel:[0,0,0] op_sel_hi:[1,0,0]\n"
"v_cvt_pk_bf16_f32 v137, v224, v225\n"
"v_mfma_f32_16x16x32_bf16 v[44:47], a[172:175], v[196:199], v[44:47]\n"
"buffer_load_dwordx4 v[188:191], v228, s[44:47], s65 offen offset:3072 sc1\n"
"v_fma_mix_f32 v227, v141, v211, v219 op_sel:[1,0,0] op_sel_hi:[1,0,0]\n"
"v_mfma_f32_16x16x32_bf16 v[12:15], a[172:175], v[192:195], v[12:15]\n"
"s_waitcnt lgkmcnt(8)\n"
"ds_read_b64_tr_b16 a[144:145], v240 offset:49152\n"
"v_fma_mix_f32 v224, v130, v212, v220 op_sel:[0,0,0] op_sel_hi:[1,0,0]\n"
"v_mfma_f32_16x16x32_bf16 v[16:19], a[176:179], v[192:195], v[16:19]\n"
"ds_read_b64_tr_b16 a[146:147], v240 offset:53248\n"
"v_cvt_pk_bf16_f32 v141, v226, v227\n"
"v_fma_mix_f32 v225, v130, v213, v221 op_sel:[1,0,0] op_sel_hi:[1,0,0]\n"
"v_mfma_f32_16x16x32_bf16 v[48:51], a[176:179], v[196:199], v[48:51]\n"
"s_add_u32 m0, s60, 98304\n"
"s_mov_b32 s64, 4194304\n"
"ds_read_b64_tr_b16 a[148:149], v241 offset:49152\n"
"v_mfma_f32_16x16x32_bf16 v[80:83], a[176:179], v[200:203], v[80:83]\n"
"buffer_load_dwordx4 v230, s[52:55], s64 offen offset:0 lds\n"
"v_fma_mix_f32 v226, v134, v212, v220 op_sel:[0,0,0] op_sel_hi:[1,0,0]\n"
"v_mfma_f32_16x16x32_bf16 v[112:115], a[176:179], v[204:207], v[112:115]\n"
"ds_read_b64_tr_b16 a[150:151], v241 offset:53248\n"
"v_cvt_pk_bf16_f32 v130, v224, v225\n"
"v_fma_mix_f32 v227, v134, v213, v221 op_sel:[1,0,0] op_sel_hi:[1,0,0]\n"
"v_mfma_f32_16x16x32_bf16 v[116:119], a[180:183], v[204:207], v[116:119]\n"
"ds_read_b64_tr_b16 a[152:153], v242 offset:49152\n"
"v_fma_mix_f32 v224, v138, v212, v220 op_sel:[0,0,0] op_sel_hi:[1,0,0]\n"
"v_mfma_f32_16x16x32_bf16 v[84:87], a[180:183], v[200:203], v[84:87]\n"
"s_mov_b32 s64, 4226048\n"
"ds_read_b64_tr_b16 a[154:155], v242 offset:53248\n"
"v_mfma_f32_16x16x32_bf16 v[52:55], a[180:183], v[196:199], v[52:55]\n"
"buffer_load_dwordx4 v231, s[52:55], s64 offen offset:1024 lds\n"
"v_cvt_pk_bf16_f32 v134, v226, v227\n"
"v_fma_mix_f32 v225, v138, v213, v221 op_sel:[1,0,0] op_sel_hi:[1,0,0]\n"
"v_mfma_f32_16x16x32_bf16 v[20:23], a[180:183], v[192:195], v[20:23]\n"
"ds_read_b64_tr_b16 a[156:157], v243 offset:49152\n"
"v_fma_mix_f32 v226, v142, v212, v220 op_sel:[0,0,0] op_sel_hi:[1,0,0]\n"
"v_mfma_f32_16x16x32_bf16 v[24:27], a[184:187], v[192:195], v[24:27]\n"
"ds_read_b64_tr_b16 a[158:159], v243 offset:53248\n"
"v_cvt_pk_bf16_f32 v138, v224, v225\n"
"v_mfma_f32_16x16x32_bf16 v[56:59], a[184:187], v[196:199], v[56:59]\n"
"s_mov_b32 s64, 4257792\n"
"ds_read_b128 v[208:211], v229 offset:1408\n"
"v_fma_mix_f32 v227, v142, v213, v221 op_sel:[1,0,0] op_sel_hi:[1,0,0]\n"
"v_fma_mix_f32 v224, v131, v214, v222 op_sel:[0,0,0] op_sel_hi:[1,0,0]\n"
"v_mfma_f32_16x16x32_bf16 v[88:91], a[184:187], v[200:203], v[88:91]\n"
"buffer_load_dwordx4 v230, s[52:55], s64 offen offset:2048 lds\n"
"v_cvt_pk_bf16_f32 v142, v226, v227\n"
"v_mfma_f32_16x16x32_bf16 v[120:123], a[184:187], v[204:207], v[120:123]\n"
"ds_read_b128 v[216:219], v229 offset:3968\n"
"v_fma_mix_f32 v225, v131, v215, v223 op_sel:[1,0,0] op_sel_hi:[1,0,0]\n"
"v_mfma_f32_16x16x32_bf16 v[124:127], a[188:191], v[204:207], v[124:127]\n"
"v_fma_mix_f32 v226, v135, v214, v222 op_sel:[0,0,0] op_sel_hi:[1,0,0]\n"
"v_cvt_pk_bf16_f32 v131, v224, v225\n"
"v_fma_mix_f32 v227, v135, v215, v223 op_sel:[1,0,0] op_sel_hi:[1,0,0]\n"
"v_mfma_f32_16x16x32_bf16 v[92:95], a[188:191], v[200:203], v[92:95]\n"
"v_fma_mix_f32 v224, v139, v214, v222 op_sel:[0,0,0] op_sel_hi:[1,0,0]\n"
"v_cvt_pk_bf16_f32 v135, v226, v227\n"
"v_mfma_f32_16x16x32_bf16 v[60:63], a[188:191], v[196:199], v[60:63]\n"
"v_fma_mix_f32 v225, v139, v215, v223 op_sel:[1,0,0] op_sel_hi:[1,0,0]\n"
"v_fma_mix_f32 v226, v143, v214, v222 op_sel:[0,0,0] op_sel_hi:[1,0,0]\n"
"v_mfma_f32_16x16x32_bf16 v[28:31], a[188:191], v[192:195], v[28:31]\n"
"v_cvt_pk_bf16_f32 v139, v224, v225\n"
"v_fma_mix_f32 v227, v143, v215, v223 op_sel:[1,0,0] op_sel_hi:[1,0,0]\n"
"v_cvt_pk_bf16_f32 v143, v226, v227\n"
"s_waitcnt vmcnt(15) lgkmcnt(0)\n"
"v_mfma_f32_16x16x32_bf16 v[0:3], a[128:131], v[128:131], v[0:3]\n"
"ds_read_b128 v[212:215], v229 offset:1424\n"
"v_fma_mix_f32 v224, v144, v208, v216 op_sel:[0,0,0] op_sel_hi:[1,0,0]\n"
"v_mfma_f32_16x16x32_bf16 v[32:35], a[128:131], v[132:135], v[32:35]\n"
"s_add_u32 s65, %7, 57344\n"
"ds_read_b128 v[220:223], v229 offset:3984\n"
"v_fma_mix_f32 v225, v144, v209, v217 op_sel:[1,0,0] op_sel_hi:[1,0,0]\n"
"v_mfma_f32_16x16x32_bf16 v[64:67], a[128:131], v[136:139], v[64:67]\n"
"buffer_load_dwordx4 v[192:195], v228, s[44:47], s65 offen offset:0 sc1\n"
"v_fma_mix_f32 v226, v148, v208, v216 op_sel:[0,0,0] op_sel_hi:[1,0,0]\n"
"v_mfma_f32_16x16x32_bf16 v[96:99], a[128:131], v[140:143], v[96:99]\n"
"ds_read_b64_tr_b16 a[160:161], v236 offset:57344\n"
"v_cvt_pk_bf16_f32 v144, v224, v225\n"
"v_mfma_f32_16x16x32_bf16 v[100:103], a[132:135], v[140:143], v[100:103]\n"
"ds_read_b64_tr_b16 a[162:163], v236 offset:61440\n"
"v_fma_mix_f32 v227, v148, v209, v217 op_sel:[1,0,0] op_sel_hi:[1,0,0]\n"
"v_fma_mix_f32 v224, v152, v208, v216 op_sel:[0,0,0] op_sel_hi:[1,0,0]\n"
"v_mfma_f32_16x16x32_bf16 v[68:71], a[132:135], v[136:139], v[68:71]\n"
"ds_read_b64_tr_b16 a[164:165], v237 offset:57344\n"
"v_cvt_pk_bf16_f32 v148, v226, v227\n"
"v_mfma_f32_16x16x32_bf16 v[36:39], a[132:135], v[132:135], v[36:39]\n"
"buffer_load_dwordx4 v[196:199], v228, s[44:47], s65 offen offset:1024 sc1\n"
"v_fma_mix_f32 v225, v152, v209, v217 op_sel:[1,0,0] op_sel_hi:[1,0,0]\n"
"v_mfma_f32_16x16x32_bf16 v[4:7], a[132:135], v[128:131], v[4:7]\n"
"ds_read_b64_tr_b16 a[166:167], v237 offset:61440\n"
"v_fma_mix_f32 v226, v156, v208, v216 op_sel:[0,0,0] op_sel_hi:[1,0,0]\n"
"v_cvt_pk_bf16_f32 v152, v224, v225\n"
"v_mfma_f32_16x16x32_bf16 v[8:11], a[136:139], v[128:131], v[8:11]\n"
"ds_read_b64_tr_b16 a[168:169], v238 offset:57344\n"
"v_fma_mix_f32 v227, v156, v209, v217 op_sel:[1,0,0] op_sel_hi:[1,0,0]\n"
"v_mfma_f32_16x16x32_bf16 v[40:43], a[136:139], v[132:135], v[40:43]\n"
"ds_read_b64_tr_b16 a[170:171], v238 offset:61440\n"
"v_fma_mix_f32 v224, v145, v210, v218 op_sel:[0,0,0] op_sel_hi:[1,0,0]\n"
"v_mfma_f32_16x16x32_bf16 v[72:75], a[136:139], v[136:139], v[72:75]\n"
"buffer_load_dwordx4 v[200:203], v228, s[44:47], s65 offen offset:2048 sc1\n"
"v_cvt_pk_bf16_f32 v156, v226, v227\n"
"v_fma_mix_f32 v225, v145, v211, v219 op_sel:[1,0,0] op_sel_hi:[1,0,0]\n"
"v_mfma_f32_16x16x32_bf16 v[104:107], a[136:139], v[140:143], v[104:107]\n"
"ds_read_b64_tr_b16 a[172:173], v239 offset:57344\n"
"v_fma_mix_f32 v226, v149, v210, v218 op_sel:[0,0,0] op_sel_hi:[1,0,0]\n"
"v_mfma_f32_16x16x32_bf16 v[108:111], a[140:143], v[140:143], v[108:111]\n"
"ds_read_b64_tr_b16 a[174:175], v239 offset:61440\n"
"v_cvt_pk_bf16_f32 v145, v224, v225\n"
"v_mfma_f32_16x16x32_bf16 v[76:79], a[140:143], v[136:139], v[76:79]\n"
"ds_read_b64_tr_b16 a[176:177], v240 offset:57344\n"
"v_fma_mix_f32 v227, v149, v211, v219 op_sel:[1,0,0] op_sel_hi:[1,0,0]\n"
"v_fma_mix_f32 v224, v153, v210, v218 op_sel:[0,0,0] op_sel_hi:[1,0,0]\n"
"v_mfma_f32_16x16x32_bf16 v[44:47], a[140:143], v[132:135], v[44:47]\n"
"buffer_load_dwordx4 v[204:207], v228, s[44:47], s65 offen offset:3072 sc1\n"
"v_cvt_pk_bf16_f32 v149, v226, v227\n"
"v_mfma_f32_16x16x32_bf16 v[12:15], a[140:143], v[128:131], v[12:15]\n"
"ds_read_b64_tr_b16 a[178:179], v240 offset:61440\n"
"v_fma_mix_f32 v225, v153, v211, v219 op_sel:[1,0,0] op_sel_hi:[1,0,0]\n"
"v_mfma_f32_16x16x32_bf16 v[16:19], a[144:147], v[128:131], v[16:19]\n"
"ds_read_b64_tr_b16 a[180:181], v241 offset:57344\n"
"v_fma_mix_f32 v226, v157, v210, v218 op_sel:[0,0,0] op_sel_hi:[1,0,0]\n"
"v_cvt_pk_bf16_f32 v153, v224, v225\n"
"v_mfma_f32_16x16x32_bf16 v[48:51], a[144:147], v[132:135], v[48:51]\n"
"s_mov_b32 s64, 4289536\n"
"ds_read_b64_tr_b16 a[182:183], v241 offset:61440\n"
"v_mfma_f32_16x16x32_bf16 v[80:83], a[144:147], v[136:139], v[80:83]\n"
"buffer_load_dwordx4 v231, s[52:55], s64 offen offset:3072 lds\n"
"v_fma_mix_f32 v227, v157, v211, v219 op_sel:[1,0,0] op_sel_hi:[1,0,0]\n"
"v_mfma_f32_16x16x32_bf16 v[112:115], a[144:147], v[140:143], v[112:115]\n"
"s_waitcnt lgkmcnt(12)\n"
"ds_read_b64_tr_b16 a[184:185], v242 offset:57344\n"
"v_fma_mix_f32 v224, v146, v212, v220 op_sel:[0,0,0] op_sel_hi:[1,0,0]\n"
"v_cvt_pk_bf16_f32 v157, v226, v227\n"
"v_mfma_f32_16x16x32_bf16 v[116:119], a[148:151], v[140:143], v[116:119]\n"
"ds_read_b64_tr_b16 a[186:187], v242 offset:61440\n"
"v_fma_mix_f32 v225, v146, v213, v221 op_sel:[1,0,0] op_sel_hi:[1,0,0]\n"
"v_mfma_f32_16x16x32_bf16 v[84:87], a[148:151], v[136:139], v[84:87]\n"
"ds_read_b64_tr_b16 a[188:189], v243 offset:57344\n"
"v_fma_mix_f32 v226, v150, v212, v220 op_sel:[0,0,0] op_sel_hi:[1,0,0]\n"
"v_mfma_f32_16x16x32_bf16 v[52:55], a[148:151], v[132:135], v[52:55]\n"
"ds_read_b64_tr_b16 a[190:191], v243 offset:61440\n"
"v_cvt_pk_bf16_f32 v146, v224, v225\n"
"v_fma_mix_f32 v227, v150, v213, v221 op_sel:[1,0,0] op_sel_hi:[1,0,0]\n"
"v_mfma_f32_16x16x32_bf16 v[20:23], a[148:151], v[128:131], v[20:23]\n"
"v_fma_mix_f32 v224, v154, v212, v220 op_sel:[0,0,0] op_sel_hi:[1,0,0]\n"
"v_cvt_pk_bf16_f32 v150, v226, v227\n"
"v_mfma_f32_16x16x32_bf16 v[24:27], a[152:155], v[128:131], v[24:27]\n"
"ds_read_b128 v[208:211], v229 offset:1536\n"
"v_fma_mix_f32 v225, v154, v213, v221 op_sel:[1,0,0] op_sel_hi:[1,0,0]\n"
"v_mfma_f32_16x16x32_bf16 v[56:59], a[152:155], v[132:135], v[56:59]\n"
"ds_read_b128 v[216:219], v229 offset:4096\n"
"v_fma_mix_f32 v226, v158, v212, v220 op_sel:[0,0,0] op_sel_hi:[1,0,0]\n"
"v_cvt_pk_bf16_f32 v154, v224, v225\n"
"v_mfma_f32_16x16x32_bf16 v[88:91], a[152:155], v[136:139], v[88:91]\n"
"v_fma_mix_f32 v227, v158, v213, v221 op_sel:[1,0,0] op_sel_hi:[1,0,0]\n"
"v_fma_mix_f32 v224, v147, v214, v222 op_sel:[0,0,0] op_sel_hi:[1,0,0]\n"
"v_mfma_f32_16x16x32_bf16 v[120:123], a[152:155], v[140:143], v[120:123]\n"
"v_cvt_pk_bf16_f32 v158, v226, v227\n"
"v_fma_mix_f32 v225, v147, v215, v223 op_sel:[1,0,0] op_sel_hi:[1,0,0]\n"
"v_mfma_f32_16x16x32_bf16 v[124:127], a[156:159], v[140:143], v[124:127]\n"
"v_fma_mix_f32 v226, v151, v214, v222 op_sel:[0,0,0] op_sel_hi:[1,0,0]\n"
"v_cvt_pk_bf16_f32 v147, v224, v225\n"
"v_fma_mix_f32 v227, v151, v215, v223 op_sel:[1,0,0] op_sel_hi:[1,0,0]\n"
"v_mfma_f32_16x16x32_bf16 v[92:95], a[156:159], v[136:139], v[92:95]\n"
"v_fma_mix_f32 v224, v155, v214, v222 op_sel:[0,0,0] op_sel_hi:[1,0,0]\n"
"v_cvt_pk_bf16_f32 v151, v226, v227\n"
"v_mfma_f32_16x16x32_bf16 v[60:63], a[156:159], v[132:135], v[60:63]\n"
"v_fma_mix_f32 v225, v155, v215, v223 op_sel:[1,0,0] op_sel_hi:[1,0,0]\n"
"v_fma_mix_f32 v226, v159, v214, v222 op_sel:[0,0,0] op_sel_hi:[1,0,0]\n"
"v_mfma_f32_16x16x32_bf16 v[28:31], a[156:159], v[128:131], v[28:31]\n"
"v_cvt_pk_bf16_f32 v155, v224, v225\n"
"v_fma_mix_f32 v227, v159, v215, v223 op_sel:[1,0,0] op_sel_hi:[1,0,0]\n"
"v_cvt_pk_bf16_f32 v159, v226, v227\n"
"s_waitcnt vmcnt(13) lgkmcnt(0)\n"
"v_mfma_f32_16x16x32_bf16 v[0:3], a[160:163], v[144:147], v[0:3]\n"
"s_barrier\n"
"v_fma_mix_f32 v224, v160, v208, v216 op_sel:[0,0,0] op_sel_hi:[1,0,0]\n"
"v_fma_mix_f32 v225, v160, v209, v217 op_sel:[1,0,0] op_sel_hi:[1,0,0]\n"
"v_mfma_f32_16x16x32_bf16 v[32:35], a[160:163], v[148:151], v[32:35]\n"
"s_add_u32 s65, %7, 61440\n"
"ds_read_b128 v[212:215], v229 offset:1552\n"
"v_fma_mix_f32 v226, v164, v208, v216 op_sel:[0,0,0] op_sel_hi:[1,0,0]\n"
"v_mfma_f32_16x16x32_bf16 v[64:67], a[160:163], v[152:155], v[64:67]\n"
"buffer_load_dwordx4 v[128:131], v228, s[44:47], s65 offen offset:0 sc1\n"
"v_cvt_pk_bf16_f32 v160, v224, v225\n"
"v_fma_mix_f32 v227, v164, v209, v217 op_sel:[1,0,0] op_sel_hi:[1,0,0]\n"
"v_mfma_f32_16x16x32_bf16 v[96:99], a[160:163], v[156:159], v[96:99]\n"
"ds_read_b128 v[220:223], v229 offset:4112\n"
"v_fma_mix_f32 v224, v168, v208, v216 op_sel:[0,0,0] op_sel_hi:[1,0,0]\n"
"v_mfma_f32_16x16x32_bf16 v[100:103], a[164:167], v[156:159], v[100:103]\n"
"ds_read_b64_tr_b16 a[128:129], v236 offset:0\n"
"v_cvt_pk_bf16_f32 v164, v226, v227\n"
"v_fma_mix_f32 v225, v168, v209, v217 op_sel:[1,0,0] op_sel_hi:[1,0,0]\n"
"v_mfma_f32_16x16x32_bf16 v[68:71], a[164:167], v[152:155], v[68:71]\n"
"ds_read_b64_tr_b16 a[130:131], v236 offset:4096\n"
"v_fma_mix_f32 v226, v172, v208, v216 op_sel:[0,0,0] op_sel_hi:[1,0,0]\n"
"v_cvt_pk_bf16_f32 v168, v224, v225\n"
"v_mfma_f32_16x16x32_bf16 v[36:39], a[164:167], v[148:151], v[36:39]\n"
"buffer_load_dwordx4 v[132:135], v228, s[44:47], s65 offen offset:1024 sc1\n"
"v_fma_mix_f32 v227, v172, v209, v217 op_sel:[1,0,0] op_sel_hi:[1,0,0]\n"
"v_mfma_f32_16x16x32_bf16 v[4:7], a[164:167], v[144:147], v[4:7]\n"
"ds_read_b64_tr_b16 a[132:133], v237 offset:0\n"
"v_fma_mix_f32 v224, v161, v210, v218 op_sel:[0,0,0] op_sel_hi:[1,0,0]\n"
"v_cvt_pk_bf16_f32 v172, v226, v227\n"
"v_mfma_f32_16x16x32_bf16 v[8:11], a[168:171], v[144:147], v[8:11]\n"
"ds_read_b64_tr_b16 a[134:135], v237 offset:4096\n"
"v_fma_mix_f32 v225, v161, v211, v219 op_sel:[1,0,0] op_sel_hi:[1,0,0]\n"
"v_fma_mix_f32 v226, v165, v210, v218 op_sel:[0,0,0] op_sel_hi:[1,0,0]\n"
"v_mfma_f32_16x16x32_bf16 v[40:43], a[168:171], v[148:151], v[40:43]\n"
"L_w4_%=:\n"
"s_cbranch_execz L_wd0_%=\n"
"ds_read_b64_tr_b16 a[136:137], v238 offset:0\n"
"v_cvt_pk_bf16_f32 v161, v224, v225\n"
"v_mfma_f32_16x16x32_bf16 v[72:75], a[168:171], v[152:155], v[72:75]\n"
"buffer_load_dwordx4 v[136:139], v228, s[44:47], s65 offen offset:2048 sc1\n"
"v_fma_mix_f32 v227, v165, v211, v219 op_sel:[1,0,0] op_sel_hi:[1,0,0]\n"
"v_fma_mix_f32 v224, v169, v210, v218 op_sel:[0,0,0] op_sel_hi:[1,0,0]\n"
"v_mfma_f32_16x16x32_bf16 v[104:107], a[168:171], v[156:159], v[104:107]\n"
"ds_read_b64_tr_b16 a[138:139], v238 offset:4096\n"
"v_cvt_pk_bf16_f32 v165, v226, v227\n"
"v_mfma_f32_16x16x32_bf16 v[108:111], a[172:175], v[156:159], v[108:111]\n"
"ds_read_b64_tr_b16 a[140:141], v239 offset:0\n"
"v_fma_mix_f32 v225, v169, v211, v219 op_sel:[1,0,0] op_sel_hi:[1,0,0]\n"
"v_mfma_f32_16x16x32_bf16 v[76:79], a[172:175], v[152:155], v[76:79]\n"
"ds_read_b64_tr_b16 a[142:143], v239 offset:4096\n"
"v_fma_mix_f32 v226, v173, v210, v218 op_sel:[0,0,0] op_sel_hi:[1,0,0]\n"
"v_cvt_pk_bf16_f32 v169, v224, v225\n"
"v_mfma_f32_16x16x32_bf16 v[44:47], a[172:175], v[148:151], v[44:47]\n"
"buffer_load_dwordx4 v[140:143], v228, s[44:47], s65 offen offset:3072 sc1\n"
"v_fma_mix_f32 v227, v173, v211, v219 op_sel:[1,0,0] op_sel_hi:[1,0,0]\n"
"v_mfma_f32_16x16x32_bf16 v[12:15], a[172:175], v[144:147], v[12:15]\n"
"s_waitcnt lgkmcnt(8)\n"
"ds_read_b64_tr_b16 a[144:145], v240 offset:0\n"
"v_fma_mix_f32 v224, v162, v212, v220 op_sel:[0,0,0] op_sel_hi:[1,0,0]\n"
"v_mfma_f32_16x16x32_bf16 v[16:19], a[176:179], v[144:147], v[16:19]\n"
"ds_read_b64_tr_b16 a[146:147], v240 offset:4096\n"
"v_cvt_pk_bf16_f32 v173, v226, v227\n"
"v_fma_mix_f32 v225, v162, v213, v221 op_sel:[1,0,0] op_sel_hi:[1,0,0]\n"
"v_mfma_f32_16x16x32_bf16 v[48:51], a[176:179], v[148:151], v[48:51]\n"
"s_add_u32 m0, s60, 114688\n"
"s_mov_b32 s64, 4718592\n"
"ds_read_b64_tr_b16 a[148:149], v241 offset:0\n"
"v_mfma_f32_16x16x32_bf16 v[80:83], a[176:179], v[152:155], v[80:83]\n"
"buffer_load_dwordx4 v230, s[52:55], s64 offen offset:0 lds\n"
"v_fma_mix_f32 v226, v166, v212, v220 op_sel:[0,0,0] op_sel_hi:[1,0,0]\n"
"v_mfma_f32_16x16x32_bf16 v[112:115], a[176:179], v[156:159], v[112:115]\n"
"ds_read_b64_tr_b16 a[150:151], v241 offset:4096\n"
"v_cvt_pk_bf16_f32 v162, v224, v225\n"
"v_fma_mix_f32 v227, v166, v213, v221 op_sel:[1,0,0] op_sel_hi:[1,0,0]\n"
"v_mfma_f32_16x16x32_bf16 v[116:119], a[180:183], v[156:159], v[116:119]\n"
"ds_read_b64_tr_b16 a[152:153], v242 offset:0\n"
"v_fma_mix_f32 v224, v170, v212, v220 op_sel:[0,0,0] op_sel_hi:[1,0,0]\n"
"v_mfma_f32_16x16x32_bf16 v[84:87], a[180:183], v[152:155], v[84:87]\n"
"s_mov_b32 s64, 4750336\n"
"ds_read_b64_tr_b16 a[154:155], v242 offset:4096\n"
"v_mfma_f32_16x16x32_bf16 v[52:55], a[180:183], v[148:151], v[52:55]\n"
"buffer_load_dwordx4 v231, s[52:55], s64 offen offset:1024 lds\n"
"v_cvt_pk_bf16_f32 v166, v226, v227\n"
"v_fma_mix_f32 v225, v170, v213, v221 op_sel:[1,0,0] op_sel_hi:[1,0,0]\n"
"v_mfma_f32_16x16x32_bf16 v[20:23], a[180:183], v[144:147], v[20:23]\n"
"ds_read_b64_tr_b16 a[156:157], v243 offset:0\n"
"v_fma_mix_f32 v226, v174, v212, v220 op_sel:[0,0,0] op_sel_hi:[1,0,0]\n"
"v_mfma_f32_16x16x32_bf16 v[24:27], a[184:187], v[144:147], v[24:27]\n"
"ds_read_b64_tr_b16 a[158:159], v243 offset:4096\n"
"v_cvt_pk_bf16_f32 v170, v224, v225\n"
"v_mfma_f32_16x16x32_bf16 v[56:59], a[184:187], v[148:151], v[56:59]\n"
"s_mov_b32 s64, 4782080\n"
"ds_read_b128 v[208:211], v229 offset:1664\n"
"v_fma_mix_f32 v227, v174, v213, v221 op_sel:[1,0,0] op_sel_hi:[1,0,0]\n"
"v_fma_mix_f32 v224, v163, v214, v222 op_sel:[0,0,0] op_sel_hi:[1,0,0]\n"
"v_mfma_f32_16x16x32_bf16 v[88:91], a[184:187], v[152:155], v[88:91]\n"
"buffer_load_dwordx4 v230, s[52:55], s64 offen offset:2048 lds\n"
"v_cvt_pk_bf16_f32 v174, v226, v227\n"
"v_mfma_f32_16x16x32_bf16 v[120:123], a[184:187], v[156:159], v[120:123]\n"
"ds_read_b128 v[216:219], v229 offset:4224\n"
"v_fma_mix_f32 v225, v163, v215, v223 op_sel:[1,0,0] op_sel_hi:[1,0,0]\n"
"v_mfma_f32_16x16x32_bf16 v[124:127], a[188:191], v[156:159], v[124:127]\n"
"v_fma_mix_f32 v226, v167, v214, v222 op_sel:[0,0,0] op_sel_hi:[1,0,0]\n"
"v_cvt_pk_bf16_f32 v163, v224, v225\n"
"v_fma_mix_f32 v227, v167, v215, v223 op_sel:[1,0,0] op_sel_hi:[1,0,0]\n"
"v_mfma_f32_16x16x32_bf16 v[92:95], a[188:191], v[152:155], v[92:95]\n"
"v_fma_mix_f32 v224, v171, v214, v222 op_sel:[0,0,0] op_sel_hi:[1,0,0]\n"
"v_cvt_pk_bf16_f32 v167, v226, v227\n"
"v_mfma_f32_16x16x32_bf16 v[60:63], a[188:191], v[148:151], v[60:63]\n"
"v_fma_mix_f32 v225, v171, v215, v223 op_sel:[1,0,0] op_sel_hi:[1,0,0]\n"
"v_fma_mix_f32 v226, v175, v214, v222 op_sel:[0,0,0] op_sel_hi:[1,0,0]\n"
"v_mfma_f32_16x16x32_bf16 v[28:31], a[188:191], v[144:147], v[28:31]\n"
"v_cvt_pk_bf16_f32 v171, v224, v225\n"
"v_fma_mix_f32 v227, v175, v215, v223 op_sel:[1,0,0] op_sel_hi:[1,0,0]\n"
"v_cvt_pk_bf16_f32 v175, v226, v227\n"
"s_waitcnt vmcnt(15) lgkmcnt(0)\n"
"v_mfma_f32_16x16x32_bf16 v[0:3], a[128:131], v[160:163], v[0:3]\n"
"ds_read_b128 v[212:215], v229 offset:1680\n"
"v_fma_mix_f32 v224, v176, v208, v216 op_sel:[0,0,0] op_sel_hi:[1,0,0]\n"
"v_mfma_f32_16x16x32_bf16 v[32:35], a[128:131], v[164:167], v[32:35]\n"
"s_add_u32 s65, %7, 65536\n"
"ds_read_b128 v[220:223], v229 offset:4240\n"
"v_fma_mix_f32 v225, v176, v209, v217 op_sel:[1,0,0] op_sel_hi:[1,0,0]\n"
"v_mfma_f32_16x16x32_bf16 v[64:67], a[128:131], v[168:171], v[64:67]\n"
"buffer_load_dwordx4 v[144:147], v228, s[44:47], s65 offen offset:0 sc1\n"
"v_fma_mix_f32 v226, v180, v208, v216 op_sel:[0,0,0] op_sel_hi:[1,0,0]\n"
"v_cvt_pk_bf16_f32 v176, v224, v225\n"
"v_mfma_f32_16x16x32_bf16 v[96:99], a[128:131], v[172:175], v[96:99]\n"
"ds_read_b64_tr_b16 a[160:161], v236 offset:8192\n"
"v_fma_mix_f32 v227, v180, v209, v217 op_sel:[1,0,0] op_sel_hi:[1,0,0]\n"
"v_mfma_f32_16x16x32_bf16 v[100:103], a[132:135], v[172:175], v[100:103]\n"
"ds_read_b64_tr_b16 a[162:163], v236 offset:12288\n"
"v_fma_mix_f32 v224, v184, v208, v216 op_sel:[0,0,0] op_sel_hi:[1,0,0]\n"
"v_cvt_pk_bf16_f32 v180, v226, v227\n"
"v_mfma_f32_16x16x32_bf16 v[68:71], a[132:135], v[168:171], v[68:71]\n"
"ds_read_b64_tr_b16 a[164:165], v237 offset:8192\n"
"v_fma_mix_f32 v225, v184, v209, v217 op_sel:[1,0,0] op_sel_hi:[1,0,0]\n"
"v_fma_mix_f32 v226, v188, v208, v216 op_sel:[0,0,0] op_sel_hi:[1,0,0]\n"
"v_mfma_f32_16x16x32_bf16 v[36:39], a[132:135], v[164:167], v[36:39]\n"
"buffer_load_dwordx4 v[148:151], v228, s[44:47], s65 offen offset:1024 sc1\n"
"v_cvt_pk_bf16_f32 v184, v224, v225\n"
"v_mfma_f32_16x16x32_bf16 v[4:7], a[132:135], v[160:163], v[4:7]\n"
"ds_read_b64_tr_b16 a[166:167], v237 offset:12288\n"
"v_fma_mix_f32 v227, v188, v209, v217 op_sel:[1,0,0] op_sel_hi:[1,0,0]\n"
"v_fma_mix_f32 v224, v177, v210, v218 op_sel:[0,0,0] op_sel_hi:[1,0,0]\n"
"v_mfma_f32_16x16x32_bf16 v[8:11], a[136:139], v[160:163], v[8:11]\n"
"ds_read_b64_tr_b16 a[168:169], v238 offset:8192\n"
"v_cvt_pk_bf16_f32 v188, v226, v227\n"
"v_fma_mix_f32 v225, v177, v211, v219 op_sel:[1,0,0] op_sel_hi:[1,0,0]\n"
"v_mfma_f32_16x16x32_bf16 v[40:43], a[136:139], v[164:167], v[40:43]\n"
"ds_read_b64_tr_b16 a[170:171], v238 offset:12288\n"
"v_fma_mix_f32 v226, v181, v210, v218 op_sel:[0,0,0] op_sel_hi:[1,0,0]\n"
"v_mfma_f32_16x16x32_bf16 v[72:75], a[136:139], v[168:171], v[72:75]\n"
"buffer_load_dwordx4 v[152:155], v228, s[44:47], s65 offen offset:2048 sc1\n"
"v_cvt_pk_bf16_f32 v177, v224, v225\n"
"v_fma_mix_f32 v227, v181, v211, v219 op_sel:[1,0,0] op_sel_hi:[1,0,0]\n"
"v_mfma_f32_16x16x32_bf16 v[104:107], a[136:139], v[172:175], v[104:107]\n"
"ds_read_b64_tr_b16 a[172:173], v239 offset:8192\n"
"v_fma_mix_f32 v224, v185, v210, v218 op_sel:[0,0,0] op_sel_hi:[1,0,0]\n"
"v_mfma_f32_16x16x32_bf16 v[108:111], a[140:143], v[172:175], v[108:111]\n"
"ds_read_b64_tr_b16 a[174:175], v239 offset:12288\n"
"v_cvt_pk_bf16_f32 v181, v226, v227\n"
"v_mfma_f32_16x16x32_bf16 v[76:79], a[140:143], v[168:171], v[76:79]\n"
"ds_read_b64_tr_b16 a[176:177], v240 offset:8192\n"
"v_fma_mix_f32 v225, v185, v211, v219 op_sel:[1,0,0] op_sel_hi:[1,0,0]\n"
"v_fma_mix_f32 v226, v189, v210, v218 op_sel:[0,0,0] op_sel_hi:[1,0,0]\n"
"v_mfma_f32_16x16x32_bf16 v[44:47], a[140:143], v[164:167], v[44:47]\n"
"buffer_load_dwordx4 v[156:159], v228, s[44:47], s65 offen offset:3072 sc1\n"
"v_cvt_pk_bf16_f32 v185, v224, v225\n"
"v_mfma_f32_16x16x32_bf16 v[12:15], a[140:143], v[160:163], v[12:15]\n"
"ds_read_b64_tr_b16 a[178:179], v240 offset:12288\n"
"v_fma_mix_f32 v227, v189, v211, v219 op_sel:[1,0,0] op_sel_hi:[1,0,0]\n"
"v_mfma_f32_16x16x32_bf16 v[16:19], a[144:147], v[160:163], v[16:19]\n"
"s_waitcnt lgkmcnt(10)\n"
"ds_read_b64_tr_b16 a[180:181], v241 offset:8192\n"
"v_fma_mix_f32 v224, v178, v212, v220 op_sel:[0,0,0] op_sel_hi:[1,0,0]\n"
"v_cvt_pk_bf16_f32 v189, v226, v227\n"
"v_mfma_f32_16x16x32_bf16 v[48:51], a[144:147], v[164:167], v[48:51]\n"
"s_mov_b32 s64, 4813824\n"
"ds_read_b64_tr_b16 a[182:183], v241 offset:12288\n"
"v_mfma_f32_16x16x32_bf16 v[80:83], a[144:147], v[168:171], v[80:83]\n"
"buffer_load_dwordx4 v231, s[52:55], s64 offen offset:3072 lds\n"
"v_fma_mix_f32 v225, v178, v213, v221 op_sel:[1,0,0] op_sel_hi:[1,0,0]\n"
"v_mfma_f32_16x16x32_bf16 v[112:115], a[144:147], v[172:175], v[112:115]\n"
"ds_read_b64_tr_b16 a[184:185], v242 offset:8192\n"
"v_fma_mix_f32 v226, v182, v212, v220 op_sel:[0,0,0] op_sel_hi:[1,0,0]\n"
"v_cvt_pk_bf16_f32 v178, v224, v225\n"
"v_mfma_f32_16x16x32_bf16 v[116:119], a[148:151], v[172:175], v[116:119]\n"
"ds_read_b64_tr_b16 a[186:187], v242 offset:12288\n"
"v_fma_mix_f32 v227, v182, v213, v221 op_sel:[1,0,0] op_sel_hi:[1,0,0]\n"
"v_mfma_f32_16x16x32_bf16 v[84:87], a[148:151], v[168:171], v[84:87]\n"
"s_add_u32 m0, s60, 0\n"
"s_mov_b32 s61, 0\n"
"ds_read_b64_tr_b16 a[188:189], v243 offset:8192\n"
"v_mfma_f32_16x16x32_bf16 v[52:55], a[148:151], v[164:167], v[52:55]\n"
"buffer_load_dwordx4 v244, s[36:39], s61 offen offset:0 lds\n"
"v_fma_mix_f32 v224, v186, v212, v220 op_sel:[0,0,0] op_sel_hi:[1,0,0]\n"
"v_cvt_pk_bf16_f32 v182, v226, v227\n"
"v_mfma_f32_16x16x32_bf16 v[20:23], a[148:151], v[160:163], v[20:23]\n"
"ds_read_b64_tr_b16 a[190:191], v243 offset:12288\n"
"v_fma_mix_f32 v225, v186, v213, v221 op_sel:[1,0,0] op_sel_hi:[1,0,0]\n"
"v_mfma_f32_16x16x32_bf16 v[24:27], a[152:155], v[160:163], v[24:27]\n"
"ds_read_b128 v[208:211], v229 offset:1792\n"
"v_fma_mix_f32 v226, v190, v212, v220 op_sel:[0,0,0] op_sel_hi:[1,0,0]\n"
"v_mfma_f32_16x16x32_bf16 v[56:59], a[152:155], v[164:167], v[56:59]\n"
"ds_read_b128 v[216:219], v229 offset:4352\n"
"v_cvt_pk_bf16_f32 v186, v224, v225\n"
"v_fma_mix_f32 v227, v190, v213, v221 op_sel:[1,0,0] op_sel_hi:[1,0,0]\n"
"v_mfma_f32_16x16x32_bf16 v[88:91], a[152:155], v[168:171], v[88:91]\n"
"buffer_load_dwordx4 v245, s[36:39], s61 offen offset:1024 lds\n"
"v_fma_mix_f32 v224, v179, v214, v222 op_sel:[0,0,0] op_sel_hi:[1,0,0]\n"
"v_mfma_f32_16x16x32_bf16 v[120:123], a[152:155], v[172:175], v[120:123]\n"
"v_cvt_pk_bf16_f32 v190, v226, v227\n"
"v_fma_mix_f32 v225, v179, v215, v223 op_sel:[1,0,0] op_sel_hi:[1,0,0]\n"
"v_mfma_f32_16x16x32_bf16 v[124:127], a[156:159], v[172:175], v[124:127]\n"
"v_fma_mix_f32 v226, v183, v214, v222 op_sel:[0,0,0] op_sel_hi:[1,0,0]\n"
"v_cvt_pk_bf16_f32 v179, v224, v225\n"
"v_fma_mix_f32 v227, v183, v215, v223 op_sel:[1,0,0] op_sel_hi:[1,0,0]\n"
"v_mfma_f32_16x16x32_bf16 v[92:95], a[156:159], v[168:171], v[92:95]\n"
"v_fma_mix_f32 v224, v187, v214, v222 op_sel:[0,0,0] op_sel_hi:[1,0,0]\n"
"v_cvt_pk_bf16_f32 v183, v226, v227\n"
"v_mfma_f32_16x16x32_bf16 v[60:63], a[156:159], v[164:167], v[60:63]\n"
"v_fma_mix_f32 v225, v187, v215, v223 op_sel:[1,0,0] op_sel_hi:[1,0,0]\n"
"v_fma_mix_f32 v226, v191, v214, v222 op_sel:[0,0,0] op_sel_hi:[1,0,0]\n"
"v_mfma_f32_16x16x32_bf16 v[28:31], a[156:159], v[160:163], v[28:31]\n"
"v_cvt_pk_bf16_f32 v187, v224, v225\n"
"v_fma_mix_f32 v227, v191, v215, v223 op_sel:[1,0,0] op_sel_hi:[1,0,0]\n"
"v_cvt_pk_bf16_f32 v191, v226, v227\n"
"s_waitcnt vmcnt(15) lgkmcnt(0)\n"
"v_mfma_f32_16x16x32_bf16 v[0:3], a[160:163], v[176:179], v[0:3]\n"
"s_barrier\n"
"v_fma_mix_f32 v224, v192, v208, v216 op_sel:[0,0,0] op_sel_hi:[1,0,0]\n"
"v_fma_mix_f32 v225, v192, v209, v217 op_sel:[1,0,0] op_sel_hi:[1,0,0]\n"
"v_mfma_f32_16x16x32_bf16 v[32:35], a[160:163], v[180:183], v[32:35]\n"
"s_add_u32 s65, %7, 69632\n"
"ds_read_b128 v[212:215], v229 offset:1808\n"
"v_fma_mix_f32 v226, v196, v208, v216 op_sel:[0,0,0] op_sel_hi:[1,0,0]\n"
"v_mfma_f32_16x16x32_bf16 v[64:67], a[160:163], v[184:187], v[64:67]\n"
"buffer_load_dwordx4 v[160:163], v228, s[44:47], s65 offen offset:0 sc1\n"
"v_cvt_pk_bf16_f32 v192, v224, v225\n"
"v_fma_mix_f32 v227, v196, v209, v217 op_sel:[1,0,0] op_sel_hi:[1,0,0]\n"
"v_mfma_f32_16x16x32_bf16 v[96:99], a[160:163], v[188:191], v[96:99]\n"
"ds_read_b128 v[220:223], v229 offset:4368\n"
"v_fma_mix_f32 v224, v200, v208, v216 op_sel:[0,0,0] op_sel_hi:[1,0,0]\n"
"v_mfma_f32_16x16x32_bf16 v[100:103], a[164:167], v[188:191], v[100:103]\n"
"ds_read_b64_tr_b16 a[128:129], v236 offset:16384\n"
"v_cvt_pk_bf16_f32 v196, v226, v227\n"
"v_fma_mix_f32 v225, v200, v209, v217 op_sel:[1,0,0] op_sel_hi:[1,0,0]\n"
"v_mfma_f32_16x16x32_bf16 v[68:71], a[164:167], v[184:187], v[68:71]\n"
"ds_read_b64_tr_b16 a[130:131], v236 offset:20480\n"
"v_fma_mix_f32 v226, v204, v208, v216 op_sel:[0,0,0] op_sel_hi:[1,0,0]\n"
"v_cvt_pk_bf16_f32 v200, v224, v225\n"
"v_mfma_f32_16x16x32_bf16 v[36:39], a[164:167], v[180:183], v[36:39]\n"
"buffer_load_dwordx4 v[164:167], v228, s[44:47], s65 offen offset:1024 sc1\n"
"v_fma_mix_f32 v227, v204, v209, v217 op_sel:[1,0,0] op_sel_hi:[1,0,0]\n"
"v_mfma_f32_16x16x32_bf16 v[4:7], a[164:167], v[176:179], v[4:7]\n"
"ds_read_b64_tr_b16 a[132:133], v237 offset:16384\n"
"v_fma_mix_f32 v224, v193, v210, v218 op_sel:[0,0,0] op_sel_hi:[1,0,0]\n"
"v_cvt_pk_bf16_f32 v204, v226, v227\n"
"v_mfma_f32_16x16x32_bf16 v[8:11], a[168:171], v[176:179], v[8:11]\n"
"ds_read_b64_tr_b16 a[134:135], v237 offset:20480\n"
"v_fma_mix_f32 v225, v193, v211, v219 op_sel:[1,0,0] op_sel_hi:[1,0,0]\n"
"v_mfma_f32_16x16x32_bf16 v[40:43], a[168:171], v[180:183], v[40:43]\n"
"ds_read_b64_tr_b16 a[136:137], v238 offset:16384\n"
"v_fma_mix_f32 v226, v197, v210, v218 op_sel:[0,0,0] op_sel_hi:[1,0,0]\n"
"v_mfma_f32_16x16x32_bf16 v[72:75], a[168:171], v[184:187], v[72:75]\n"
"buffer_load_dwordx4 v[168:171], v228, s[44:47], s65 offen offset:2048 sc1\n"
"v_cvt_pk_bf16_f32 v193, v224, v225\n"
"v_fma_mix_f32 v227, v197, v211, v219 op_sel:[1,0,0] op_sel_hi:[1,0,0]\n"
"v_mfma_f32_16x16x32_bf16 v[104:107], a[168:171], v[188:191], v[104:107]\n"
"ds_read_b64_tr_b16 a[138:139], v238 offset:20480\n"
"v_fma_mix_f32 v224, v201, v210, v218 op_sel:[0,0,0] op_sel_hi:[1,0,0]\n"
"v_mfma_f32_16x16x32_bf16 v[108:111], a[172:175], v[188:191], v[108:111]\n"
"ds_read_b64_tr_b16 a[140:141], v239 offset:16384\n"
"v_cvt_pk_bf16_f32 v197, v226, v227\n"
"v_mfma_f32_16x16x32_bf16 v[76:79], a[172:175], v[184:187], v[76:79]\n"
"ds_read_b64_tr_b16 a[142:143], v239 offset:20480\n"
"v_fma_mix_f32 v225, v201, v211, v219 op_sel:[1,0,0] op_sel_hi:[1,0,0]\n"
"v_fma_mix_f32 v226, v205, v210, v218 op_sel:[0,0,0] op_sel_hi:[1,0,0]\n"
"v_mfma_f32_16x16x32_bf16 v[44:47], a[172:175], v[180:183], v[44:47]\n"
"buffer_load_dwordx4 v[172:175], v228, s[44:47], s65 offen offset:3072 sc1\n"
"v_cvt_pk_bf16_f32 v201, v224, v225\n"
"v_mfma_f32_16x16x32_bf16 v[12:15], a[172:175], v[176:179], v[12:15]\n"
"ds_read_b64_tr_b16 a[144:145], v240 offset:16384\n"
"v_fma_mix_f32 v227, v205, v211, v219 op_sel:[1,0,0] op_sel_hi:[1,0,0]\n"
"v_mfma_f32_16x16x32_bf16 v[16:19], a[176:179], v[176:179], v[16:19]\n"
"s_waitcnt lgkmcnt(9)\n"
"ds_read_b64_tr_b16 a[146:147], v240 offset:20480\n"
"v_fma_mix_f32 v224, v194, v212, v220 op_sel:[0,0,0] op_sel_hi:[1,0,0]\n"
"v_cvt_pk_bf16_f32 v205, v226, v227\n"
"v_mfma_f32_16x16x32_bf16 v[48:51], a[176:179], v[180:183], v[48:51]\n"
"ds_read_b64_tr_b16 a[148:149], v241 offset:16384\n"
"v_fma_mix_f32 v225, v194, v213, v221 op_sel:[1,0,0] op_sel_hi:[1,0,0]\n"
"v_mfma_f32_16x16x32_bf16 v[80:83], a[176:179], v[184:187], v[80:83]\n"
"buffer_load_dwordx4 v246, s[36:39], s61 offen offset:2048 lds\n"
"v_fma_mix_f32 v226, v198, v212, v220 op_sel:[0,0,0] op_sel_hi:[1,0,0]\n"
"v_mfma_f32_16x16x32_bf16 v[112:115], a[176:179], v[188:191], v[112:115]\n"
"ds_read_b64_tr_b16 a[150:151], v241 offset:20480\n"
"v_cvt_pk_bf16_f32 v194, v224, v225\n"
"v_fma_mix_f32 v227, v198, v213, v221 op_sel:[1,0,0] op_sel_hi:[1,0,0]\n"
"v_mfma_f32_16x16x32_bf16 v[116:119], a[180:183], v[188:191], v[116:119]\n"
"ds_read_b64_tr_b16 a[152:153], v242 offset:16384\n"
"v_fma_mix_f32 v224, v202, v212, v220 op_sel:[0,0,0] op_sel_hi:[1,0,0]\n"
"v_mfma_f32_16x16x32_bf16 v[84:87], a[180:183], v[184:187], v[84:87]\n"
"ds_read_b64_tr_b16 a[154:155], v242 offset:20480\n"
"v_cvt_pk_bf16_f32 v198, v226, v227\n"
"v_mfma_f32_16x16x32_bf16 v[52:55], a[180:183], v[180:183], v[52:55]\n"
"buffer_load_dwordx4 v247, s[36:39], s61 offen offset:3072 lds\n"
"v_fma_mix_f32 v225, v202, v213, v221 op_sel:[1,0,0] op_sel_hi:[1,0,0]\n"
"v_fma_mix_f32 v226, v206, v212, v220 op_sel:[0,0,0] op_sel_hi:[1,0,0]\n"
"v_mfma_f32_16x16x32_bf16 v[20:23], a[180:183], v[176:179], v[20:23]\n"
"ds_read_b64_tr_b16 a[156:157], v243 offset:16384\n"
"v_cvt_pk_bf16_f32 v202, v224, v225\n"
"v_mfma_f32_16x16x32_bf16 v[24:27], a[184:187], v[176:179], v[24:27]\n"
"ds_read_b64_tr_b16 a[158:159], v243 offset:20480\n"
"v_fma_mix_f32 v227, v206, v213, v221 op_sel:[1,0,0] op_sel_hi:[1,0,0]\n"
"v_mfma_f32_16x16x32_bf16 v[56:59], a[184:187], v[180:183], v[56:59]\n"
"s_add_u32 m0, s60, 65536\n"
"s_mov_b32 s62, 0\n"
"ds_read_b128 v[208:211], v229 offset:1920\n"
"v_fma_mix_f32 v224, v195, v214, v222 op_sel:[0,0,0] op_sel_hi:[1,0,0]\n"
"v_mfma_f32_16x16x32_bf16 v[88:91], a[184:187], v[184:187], v[88:91]\n"
"buffer_load_dwordx4 v248, s[40:43], s62 offen offset:0 lds\n"
"v_cvt_pk_bf16_f32 v206, v226, v227\n"
"v_mfma_f32_16x16x32_bf16 v[120:123], a[184:187], v[188:191], v[120:123]\n"
"ds_read_b128 v[216:219], v229 offset:4480\n"
"v_fma_mix_f32 v225, v195, v215, v223 op_sel:[1,0,0] op_sel_hi:[1,0,0]\n"
"v_mfma_f32_16x16x32_bf16 v[124:127], a[188:191], v[188:191], v[124:127]\n"
"v_fma_mix_f32 v226, v199, v214, v222 op_sel:[0,0,0] op_sel_hi:[1,0,0]\n"
"v_cvt_pk_bf16_f32 v195, v224, v225\n"
"v_fma_mix_f32 v227, v199, v215, v223 op_sel:[1,0,0] op_sel_hi:[1,0,0]\n"
"v_mfma_f32_16x16x32_bf16 v[92:95], a[188:191], v[184:187], v[92:95]\n"
"v_fma_mix_f32 v224, v203, v214, v222 op_sel:[0,0,0] op_sel_hi:[1,0,0]\n"
"v_cvt_pk_bf16_f32 v199, v226, v227\n"
"v_mfma_f32_16x16x32_bf16 v[60:63], a[188:191], v[180:183], v[60:63]\n"
"v_fma_mix_f32 v225, v203, v215, v223 op_sel:[1,0,0] op_sel_hi:[1,0,0]\n"
"v_fma_mix_f32 v226, v207, v214, v222 op_sel:[0,0,0] op_sel_hi:[1,0,0]\n"
"v_mfma_f32_16x16x32_bf16 v[28:31], a[188:191], v[176:179], v[28:31]\n"
"v_cvt_pk_bf16_f32 v203, v224, v225\n"
"v_fma_mix_f32 v227, v207, v215, v223 op_sel:[1,0,0] op_sel_hi:[1,0,0]\n"
"v_cvt_pk_bf16_f32 v207, v226, v227\n"
"s_waitcnt vmcnt(17) lgkmcnt(0)\n"
"v_mfma_f32_16x16x32_bf16 v[0:3], a[128:131], v[192:195], v[0:3]\n"
"ds_read_b128 v[212:215], v229 offset:1936\n"
"v_fma_mix_f32 v224, v128, v208, v216 op_sel:[0,0,0] op_sel_hi:[1,0,0]\n"
"v_mfma_f32_16x16x32_bf16 v[32:35], a[128:131], v[196:199], v[32:35]\n"
"s_add_u32 s65, %7, 73728\n"
"ds_read_b128 v[220:223], v229 offset:4496\n"
"v_fma_mix_f32 v225, v128, v209, v217 op_sel:[1,0,0] op_sel_hi:[1,0,0]\n"
"v_mfma_f32_16x16x32_bf16 v[64:67], a[128:131], v[200:203], v[64:67]\n"
"buffer_load_dwordx4 v[176:179], v228, s[44:47], s65 offen offset:0 sc1\n"
"v_fma_mix_f32 v226, v132, v208, v216 op_sel:[0,0,0] op_sel_hi:[1,0,0]\n"
"v_cvt_pk_bf16_f32 v128, v224, v225\n"
"v_mfma_f32_16x16x32_bf16 v[96:99], a[128:131], v[204:207], v[96:99]\n"
"ds_read_b64_tr_b16 a[160:161], v236 offset:24576\n"
"v_fma_mix_f32 v227, v132, v209, v217 op_sel:[1,0,0] op_sel_hi:[1,0,0]\n"
"v_mfma_f32_16x16x32_bf16 v[100:103], a[132:135], v[204:207], v[100:103]\n"
"ds_read_b64_tr_b16 a[162:163], v236 offset:28672\n"
"v_fma_mix_f32 v224, v136, v208, v216 op_sel:[0,0,0] op_sel_hi:[1,0,0]\n"
"v_cvt_pk_bf16_f32 v132, v226, v227\n"
"v_mfma_f32_16x16x32_bf16 v[68:71], a[132:135], v[200:203], v[68:71]\n"
"ds_read_b64_tr_b16 a[164:165], v237 offset:24576\n"
"v_fma_mix_f32 v225, v136, v209, v217 op_sel:[1,0,0] op_sel_hi:[1,0,0]\n"
"v_mfma_f32_16x16x32_bf16 v[36:39], a[132:135], v[196:199], v[36:39]\n"
"buffer_load_dwordx4 v[180:183], v228, s[44:47], s65 offen offset:1024 sc1\n"
"v_fma_mix_f32 v226, v140, v208, v216 op_sel:[0,0,0] op_sel_hi:[1,0,0]\n"
"v_mfma_f32_16x16x32_bf16 v[4:7], a[132:135], v[192:195], v[4:7]\n"
"ds_read_b64_tr_b16 a[166:167], v237 offset:28672\n"
"v_cvt_pk_bf16_f32 v136, v224, v225\n"
"v_fma_mix_f32 v227, v140, v209, v217 op_sel:[1,0,0] op_sel_hi:[1,0,0]\n"
"v_mfma_f32_16x16x32_bf16 v[8:11], a[136:139], v[192:195], v[8:11]\n"
"ds_read_b64_tr_b16 a[168:169], v238 offset:24576\n"
"v_fma_mix_f32 v224, v129, v210, v218 op_sel:[0,0,0] op_sel_hi:[1,0,0]\n"
"v_mfma_f32_16x16x32_bf16 v[40:43], a[136:139], v[196:199], v[40:43]\n"
"ds_read_b64_tr_b16 a[170:171], v238 offset:28672\n"
"v_cvt_pk_bf16_f32 v140, v226, v227\n"
"v_mfma_f32_16x16x32_bf16 v[72:75], a[136:139], v[200:203], v[72:75]\n"
"buffer_load_dwordx4 v[184:187], v228, s[44:47], s65 offen offset:2048 sc1\n"
"v_fma_mix_f32 v225, v129, v211, v219 op_sel:[1,0,0] op_sel_hi:[1,0,0]\n"
"v_fma_mix_f32 v226, v133, v210, v218 op_sel:[0,0,0] op_sel_hi:[1,0,0]\n"
"v_mfma_f32_16x16x32_bf16 v[104:107], a[136:139], v[204:207], v[104:107]\n"
"ds_read_b64_tr_b16 a[172:173], v239 offset:24576\n"
"v_cvt_pk_bf16_f32 v129, v224, v225\n"
"v_mfma_f32_16x16x32_bf16 v[108:111], a[140:143], v[204:207], v[108:111]\n"
"ds_read_b64_tr_b16 a[174:175], v239 offset:28672\n"
"v_fma_mix_f32 v227, v133, v211, v219 op_sel:[1,0,0] op_sel_hi:[1,0,0]\n"
"v_mfma_f32_16x16x32_bf16 v[76:79], a[140:143], v[200:203], v[76:79]\n"
"ds_read_b64_tr_b16 a[176:177], v240 offset:24576\n"
"v_fma_mix_f32 v224, v137, v210, v218 op_sel:[0,0,0] op_sel_hi:[1,0,0]\n"
"v_cvt_pk_bf16_f32 v133, v226, v227\n"
"v_mfma_f32_16x16x32_bf16 v[44:47], a[140:143], v[196:199], v[44:47]\n"
"buffer_load_dwordx4 v[188:191], v228, s[44:47], s65 offen offset:3072 sc1\n"
"v_fma_mix_f32 v225, v137, v211, v219 op_sel:[1,0,0] op_sel_hi:[1,0,0]\n"
"v_mfma_f32_16x16x32_bf16 v[12:15], a[140:143], v[192:195], v[12:15]\n"
"ds_read_b64_tr_b16 a[178:179], v240 offset:28672\n"
"v_fma_mix_f32 v226, v141, v210, v218 op_sel:[0,0,0] op_sel_hi:[1,0,0]\n"
"v_mfma_f32_16x16x32_bf16 v[16:19], a[144:147], v[192:195], v[16:19]\n"
"ds_read_b64_tr_b16 a[180:181], v241 offset:24576\n"
"v_cvt_pk_bf16_f32 v137, v224, v225\n"
"v_fma_mix_f32 v227, v141, v211, v219 op_sel:[1,0,0] op_sel_hi:[1,0,0]\n"
"v_mfma_f32_16x16x32_bf16 v[48:51], a[144:147], v[196:199], v[48:51]\n"
"s_waitcnt lgkmcnt(11)\n"
"ds_read_b64_tr_b16 a[182:183], v241 offset:28672\n"
"v_fma_mix_f32 v224, v130, v212, v220 op_sel:[0,0,0] op_sel_hi:[1,0,0]\n"
"v_mfma_f32_16x16x32_bf16 v[80:83], a[144:147], v[200:203], v[80:83]\n"
"buffer_load_dwordx4 v249, s[40:43], s62 offen offset:1024 lds\n"
"v_cvt_pk_bf16_f32 v141, v226, v227\n"
"v_mfma_f32_16x16x32_bf16 v[112:115], a[144:147], v[204:207], v[112:115]\n"
"ds_read_b64_tr_b16 a[184:185], v242 offset:24576\n"
"v_fma_mix_f32 v225, v130, v213, v221 op_sel:[1,0,0] op_sel_hi:[1,0,0]\n"
"v_fma_mix_f32 v226, v134, v212, v220 op_sel:[0,0,0] op_sel_hi:[1,0,0]\n"
"v_mfma_f32_16x16x32_bf16 v[116:119], a[148:151], v[204:207], v[116:119]\n"
"ds_read_b64_tr_b16 a[186:187], v242 offset:28672\n"
"v_cvt_pk_bf16_f32 v130, v224, v225\n"
"v_mfma_f32_16x16x32_bf16 v[84:87], a[148:151], v[200:203], v[84:87]\n"
"ds_read_b64_tr_b16 a[188:189], v243 offset:24576\n"
"v_fma_mix_f32 v227, v134, v213, v221 op_sel:[1,0,0] op_sel_hi:[1,0,0]\n"
"v_mfma_f32_16x16x32_bf16 v[52:55], a[148:151], v[196:199], v[52:55]\n"
"buffer_load_dwordx4 v248, s[40:43], s62 offen offset:2048 lds\n"
"v_fma_mix_f32 v224, v138, v212, v220 op_sel:[0,0,0] op_sel_hi:[1,0,0]\n"
"v_cvt_pk_bf16_f32 v134, v226, v227\n"
"v_mfma_f32_16x16x32_bf16 v[20:23], a[148:151], v[192:195], v[20:23]\n"
"ds_read_b64_tr_b16 a[190:191], v243 offset:28672\n"
"v_fma_mix_f32 v225, v138, v213, v221 op_sel:[1,0,0] op_sel_hi:[1,0,0]\n"
"v_mfma_f32_16x16x32_bf16 v[24:27], a[152:155], v[192:195], v[24:27]\n"
"ds_read_b128 v[208:211], v229 offset:2048\n"
"v_fma_mix_f32 v226, v142, v212, v220 op_sel:[0,0,0] op_sel_hi:[1,0,0]\n"
"v_mfma_f32_16x16x32_bf16 v[56:59], a[152:155], v[196:199], v[56:59]\n"
"ds_read_b128 v[216:219], v229 offset:4608\n"
"v_cvt_pk_bf16_f32 v138, v224, v225\n"
"v_fma_mix_f32 v227, v142, v213, v221 op_sel:[1,0,0] op_sel_hi:[1,0,0]\n"
"v_mfma_f32_16x16x32_bf16 v[88:91], a[152:155], v[200:203], v[88:91]\n"
"buffer_load_dwordx4 v249, s[40:43], s62 offen offset:3072 lds\n"
"v_fma_mix_f32 v224, v131, v214, v222 op_sel:[0,0,0] op_sel_hi:[1,0,0]\n"
"v_mfma_f32_16x16x32_bf16 v[120:123], a[152:155], v[204:207], v[120:123]\n"
"v_cvt_pk_bf16_f32 v142, v226, v227\n"
"v_fma_mix_f32 v225, v131, v215, v223 op_sel:[1,0,0] op_sel_hi:[1,0,0]\n"
"v_mfma_f32_16x16x32_bf16 v[124:127], a[156:159], v[204:207], v[124:127]\n"
"v_fma_mix_f32 v226, v135, v214, v222 op_sel:[0,0,0] op_sel_hi:[1,0,0]\n"
"v_cvt_pk_bf16_f32 v131, v224, v225\n"
"v_fma_mix_f32 v227, v135, v215, v223 op_sel:[1,0,0] op_sel_hi:[1,0,0]\n"
"v_mfma_f32_16x16x32_bf16 v[92:95], a[156:159], v[200:203], v[92:95]\n"
"v_fma_mix_f32 v224, v139, v214, v222 op_sel:[0,0,0] op_sel_hi:[1,0,0]\n"
"v_cvt_pk_bf16_f32 v135, v226, v227\n"
"v_mfma_f32_16x16x32_bf16 v[60:63], a[156:159], v[196:199], v[60:63]\n"
"v_fma_mix_f32 v225, v139, v215, v223 op_sel:[1,0,0] op_sel_hi:[1,0,0]\n"
"v_fma_mix_f32 v226, v143, v214, v222 op_sel:[0,0,0] op_sel_hi:[1,0,0]\n"
"v_mfma_f32_16x16x32_bf16 v[28:31], a[156:159], v[192:195], v[28:31]\n"
"v_cvt_pk_bf16_f32 v139, v224, v225\n"
"v_fma_mix_f32 v227, v143, v215, v223 op_sel:[1,0,0] op_sel_hi:[1,0,0]\n"
"v_cvt_pk_bf16_f32 v143, v226, v227\n"
"s_waitcnt vmcnt(17) lgkmcnt(0)\n"
"v_mfma_f32_16x16x32_bf16 v[0:3], a[160:163], v[128:131], v[0:3]\n"
"s_barrier\n"
"v_fma_mix_f32 v224, v144, v208, v216 op_sel:[0,0,0] op_sel_hi:[1,0,0]\n"
"v_fma_mix_f32 v225, v144, v209, v217 op_sel:[1,0,0] op_sel_hi:[1,0,0]\n"
"v_mfma_f32_16x16x32_bf16 v[32:35], a[160:163], v[132:135], v[32:35]\n"
"s_add_u32 s65, %7, 77824\n"
"ds_read_b128 v[212:215], v229 offset:2064\n"
"v_fma_mix_f32 v226, v148, v208, v216 op_sel:[0,0,0] op_sel_hi:[1,0,0]\n"
"v_mfma_f32_16x16x32_bf16 v[64:67], a[160:163], v[136:139], v[64:67]\n"
"buffer_load_dwordx4 v[192:195], v228, s[44:47], s65 offen offset:0 sc1\n"
"v_cvt_pk_bf16_f32 v144, v224, v225\n"
"v_fma_mix_f32 v227, v148, v209, v217 op_sel:[1,0,0] op_sel_hi:[1,0,0]\n"
"v_mfma_f32_16x16x32_bf16 v[96:99], a[160:163], v[140:143], v[96:99]\n"
"ds_read_b128 v[220:223], v229 offset:4624\n"
"v_fma_mix_f32 v224, v152, v208, v216 op_sel:[0,0,0] op_sel_hi:[1,0,0]\n"
"v_mfma_f32_16x16x32_bf16 v[100:103], a[164:167], v[140:143], v[100:103]\n"
"ds_read_b64_tr_b16 a[128:129], v236 offset:32768\n"
"v_cvt_pk_bf16_f32 v148, v226, v227\n"
"v_fma_mix_f32 v225, v152, v209, v217 op_sel:[1,0,0] op_sel_hi:[1,0,0]\n"
"v_mfma_f32_16x16x32_bf16 v[68:71], a[164:167], v[136:139], v[68:71]\n"
"ds_read_b64_tr_b16 a[130:131], v236 offset:36864\n"
"v_fma_mix_f32 v226, v156, v208, v216 op_sel:[0,0,0] op_sel_hi:[1,0,0]\n"
"v_cvt_pk_bf16_f32 v152, v224, v225\n"
"v_mfma_f32_16x16x32_bf16 v[36:39], a[164:167], v[132:135], v[36:39]\n"
"buffer_load_dwordx4 v[196:199], v228, s[44:47], s65 offen offset:1024 sc1\n"
"v_fma_mix_f32 v227, v156, v209, v217 op_sel:[1,0,0] op_sel_hi:[1,0,0]\n"
"v_mfma_f32_16x16x32_bf16 v[4:7], a[164:167], v[128:131], v[4:7]\n"
"ds_read_b64_tr_b16 a[132:133], v237 offset:32768\n"
"v_fma_mix_f32 v224, v145, v210, v218 op_sel:[0,0,0] op_sel_hi:[1,0,0]\n"
"v_cvt_pk_bf16_f32 v156, v226, v227\n"
"v_mfma_f32_16x16x32_bf16 v[8:11], a[168:171], v[128:131], v[8:11]\n"
"ds_read_b64_tr_b16 a[134:135], v237 offset:36864\n"
"v_fma_mix_f32 v225, v145, v211, v219 op_sel:[1,0,0] op_sel_hi:[1,0,0]\n"
"v_mfma_f32_16x16x32_bf16 v[40:43], a[168:171], v[132:135], v[40:43]\n"
"ds_read_b64_tr_b16 a[136:137], v238 offset:32768\n"
"v_fma_mix_f32 v226, v149, v210, v218 op_sel:[0,0,0] op_sel_hi:[1,0,0]\n"
"v_mfma_f32_16x16x32_bf16 v[72:75], a[168:171], v[136:139], v[72:75]\n"
"buffer_load_dwordx4 v[200:203], v228, s[44:47], s65 offen offset:2048 sc1\n"
"v_cvt_pk_bf16_f32 v145, v224, v225\n"
"v_fma_mix_f32 v227, v149, v211, v219 op_sel:[1,0,0] op_sel_hi:[1,0,0]\n"
"v_mfma_f32_16x16x32_bf16 v[104:107], a[168:171], v[140:143], v[104:107]\n"
"ds_read_b64_tr_b16 a[138:139], v238 offset:36864\n"
"v_fma_mix_f32 v224, v153, v210, v218 op_sel:[0,0,0] op_sel_hi:[1,0,0]\n"
"v_mfma_f32_16x16x32_bf16 v[108:111], a[172:175], v[140:143], v[108:111]\n"
"ds_read_b64_tr_b16 a[140:141], v239 offset:32768\n"
"v_cvt_pk_bf16_f32 v149, v226, v227\n"
"v_mfma_f32_16x16x32_bf16 v[76:79], a[172:175], v[136:139], v[76:79]\n"
"ds_read_b64_tr_b16 a[142:143], v239 offset:36864\n"
"v_fma_mix_f32 v225, v153, v211, v219 op_sel:[1,0,0] op_sel_hi:[1,0,0]\n"
"v_fma_mix_f32 v226, v157, v210, v218 op_sel:[0,0,0] op_sel_hi:[1,0,0]\n"
"v_mfma_f32_16x16x32_bf16 v[44:47], a[172:175], v[132:135], v[44:47]\n"
"buffer_load_dwordx4 v[204:207], v228, s[44:47], s65 offen offset:3072 sc1\n"
"v_cvt_pk_bf16_f32 v153, v224, v225\n"
"v_mfma_f32_16x16x32_bf16 v[12:15], a[172:175], v[128:131], v[12:15]\n"
"ds_read_b64_tr_b16 a[144:145], v240 offset:32768\n"
"v_fma_mix_f32 v227, v157, v211, v219 op_sel:[1,0,0] op_sel_hi:[1,0,0]\n"
"v_mfma_f32_16x16x32_bf16 v[16:19], a[176:179], v[128:131], v[16:19]\n"
"s_waitcnt lgkmcnt(9)\n"
"ds_read_b64_tr_b16 a[146:147], v240 offset:36864\n"
"v_fma_mix_f32 v224, v146, v212, v220 op_sel:[0,0,0] op_sel_hi:[1,0,0]\n"
"v_cvt_pk_bf16_f32 v157, v226, v227\n"
"v_mfma_f32_16x16x32_bf16 v[48:51], a[176:179], v[132:135], v[48:51]\n"
"s_add_u32 m0, s60, 16384\n"
"s_mov_b32 s61, 16384\n"
"ds_read_b64_tr_b16 a[148:149], v241 offset:32768\n"
"v_mfma_f32_16x16x32_bf16 v[80:83], a[176:179], v[136:139], v[80:83]\n"
"buffer_load_dwordx4 v244, s[36:39], s61 offen offset:0 lds\n"
"v_fma_mix_f32 v225, v146, v213, v221 op_sel:[1,0,0] op_sel_hi:[1,0,0]\n"
"v_mfma_f32_16x16x32_bf16 v[112:115], a[176:179], v[140:143], v[112:115]\n"
"ds_read_b64_tr_b16 a[150:151], v241 offset:36864\n"
"v_fma_mix_f32 v226, v150, v212, v220 op_sel:[0,0,0] op_sel_hi:[1,0,0]\n"
"v_cvt_pk_bf16_f32 v146, v224, v225\n"
"v_mfma_f32_16x16x32_bf16 v[116:119], a[180:183], v[140:143], v[116:119]\n"
"ds_read_b64_tr_b16 a[152:153], v242 offset:32768\n"
"v_fma_mix_f32 v227, v150, v213, v221 op_sel:[1,0,0] op_sel_hi:[1,0,0]\n"
"v_mfma_f32_16x16x32_bf16 v[84:87], a[180:183], v[136:139], v[84:87]\n"
"ds_read_b64_tr_b16 a[154:155], v242 offset:36864\n"
"v_fma_mix_f32 v224, v154, v212, v220 op_sel:[0,0,0] op_sel_hi:[1,0,0]\n"
"v_mfma_f32_16x16x32_bf16 v[52:55], a[180:183], v[132:135], v[52:55]\n"
"buffer_load_dwordx4 v245, s[36:39], s61 offen offset:1024 lds\n"
"v_cvt_pk_bf16_f32 v150, v226, v227\n"
"v_fma_mix_f32 v225, v154, v213, v221 op_sel:[1,0,0] op_sel_hi:[1,0,0]\n"
"v_mfma_f32_16x16x32_bf16 v[20:23], a[180:183], v[128:131], v[20:23]\n"
"ds_read_b64_tr_b16 a[156:157], v243 offset:32768\n"
"v_fma_mix_f32 v226, v158, v212, v220 op_sel:[0,0,0] op_sel_hi:[1,0,0]\n"
"v_mfma_f32_16x16x32_bf16 v[24:27], a[184:187], v[128:131], v[24:27]\n"
"ds_read_b64_tr_b16 a[158:159], v243 offset:36864\n"
"v_cvt_pk_bf16_f32 v154, v224, v225\n"
"v_mfma_f32_16x16x32_bf16 v[56:59], a[184:187], v[132:135], v[56:59]\n"
"ds_read_b128 v[208:211], v229 offset:2176\n"
"v_fma_mix_f32 v227, v158, v213, v221 op_sel:[1,0,0] op_sel_hi:[1,0,0]\n"
"v_fma_mix_f32 v224, v147, v214, v222 op_sel:[0,0,0] op_sel_hi:[1,0,0]\n"
"v_mfma_f32_16x16x32_bf16 v[88:91], a[184:187], v[136:139], v[88:91]\n"
"buffer_load_dwordx4 v246, s[36:39], s61 offen offset:2048 lds\n"
"v_cvt_pk_bf16_f32 v158, v226, v227\n"
"v_mfma_f32_16x16x32_bf16 v[120:123], a[184:187], v[140:143], v[120:123]\n"
"ds_read_b128 v[216:219], v229 offset:4736\n"
"v_fma_mix_f32 v225, v147, v215, v223 op_sel:[1,0,0] op_sel_hi:[1,0,0]\n"
"v_mfma_f32_16x16x32_bf16 v[124:127], a[188:191], v[140:143], v[124:127]\n"
"L_w5_%=:\n"
"s_cbranch_execz L_wd1_%=\n"
"v_fma_mix_f32 v226, v151, v214, v222 op_sel:[0,0,0] op_sel_hi:[1,0,0]\n"
"v_cvt_pk_bf16_f32 v147, v224, v225\n"
"v_fma_mix_f32 v227, v151, v215, v223 op_sel:[1,0,0] op_sel_hi:[1,0,0]\n"
"v_mfma_f32_16x16x32_bf16 v[92:95], a[188:191], v[136:139], v[92:95]\n"
"v_fma_mix_f32 v224, v155, v214, v222 op_sel:[0,0,0] op_sel_hi:[1,0,0]\n"
"v_cvt_pk_bf16_f32 v151, v226, v227\n"
"v_mfma_f32_16x16x32_bf16 v[60:63], a[188:191], v[132:135], v[60:63]\n"
"v_fma_mix_f32 v225, v155, v215, v223 op_sel:[1,0,0] op_sel_hi:[1,0,0]\n"
"v_fma_mix_f32 v226, v159, v214, v222 op_sel:[0,0,0] op_sel_hi:[1,0,0]\n"
"v_mfma_f32_16x16x32_bf16 v[28:31], a[188:191], v[128:131], v[28:31]\n"
"v_cvt_pk_bf16_f32 v155, v224, v225\n"
"v_fma_mix_f32 v227, v159, v215, v223 op_sel:[1,0,0] op_sel_hi:[1,0,0]\n"
"v_cvt_pk_bf16_f32 v159, v226, v227\n"
"s_waitcnt vmcnt(17) lgkmcnt(0)\n"
"v_mfma_f32_16x16x32_bf16 v[0:3], a[128:131], v[144:147], v[0:3]\n"
"ds_read_b128 v[212:215], v229 offset:2192\n"
"v_fma_mix_f32 v224, v160, v208, v216 op_sel:[0,0,0] op_sel_hi:[1,0,0]\n"
"v_mfma_f32_16x16x32_bf16 v[32:35], a[128:131], v[148:151], v[32:35]\n"
"ds_read_b128 v[220:223], v229 offset:4752\n"
"v_fma_mix_f32 v225, v160, v209, v217 op_sel:[1,0,0] op_sel_hi:[1,0,0]\n"
"v_fma_mix_f32 v226, v164, v208, v216 op_sel:[0,0,0] op_sel_hi:[1,0,0]\n"
"v_mfma_f32_16x16x32_bf16 v[64:67], a[128:131], v[152:155], v[64:67]\n"
"buffer_load_dwordx4 v247, s[36:39], s61 offen offset:3072 lds\n"
"v_cvt_pk_bf16_f32 v160, v224, v225\n"
"v_fma_mix_f32 v227, v164, v209, v217 op_sel:[1,0,0] op_sel_hi:[1,0,0]\n"
"v_mfma_f32_16x16x32_bf16 v[96:99], a[128:131], v[156:159], v[96:99]\n"
"ds_read_b64_tr_b16 a[160:161], v236 offset:40960\n"
"v_fma_mix_f32 v224, v168, v208, v216 op_sel:[0,0,0] op_sel_hi:[1,0,0]\n"
"v_mfma_f32_16x16x32_bf16 v[100:103], a[132:135], v[156:159], v[100:103]\n"
"ds_read_b64_tr_b16 a[162:163], v236 offset:45056\n"
"v_cvt_pk_bf16_f32 v164, v226, v227\n"
"v_fma_mix_f32 v225, v168, v209, v217 op_sel:[1,0,0] op_sel_hi:[1,0,0]\n"
"v_mfma_f32_16x16x32_bf16 v[68:71], a[132:135], v[152:155], v[68:71]\n"
"s_add_u32 m0, s60, 81920\n"
"s_mov_b32 s62, 16384\n"
"ds_read_b64_tr_b16 a[164:165], v237 offset:40960\n"
"v_fma_mix_f32 v226, v172, v208, v216 op_sel:[0,0,0] op_sel_hi:[1,0,0]\n"
"v_mfma_f32_16x16x32_bf16 v[36:39], a[132:135], v[148:151], v[36:39]\n"
"buffer_load_dwordx4 v248, s[40:43], s62 offen offset:0 lds\n"
"v_cvt_pk_bf16_f32 v168, v224, v225\n"
"v_mfma_f32_16x16x32_bf16 v[4:7], a[132:135], v[144:147], v[4:7]\n"
"ds_read_b64_tr_b16 a[166:167], v237 offset:45056\n"
"v_fma_mix_f32 v227, v172, v209, v217 op_sel:[1,0,0] op_sel_hi:[1,0,0]\n"
"v_fma_mix_f32 v224, v161, v210, v218 op_sel:[0,0,0] op_sel_hi:[1,0,0]\n"
"v_mfma_f32_16x16x32_bf16 v[8:11], a[136:139], v[144:147], v[8:11]\n"
"ds_read_b64_tr_b16 a[168:169], v238 offset:40960\n"
"v_cvt_pk_bf16_f32 v172, v226, v227\n"
"v_fma_mix_f32 v225, v161, v211, v219 op_sel:[1,0,0] op_sel_hi:[1,0,0]\n"
"v_mfma_f32_16x16x32_bf16 v[40:43], a[136:139], v[148:151], v[40:43]\n"
"ds_read_b64_tr_b16 a[170:171], v238 offset:45056\n"
"v_fma_mix_f32 v226, v165, v210, v218 op_sel:[0,0,0] op_sel_hi:[1,0,0]\n"
"v_mfma_f32_16x16x32_bf16 v[72:75], a[136:139], v[152:155], v[72:75]\n"
"buffer_load_dwordx4 v249, s[40:43], s62 offen offset:1024 lds\n"
"v_cvt_pk_bf16_f32 v161, v224, v225\n"
"v_fma_mix_f32 v227, v165, v211, v219 op_sel:[1,0,0] op_sel_hi:[1,0,0]\n"
"v_mfma_f32_16x16x32_bf16 v[104:107], a[136:139], v[156:159], v[104:107]\n"
"ds_read_b64_tr_b16 a[172:173], v239 offset:40960\n"
"v_fma_mix_f32 v224, v169, v210, v218 op_sel:[0,0,0] op_sel_hi:[1,0,0]\n"
"v_mfma_f32_16x16x32_bf16 v[108:111], a[140:143], v[156:159], v[108:111]\n"
"ds_read_b64_tr_b16 a[174:175], v239 offset:45056\n"
"v_cvt_pk_bf16_f32 v165, v226, v227\n"
"v_mfma_f32_16x16x32_bf16 v[76:79], a[140:143], v[152:155], v[76:79]\n"
"ds_read_b64_tr_b16 a[176:177], v240 offset:40960\n"
"v_fma_mix_f32 v225, v169, v211, v219 op_sel:[1,0,0] op_sel_hi:[1,0,0]\n"
"v_fma_mix_f32 v226, v173, v210, v218 op_sel:[0,0,0] op_sel_hi:[1,0,0]\n"
"v_mfma_f32_16x16x32_bf16 v[44:47], a[140:143], v[148:151], v[44:47]\n"
"buffer_load_dwordx4 v248, s[40:43], s62 offen offset:2048 lds\n"
"v_cvt_pk_bf16_f32 v169, v224, v225\n"
"v_mfma_f32_16x16x32_bf16 v[12:15], a[140:143], v[144:147], v[12:15]\n"
"ds_read_b64_tr_b16 a[178:179], v240 offset:45056\n"
"v_fma_mix_f32 v227, v173, v211, v219 op_sel:[1,0,0] op_sel_hi:[1,0,0]\n"
"v_mfma_f32_16x16x32_bf16 v[16:19], a[144:147], v[144:147], v[16:19]\n"
"s_waitcnt lgkmcnt(10)\n"
"ds_read_b64_tr_b16 a[180:181], v241 offset:40960\n"
"v_fma_mix_f32 v224, v162, v212, v220 op_sel:[0,0,0] op_sel_hi:[1,0,0]\n"
"v_cvt_pk_bf16_f32 v173, v226, v227\n"
"v_mfma_f32_16x16x32_bf16 v[48:51], a[144:147], v[148:151], v[48:51]\n"
"ds_read_b64_tr_b16 a[182:183], v241 offset:45056\n"
"v_fma_mix_f32 v225, v162, v213, v221 op_sel:[1,0,0] op_sel_hi:[1,0,0]\n"
"v_mfma_f32_16x16x32_bf16 v[80:83], a[144:147], v[152:155], v[80:83]\n"
"buffer_load_dwordx4 v249, s[40:43], s62 offen offset:3072 lds\n"
"v_fma_mix_f32 v226, v166, v212, v220 op_sel:[0,0,0] op_sel_hi:[1,0,0]\n"
"v_mfma_f32_16x16x32_bf16 v[112:115], a[144:147], v[156:159], v[112:115]\n"
"ds_read_b64_tr_b16 a[184:185], v242 offset:40960\n"
"v_cvt_pk_bf16_f32 v162, v224, v225\n"
"v_fma_mix_f32 v227, v166, v213, v221 op_sel:[1,0,0] op_sel_hi:[1,0,0]\n"
"v_mfma_f32_16x16x32_bf16 v[116:119], a[148:151], v[156:159], v[116:119]\n"
"ds_read_b64_tr_b16 a[186:187], v242 offset:45056\n"
"v_fma_mix_f32 v224, v170, v212, v220 op_sel:[0,0,0] op_sel_hi:[1,0,0]\n"
"v_mfma_f32_16x16x32_bf16 v[84:87], a[148:151], v[152:155], v[84:87]\n"
"s_add_u32 m0, s60, 32768\n"
"s_mov_b32 s61, 32768\n"
"ds_read_b64_tr_b16 a[188:189], v243 offset:40960\n"
"v_mfma_f32_16x16x32_bf16 v[52:55], a[148:151], v[148:151], v[52:55]\n"
"buffer_load_dwordx4 v244, s[36:39], s61 offen offset:0 lds\n"
"v_cvt_pk_bf16_f32 v166, v226, v227\n"
"v_fma_mix_f32 v225, v170, v213, v221 op_sel:[1,0,0] op_sel_hi:[1,0,0]\n"
"v_mfma_f32_16x16x32_bf16 v[20:23], a[148:151], v[144:147], v[20:23]\n"
"ds_read_b64_tr_b16 a[190:191], v243 offset:45056\n"
"v_fma_mix_f32 v226, v174, v212, v220 op_sel:[0,0,0] op_sel_hi:[1,0,0]\n"
"v_mfma_f32_16x16x32_bf16 v[24:27], a[152:155], v[144:147], v[24:27]\n"
"ds_read_b128 v[208:211], v229 offset:2304\n"
"v_cvt_pk_bf16_f32 v170, v224, v225\n"
"v_mfma_f32_16x16x32_bf16 v[56:59], a[152:155], v[148:151], v[56:59]\n"
"ds_read_b128 v[216:219], v229 offset:4864\n"
"v_fma_mix_f32 v227, v174, v213, v221 op_sel:[1,0,0] op_sel_hi:[1,0,0]\n"
"v_fma_mix_f32 v224, v163, v214, v222 op_sel:[0,0,0] op_sel_hi:[1,0,0]\n"
"v_mfma_f32_16x16x32_bf16 v[88:91], a[152:155], v[152:155], v[88:91]\n"
"buffer_load_dwordx4 v245, s[36:39], s61 offen offset:1024 lds\n"
"v_cvt_pk_bf16_f32 v174, v226, v227\n"
"v_mfma_f32_16x16x32_bf16 v[120:123], a[152:155], v[156:159], v[120:123]\n"
"v_fma_mix_f32 v225, v163, v215, v223 op_sel:[1,0,0] op_sel_hi:[1,0,0]\n"
"v_fma_mix_f32 v226, v167, v214, v222 op_sel:[0,0,0] op_sel_hi:[1,0,0]\n"
"v_mfma_f32_16x16x32_bf16 v[124:127], a[156:159], v[156:159], v[124:127]\n"
"v_cvt_pk_bf16_f32 v163, v224, v225\n"
"v_fma_mix_f32 v227, v167, v215, v223 op_sel:[1,0,0] op_sel_hi:[1,0,0]\n"
"v_fma_mix_f32 v224, v171, v214, v222 op_sel:[0,0,0] op_sel_hi:[1,0,0]\n"
"v_mfma_f32_16x16x32_bf16 v[92:95], a[156:159], v[152:155], v[92:95]\n"
"v_cvt_pk_bf16_f32 v167, v226, v227\n"
"v_fma_mix_f32 v225, v171, v215, v223 op_sel:[1,0,0] op_sel_hi:[1,0,0]\n"
"v_mfma_f32_16x16x32_bf16 v[60:63], a[156:159], v[148:151], v[60:63]\n"
"buffer_load_dwordx4 v246, s[36:39], s61 offen offset:2048 lds\n"
"v_fma_mix_f32 v226, v175, v214, v222 op_sel:[0,0,0] op_sel_hi:[1,0,0]\n"
"v_mfma_f32_16x16x32_bf16 v[28:31], a[156:159], v[144:147], v[28:31]\n"
"v_cvt_pk_bf16_f32 v171, v224, v225\n"
"v_fma_mix_f32 v227, v175, v215, v223 op_sel:[1,0,0] op_sel_hi:[1,0,0]\n"
"v_cvt_pk_bf16_f32 v175, v226, v227\n"
"s_waitcnt vmcnt(18) lgkmcnt(0)\n"
"v_mfma_f32_16x16x32_bf16 v[0:3], a[160:163], v[160:163], v[0:3]\n"
"s_barrier\n"
"v_fma_mix_f32 v224, v176, v208, v216 op_sel:[0,0,0] op_sel_hi:[1,0,0]\n"
"v_fma_mix_f32 v225, v176, v209, v217 op_sel:[1,0,0] op_sel_hi:[1,0,0]\n"
"v_mfma_f32_16x16x32_bf16 v[32:35], a[160:163], v[164:167], v[32:35]\n"
"ds_read_b128 v[212:215], v229 offset:2320\n"
"v_fma_mix_f32 v226, v180, v208, v216 op_sel:[0,0,0] op_sel_hi:[1,0,0]\n"
"v_cvt_pk_bf16_f32 v176, v224, v225\n"
"v_mfma_f32_16x16x32_bf16 v[64:67], a[160:163], v[168:171], v[64:67]\n"
"buffer_load_dwordx4 v247, s[36:39], s61 offen offset:3072 lds\n"
"v_fma_mix_f32 v227, v180, v209, v217 op_sel:[1,0,0] op_sel_hi:[1,0,0]\n"
"v_mfma_f32_16x16x32_bf16 v[96:99], a[160:163], v[172:175], v[96:99]\n"
"ds_read_b128 v[220:223], v229 offset:4880\n"
"v_fma_mix_f32 v224, v184, v208, v216 op_sel:[0,0,0] op_sel_hi:[1,0,0]\n"
"v_mfma_f32_16x16x32_bf16 v[100:103], a[164:167], v[172:175], v[100:103]\n"
"ds_read_b64_tr_b16 a[128:129], v236 offset:49152\n"
"v_cvt_pk_bf16_f32 v180, v226, v227\n"
"v_fma_mix_f32 v225, v184, v209, v217 op_sel:[1,0,0] op_sel_hi:[1,0,0]\n"
"v_mfma_f32_16x16x32_bf16 v[68:71], a[164:167], v[168:171], v[68:71]\n"
"s_add_u32 m0, s60, 49152\n"
"s_mov_b32 s61, 49152\n"
"ds_read_b64_tr_b16 a[130:131], v236 offset:53248\n"
"v_mfma_f32_16x16x32_bf16 v[36:39], a[164:167], v[164:167], v[36:39]\n"
"buffer_load_dwordx4 v244, s[36:39], s61 offen offset:0 lds\n"
"v_fma_mix_f32 v226, v188, v208, v216 op_sel:[0,0,0] op_sel_hi:[1,0,0]\n"
"v_mfma_f32_16x16x32_bf16 v[4:7], a[164:167], v[160:163], v[4:7]\n"
"ds_read_b64_tr_b16 a[132:133], v237 offset:49152\n"
"v_cvt_pk_bf16_f32 v184, v224, v225\n"
"v_fma_mix_f32 v227, v188, v209, v217 op_sel:[1,0,0] op_sel_hi:[1,0,0]\n"
"v_mfma_f32_16x16x32_bf16 v[8:11], a[168:171], v[160:163], v[8:11]\n"
"ds_read_b64_tr_b16 a[134:135], v237 offset:53248\n"
"v_fma_mix_f32 v224, v177, v210, v218 op_sel:[0,0,0] op_sel_hi:[1,0,0]\n"
"v_mfma_f32_16x16x32_bf16 v[40:43], a[168:171], v[164:167], v[40:43]\n"
"ds_read_b64_tr_b16 a[136:137], v238 offset:49152\n"
"v_cvt_pk_bf16_f32 v188, v226, v227\n"
"v_mfma_f32_16x16x32_bf16 v[72:75], a[168:171], v[168:171], v[72:75]\n"
"buffer_load_dwordx4 v245, s[36:39], s61 offen offset:1024 lds\n"
"v_fma_mix_f32 v225, v177, v211, v219 op_sel:[1,0,0] op_sel_hi:[1,0,0]\n"
"v_fma_mix_f32 v226, v181, v210, v218 op_sel:[0,0,0] op_sel_hi:[1,0,0]\n"
"v_mfma_f32_16x16x32_bf16 v[104:107], a[168:171], v[172:175], v[104:107]\n"
"ds_read_b64_tr_b16 a[138:139], v238 offset:53248\n"
"v_cvt_pk_bf16_f32 v177, v224, v225\n"
"v_mfma_f32_16x16x32_bf16 v[108:111], a[172:175], v[172:175], v[108:111]\n"
"ds_read_b64_tr_b16 a[140:141], v239 offset:49152\n"
"v_fma_mix_f32 v227, v181, v211, v219 op_sel:[1,0,0] op_sel_hi:[1,0,0]\n"
"v_mfma_f32_16x16x32_bf16 v[76:79], a[172:175], v[168:171], v[76:79]\n"
"ds_read_b64_tr_b16 a[142:143], v239 offset:53248\n"
"v_fma_mix_f32 v224, v185, v210, v218 op_sel:[0,0,0] op_sel_hi:[1,0,0]\n"
"v_cvt_pk_bf16_f32 v181, v226, v227\n"
"v_mfma_f32_16x16x32_bf16 v[44:47], a[172:175], v[164:167], v[44:47]\n"
"buffer_load_dwordx4 v246, s[36:39], s61 offen offset:2048 lds\n"
"v_fma_mix_f32 v225, v185, v211, v219 op_sel:[1,0,0] op_sel_hi:[1,0,0]\n"
"v_mfma_f32_16x16x32_bf16 v[12:15], a[172:175], v[160:163], v[12:15]\n"
"ds_read_b64_tr_b16 a[144:145], v240 offset:49152\n"
"v_fma_mix_f32 v226, v189, v210, v218 op_sel:[0,0,0] op_sel_hi:[1,0,0]\n"
"v_mfma_f32_16x16x32_bf16 v[16:19], a[176:179], v[160:163], v[16:19]\n"
"ds_read_b64_tr_b16 a[146:147], v240 offset:53248\n"
"v_cvt_pk_bf16_f32 v185, v224, v225\n"
"v_fma_mix_f32 v227, v189, v211, v219 op_sel:[1,0,0] op_sel_hi:[1,0,0]\n"
"v_mfma_f32_16x16x32_bf16 v[48:51], a[176:179], v[164:167], v[48:51]\n"
"s_waitcnt lgkmcnt(10)\n"
"ds_read_b64_tr_b16 a[148:149], v241 offset:49152\n"
"v_fma_mix_f32 v224, v178, v212, v220 op_sel:[0,0,0] op_sel_hi:[1,0,0]\n"
"v_mfma_f32_16x16x32_bf16 v[80:83], a[176:179], v[168:171], v[80:83]\n"
"buffer_load_dwordx4 v247, s[36:39], s61 offen offset:3072 lds\n"
"v_cvt_pk_bf16_f32 v189, v226, v227\n"
"v_mfma_f32_16x16x32_bf16 v[112:115], a[176:179], v[172:175], v[112:115]\n"
"ds_read_b64_tr_b16 a[150:151], v241 offset:53248\n"
"v_fma_mix_f32 v225, v178, v213, v221 op_sel:[1,0,0] op_sel_hi:[1,0,0]\n"
"v_fma_mix_f32 v226, v182, v212, v220 op_sel:[0,0,0] op_sel_hi:[1,0,0]\n"
"v_mfma_f32_16x16x32_bf16 v[116:119], a[180:183], v[172:175], v[116:119]\n"
"ds_read_b64_tr_b16 a[152:153], v242 offset:49152\n"
"v_cvt_pk_bf16_f32 v178, v224, v225\n"
"v_mfma_f32_16x16x32_bf16 v[84:87], a[180:183], v[168:171], v[84:87]\n"
"ds_read_b64_tr_b16 a[154:155], v242 offset:53248\n"
"v_fma_mix_f32 v227, v182, v213, v221 op_sel:[1,0,0] op_sel_hi:[1,0,0]\n"
"v_mfma_f32_16x16x32_bf16 v[52:55], a[180:183], v[164:167], v[52:55]\n"
"ds_read_b64_tr_b16 a[156:157], v243 offset:49152\n"
"v_fma_mix_f32 v224, v186, v212, v220 op_sel:[0,0,0] op_sel_hi:[1,0,0]\n"
"v_cvt_pk_bf16_f32 v182, v226, v227\n"
"v_mfma_f32_16x16x32_bf16 v[20:23], a[180:183], v[160:163], v[20:23]\n"
"ds_read_b64_tr_b16 a[158:159], v243 offset:53248\n"
"v_fma_mix_f32 v225, v186, v213, v221 op_sel:[1,0,0] op_sel_hi:[1,0,0]\n"
"v_mfma_f32_16x16x32_bf16 v[24:27], a[184:187], v[160:163], v[24:27]\n"
"ds_read_b128 v[208:211], v229 offset:2432\n"
"v_fma_mix_f32 v226, v190, v212, v220 op_sel:[0,0,0] op_sel_hi:[1,0,0]\n"
"v_mfma_f32_16x16x32_bf16 v[56:59], a[184:187], v[164:167], v[56:59]\n"
"ds_read_b128 v[216:219], v229 offset:4992\n"
"v_cvt_pk_bf16_f32 v186, v224, v225\n"
"v_fma_mix_f32 v227, v190, v213, v221 op_sel:[1,0,0] op_sel_hi:[1,0,0]\n"
"v_mfma_f32_16x16x32_bf16 v[88:91], a[184:187], v[168:171], v[88:91]\n"
"v_fma_mix_f32 v224, v179, v214, v222 op_sel:[0,0,0] op_sel_hi:[1,0,0]\n"
"v_cvt_pk_bf16_f32 v190, v226, v227\n"
"v_mfma_f32_16x16x32_bf16 v[120:123], a[184:187], v[172:175], v[120:123]\n"
"v_fma_mix_f32 v225, v179, v215, v223 op_sel:[1,0,0] op_sel_hi:[1,0,0]\n"
"v_fma_mix_f32 v226, v183, v214, v222 op_sel:[0,0,0] op_sel_hi:[1,0,0]\n"
"v_mfma_f32_16x16x32_bf16 v[124:127], a[188:191], v[172:175], v[124:127]\n"
"v_cvt_pk_bf16_f32 v179, v224, v225\n"
"v_fma_mix_f32 v227, v183, v215, v223 op_sel:[1,0,0] op_sel_hi:[1,0,0]\n"
"v_fma_mix_f32 v224, v187, v214, v222 op_sel:[0,0,0] op_sel_hi:[1,0,0]\n"
"v_mfma_f32_16x16x32_bf16 v[92:95], a[188:191], v[168:171], v[92:95]\n"
"v_cvt_pk_bf16_f32 v183, v226, v227\n"
"v_fma_mix_f32 v225, v187, v215, v223 op_sel:[1,0,0] op_sel_hi:[1,0,0]\n"
"v_mfma_f32_16x16x32_bf16 v[60:63], a[188:191], v[164:167], v[60:63]\n"
"v_fma_mix_f32 v226, v191, v214, v222 op_sel:[0,0,0] op_sel_hi:[1,0,0]\n"
"v_cvt_pk_bf16_f32 v187, v224, v225\n"
"v_mfma_f32_16x16x32_bf16 v[28:31], a[188:191], v[160:163], v[28:31]\n"
"v_fma_mix_f32 v227, v191, v215, v223 op_sel:[1,0,0] op_sel_hi:[1,0,0]\n"
"v_cvt_pk_bf16_f32 v191, v226, v227\n"
"s_mov_b64 exec, 0\n"
"s_cmp_eq_u32 s60, 0\n"
"s_cbranch_scc1 L_w9_%=\n"
"s_cmp_eq_u32 s60, 4096\n"
"s_cbranch_scc1 L_w10_%=\n"
"s_cmp_eq_u32 s60, 8192\n"
"s_cbranch_scc1 L_w11_%=\n"
"s_branch L_w12_%=\n"
"L_wd3_%=:\n"
"s_mov_b64 exec, -1\n"
"s_waitcnt vmcnt(16) lgkmcnt(0)\n"
"v_mfma_f32_16x16x32_bf16 v[0:3], a[128:131], v[176:179], v[0:3]\n"
"ds_read_b128 v[212:215], v229 offset:2448\n"
"v_fma_mix_f32 v224, v192, v208, v216 op_sel:[0,0,0] op_sel_hi:[1,0,0]\n"
"v_mfma_f32_16x16x32_bf16 v[32:35], a[128:131], v[180:183], v[32:35]\n"
"ds_read_b128 v[220:223], v229 offset:5008\n"
"v_fma_mix_f32 v225, v192, v209, v217 op_sel:[1,0,0] op_sel_hi:[1,0,0]\n"
"v_fma_mix_f32 v226, v196, v208, v216 op_sel:[0,0,0] op_sel_hi:[1,0,0]\n"
"v_mfma_f32_16x16x32_bf16 v[64:67], a[128:131], v[184:187], v[64:67]\n"
"ds_read_b64_tr_b16 a[160:161], v236 offset:57344\n"
"v_cvt_pk_bf16_f32 v192, v224, v225\n"
"v_mfma_f32_16x16x32_bf16 v[96:99], a[128:131], v[188:191], v[96:99]\n"
"ds_read_b64_tr_b16 a[162:163], v236 offset:61440\n"
"v_fma_mix_f32 v227, v196, v209, v217 op_sel:[1,0,0] op_sel_hi:[1,0,0]\n"
"v_mfma_f32_16x16x32_bf16 v[100:103], a[132:135], v[188:191], v[100:103]\n"
"ds_read_b64_tr_b16 a[164:165], v237 offset:57344\n"
"v_fma_mix_f32 v224, v200, v208, v216 op_sel:[0,0,0] op_sel_hi:[1,0,0]\n"
"v_cvt_pk_bf16_f32 v196, v226, v227\n"
"v_mfma_f32_16x16x32_bf16 v[68:71], a[132:135], v[184:187], v[68:71]\n"
"ds_read_b64_tr_b16 a[166:167], v237 offset:61440\n"
"v_fma_mix_f32 v225, v200, v209, v217 op_sel:[1,0,0] op_sel_hi:[1,0,0]\n"
"v_mfma_f32_16x16x32_bf16 v[36:39], a[132:135], v[180:183], v[36:39]\n"
"ds_read_b64_tr_b16 a[168:169], v238 offset:57344\n"
"v_fma_mix_f32 v226, v204, v208, v216 op_sel:[0,0,0] op_sel_hi:[1,0,0]\n"
"v_mfma_f32_16x16x32_bf16 v[4:7], a[132:135], v[176:179], v[4:7]\n"
"ds_read_b64_tr_b16 a[170:171], v238 offset:61440\n"
"v_cvt_pk_bf16_f32 v200, v224, v225\n"
"v_mfma_f32_16x16x32_bf16 v[8:11], a[136:139], v[176:179], v[8:11]\n"
"ds_read_b64_tr_b16 a[172:173], v239 offset:57344\n"
"v_fma_mix_f32 v227, v204, v209, v217 op_sel:[1,0,0] op_sel_hi:[1,0,0]\n"
"v_mfma_f32_16x16x32_bf16 v[40:43], a[136:139], v[180:183], v[40:43]\n"
"ds_read_b64_tr_b16 a[174:175], v239 offset:61440\n"
"v_fma_mix_f32 v224, v193, v210, v218 op_sel:[0,0,0] op_sel_hi:[1,0,0]\n"
"v_mfma_f32_16x16x32_bf16 v[72:75], a[136:139], v[184:187], v[72:75]\n"
"ds_read_b64_tr_b16 a[176:177], v240 offset:57344\n"
"v_cvt_pk_bf16_f32 v204, v226, v227\n"
"v_mfma_f32_16x16x32_bf16 v[104:107], a[136:139], v[188:191], v[104:107]\n"
"ds_read_b64_tr_b16 a[178:179], v240 offset:61440\n"
"v_fma_mix_f32 v225, v193, v211, v219 op_sel:[1,0,0] op_sel_hi:[1,0,0]\n"
"v_mfma_f32_16x16x32_bf16 v[108:111], a[140:143], v[188:191], v[108:111]\n"
"ds_read_b64_tr_b16 a[180:181], v241 offset:57344\n"
"v_fma_mix_f32 v226, v197, v210, v218 op_sel:[0,0,0] op_sel_hi:[1,0,0]\n"
"v_mfma_f32_16x16x32_bf16 v[76:79], a[140:143], v[184:187], v[76:79]\n"
"ds_read_b64_tr_b16 a[182:183], v241 offset:61440\n"
"v_cvt_pk_bf16_f32 v193, v224, v225\n"
"v_mfma_f32_16x16x32_bf16 v[44:47], a[140:143], v[180:183], v[44:47]\n"
"ds_read_b64_tr_b16 a[184:185], v242 offset:57344\n"
"v_fma_mix_f32 v227, v197, v211, v219 op_sel:[1,0,0] op_sel_hi:[1,0,0]\n"
"v_mfma_f32_16x16x32_bf16 v[12:15], a[140:143], v[176:179], v[12:15]\n"
"ds_read_b64_tr_b16 a[186:187], v242 offset:61440\n"
"v_fma_mix_f32 v224, v201, v210, v218 op_sel:[0,0,0] op_sel_hi:[1,0,0]\n"
"v_mfma_f32_16x16x32_bf16 v[16:19], a[144:147], v[176:179], v[16:19]\n"
"ds_read_b64_tr_b16 a[188:189], v243 offset:57344\n"
"v_cvt_pk_bf16_f32 v197, v226, v227\n"
"v_mfma_f32_16x16x32_bf16 v[48:51], a[144:147], v[180:183], v[48:51]\n"
"ds_read_b64_tr_b16 a[190:191], v243 offset:61440\n"
"v_fma_mix_f32 v225, v201, v211, v219 op_sel:[1,0,0] op_sel_hi:[1,0,0]\n"
"v_mfma_f32_16x16x32_bf16 v[80:83], a[144:147], v[184:187], v[80:83]\n"
"v_fma_mix_f32 v226, v205, v210, v218 op_sel:[0,0,0] op_sel_hi:[1,0,0]\n"
"v_cvt_pk_bf16_f32 v201, v224, v225\n"
"v_mfma_f32_16x16x32_bf16 v[112:115], a[144:147], v[188:191], v[112:115]\n"
"s_waitcnt lgkmcnt(15)\n"
"v_fma_mix_f32 v227, v205, v211, v219 op_sel:[1,0,0] op_sel_hi:[1,0,0]\n"
"v_fma_mix_f32 v224, v194, v212, v220 op_sel:[0,0,0] op_sel_hi:[1,0,0]\n"
"v_mfma_f32_16x16x32_bf16 v[116:119], a[148:151], v[188:191], v[116:119]\n"
"v_cvt_pk_bf16_f32 v205, v226, v227\n"
"v_fma_mix_f32 v225, v194, v213, v221 op_sel:[1,0,0] op_sel_hi:[1,0,0]\n"
"v_mfma_f32_16x16x32_bf16 v[84:87], a[148:151], v[184:187], v[84:87]\n"
"v_fma_mix_f32 v226, v198, v212, v220 op_sel:[0,0,0] op_sel_hi:[1,0,0]\n"
"v_cvt_pk_bf16_f32 v194, v224, v225\n"
"v_mfma_f32_16x16x32_bf16 v[52:55], a[148:151], v[180:183], v[52:55]\n"
"v_fma_mix_f32 v227, v198, v213, v221 op_sel:[1,0,0] op_sel_hi:[1,0,0]\n"
"v_fma_mix_f32 v224, v202, v212, v220 op_sel:[0,0,0] op_sel_hi:[1,0,0]\n"
"v_mfma_f32_16x16x32_bf16 v[20:23], a[148:151], v[176:179], v[20:23]\n"
"v_cvt_pk_bf16_f32 v198, v226, v227\n"
"v_fma_mix_f32 v225, v202, v213, v221 op_sel:[1,0,0] op_sel_hi:[1,0,0]\n"
"v_mfma_f32_16x16x32_bf16 v[24:27], a[152:155], v[176:179], v[24:27]\n"
"v_fma_mix_f32 v226, v206, v212, v220 op_sel:[0,0,0] op_sel_hi:[1,0,0]\n"
"v_cvt_pk_bf16_f32 v202, v224, v225\n"
"v_mfma_f32_16x16x32_bf16 v[56:59], a[152:155], v[180:183], v[56:59]\n"
"v_fma_mix_f32 v227, v206, v213, v221 op_sel:[1,0,0] op_sel_hi:[1,0,0]\n"
"v_fma_mix_f32 v224, v195, v214, v222 op_sel:[0,0,0] op_sel_hi:[1,0,0]\n"
"v_mfma_f32_16x16x32_bf16 v[88:91], a[152:155], v[184:187], v[88:91]\n"
"v_cvt_pk_bf16_f32 v206, v226, v227\n"
"v_fma_mix_f32 v225, v195, v215, v223 op_sel:[1,0,0] op_sel_hi:[1,0,0]\n"
"v_mfma_f32_16x16x32_bf16 v[120:123], a[152:155], v[188:191], v[120:123]\n"
"v_fma_mix_f32 v226, v199, v214, v222 op_sel:[0,0,0] op_sel_hi:[1,0,0]\n"
"v_cvt_pk_bf16_f32 v195, v224, v225\n"
"v_mfma_f32_16x16x32_bf16 v[124:127], a[156:159], v[188:191], v[124:127]\n"
"v_fma_mix_f32 v227, v199, v215, v223 op_sel:[1,0,0] op_sel_hi:[1,0,0]\n"
"v_fma_mix_f32 v224, v203, v214, v222 op_sel:[0,0,0] op_sel_hi:[1,0,0]\n"
"v_mfma_f32_16x16x32_bf16 v[92:95], a[156:159], v[184:187], v[92:95]\n"
"v_cvt_pk_bf16_f32 v199, v226, v227\n"
"v_fma_mix_f32 v225, v203, v215, v223 op_sel:[1,0,0] op_sel_hi:[1,0,0]\n"
"v_mfma_f32_16x16x32_bf16 v[60:63], a[156:159], v[180:183], v[60:63]\n"
"v_fma_mix_f32 v226, v207, v214, v222 op_sel:[0,0,0] op_sel_hi:[1,0,0]\n"
"v_cvt_pk_bf16_f32 v203, v224, v225\n"
"v_mfma_f32_16x16x32_bf16 v[28:31], a[156:159], v[176:179], v[28:31]\n"
"v_fma_mix_f32 v227, v207, v215, v223 op_sel:[1,0,0] op_sel_hi:[1,0,0]\n"
"v_cvt_pk_bf16_f32 v207, v226, v227\n"
"s_waitcnt vmcnt(28) lgkmcnt(0)\n"
"v_mov_b32 v224, -1\n"
"v_mov_b32 v225, -1\n"
"v_mov_b32 v226, -1\n"
"v_mov_b32 v227, -1\n"
"v_mfma_f32_16x16x32_bf16 v[0:3], a[160:163], v[192:195], v[0:3]\n"
"s_barrier\n"
"v_mfma_f32_16x16x32_bf16 v[4:7], a[164:167], v[192:195], v[4:7]\n"
"ds_read_b128 a[128:131], v232 offset:0\n"
"v_mfma_f32_16x16x32_bf16 v[8:11], a[168:171], v[192:195], v[8:11]\n"
"ds_read_b128 a[132:135], v233 offset:0\n"
"v_mfma_f32_16x16x32_bf16 v[12:15], a[172:175], v[192:195], v[12:15]\n"
"ds_read_b128 a[136:139], v234 offset:0\n"
"v_mfma_f32_16x16x32_bf16 v[16:19], a[176:179], v[192:195], v[16:19]\n"
"ds_read_b128 a[140:143], v235 offset:0\n"
"v_mfma_f32_16x16x32_bf16 v[20:23], a[180:183], v[192:195], v[20:23]\n"
"ds_read_b128 a[144:147], v232 offset:4096\n"
"v_mfma_f32_16x16x32_bf16 v[24:27], a[184:187], v[192:195], v[24:27]\n"
"ds_read_b128 a[148:151], v233 offset:4096\n"
"v_mfma_f32_16x16x32_bf16 v[28:31], a[188:191], v[192:195], v[28:31]\n"
"ds_read_b128 a[152:155], v234 offset:4096\n"
"v_mfma_f32_16x16x32_bf16 v[32:35], a[160:163], v[196:199], v[32:35]\n"
"ds_read_b128 a[156:159], v235 offset:4096\n"
"v_mfma_f32_16x16x32_bf16 v[36:39], a[164:167], v[196:199], v[36:39]\n"
"v_mfma_f32_16x16x32_bf16 v[40:43], a[168:171], v[196:199], v[40:43]\n"
"v_mfma_f32_16x16x32_bf16 v[44:47], a[172:175], v[196:199], v[44:47]\n"
"v_cvt_pk_bf16_f32 v128, v0, v1\n"
"v_cvt_pk_bf16_f32 v129, v2, v3\n"
"v_mfma_f32_16x16x32_bf16 v[48:51], a[176:179], v[196:199], v[48:51]\n"
"v_cvt_pk_bf16_f32 v130, v4, v5\n"
"v_cvt_pk_bf16_f32 v131, v6, v7\n"
"v_mfma_f32_16x16x32_bf16 v[52:55], a[180:183], v[196:199], v[52:55]\n"
"buffer_store_dwordx4 v[224:227], v228, s[44:47], %9 offen offset:0 sc1\n"
"v_cvt_pk_bf16_f32 v132, v8, v9\n"
"v_cvt_pk_bf16_f32 v133, v10, v11\n"
"v_mfma_f32_16x16x32_bf16 v[56:59], a[184:187], v[196:199], v[56:59]\n"
"v_cvt_pk_bf16_f32 v134, v12, v13\n"
"v_cvt_pk_bf16_f32 v135, v14, v15\n"
"v_mfma_f32_16x16x32_bf16 v[60:63], a[188:191], v[196:199], v[60:63]\n"
"v_permlane16_swap_b32 v128, v130\n"
"v_mfma_f32_16x16x32_bf16 v[64:67], a[160:163], v[200:203], v[64:67]\n"
"v_permlane16_swap_b32 v129, v131\n"
"v_mfma_f32_16x16x32_bf16 v[68:71], a[164:167], v[200:203], v[68:71]\n"
"buffer_store_dwordx4 v[224:227], v228, s[44:47], %9 offen offset:1024 sc1\n"
"v_cvt_pk_bf16_f32 v136, v16, v17\n"
"v_cvt_pk_bf16_f32 v137, v18, v19\n"
"v_mfma_f32_16x16x32_bf16 v[72:75], a[168:171], v[200:203], v[72:75]\n"
"v_cvt_pk_bf16_f32 v138, v20, v21\n"
"v_cvt_pk_bf16_f32 v139, v22, v23\n"
"v_mfma_f32_16x16x32_bf16 v[76:79], a[172:175], v[200:203], v[76:79]\n"
"v_permlane16_swap_b32 v132, v134\n"
"v_mfma_f32_16x16x32_bf16 v[80:83], a[176:179], v[200:203], v[80:83]\n"
"v_permlane16_swap_b32 v133, v135\n"
"v_mfma_f32_16x16x32_bf16 v[84:87], a[180:183], v[200:203], v[84:87]\n"
"buffer_store_dwordx4 v[224:227], v228, s[44:47], %9 offen offset:1536 sc1\n"
"v_cvt_pk_bf16_f32 v140, v24, v25\n"
"v_cvt_pk_bf16_f32 v141, v26, v27\n"
"v_mfma_f32_16x16x32_bf16 v[88:91], a[184:187], v[200:203], v[88:91]\n"
"v_cvt_pk_bf16_f32 v142, v28, v29\n"
"v_cvt_pk_bf16_f32 v143, v30, v31\n"
"v_mfma_f32_16x16x32_bf16 v[92:95], a[188:191], v[200:203], v[92:95]\n"
"v_permlane16_swap_b32 v136, v138\n"
"v_mfma_f32_16x16x32_bf16 v[96:99], a[160:163], v[204:207], v[96:99]\n"
"v_permlane16_swap_b32 v137, v139\n"
"v_mfma_f32_16x16x32_bf16 v[100:103], a[164:167], v[204:207], v[100:103]\n"
"v_cvt_pk_bf16_f32 v144, v32, v33\n"
"v_cvt_pk_bf16_f32 v145, v34, v35\n"
"v_mfma_f32_16x16x32_bf16 v[104:107], a[168:171], v[204:207], v[104:107]\n"
"v_cvt_pk_bf16_f32 v146, v36, v37\n"
"v_cvt_pk_bf16_f32 v147, v38, v39\n"
"v_mfma_f32_16x16x32_bf16 v[108:111], a[172:175], v[204:207], v[108:111]\n"
"v_permlane16_swap_b32 v140, v142\n"
"v_mfma_f32_16x16x32_bf16 v[112:115], a[176:179], v[204:207], v[112:115]\n"
"v_permlane16_swap_b32 v141, v143\n"
"v_mfma_f32_16x16x32_bf16 v[116:119], a[180:183], v[204:207], v[116:119]\n"
"v_cvt_pk_bf16_f32 v148, v40, v41\n"
"v_cvt_pk_bf16_f32 v149, v42, v43\n"
"v_mfma_f32_16x16x32_bf16 v[120:123], a[184:187], v[204:207], v[120:123]\n"
"v_cvt_pk_bf16_f32 v150, v44, v45\n"
"v_cvt_pk_bf16_f32 v151, v46, v47\n"
"v_mfma_f32_16x16x32_bf16 v[124:127], a[188:191], v[204:207], v[124:127]\n"
"v_permlane16_swap_b32 v144, v146\n"
"s_waitcnt lgkmcnt(0)\n"
"s_nop 1\n"
"v_mfma_f32_16x16x32_bf16 v[0:3], a[128:131], v[128:131], 0\n"
"v_mfma_f32_16x16x32_bf16 v[0:3], a[132:135], v[132:135], v[0:3]\n"
"v_mfma_f32_16x16x32_bf16 v[0:3], a[136:139], v[136:139], v[0:3]\n"
"v_mfma_f32_16x16x32_bf16 v[0:3], a[140:143], v[140:143], v[0:3]\n"
"v_permlane16_swap_b32 v145, v147\n"
"v_mfma_f32_16x16x32_bf16 v[4:7], a[144:147], v[128:131], 0\n"
"v_cvt_pk_bf16_f32 v152, v48, v49\n"
"v_cvt_pk_bf16_f32 v153, v50, v51\n"
"v_mfma_f32_16x16x32_bf16 v[4:7], a[148:151], v[132:135], v[4:7]\n"
"v_cvt_pk_bf16_f32 v154, v52, v53\n"
"v_cvt_pk_bf16_f32 v155, v54, v55\n"
"v_mfma_f32_16x16x32_bf16 v[4:7], a[152:155], v[136:139], v[4:7]\n"
"v_permlane16_swap_b32 v148, v150\n"
"v_mfma_f32_16x16x32_bf16 v[4:7], a[156:159], v[140:143], v[4:7]\n"
"v_permlane16_swap_b32 v149, v151\n"
"v_cvt_pk_bf16_f32 v156, v56, v57\n"
"v_cvt_pk_bf16_f32 v157, v58, v59\n"
"v_cvt_pk_bf16_f32 v158, v60, v61\n"
"v_cvt_pk_bf16_f32 v159, v62, v63\n"
"v_permlane16_swap_b32 v152, v154\n"
"v_permlane16_swap_b32 v153, v155\n"
"v_cvt_pk_bf16_f32 v160, v64, v65\n"
"v_cvt_pk_bf16_f32 v161, v66, v67\n"
"v_cvt_pk_bf16_f32 v162, v68, v69\n"
"v_cvt_pk_bf16_f32 v163, v70, v71\n"
"v_permlane16_swap_b32 v156, v158\n"
"v_permlane16_swap_b32 v157, v159\n"
"s_nop 1\n"
"v_mfma_f32_16x16x32_bf16 v[8:11], a[128:131], v[144:147], 0\n"
"v_cvt_pk_bf16_f32 v164, v72, v73\n"
"v_cvt_pk_bf16_f32 v165, v74, v75\n"
"v_mfma_f32_16x16x32_bf16 v[8:11], a[132:135], v[148:151], v[8:11]\n"
"v_cvt_pk_bf16_f32 v166, v76, v77\n"
"v_cvt_pk_bf16_f32 v167, v78, v79\n"
"v_mfma_f32_16x16x32_bf16 v[8:11], a[136:139], v[152:155], v[8:11]\n"
"v_permlane16_swap_b32 v160, v162\n"
"v_mfma_f32_16x16x32_bf16 v[8:11], a[140:143], v[156:159], v[8:11]\n"
"v_permlane16_swap_b32 v161, v163\n"
"v_mfma_f32_16x16x32_bf16 v[12:15], a[144:147], v[144:147], 0\n"
"v_cvt_pk_bf16_f32 v168, v80, v81\n"
"v_cvt_pk_bf16_f32 v169, v82, v83\n"
"v_mfma_f32_16x16x32_bf16 v[12:15], a[148:151], v[148:151], v[12:15]\n"
"v_cvt_pk_bf16_f32 v170, v84, v85\n"
"v_cvt_pk_bf16_f32 v171, v86, v87\n"
"v_mfma_f32_16x16x32_bf16 v[12:15], a[152:155], v[152:155], v[12:15]\n"
"v_permlane16_swap_b32 v164, v166\n"
"v_mfma_f32_16x16x32_bf16 v[12:15], a[156:159], v[156:159], v[12:15]\n"
"v_permlane16_swap_b32 v165, v167\n"
"v_cvt_pk_bf16_f32 v172, v88, v89\n"
"v_cvt_pk_bf16_f32 v173, v90, v91\n"
"v_cvt_pk_bf16_f32 v174, v92, v93\n"
"v_cvt_pk_bf16_f32 v175, v94, v95\n"
"v_permlane16_swap_b32 v168, v170\n"
"v_permlane16_swap_b32 v169, v171\n"
"v_cvt_pk_bf16_f32 v176, v96, v97\n"
"v_cvt_pk_bf16_f32 v177, v98, v99\n"
"v_cvt_pk_bf16_f32 v178, v100, v101\n"
"v_cvt_pk_bf16_f32 v179, v102, v103\n"
"v_permlane16_swap_b32 v172, v174\n"
"v_permlane16_swap_b32 v173, v175\n"
"s_nop 1\n"
"v_mfma_f32_16x16x32_bf16 v[16:19], a[128:131], v[160:163], 0\n"
"v_cvt_pk_bf16_f32 v180, v104, v105\n"
"v_cvt_pk_bf16_f32 v181, v106, v107\n"
"v_mfma_f32_16x16x32_bf16 v[16:19], a[132:135], v[164:167], v[16:19]\n"
"v_cvt_pk_bf16_f32 v182, v108, v109\n"
"v_cvt_pk_bf16_f32 v183, v110, v111\n"
"v_mfma_f32_16x16x32_bf16 v[16:19], a[136:139], v[168:171], v[16:19]\n"
"v_permlane16_swap_b32 v176, v178\n"
"v_mfma_f32_16x16x32_bf16 v[16:19], a[140:143], v[172:175], v[16:19]\n"
"v_permlane16_swap_b32 v177, v179\n"
"v_mfma_f32_16x16x32_bf16 v[20:23], a[144:147], v[160:163], 0\n"
"v_cvt_pk_bf16_f32 v184, v112, v113\n"
"v_cvt_pk_bf16_f32 v185, v114, v115\n"
"v_mfma_f32_16x16x32_bf16 v[20:23], a[148:151], v[164:167], v[20:23]\n"
"v_cvt_pk_bf16_f32 v186, v116, v117\n"
"v_cvt_pk_bf16_f32 v187, v118, v119\n"
"v_mfma_f32_16x16x32_bf16 v[20:23], a[152:155], v[168:171], v[20:23]\n"
"v_permlane16_swap_b32 v180, v182\n"
"v_mfma_f32_16x16x32_bf16 v[20:23], a[156:159], v[172:175], v[20:23]\n"
"v_permlane16_swap_b32 v181, v183\n"
"v_cvt_pk_bf16_f32 v188, v120, v121\n"
"v_cvt_pk_bf16_f32 v189, v122, v123\n"
"v_cvt_pk_bf16_f32 v190, v124, v125\n"
"v_cvt_pk_bf16_f32 v191, v126, v127\n"
"v_permlane16_swap_b32 v184, v186\n"
"v_permlane16_swap_b32 v185, v187\n"
"s_nop 0\n"
"v_permlane16_swap_b32 v188, v190\n"
"v_permlane16_swap_b32 v189, v191\n"
"s_nop 1\n"
"v_mfma_f32_16x16x32_bf16 v[24:27], a[128:131], v[176:179], 0\n"
"v_mfma_f32_16x16x32_bf16 v[24:27], a[132:135], v[180:183], v[24:27]\n"
"v_mfma_f32_16x16x32_bf16 v[24:27], a[136:139], v[184:187], v[24:27]\n"
"v_mfma_f32_16x16x32_bf16 v[24:27], a[140:143], v[188:191], v[24:27]\n"
"v_mfma_f32_16x16x32_bf16 v[28:31], a[144:147], v[176:179], 0\n"
"v_mfma_f32_16x16x32_bf16 v[28:31], a[148:151], v[180:183], v[28:31]\n"
"v_mfma_f32_16x16x32_bf16 v[28:31], a[152:155], v[184:187], v[28:31]\n"
"v_mfma_f32_16x16x32_bf16 v[28:31], a[156:159], v[188:191], v[28:31]\n"
"v_mov_b32 v80, 0xff800000\n"
"v_mov_b32 v81, 0xff800000\n"
"v_mov_b32 v82, 0xff800000\n"
"v_mov_b32 v83, 0xff800000\n"
"v_bfe_u32 v230, v228, 7, 1\n"
"v_bfe_u32 v231, v228, 8, 1\n"
"v_and_b32 v229, 16, v228\n"
"s_mov_b32 s50, 0x7fffffff\n"
"s_mov_b32 s51, 0x20000\n"
"L_pipe_%=:\n"
"s_load_dwordx8 s[52:59], %0, s59\n"
"ds_read_b128 a[128:131], v232 offset:8192\n"
"ds_read_b128 a[132:135], v233 offset:8192\n"
"ds_read_b128 a[136:139], v234 offset:8192\n"
"ds_read_b128 a[140:143], v235 offset:8192\n"
"ds_read_b128 a[144:147], v232 offset:12288\n"
"ds_read_b128 a[148:151], v233 offset:12288\n"
"L_w6_%=:\n"
"s_cbranch_execz L_wd1_%=\n"
"ds_read_b128 a[152:155], v234 offset:12288\n"
"ds_read_b128 a[156:159], v235 offset:12288\n"
"v_max3_f32 v80, v80, v0, v1\n"
"v_max3_f32 v81, v81, v8, v9\n"
"v_max3_f32 v82, v82, v16, v17\n"
"v_max3_f32 v83, v83, v24, v25\n"
"v_max3_f32 v80, v80, v2, v3\n"
"v_max3_f32 v81, v81, v10, v11\n"
"v_max3_f32 v82, v82, v18, v19\n"
"v_max3_f32 v83, v83, v26, v27\n"
"v_max3_f32 v80, v80, v4, v5\n"
"v_max3_f32 v81, v81, v12, v13\n"
"v_max3_f32 v82, v82, v20, v21\n"
"v_max3_f32 v83, v83, v28, v29\n"
"v_max3_f32 v80, v80, v6, v7\n"
"v_max3_f32 v81, v81, v14, v15\n"
"v_max3_f32 v82, v82, v22, v23\n"
"v_max3_f32 v83, v83, v30, v31\n"
"v_mov_b32 v224, v80\n"
"v_mov_b32 v225, v81\n"
"v_mov_b32 v226, v82\n"
"v_mov_b32 v227, v83\n"
"s_nop 0\n"
"v_permlane16_swap_b32 v80, v224\n"
"v_permlane16_swap_b32 v81, v225\n"
"v_permlane16_swap_b32 v82, v226\n"
"v_permlane16_swap_b32 v83, v227\n"
"v_max_f32 v80, v80, v224\n"
"v_max_f32 v81, v81, v225\n"
"v_max_f32 v82, v82, v226\n"
"v_max_f32 v83, v83, v227\n"
"v_mov_b32 v224, v80\n"
"v_mov_b32 v225, v81\n"
"v_mov_b32 v226, v82\n"
"v_mov_b32 v227, v83\n"
"s_nop 0\n"
"v_permlane32_swap_b32 v80, v224\n"
"v_permlane32_swap_b32 v81, v225\n"
"v_permlane32_swap_b32 v82, v226\n"
"v_permlane32_swap_b32 v83, v227\n"
"v_max_f32 v80, v80, v224\n"
"v_max_f32 v81, v81, v225\n"
"v_max_f32 v82, v82, v226\n"
"v_max_f32 v83, v83, v227\n"
"v_sub_f32_e64 v64, -v80, s67\n"
"v_sub_f32_e64 v68, -v81, s67\n"
"v_sub_f32_e64 v72, -v82, s67\n"
"v_sub_f32_e64 v76, -v83, s67\n"
"v_pk_add_f32 v[0:1], v[0:1], v[64:65] op_sel_hi:[1,0]\n"
"v_pk_add_f32 v[2:3], v[2:3], v[64:65] op_sel_hi:[1,0]\n"
"v_pk_add_f32 v[4:5], v[4:5], v[64:65] op_sel_hi:[1,0]\n"
"v_pk_add_f32 v[6:7], v[6:7], v[64:65] op_sel_hi:[1,0]\n"
"v_pk_add_f32 v[8:9], v[8:9], v[68:69] op_sel_hi:[1,0]\n"
"v_pk_add_f32 v[10:11], v[10:11], v[68:69] op_sel_hi:[1,0]\n"
"v_pk_add_f32 v[12:13], v[12:13], v[68:69] op_sel_hi:[1,0]\n"
"v_pk_add_f32 v[14:15], v[14:15], v[68:69] op_sel_hi:[1,0]\n"
"v_pk_add_f32 v[16:17], v[16:17], v[72:73] op_sel_hi:[1,0]\n"
"v_pk_add_f32 v[18:19], v[18:19], v[72:73] op_sel_hi:[1,0]\n"
"v_pk_add_f32 v[20:21], v[20:21], v[72:73] op_sel_hi:[1,0]\n"
"v_pk_add_f32 v[22:23], v[22:23], v[72:73] op_sel_hi:[1,0]\n"
"v_pk_add_f32 v[24:25], v[24:25], v[76:77] op_sel_hi:[1,0]\n"
"v_pk_add_f32 v[26:27], v[26:27], v[76:77] op_sel_hi:[1,0]\n"
"v_pk_add_f32 v[28:29], v[28:29], v[76:77] op_sel_hi:[1,0]\n"
"v_pk_add_f32 v[30:31], v[30:31], v[76:77] op_sel_hi:[1,0]\n"
"v_mov_b32 v65, v64\n"
"v_pk_mov_b32 v[66:67], v[64:65], v[64:65] op_sel:[0,0]\n"
"v_mov_b32 v69, v68\n"
"v_pk_mov_b32 v[70:71], v[68:69], v[68:69] op_sel:[0,0]\n"
"v_mov_b32 v73, v72\n"
"v_pk_mov_b32 v[74:75], v[72:73], v[72:73] op_sel:[0,0]\n"
"v_mov_b32 v77, v76\n"
"v_pk_mov_b32 v[78:79], v[76:77], v[76:77] op_sel:[0,0]\n"
"v_exp_f32 v0, v0\n"
"v_exp_f32 v1, v1\n"
"v_exp_f32 v2, v2\n"
"v_exp_f32 v3, v3\n"
"v_exp_f32 v4, v4\n"
"v_exp_f32 v5, v5\n"
"v_exp_f32 v6, v6\n"
"v_exp_f32 v7, v7\n"
"v_exp_f32 v8, v8\n"
"v_exp_f32 v9, v9\n"
"v_exp_f32 v10, v10\n"
"v_exp_f32 v11, v11\n"
"v_exp_f32 v12, v12\n"
"v_exp_f32 v13, v13\n"
"v_exp_f32 v14, v14\n"
"v_exp_f32 v15, v15\n"
"v_exp_f32 v16, v16\n"
"v_exp_f32 v17, v17\n"
"v_exp_f32 v18, v18\n"
"v_exp_f32 v19, v19\n"
"v_exp_f32 v20, v20\n"
"v_exp_f32 v21, v21\n"
"v_exp_f32 v22, v22\n"
"v_exp_f32 v23, v23\n"
"v_exp_f32 v24, v24\n"
"v_exp_f32 v25, v25\n"
"v_exp_f32 v26, v26\n"
"v_exp_f32 v27, v27\n"
"s_nop 0\n"
"v_cvt_pk_bf16_f32 v32, v0, v1\n"
"v_cvt_pk_bf16_f32 v33, v2, v3\n"
"v_cvt_pk_bf16_f32 v34, v4, v5\n"
"v_cvt_pk_bf16_f32 v35, v6, v7\n"
"v_cvt_pk_bf16_f32 v40, v8, v9\n"
"v_cvt_pk_bf16_f32 v41, v10, v11\n"
"v_cvt_pk_bf16_f32 v42, v12, v13\n"
"v_cvt_pk_bf16_f32 v43, v14, v15\n"
"v_cvt_pk_bf16_f32 v48, v16, v17\n"
"v_cvt_pk_bf16_f32 v49, v18, v19\n"
"v_cvt_pk_bf16_f32 v50, v20, v21\n"
"v_cvt_pk_bf16_f32 v51, v22, v23\n"
"v_cvt_pk_bf16_f32 v56, v24, v25\n"
"s_mov_b32 s61, 65536\n"
"s_mov_b32 s62, 32768\n"
"s_mov_b32 s63, 7\n"
"v_mov_b32 v88, 0\n"
"v_mov_b32 v89, 0\n"
"v_mov_b32 v90, 0\n"
"v_mov_b32 v91, 0\n"
"v_mov_b32 v96, 0\n"
"v_mov_b32 v97, 0\n"
"v_mov_b32 v98, 0\n"
"v_mov_b32 v99, 0\n"
"v_mov_b32 v104, 0\n"
"v_mov_b32 v105, 0\n"
"v_mov_b32 v106, 0\n"
"v_mov_b32 v107, 0\n"
"v_mov_b32 v112, 0\n"
"v_mov_b32 v113, 0\n"
"v_mov_b32 v114, 0\n"
"v_mov_b32 v115, 0\n"
"v_bfe_u32 v192, v228, 5, 2\n"
"v_lshrrev_b32 v193, 9, v228\n"
"v_lshrrev_b32 v194, 1, v228\n"
"s_add_u32 s64, s60, 131072\n"
"v_add_u32 v194, s64, v194\n"
"s_waitcnt vmcnt(12) lgkmcnt(0)\n"
"s_barrier\n"
"v_bfe_u32 v252, v228, 4, 4\n"
"v_mul_u32_u24 v252, 1280, v252\n"
"v_lshrrev_b32 v254, 8, v228\n"
"v_lshl_add_u32 v252, v254, 3, v252\n"
"s_lshl_b32 s64, %11, 2\n"
"s_add_u32 s64, s64, %17\n"
"s_mul_i32 s64, s64, 20480\n"
"s_mul_i32 s65, %12, 160\n"
"s_add_u32 s64, s64, s65\n"
"v_add_u32 v252, s64, v252\n"
"global_load_dwordx2 a[244:245], v252, %1 offset:0\n"
"global_load_dwordx2 a[246:247], v252, %1 offset:32\n"
"global_load_dwordx2 a[248:249], v252, %1 offset:64\n"
"global_load_dwordx2 a[250:251], v252, %1 offset:96\n"
"global_load_dwordx2 a[252:253], v252, %1 offset:128\n"
"s_branch L_loop_%=\n"
"L_sig_%=:\n"
"s_waitcnt vmcnt(0)\n"
"s_lshl_b32 s64, %18, 2\n"
"s_add_u32 s64, s64, %17\n"
"s_and_b32 s64, s64, 15\n"
"s_lshl_b32 s64, s64, 7\n"
"s_add_u32 s64, s64, 626688\n"
"v_mov_b32 v224, s64\n"
"v_mov_b32 v225, 1\n"
"s_mov_b64 exec, 1\n"
"global_atomic_add v224, v225, %0\n"
"s_mov_b64 exec, -1\n"
"s_branch L_sigret_%=\n"
".p2align 6\n"
"L_loop_%=:\n"
"v_mfma_f32_16x16x32_bf16 v[0:3], a[128:131], v[128:131], v[64:67]\n"
"v_exp_f32 v28, v28\n"
"v_mfma_f32_16x16x32_bf16 v[0:3], a[132:135], v[132:135], v[0:3]\n"
"ds_read_b64_tr_b16 a[160:161], v236 offset:57344\n"
"v_cvt_pk_bf16_f32 v57, v26, v27\n"
"v_mfma_f32_16x16x32_bf16 v[0:3], a[136:139], v[136:139], v[0:3]\n"
"v_exp_f32 v29, v29\n"
"v_mfma_f32_16x16x32_bf16 v[0:3], a[140:143], v[140:143], v[0:3]\n"
"ds_read_b64_tr_b16 a[162:163], v236 offset:61440\n"
"v_mad_u32_u24 v204, v230, s54, v229\n"
"v_mfma_f32_16x16x32_bf16 v[4:7], a[156:159], v[140:143], v[64:67]\n"
"v_exp_f32 v30, v30\n"
"v_mfma_f32_16x16x32_bf16 v[4:7], a[152:155], v[136:139], v[4:7]\n"
"ds_read_b64_tr_b16 a[164:165], v236 offset:0\n"
"v_cvt_pk_bf16_f32 v58, v28, v29\n"
"v_mfma_f32_16x16x32_bf16 v[4:7], a[148:151], v[132:135], v[4:7]\n"
"v_exp_f32 v31, v31\n"
"v_mfma_f32_16x16x32_bf16 v[4:7], a[144:147], v[128:131], v[4:7]\n"
"ds_read_b64_tr_b16 a[166:167], v236 offset:4096\n"
"v_mad_u32_u24 v204, v231, s55, v204\n"
"v_mfma_f32_16x16x32_bf16 v[12:15], a[144:147], v[144:147], v[68:71]\n"
"v_exp_f32 v0, v0\n"
"v_mfma_f32_16x16x32_bf16 v[12:15], a[148:151], v[148:151], v[12:15]\n"
"ds_read_b64_tr_b16 a[168:169], v237 offset:57344\n"
"v_cvt_pk_bf16_f32 v59, v30, v31\n"
"v_mfma_f32_16x16x32_bf16 v[12:15], a[152:155], v[152:155], v[12:15]\n"
"v_exp_f32 v1, v1\n"
"v_mfma_f32_16x16x32_bf16 v[12:15], a[156:159], v[156:159], v[12:15]\n"
"ds_read_b64_tr_b16 a[170:171], v237 offset:61440\n"
"v_mad_u32_u24 v204, v192, s56, v204\n"
"v_mfma_f32_16x16x32_bf16 v[8:11], a[140:143], v[156:159], v[68:71]\n"
"s_mov_b64 s[48:49], s[52:53]\n"
"v_exp_f32 v2, v2\n"
"v_mfma_f32_16x16x32_bf16 v[8:11], a[136:139], v[152:155], v[8:11]\n"
"ds_read_b64_tr_b16 a[172:173], v237 offset:0\n"
"v_cvt_pk_bf16_f32 v36, v0, v1\n"
"v_mfma_f32_16x16x32_bf16 v[8:11], a[132:135], v[148:151], v[8:11]\n"
"v_exp_f32 v3, v3\n"
"v_mfma_f32_16x16x32_bf16 v[8:11], a[128:131], v[144:147], v[8:11]\n"
"ds_read_b64_tr_b16 a[174:175], v237 offset:4096\n"
"v_mad_u32_u24 v204, v193, s57, v204\n"
"v_mfma_f32_16x16x32_bf16 v[16:19], a[128:131], v[160:163], v[72:75]\n"
"v_exp_f32 v4, v4\n"
"v_mfma_f32_16x16x32_bf16 v[16:19], a[132:135], v[164:167], v[16:19]\n"
"ds_read_b64_tr_b16 a[176:177], v238 offset:57344\n"
"v_cvt_pk_bf16_f32 v37, v2, v3\n"
"v_mfma_f32_16x16x32_bf16 v[16:19], a[136:139], v[168:171], v[16:19]\n"
"v_exp_f32 v5, v5\n"
"v_mfma_f32_16x16x32_bf16 v[16:19], a[140:143], v[172:175], v[16:19]\n"
"ds_read_b64_tr_b16 a[178:179], v238 offset:61440\n"
"v_mfma_f32_16x16x32_bf16 v[20:23], a[156:159], v[172:175], v[72:75]\n"
"v_exp_f32 v6, v6\n"
"v_mfma_f32_16x16x32_bf16 v[20:23], a[152:155], v[168:171], v[20:23]\n"
"ds_read_b64_tr_b16 a[180:181], v238 offset:0\n"
"v_cvt_pk_bf16_f32 v38, v4, v5\n"
"v_mfma_f32_16x16x32_bf16 v[20:23], a[148:151], v[164:167], v[20:23]\n"
"v_exp_f32 v7, v7\n"
"v_mfma_f32_16x16x32_bf16 v[20:23], a[144:147], v[160:163], v[20:23]\n"
"ds_read_b64_tr_b16 a[182:183], v238 offset:4096\n"
"v_mfma_f32_16x16x32_bf16 v[28:31], a[144:147], v[176:179], v[76:79]\n"
"v_exp_f32 v8, v8\n"
"v_mfma_f32_16x16x32_bf16 v[28:31], a[148:151], v[180:183], v[28:31]\n"
"ds_read_b64_tr_b16 a[184:185], v239 offset:57344\n"
"v_cvt_pk_bf16_f32 v39, v6, v7\n"
"v_mfma_f32_16x16x32_bf16 v[28:31], a[152:155], v[184:187], v[28:31]\n"
"v_exp_f32 v9, v9\n"
"v_mfma_f32_16x16x32_bf16 v[28:31], a[156:159], v[188:191], v[28:31]\n"
"ds_read_b64_tr_b16 a[186:187], v239 offset:61440\n"
"v_mfma_f32_16x16x32_bf16 v[24:27], a[140:143], v[188:191], v[76:79]\n"
"v_exp_f32 v10, v10\n"
"v_mfma_f32_16x16x32_bf16 v[24:27], a[136:139], v[184:187], v[24:27]\n"
"ds_read_b64_tr_b16 a[188:189], v239 offset:0\n"
"v_cvt_pk_bf16_f32 v44, v8, v9\n"
"v_mfma_f32_16x16x32_bf16 v[24:27], a[132:135], v[180:183], v[24:27]\n"
"s_waitcnt lgkmcnt(7)\n"
"v_exp_f32 v11, v11\n"
"v_mfma_f32_16x16x32_bf16 v[24:27], a[128:131], v[176:179], v[24:27]\n"
"ds_read_b64_tr_b16 a[190:191], v239 offset:4096\n"
"v_mfma_f32_16x16x32_bf16 a[0:3], a[160:163], v[88:91], a[0:3]\n"
"v_exp_f32 v12, v12\n"
"v_mfma_f32_16x16x32_bf16 a[0:3], a[164:167], v[32:35], a[0:3]\n"
"ds_read_b128 a[128:131], v232 offset:16384\n"
"v_cvt_pk_bf16_f32 v45, v10, v11\n"
"v_mfma_f32_16x16x32_bf16 a[32:35], a[164:167], v[40:43], a[32:35]\n"
"s_add_u32 m0, s60, 98304\n"
"v_exp_f32 v13, v13\n"
"v_mfma_f32_16x16x32_bf16 a[32:35], a[160:163], v[96:99], a[32:35]\n"
"buffer_load_dwordx4 v248, s[40:43], s62 offen offset:0 lds\n"
"v_mfma_f32_16x16x32_bf16 a[64:67], a[160:163], v[104:107], a[64:67]\n"
"v_exp_f32 v14, v14\n"
"v_mfma_f32_16x16x32_bf16 a[64:67], a[164:167], v[48:51], a[64:67]\n"
"ds_read_b128 a[132:135], v233 offset:16384\n"
"v_cvt_pk_bf16_f32 v46, v12, v13\n"
"v_mfma_f32_16x16x32_bf16 a[96:99], a[164:167], v[56:59], a[96:99]\n"
"v_exp_f32 v15, v15\n"
"v_mfma_f32_16x16x32_bf16 a[96:99], a[160:163], v[112:115], a[96:99]\n"
"buffer_load_dwordx4 v249, s[40:43], s62 offen offset:1024 lds\n"
"v_mfma_f32_16x16x32_bf16 a[100:103], a[168:171], v[112:115], a[100:103]\n"
"v_exp_f32 v16, v16\n"
"v_mfma_f32_16x16x32_bf16 a[100:103], a[172:175], v[56:59], a[100:103]\n"
"ds_read_b128 a[136:139], v234 offset:16384\n"
"v_cvt_pk_bf16_f32 v47, v14, v15\n"
"v_mfma_f32_16x16x32_bf16 a[68:71], a[172:175], v[48:51], a[68:71]\n"
"v_exp_f32 v17, v17\n"
"v_mfma_f32_16x16x32_bf16 a[68:71], a[168:171], v[104:107], a[68:71]\n"
"buffer_load_dwordx4 v248, s[40:43], s62 offen offset:2048 lds\n"
"v_mfma_f32_16x16x32_bf16 a[36:39], a[168:171], v[96:99], a[36:39]\n"
"v_exp_f32 v18, v18\n"
"v_mfma_f32_16x16x32_bf16 a[36:39], a[172:175], v[40:43], a[36:39]\n"
"ds_read_b128 a[140:143], v235 offset:16384\n"
"v_cvt_pk_bf16_f32 v52, v16, v17\n"
"v_mfma_f32_16x16x32_bf16 a[4:7], a[172:175], v[32:35], a[4:7]\n"
"s_waitcnt lgkmcnt(4)\n"
"v_exp_f32 v19, v19\n"
"v_mfma_f32_16x16x32_bf16 a[4:7], a[168:171], v[88:91], a[4:7]\n"
"buffer_load_dwordx4 v249, s[40:43], s62 offen offset:3072 lds\n"
"v_mfma_f32_16x16x32_bf16 a[8:11], a[176:179], v[88:91], a[8:11]\n"
"v_exp_f32 v20, v20\n"
"v_mfma_f32_16x16x32_bf16 a[8:11], a[180:183], v[32:35], a[8:11]\n"
"ds_read_b128 a[156:159], v235 offset:20480\n"
"v_cvt_pk_bf16_f32 v53, v18, v19\n"
"v_mfma_f32_16x16x32_bf16 a[40:43], a[180:183], v[40:43], a[40:43]\n"
"s_add_u32 m0, s60, 0\n"
"v_exp_f32 v21, v21\n"
"v_mfma_f32_16x16x32_bf16 a[40:43], a[176:179], v[96:99], a[40:43]\n"
"buffer_load_dwordx4 v244, s[36:39], s61 offen offset:0 lds\n"
"v_mfma_f32_16x16x32_bf16 a[72:75], a[176:179], v[104:107], a[72:75]\n"
"v_exp_f32 v22, v22\n"
"v_mfma_f32_16x16x32_bf16 a[72:75], a[180:183], v[48:51], a[72:75]\n"
"ds_read_b128 a[152:155], v234 offset:20480\n"
"v_cvt_pk_bf16_f32 v54, v20, v21\n"
"v_mfma_f32_16x16x32_bf16 a[104:107], a[180:183], v[56:59], a[104:107]\n"
"v_exp_f32 v23, v23\n"
"v_mfma_f32_16x16x32_bf16 a[104:107], a[176:179], v[112:115], a[104:107]\n"
"buffer_load_dwordx4 v245, s[36:39], s61 offen offset:1024 lds\n"
"v_mfma_f32_16x16x32_bf16 a[108:111], a[184:187], v[112:115], a[108:111]\n"
"v_exp_f32 v24, v24\n"
"v_mfma_f32_16x16x32_bf16 a[108:111], a[188:191], v[56:59], a[108:111]\n"
"ds_read_b128 a[148:151], v233 offset:20480\n"
"v_cvt_pk_bf16_f32 v55, v22, v23\n"
"v_mfma_f32_16x16x32_bf16 a[76:79], a[188:191], v[48:51], a[76:79]\n"
"v_exp_f32 v25, v25\n"
"v_mfma_f32_16x16x32_bf16 a[76:79], a[184:187], v[104:107], a[76:79]\n"
"buffer_load_dwordx4 v246, s[36:39], s61 offen offset:2048 lds\n"
"v_mfma_f32_16x16x32_bf16 a[44:47], a[184:187], v[96:99], a[44:47]\n"
"v_exp_f32 v26, v26\n"
"v_mfma_f32_16x16x32_bf16 a[44:47], a[188:191], v[40:43], a[44:47]\n"
"ds_read_b128 a[144:147], v232 offset:20480\n"
"v_cvt_pk_bf16_f32 v60, v24, v25\n"
"v_mfma_f32_16x16x32_bf16 a[12:15], a[188:191], v[32:35], a[12:15]\n"
"s_waitcnt lgkmcnt(4)\n"
"v_exp_f32 v27, v27\n"
"v_mfma_f32_16x16x32_bf16 a[12:15], a[184:187], v[88:91], a[12:15]\n"
"buffer_load_dwordx4 v247, s[36:39], s61 offen offset:3072 lds\n"
"v_mfma_f32_16x16x32_bf16 v[0:3], a[128:131], v[128:131], v[64:67]\n"
"s_add_u32 s61, s61, 16384\n"
"v_exp_f32 v28, v28\n"
"v_mfma_f32_16x16x32_bf16 v[0:3], a[132:135], v[132:135], v[0:3]\n"
"ds_read_b64_tr_b16 a[160:161], v240 offset:0\n"
"v_cvt_pk_bf16_f32 v61, v26, v27\n"
"v_mfma_f32_16x16x32_bf16 v[0:3], a[136:139], v[136:139], v[0:3]\n"
"s_waitcnt lgkmcnt(3)\n"
"v_exp_f32 v29, v29\n"
"v_mfma_f32_16x16x32_bf16 v[0:3], a[140:143], v[140:143], v[0:3]\n"
"ds_read_b64_tr_b16 a[162:163], v240 offset:4096\n"
"v_mfma_f32_16x16x32_bf16 v[4:7], a[156:159], v[140:143], v[64:67]\n"
"s_waitcnt lgkmcnt(2)\n"
"v_exp_f32 v30, v30\n"
"v_mfma_f32_16x16x32_bf16 v[4:7], a[152:155], v[136:139], v[4:7]\n"
"ds_read_b64_tr_b16 a[164:165], v240 offset:8192\n"
"v_cvt_pk_bf16_f32 v62, v28, v29\n"
"v_mfma_f32_16x16x32_bf16 v[4:7], a[148:151], v[132:135], v[4:7]\n"
"s_add_u32 m0, s60, 131072\n"
"v_exp_f32 v31, v31\n"
"v_mfma_f32_16x16x32_bf16 v[4:7], a[144:147], v[128:131], v[4:7]\n"
"ds_read_b64_tr_b16 a[166:167], v240 offset:12288\n"
"v_smfmac_f32_16x16x64_bf16 a[192:195], a[208:211], v[32:39], v250\n"
"buffer_load_dwordx4 v204, s[48:51], 0 offen lds\n"
"v_mfma_f32_16x16x32_bf16 v[12:15], a[144:147], v[144:147], v[68:71]\n"
"v_exp_f32 v0, v0\n"
"v_mfma_f32_16x16x32_bf16 v[12:15], a[148:151], v[148:151], v[12:15]\n"
"ds_read_b64_tr_b16 a[168:169], v241 offset:0\n"
"v_cvt_pk_bf16_f32 v63, v30, v31\n"
"v_mfma_f32_16x16x32_bf16 v[12:15], a[152:155], v[152:155], v[12:15]\n"
"v_exp_f32 v1, v1\n"
"v_mfma_f32_16x16x32_bf16 v[12:15], a[156:159], v[156:159], v[12:15]\n"
"ds_read_b64_tr_b16 a[170:171], v241 offset:4096\n"
"v_mfma_f32_16x16x32_bf16 v[8:11], a[140:143], v[156:159], v[68:71]\n"
"v_exp_f32 v2, v2\n"
"v_mfma_f32_16x16x32_bf16 v[8:11], a[136:139], v[152:155], v[8:11]\n"
"ds_read_b64_tr_b16 a[172:173], v241 offset:8192\n"
"v_cvt_pk_bf16_f32 v84, v0, v1\n"
"v_mfma_f32_16x16x32_bf16 v[8:11], a[132:135], v[148:151], v[8:11]\n"
"v_exp_f32 v3, v3\n"
"v_mfma_f32_16x16x32_bf16 v[8:11], a[128:131], v[144:147], v[8:11]\n"
"ds_read_b64_tr_b16 a[174:175], v241 offset:12288\n"
"v_smfmac_f32_16x16x64_bf16 a[196:199], a[208:211], v[40:47], v250\n"
"v_mfma_f32_16x16x32_bf16 v[16:19], a[128:131], v[160:163], v[72:75]\n"
"v_exp_f32 v4, v4\n"
"v_mfma_f32_16x16x32_bf16 v[16:19], a[132:135], v[164:167], v[16:19]\n"
"ds_read_b64_tr_b16 a[176:177], v242 offset:0\n"
"v_cvt_pk_bf16_f32 v85, v2, v3\n"
"v_mfma_f32_16x16x32_bf16 v[16:19], a[136:139], v[168:171], v[16:19]\n"
"v_exp_f32 v5, v5\n"
"v_mfma_f32_16x16x32_bf16 v[16:19], a[140:143], v[172:175], v[16:19]\n"
"ds_read_b64_tr_b16 a[178:179], v242 offset:4096\n"
"v_mfma_f32_16x16x32_bf16 v[20:23], a[156:159], v[172:175], v[72:75]\n"
"v_exp_f32 v6, v6\n"
"v_mfma_f32_16x16x32_bf16 v[20:23], a[152:155], v[168:171], v[20:23]\n"
"ds_read_b64_tr_b16 a[180:181], v242 offset:8192\n"
"v_cvt_pk_bf16_f32 v86, v4, v5\n"
"v_mfma_f32_16x16x32_bf16 v[20:23], a[148:151], v[164:167], v[20:23]\n"
"v_exp_f32 v7, v7\n"
"v_mfma_f32_16x16x32_bf16 v[20:23], a[144:147], v[160:163], v[20:23]\n"
"ds_read_b64_tr_b16 a[182:183], v242 offset:12288\n"
"v_smfmac_f32_16x16x64_bf16 a[200:203], a[208:211], v[48:55], v250\n"
"v_mfma_f32_16x16x32_bf16 v[28:31], a[144:147], v[176:179], v[76:79]\n"
"v_exp_f32 v8, v8\n"
"v_mfma_f32_16x16x32_bf16 v[28:31], a[148:151], v[180:183], v[28:31]\n"
"ds_read_b64_tr_b16 a[184:185], v243 offset:0\n"
"v_cvt_pk_bf16_f32 v87, v6, v7\n"
"v_mfma_f32_16x16x32_bf16 v[28:31], a[152:155], v[184:187], v[28:31]\n"
"v_exp_f32 v9, v9\n"
"v_mfma_f32_16x16x32_bf16 v[28:31], a[156:159], v[188:191], v[28:31]\n"
"ds_read_b64_tr_b16 a[186:187], v243 offset:4096\n"
"v_mfma_f32_16x16x32_bf16 v[24:27], a[140:143], v[188:191], v[76:79]\n"
"v_exp_f32 v10, v10\n"
"v_mfma_f32_16x16x32_bf16 v[24:27], a[136:139], v[184:187], v[24:27]\n"
"ds_read_b64_tr_b16 a[188:189], v243 offset:8192\n"
"v_cvt_pk_bf16_f32 v92, v8, v9\n"
"v_mfma_f32_16x16x32_bf16 v[24:27], a[132:135], v[180:183], v[24:27]\n"
"s_waitcnt lgkmcnt(7)\n"
"v_exp_f32 v11, v11\n"
"v_mfma_f32_16x16x32_bf16 v[24:27], a[128:131], v[176:179], v[24:27]\n"
"ds_read_b64_tr_b16 a[190:191], v243 offset:12288\n"
"v_smfmac_f32_16x16x64_bf16 a[204:207], a[208:211], v[56:63], v250\n"
"v_mfma_f32_16x16x32_bf16 a[16:19], a[160:163], v[32:35], a[16:19]\n"
"v_exp_f32 v12, v12\n"
"v_mfma_f32_16x16x32_bf16 a[16:19], a[164:167], v[36:39], a[16:19]\n"
"ds_read_b128 a[128:131], v232 offset:24576\n"
"v_cvt_pk_bf16_f32 v93, v10, v11\n"
"v_mfma_f32_16x16x32_bf16 a[48:51], a[164:167], v[44:47], a[48:51]\n"
"v_exp_f32 v13, v13\n"
"v_mfma_f32_16x16x32_bf16 a[48:51], a[160:163], v[40:43], a[48:51]\n"
"ds_read_b128 a[132:135], v233 offset:24576\n"
"v_mfma_f32_16x16x32_bf16 a[80:83], a[160:163], v[48:51], a[80:83]\n"
"v_exp_f32 v14, v14\n"
"v_mfma_f32_16x16x32_bf16 a[80:83], a[164:167], v[52:55], a[80:83]\n"
"ds_read_b128 a[136:139], v234 offset:24576\n"
"v_cvt_pk_bf16_f32 v94, v12, v13\n"
"v_mfma_f32_16x16x32_bf16 a[112:115], a[164:167], v[60:63], a[112:115]\n"
"v_exp_f32 v15, v15\n"
"v_mfma_f32_16x16x32_bf16 a[112:115], a[160:163], v[56:59], a[112:115]\n"
"ds_read_b128 a[140:143], v235 offset:24576\n"
"v_mfma_f32_16x16x32_bf16 a[116:119], a[168:171], v[56:59], a[116:119]\n"
"v_exp_f32 v16, v16\n"
"v_mfma_f32_16x16x32_bf16 a[116:119], a[172:175], v[60:63], a[116:119]\n"
"ds_read_b128 a[144:147], v232 offset:28672\n"
"v_cvt_pk_bf16_f32 v95, v14, v15\n"
"v_mfma_f32_16x16x32_bf16 a[84:87], a[172:175], v[52:55], a[84:87]\n"
"v_exp_f32 v17, v17\n"
"v_mfma_f32_16x16x32_bf16 a[84:87], a[168:171], v[48:51], a[84:87]\n"
"v_mfma_f32_16x16x32_bf16 a[52:55], a[168:171], v[40:43], a[52:55]\n"
"v_exp_f32 v18, v18\n"
"v_mfma_f32_16x16x32_bf16 a[52:55], a[172:175], v[44:47], a[52:55]\n"
"ds_read_b128 a[148:151], v233 offset:28672\n"
"v_cvt_pk_bf16_f32 v100, v16, v17\n"
"v_mfma_f32_16x16x32_bf16 a[20:23], a[172:175], v[36:39], a[20:23]\n"
"s_waitcnt lgkmcnt(6)\n"
"v_exp_f32 v19, v19\n"
"v_mfma_f32_16x16x32_bf16 a[20:23], a[168:171], v[32:35], a[20:23]\n"
"v_mfma_f32_16x16x32_bf16 a[24:27], a[176:179], v[32:35], a[24:27]\n"
"v_exp_f32 v20, v20\n"
"v_mfma_f32_16x16x32_bf16 a[24:27], a[180:183], v[36:39], a[24:27]\n"
"ds_read_b128 a[152:155], v234 offset:28672\n"
"v_cvt_pk_bf16_f32 v101, v18, v19\n"
"v_mfma_f32_16x16x32_bf16 a[56:59], a[180:183], v[44:47], a[56:59]\n"
"v_exp_f32 v21, v21\n"
"v_mfma_f32_16x16x32_bf16 a[56:59], a[176:179], v[40:43], a[56:59]\n"
"v_mfma_f32_16x16x32_bf16 a[88:91], a[176:179], v[48:51], a[88:91]\n"
"v_exp_f32 v22, v22\n"
"v_mfma_f32_16x16x32_bf16 a[88:91], a[180:183], v[52:55], a[88:91]\n"
"ds_read_b128 a[156:159], v235 offset:28672\n"
"v_cvt_pk_bf16_f32 v102, v20, v21\n"
"v_mfma_f32_16x16x32_bf16 a[120:123], a[180:183], v[60:63], a[120:123]\n"
"v_exp_f32 v23, v23\n"
"v_mfma_f32_16x16x32_bf16 a[120:123], a[176:179], v[56:59], a[120:123]\n"
"v_mfma_f32_16x16x32_bf16 a[124:127], a[184:187], v[56:59], a[124:127]\n"
"v_exp_f32 v24, v24\n"
"v_mfma_f32_16x16x32_bf16 a[124:127], a[188:191], v[60:63], a[124:127]\n"
"v_cvt_pk_bf16_f32 v103, v22, v23\n"
"v_mfma_f32_16x16x32_bf16 a[92:95], a[188:191], v[52:55], a[92:95]\n"
"v_exp_f32 v25, v25\n"
"v_mfma_f32_16x16x32_bf16 a[92:95], a[184:187], v[48:51], a[92:95]\n"
"v_mfma_f32_16x16x32_bf16 a[60:63], a[184:187], v[40:43], a[60:63]\n"
"s_add_u32 s62, s62, 16384\n"
"v_exp_f32 v26, v26\n"
"v_mfma_f32_16x16x32_bf16 a[60:63], a[188:191], v[44:47], a[60:63]\n"
"v_cvt_pk_bf16_f32 v108, v24, v25\n"
"v_mfma_f32_16x16x32_bf16 a[28:31], a[188:191], v[36:39], a[28:31]\n"
"s_waitcnt vmcnt(13) lgkmcnt(0)\n"
"v_exp_f32 v27, v27\n"
"v_mfma_f32_16x16x32_bf16 a[28:31], a[184:187], v[32:35], a[28:31]\n"
"s_barrier\n"
"v_mfma_f32_16x16x32_bf16 v[0:3], a[128:131], v[128:131], v[64:67]\n"
"v_exp_f32 v28, v28\n"
"v_mfma_f32_16x16x32_bf16 v[0:3], a[132:135], v[132:135], v[0:3]\n"
"ds_read_b64_tr_b16 a[160:161], v236 offset:8192\n"
"v_cvt_pk_bf16_f32 v109, v26, v27\n"
"v_mfma_f32_16x16x32_bf16 v[0:3], a[136:139], v[136:139], v[0:3]\n"
"v_exp_f32 v29, v29\n"
"v_mfma_f32_16x16x32_bf16 v[0:3], a[140:143], v[140:143], v[0:3]\n"
"ds_read_b64_tr_b16 a[162:163], v236 offset:12288\n"
"v_mfma_f32_16x16x32_bf16 v[4:7], a[156:159], v[140:143], v[64:67]\n"
"s_mov_b32 s65, s58\n"
"v_exp_f32 v30, v30\n"
"v_mfma_f32_16x16x32_bf16 v[4:7], a[152:155], v[136:139], v[4:7]\n"
"ds_read_b64_tr_b16 a[164:165], v236 offset:16384\n"
"v_cvt_pk_bf16_f32 v110, v28, v29\n"
"v_mfma_f32_16x16x32_bf16 v[4:7], a[148:151], v[132:135], v[4:7]\n"
"v_exp_f32 v31, v31\n"
"v_mfma_f32_16x16x32_bf16 v[4:7], a[144:147], v[128:131], v[4:7]\n"
"ds_read_b64_tr_b16 a[166:167], v236 offset:20480\n"
"v_mfma_f32_16x16x32_bf16 v[12:15], a[144:147], v[144:147], v[68:71]\n"
"v_exp_f32 v0, v0\n"
"v_mfma_f32_16x16x32_bf16 v[12:15], a[148:151], v[148:151], v[12:15]\n"
"ds_read_b64_tr_b16 a[168:169], v237 offset:8192\n"
"v_cvt_pk_bf16_f32 v111, v30, v31\n"
"v_mfma_f32_16x16x32_bf16 v[12:15], a[152:155], v[152:155], v[12:15]\n"
"v_exp_f32 v1, v1\n"
"v_mfma_f32_16x16x32_bf16 v[12:15], a[156:159], v[156:159], v[12:15]\n"
"ds_read_b64_tr_b16 a[170:171], v237 offset:12288\n"
"v_mfma_f32_16x16x32_bf16 v[8:11], a[140:143], v[156:159], v[68:71]\n"
"v_exp_f32 v2, v2\n"
"v_mfma_f32_16x16x32_bf16 v[8:11], a[136:139], v[152:155], v[8:11]\n"
"ds_read_b64_tr_b16 a[172:173], v237 offset:16384\n"
"v_cvt_pk_bf16_f32 v88, v0, v1\n"
"v_mfma_f32_16x16x32_bf16 v[8:11], a[132:135], v[148:151], v[8:11]\n"
"v_exp_f32 v3, v3\n"
"v_mfma_f32_16x16x32_bf16 v[8:11], a[128:131], v[144:147], v[8:11]\n"
"ds_read_b64_tr_b16 a[174:175], v237 offset:20480\n"
"v_mfma_f32_16x16x32_bf16 v[16:19], a[128:131], v[160:163], v[72:75]\n"
"v_exp_f32 v4, v4\n"
"v_mfma_f32_16x16x32_bf16 v[16:19], a[132:135], v[164:167], v[16:19]\n"
"ds_read_b64_tr_b16 a[176:177], v238 offset:8192\n"
"v_cvt_pk_bf16_f32 v89, v2, v3\n"
"v_mfma_f32_16x16x32_bf16 v[16:19], a[136:139], v[168:171], v[16:19]\n"
"v_exp_f32 v5, v5\n"
"v_mfma_f32_16x16x32_bf16 v[16:19], a[140:143], v[172:175], v[16:19]\n"
"ds_read_b64_tr_b16 a[178:179], v238 offset:12288\n"
"v_mfma_f32_16x16x32_bf16 v[20:23], a[156:159], v[172:175], v[72:75]\n"
"v_exp_f32 v6, v6\n"
"v_mfma_f32_16x16x32_bf16 v[20:23], a[152:155], v[168:171], v[20:23]\n"
"ds_read_b64_tr_b16 a[180:181], v238 offset:16384\n"
"v_cvt_pk_bf16_f32 v90, v4, v5\n"
"v_mfma_f32_16x16x32_bf16 v[20:23], a[148:151], v[164:167], v[20:23]\n"
"v_exp_f32 v7, v7\n"
"v_mfma_f32_16x16x32_bf16 v[20:23], a[144:147], v[160:163], v[20:23]\n"
"ds_read_b64_tr_b16 a[182:183], v238 offset:20480\n"
"v_mfma_f32_16x16x32_bf16 v[28:31], a[144:147], v[176:179], v[76:79]\n"
"v_exp_f32 v8, v8\n"
"v_mfma_f32_16x16x32_bf16 v[28:31], a[148:151], v[180:183], v[28:31]\n"
"ds_read_b64_tr_b16 a[184:185], v239 offset:8192\n"
"v_cvt_pk_bf16_f32 v91, v6, v7\n"
"v_mfma_f32_16x16x32_bf16 v[28:31], a[152:155], v[184:187], v[28:31]\n"
"v_exp_f32 v9, v9\n"
"v_mfma_f32_16x16x32_bf16 v[28:31], a[156:159], v[188:191], v[28:31]\n"
"ds_read_b64_tr_b16 a[186:187], v239 offset:12288\n"
"v_mfma_f32_16x16x32_bf16 v[24:27], a[140:143], v[188:191], v[76:79]\n"
"v_exp_f32 v10, v10\n"
"v_mfma_f32_16x16x32_bf16 v[24:27], a[136:139], v[184:187], v[24:27]\n"
"ds_read_b64_tr_b16 a[188:189], v239 offset:16384\n"
"v_cvt_pk_bf16_f32 v96, v8, v9\n"
"v_mfma_f32_16x16x32_bf16 v[24:27], a[132:135], v[180:183], v[24:27]\n"
"s_waitcnt lgkmcnt(7)\n"
"v_exp_f32 v11, v11\n"
"v_mfma_f32_16x16x32_bf16 v[24:27], a[128:131], v[176:179], v[24:27]\n"
"ds_read_b64_tr_b16 a[190:191], v239 offset:20480\n"
"v_mfma_f32_16x16x32_bf16 a[0:3], a[160:163], v[36:39], a[0:3]\n"
"v_exp_f32 v12, v12\n"
"v_mfma_f32_16x16x32_bf16 a[0:3], a[164:167], v[84:87], a[0:3]\n"
"ds_read_b128 a[128:131], v232 offset:32768\n"
"v_cvt_pk_bf16_f32 v97, v10, v11\n"
"v_mfma_f32_16x16x32_bf16 a[32:35], a[164:167], v[92:95], a[32:35]\n"
"s_add_u32 m0, s60, 114688\n"
"v_exp_f32 v13, v13\n"
"v_mfma_f32_16x16x32_bf16 a[32:35], a[160:163], v[44:47], a[32:35]\n"
"buffer_load_dwordx4 v248, s[40:43], s62 offen offset:0 lds\n"
"v_mfma_f32_16x16x32_bf16 a[64:67], a[160:163], v[52:55], a[64:67]\n"
"v_exp_f32 v14, v14\n"
"v_mfma_f32_16x16x32_bf16 a[64:67], a[164:167], v[100:103], a[64:67]\n"
"ds_read_b128 a[132:135], v233 offset:32768\n"
"v_cvt_pk_bf16_f32 v98, v12, v13\n"
"v_mfma_f32_16x16x32_bf16 a[96:99], a[164:167], v[108:111], a[96:99]\n"
"v_exp_f32 v15, v15\n"
"v_mfma_f32_16x16x32_bf16 a[96:99], a[160:163], v[60:63], a[96:99]\n"
"buffer_load_dwordx4 v249, s[40:43], s62 offen offset:1024 lds\n"
"v_mfma_f32_16x16x32_bf16 a[100:103], a[168:171], v[60:63], a[100:103]\n"
"v_exp_f32 v16, v16\n"
"v_mfma_f32_16x16x32_bf16 a[100:103], a[172:175], v[108:111], a[100:103]\n"
"ds_read_b128 a[136:139], v234 offset:32768\n"
"v_cvt_pk_bf16_f32 v99, v14, v15\n"
"v_mfma_f32_16x16x32_bf16 a[68:71], a[172:175], v[100:103], a[68:71]\n"
"v_exp_f32 v17, v17\n"
"v_mfma_f32_16x16x32_bf16 a[68:71], a[168:171], v[52:55], a[68:71]\n"
"buffer_load_dwordx4 v248, s[40:43], s62 offen offset:2048 lds\n"
"v_mfma_f32_16x16x32_bf16 a[36:39], a[168:171], v[44:47], a[36:39]\n"
"v_exp_f32 v18, v18\n"
"v_mfma_f32_16x16x32_bf16 a[36:39], a[172:175], v[92:95], a[36:39]\n"
"ds_read_b128 a[140:143], v235 offset:32768\n"
"v_cvt_pk_bf16_f32 v104, v16, v17\n"
"v_mfma_f32_16x16x32_bf16 a[4:7], a[172:175], v[84:87], a[4:7]\n"
"s_waitcnt lgkmcnt(4)\n"
"v_exp_f32 v19, v19\n"
"v_mfma_f32_16x16x32_bf16 a[4:7], a[168:171], v[36:39], a[4:7]\n"
"buffer_load_dwordx4 v249, s[40:43], s62 offen offset:3072 lds\n"
"v_mfma_f32_16x16x32_bf16 a[8:11], a[176:179], v[36:39], a[8:11]\n"
"v_exp_f32 v20, v20\n"
"v_mfma_f32_16x16x32_bf16 a[8:11], a[180:183], v[84:87], a[8:11]\n"
"ds_read_b128 a[156:159], v235 offset:36864\n"
"v_cvt_pk_bf16_f32 v105, v18, v19\n"
"v_mfma_f32_16x16x32_bf16 a[40:43], a[180:183], v[92:95], a[40:43]\n"
"s_add_u32 m0, s60, 16384\n"
"v_exp_f32 v21, v21\n"
"v_mfma_f32_16x16x32_bf16 a[40:43], a[176:179], v[44:47], a[40:43]\n"
"buffer_load_dwordx4 v244, s[36:39], s61 offen offset:0 lds\n"
"v_mfma_f32_16x16x32_bf16 a[72:75], a[176:179], v[52:55], a[72:75]\n"
"v_exp_f32 v22, v22\n"
"v_mfma_f32_16x16x32_bf16 a[72:75], a[180:183], v[100:103], a[72:75]\n"
"ds_read_b128 a[152:155], v234 offset:36864\n"
"v_cvt_pk_bf16_f32 v106, v20, v21\n"
"v_mfma_f32_16x16x32_bf16 a[104:107], a[180:183], v[108:111], a[104:107]\n"
"v_exp_f32 v23, v23\n"
"v_mfma_f32_16x16x32_bf16 a[104:107], a[176:179], v[60:63], a[104:107]\n"
"buffer_load_dwordx4 v245, s[36:39], s61 offen offset:1024 lds\n"
"v_mfma_f32_16x16x32_bf16 a[108:111], a[184:187], v[60:63], a[108:111]\n"
"v_exp_f32 v24, v24\n"
"v_mfma_f32_16x16x32_bf16 a[108:111], a[188:191], v[108:111], a[108:111]\n"
"L_w7_%=:\n"
"s_cbranch_execz L_wd2_%=\n"
"ds_read_b128 a[148:151], v233 offset:36864\n"
"v_cvt_pk_bf16_f32 v107, v22, v23\n"
"v_mfma_f32_16x16x32_bf16 a[76:79], a[188:191], v[100:103], a[76:79]\n"
"v_exp_f32 v25, v25\n"
"v_mfma_f32_16x16x32_bf16 a[76:79], a[184:187], v[52:55], a[76:79]\n"
"buffer_load_dwordx4 v246, s[36:39], s61 offen offset:2048 lds\n"
"v_mfma_f32_16x16x32_bf16 a[44:47], a[184:187], v[44:47], a[44:47]\n"
"v_exp_f32 v26, v26\n"
"v_mfma_f32_16x16x32_bf16 a[44:47], a[188:191], v[92:95], a[44:47]\n"
"ds_read_b128 a[144:147], v232 offset:36864\n"
"v_cvt_pk_bf16_f32 v112, v24, v25\n"
"v_mfma_f32_16x16x32_bf16 a[12:15], a[188:191], v[84:87], a[12:15]\n"
"s_waitcnt lgkmcnt(4)\n"
"v_exp_f32 v27, v27\n"
"v_mfma_f32_16x16x32_bf16 a[12:15], a[184:187], v[36:39], a[12:15]\n"
"buffer_load_dwordx4 v247, s[36:39], s61 offen offset:3072 lds\n"
"v_mfma_f32_16x16x32_bf16 v[0:3], a[128:131], v[128:131], v[64:67]\n"
"s_add_u32 s61, s61, 16384\n"
"v_exp_f32 v28, v28\n"
"v_mfma_f32_16x16x32_bf16 v[0:3], a[132:135], v[132:135], v[0:3]\n"
"ds_read_b64_tr_b16 a[160:161], v240 offset:16384\n"
"v_cvt_pk_bf16_f32 v113, v26, v27\n"
"v_mfma_f32_16x16x32_bf16 v[0:3], a[136:139], v[136:139], v[0:3]\n"
"s_waitcnt lgkmcnt(3)\n"
"v_exp_f32 v29, v29\n"
"v_mfma_f32_16x16x32_bf16 v[0:3], a[140:143], v[140:143], v[0:3]\n"
"ds_read_b64_tr_b16 a[162:163], v240 offset:20480\n"
"v_mfma_f32_16x16x32_bf16 v[4:7], a[156:159], v[140:143], v[64:67]\n"
"s_waitcnt lgkmcnt(2)\n"
"v_exp_f32 v30, v30\n"
"v_mfma_f32_16x16x32_bf16 v[4:7], a[152:155], v[136:139], v[4:7]\n"
"ds_read_b64_tr_b16 a[164:165], v240 offset:24576\n"
"v_cvt_pk_bf16_f32 v114, v28, v29\n"
"v_mfma_f32_16x16x32_bf16 v[4:7], a[148:151], v[132:135], v[4:7]\n"
"v_exp_f32 v31, v31\n"
"v_mfma_f32_16x16x32_bf16 v[4:7], a[144:147], v[128:131], v[4:7]\n"
"ds_read_b64_tr_b16 a[166:167], v240 offset:28672\n"
"v_smfmac_f32_16x16x64_bf16 a[192:195], a[208:211], v[84:91], v250\n"
"v_mfma_f32_16x16x32_bf16 v[12:15], a[144:147], v[144:147], v[68:71]\n"
"v_exp_f32 v0, v0\n"
"v_mfma_f32_16x16x32_bf16 v[12:15], a[148:151], v[148:151], v[12:15]\n"
"ds_read_b64_tr_b16 a[168:169], v241 offset:16384\n"
"v_cvt_pk_bf16_f32 v115, v30, v31\n"
"v_mfma_f32_16x16x32_bf16 v[12:15], a[152:155], v[152:155], v[12:15]\n"
"v_exp_f32 v1, v1\n"
"v_mfma_f32_16x16x32_bf16 v[12:15], a[156:159], v[156:159], v[12:15]\n"
"ds_read_b64_tr_b16 a[170:171], v241 offset:20480\n"
"v_mfma_f32_16x16x32_bf16 v[8:11], a[140:143], v[156:159], v[68:71]\n"
"v_exp_f32 v2, v2\n"
"v_mfma_f32_16x16x32_bf16 v[8:11], a[136:139], v[152:155], v[8:11]\n"
"ds_read_b64_tr_b16 a[172:173], v241 offset:24576\n"
"v_cvt_pk_bf16_f32 v32, v0, v1\n"
"v_mfma_f32_16x16x32_bf16 v[8:11], a[132:135], v[148:151], v[8:11]\n"
"v_exp_f32 v3, v3\n"
"v_mfma_f32_16x16x32_bf16 v[8:11], a[128:131], v[144:147], v[8:11]\n"
"ds_read_b64_tr_b16 a[174:175], v241 offset:28672\n"
"v_smfmac_f32_16x16x64_bf16 a[196:199], a[208:211], v[92:99], v250\n"
"v_mfma_f32_16x16x32_bf16 v[16:19], a[128:131], v[160:163], v[72:75]\n"
"v_exp_f32 v4, v4\n"
"v_mfma_f32_16x16x32_bf16 v[16:19], a[132:135], v[164:167], v[16:19]\n"
"ds_read_b64_tr_b16 a[176:177], v242 offset:16384\n"
"v_cvt_pk_bf16_f32 v33, v2, v3\n"
"v_mfma_f32_16x16x32_bf16 v[16:19], a[136:139], v[168:171], v[16:19]\n"
"v_exp_f32 v5, v5\n"
"v_mfma_f32_16x16x32_bf16 v[16:19], a[140:143], v[172:175], v[16:19]\n"
"ds_read_b64_tr_b16 a[178:179], v242 offset:20480\n"
"v_mfma_f32_16x16x32_bf16 v[20:23], a[156:159], v[172:175], v[72:75]\n"
"v_exp_f32 v6, v6\n"
"v_mfma_f32_16x16x32_bf16 v[20:23], a[152:155], v[168:171], v[20:23]\n"
"ds_read_b64_tr_b16 a[180:181], v242 offset:24576\n"
"v_cvt_pk_bf16_f32 v34, v4, v5\n"
"v_mfma_f32_16x16x32_bf16 v[20:23], a[148:151], v[164:167], v[20:23]\n"
"v_exp_f32 v7, v7\n"
"v_mfma_f32_16x16x32_bf16 v[20:23], a[144:147], v[160:163], v[20:23]\n"
"ds_read_b64_tr_b16 a[182:183], v242 offset:28672\n"
"v_smfmac_f32_16x16x64_bf16 a[200:203], a[208:211], v[100:107], v250\n"
"v_mfma_f32_16x16x32_bf16 v[28:31], a[144:147], v[176:179], v[76:79]\n"
"v_exp_f32 v8, v8\n"
"v_mfma_f32_16x16x32_bf16 v[28:31], a[148:151], v[180:183], v[28:31]\n"
"ds_read_b64_tr_b16 a[184:185], v243 offset:16384\n"
"v_cvt_pk_bf16_f32 v35, v6, v7\n"
"v_mfma_f32_16x16x32_bf16 v[28:31], a[152:155], v[184:187], v[28:31]\n"
"v_exp_f32 v9, v9\n"
"v_mfma_f32_16x16x32_bf16 v[28:31], a[156:159], v[188:191], v[28:31]\n"
"ds_read_b64_tr_b16 a[186:187], v243 offset:20480\n"
"v_mfma_f32_16x16x32_bf16 v[24:27], a[140:143], v[188:191], v[76:79]\n"
"v_exp_f32 v10, v10\n"
"v_mfma_f32_16x16x32_bf16 v[24:27], a[136:139], v[184:187], v[24:27]\n"
"ds_read_b64_tr_b16 a[188:189], v243 offset:24576\n"
"v_cvt_pk_bf16_f32 v40, v8, v9\n"
"v_mfma_f32_16x16x32_bf16 v[24:27], a[132:135], v[180:183], v[24:27]\n"
"s_waitcnt lgkmcnt(7)\n"
"v_exp_f32 v11, v11\n"
"v_mfma_f32_16x16x32_bf16 v[24:27], a[128:131], v[176:179], v[24:27]\n"
"ds_read_b64_tr_b16 a[190:191], v243 offset:28672\n"
"v_smfmac_f32_16x16x64_bf16 a[204:207], a[208:211], v[108:115], v250\n"
"v_mfma_f32_16x16x32_bf16 a[16:19], a[160:163], v[84:87], a[16:19]\n"
"v_exp_f32 v12, v12\n"
"v_mfma_f32_16x16x32_bf16 a[16:19], a[164:167], v[88:91], a[16:19]\n"
"ds_read_b128 a[128:131], v232 offset:40960\n"
"v_cvt_pk_bf16_f32 v41, v10, v11\n"
"v_mfma_f32_16x16x32_bf16 a[48:51], a[164:167], v[96:99], a[48:51]\n"
"s_load_dwordx8 s[52:59], %0, s59\n"
"v_exp_f32 v13, v13\n"
"v_mfma_f32_16x16x32_bf16 a[48:51], a[160:163], v[92:95], a[48:51]\n"
"ds_read_b128 a[132:135], v233 offset:40960\n"
"v_mfma_f32_16x16x32_bf16 a[80:83], a[160:163], v[100:103], a[80:83]\n"
"v_exp_f32 v14, v14\n"
"v_mfma_f32_16x16x32_bf16 a[80:83], a[164:167], v[104:107], a[80:83]\n"
"ds_read_b128 a[136:139], v234 offset:40960\n"
"v_cvt_pk_bf16_f32 v42, v12, v13\n"
"v_mfma_f32_16x16x32_bf16 a[112:115], a[164:167], v[112:115], a[112:115]\n"
"v_exp_f32 v15, v15\n"
"v_mfma_f32_16x16x32_bf16 a[112:115], a[160:163], v[108:111], a[112:115]\n"
"ds_read_b128 a[140:143], v235 offset:40960\n"
"v_mfma_f32_16x16x32_bf16 a[116:119], a[168:171], v[108:111], a[116:119]\n"
"v_exp_f32 v16, v16\n"
"v_mfma_f32_16x16x32_bf16 a[116:119], a[172:175], v[112:115], a[116:119]\n"
"ds_read_b128 a[144:147], v232 offset:45056\n"
"v_cvt_pk_bf16_f32 v43, v14, v15\n"
"v_mfma_f32_16x16x32_bf16 a[84:87], a[172:175], v[104:107], a[84:87]\n"
"s_waitcnt vmcnt(8)\n"
"v_exp_f32 v17, v17\n"
"v_mfma_f32_16x16x32_bf16 a[84:87], a[168:171], v[100:103], a[84:87]\n"
"ds_read_b64_tr_b16 v[200:201], v194 offset:0\n"
"ds_read_b64_tr_b16 v[202:203], v194 offset:512\n"
"v_mfma_f32_16x16x32_bf16 a[52:55], a[168:171], v[92:95], a[52:55]\n"
"v_exp_f32 v18, v18\n"
"v_mfma_f32_16x16x32_bf16 a[52:55], a[172:175], v[96:99], a[52:55]\n"
"ds_read_b128 a[148:151], v233 offset:45056\n"
"v_cvt_pk_bf16_f32 v48, v16, v17\n"
"v_mfma_f32_16x16x32_bf16 a[20:23], a[172:175], v[88:91], a[20:23]\n"
"s_waitcnt lgkmcnt(8)\n"
"v_exp_f32 v19, v19\n"
"v_mfma_f32_16x16x32_bf16 a[20:23], a[168:171], v[84:87], a[20:23]\n"
"v_mfma_f32_16x16x32_bf16 a[24:27], a[176:179], v[84:87], a[24:27]\n"
"v_exp_f32 v20, v20\n"
"v_mfma_f32_16x16x32_bf16 a[24:27], a[180:183], v[88:91], a[24:27]\n"
"ds_read_b128 a[152:155], v234 offset:45056\n"
"v_cvt_pk_bf16_f32 v49, v18, v19\n"
"v_mfma_f32_16x16x32_bf16 a[56:59], a[180:183], v[96:99], a[56:59]\n"
"v_exp_f32 v21, v21\n"
"v_mfma_f32_16x16x32_bf16 a[56:59], a[176:179], v[92:95], a[56:59]\n"
"v_mfma_f32_16x16x32_bf16 a[88:91], a[176:179], v[100:103], a[88:91]\n"
"v_exp_f32 v22, v22\n"
"v_mfma_f32_16x16x32_bf16 a[88:91], a[180:183], v[104:107], a[88:91]\n"
"ds_read_b128 a[156:159], v235 offset:45056\n"
"v_cvt_pk_bf16_f32 v50, v20, v21\n"
"v_mfma_f32_16x16x32_bf16 a[120:123], a[180:183], v[112:115], a[120:123]\n"
"s_waitcnt lgkmcnt(3)\n"
"v_exp_f32 v23, v23\n"
"v_mfma_f32_16x16x32_bf16 a[120:123], a[176:179], v[108:111], a[120:123]\n"
"buffer_store_dwordx4 v[200:203], v228, s[44:47], s65 offen sc1\n"
"v_mfma_f32_16x16x32_bf16 a[124:127], a[184:187], v[108:111], a[124:127]\n"
"v_exp_f32 v24, v24\n"
"v_mfma_f32_16x16x32_bf16 a[124:127], a[188:191], v[112:115], a[124:127]\n"
"v_cvt_pk_bf16_f32 v51, v22, v23\n"
"v_mfma_f32_16x16x32_bf16 a[92:95], a[188:191], v[104:107], a[92:95]\n"
"v_exp_f32 v25, v25\n"
"v_mfma_f32_16x16x32_bf16 a[92:95], a[184:187], v[100:103], a[92:95]\n"
"v_mfma_f32_16x16x32_bf16 a[60:63], a[184:187], v[92:95], a[60:63]\n"
"s_add_u32 s62, s62, 16384\n"
"v_exp_f32 v26, v26\n"
"v_mfma_f32_16x16x32_bf16 a[60:63], a[188:191], v[96:99], a[60:63]\n"
"v_cvt_pk_bf16_f32 v56, v24, v25\n"
"v_mfma_f32_16x16x32_bf16 a[28:31], a[188:191], v[88:91], a[28:31]\n"
"s_waitcnt vmcnt(13) lgkmcnt(0)\n"
"v_exp_f32 v27, v27\n"
"v_mfma_f32_16x16x32_bf16 a[28:31], a[184:187], v[84:87], a[28:31]\n"
"s_barrier\n"
"v_mfma_f32_16x16x32_bf16 v[0:3], a[128:131], v[128:131], v[64:67]\n"
"v_exp_f32 v28, v28\n"
"v_mfma_f32_16x16x32_bf16 v[0:3], a[132:135], v[132:135], v[0:3]\n"
"ds_read_b64_tr_b16 a[160:161], v236 offset:24576\n"
"v_cvt_pk_bf16_f32 v57, v26, v27\n"
"v_mfma_f32_16x16x32_bf16 v[0:3], a[136:139], v[136:139], v[0:3]\n"
"v_exp_f32 v29, v29\n"
"v_mfma_f32_16x16x32_bf16 v[0:3], a[140:143], v[140:143], v[0:3]\n"
"ds_read_b64_tr_b16 a[162:163], v236 offset:28672\n"
"v_mad_u32_u24 v204, v230, s54, v229\n"
"v_mfma_f32_16x16x32_bf16 v[4:7], a[156:159], v[140:143], v[64:67]\n"
"v_exp_f32 v30, v30\n"
"v_mfma_f32_16x16x32_bf16 v[4:7], a[152:155], v[136:139], v[4:7]\n"
"ds_read_b64_tr_b16 a[164:165], v236 offset:32768\n"
"v_cvt_pk_bf16_f32 v58, v28, v29\n"
"v_mfma_f32_16x16x32_bf16 v[4:7], a[148:151], v[132:135], v[4:7]\n"
"v_exp_f32 v31, v31\n"
"v_mfma_f32_16x16x32_bf16 v[4:7], a[144:147], v[128:131], v[4:7]\n"
"ds_read_b64_tr_b16 a[166:167], v236 offset:36864\n"
"v_mad_u32_u24 v204, v231, s55, v204\n"
"v_mfma_f32_16x16x32_bf16 v[12:15], a[144:147], v[144:147], v[68:71]\n"
"v_exp_f32 v0, v0\n"
"v_mfma_f32_16x16x32_bf16 v[12:15], a[148:151], v[148:151], v[12:15]\n"
"ds_read_b64_tr_b16 a[168:169], v237 offset:24576\n"
"v_cvt_pk_bf16_f32 v59, v30, v31\n"
"v_mfma_f32_16x16x32_bf16 v[12:15], a[152:155], v[152:155], v[12:15]\n"
"v_exp_f32 v1, v1\n"
"v_mfma_f32_16x16x32_bf16 v[12:15], a[156:159], v[156:159], v[12:15]\n"
"ds_read_b64_tr_b16 a[170:171], v237 offset:28672\n"
"v_mad_u32_u24 v204, v192, s56, v204\n"
"v_mfma_f32_16x16x32_bf16 v[8:11], a[140:143], v[156:159], v[68:71]\n"
"s_mov_b64 s[48:49], s[52:53]\n"
"v_exp_f32 v2, v2\n"
"v_mfma_f32_16x16x32_bf16 v[8:11], a[136:139], v[152:155], v[8:11]\n"
"ds_read_b64_tr_b16 a[172:173], v237 offset:32768\n"
"v_cvt_pk_bf16_f32 v36, v0, v1\n"
"v_mfma_f32_16x16x32_bf16 v[8:11], a[132:135], v[148:151], v[8:11]\n"
"v_exp_f32 v3, v3\n"
"v_mfma_f32_16x16x32_bf16 v[8:11], a[128:131], v[144:147], v[8:11]\n"
"ds_read_b64_tr_b16 a[174:175], v237 offset:36864\n"
"v_mad_u32_u24 v204, v193, s57, v204\n"
"v_mfma_f32_16x16x32_bf16 v[16:19], a[128:131], v[160:163], v[72:75]\n"
"v_exp_f32 v4, v4\n"
"v_mfma_f32_16x16x32_bf16 v[16:19], a[132:135], v[164:167], v[16:19]\n"
"ds_read_b64_tr_b16 a[176:177], v238 offset:24576\n"
"v_cvt_pk_bf16_f32 v37, v2, v3\n"
"v_mfma_f32_16x16x32_bf16 v[16:19], a[136:139], v[168:171], v[16:19]\n"
"v_exp_f32 v5, v5\n"
"v_mfma_f32_16x16x32_bf16 v[16:19], a[140:143], v[172:175], v[16:19]\n"
"ds_read_b64_tr_b16 a[178:179], v238 offset:28672\n"
"v_mfma_f32_16x16x32_bf16 v[20:23], a[156:159], v[172:175], v[72:75]\n"
"v_exp_f32 v6, v6\n"
"v_mfma_f32_16x16x32_bf16 v[20:23], a[152:155], v[168:171], v[20:23]\n"
"ds_read_b64_tr_b16 a[180:181], v238 offset:32768\n"
"v_cvt_pk_bf16_f32 v38, v4, v5\n"
"v_mfma_f32_16x16x32_bf16 v[20:23], a[148:151], v[164:167], v[20:23]\n"
"v_exp_f32 v7, v7\n"
"v_mfma_f32_16x16x32_bf16 v[20:23], a[144:147], v[160:163], v[20:23]\n"
"ds_read_b64_tr_b16 a[182:183], v238 offset:36864\n"
"v_mfma_f32_16x16x32_bf16 v[28:31], a[144:147], v[176:179], v[76:79]\n"
"v_exp_f32 v8, v8\n"
"v_mfma_f32_16x16x32_bf16 v[28:31], a[148:151], v[180:183], v[28:31]\n"
"ds_read_b64_tr_b16 a[184:185], v239 offset:24576\n"
"v_cvt_pk_bf16_f32 v39, v6, v7\n"
"v_mfma_f32_16x16x32_bf16 v[28:31], a[152:155], v[184:187], v[28:31]\n"
"v_exp_f32 v9, v9\n"
"v_mfma_f32_16x16x32_bf16 v[28:31], a[156:159], v[188:191], v[28:31]\n"
"ds_read_b64_tr_b16 a[186:187], v239 offset:28672\n"
"v_mfma_f32_16x16x32_bf16 v[24:27], a[140:143], v[188:191], v[76:79]\n"
"s_cmp_eq_u32 s63, s35\n"
"v_exp_f32 v10, v10\n"
"v_mfma_f32_16x16x32_bf16 v[24:27], a[136:139], v[184:187], v[24:27]\n"
"ds_read_b64_tr_b16 a[188:189], v239 offset:32768\n"
"v_cvt_pk_bf16_f32 v44, v8, v9\n"
"v_mfma_f32_16x16x32_bf16 v[24:27], a[132:135], v[180:183], v[24:27]\n"
"s_waitcnt lgkmcnt(7)\n"
"v_exp_f32 v11, v11\n"
"v_mfma_f32_16x16x32_bf16 v[24:27], a[128:131], v[176:179], v[24:27]\n"
"ds_read_b64_tr_b16 a[190:191], v239 offset:36864\n"
"v_mfma_f32_16x16x32_bf16 a[0:3], a[160:163], v[88:91], a[0:3]\n"
"s_cbranch_scc1 L_sig_%=\n"
"L_sigret_%=:\n"
"v_exp_f32 v12, v12\n"
"v_mfma_f32_16x16x32_bf16 a[0:3], a[164:167], v[32:35], a[0:3]\n"
"ds_read_b128 a[128:131], v232 offset:49152\n"
"v_cvt_pk_bf16_f32 v45, v10, v11\n"
"v_mfma_f32_16x16x32_bf16 a[32:35], a[164:167], v[40:43], a[32:35]\n"
"s_add_u32 m0, s60, 65536\n"
"v_exp_f32 v13, v13\n"
"v_mfma_f32_16x16x32_bf16 a[32:35], a[160:163], v[96:99], a[32:35]\n"
"buffer_load_dwordx4 v248, s[40:43], s62 offen offset:0 lds\n"
"v_mfma_f32_16x16x32_bf16 a[64:67], a[160:163], v[104:107], a[64:67]\n"
"v_exp_f32 v14, v14\n"
"v_mfma_f32_16x16x32_bf16 a[64:67], a[164:167], v[48:51], a[64:67]\n"
"ds_read_b128 a[132:135], v233 offset:49152\n"
"v_cvt_pk_bf16_f32 v46, v12, v13\n"
"v_mfma_f32_16x16x32_bf16 a[96:99], a[164:167], v[56:59], a[96:99]\n"
"v_exp_f32 v15, v15\n"
"v_mfma_f32_16x16x32_bf16 a[96:99], a[160:163], v[112:115], a[96:99]\n"
"buffer_load_dwordx4 v249, s[40:43], s62 offen offset:1024 lds\n"
"v_mfma_f32_16x16x32_bf16 a[100:103], a[168:171], v[112:115], a[100:103]\n"
"v_exp_f32 v16, v16\n"
"v_mfma_f32_16x16x32_bf16 a[100:103], a[172:175], v[56:59], a[100:103]\n"
"ds_read_b128 a[136:139], v234 offset:49152\n"
"v_cvt_pk_bf16_f32 v47, v14, v15\n"
"v_mfma_f32_16x16x32_bf16 a[68:71], a[172:175], v[48:51], a[68:71]\n"
"v_exp_f32 v17, v17\n"
"v_mfma_f32_16x16x32_bf16 a[68:71], a[168:171], v[104:107], a[68:71]\n"
"buffer_load_dwordx4 v248, s[40:43], s62 offen offset:2048 lds\n"
"v_mfma_f32_16x16x32_bf16 a[36:39], a[168:171], v[96:99], a[36:39]\n"
"v_exp_f32 v18, v18\n"
"v_mfma_f32_16x16x32_bf16 a[36:39], a[172:175], v[40:43], a[36:39]\n"
"ds_read_b128 a[140:143], v235 offset:49152\n"
"v_cvt_pk_bf16_f32 v52, v16, v17\n"
"v_mfma_f32_16x16x32_bf16 a[4:7], a[172:175], v[32:35], a[4:7]\n"
"s_waitcnt lgkmcnt(4)\n"
"v_exp_f32 v19, v19\n"
"v_mfma_f32_16x16x32_bf16 a[4:7], a[168:171], v[88:91], a[4:7]\n"
"buffer_load_dwordx4 v249, s[40:43], s62 offen offset:3072 lds\n"
"v_mfma_f32_16x16x32_bf16 a[8:11], a[176:179], v[88:91], a[8:11]\n"
"v_exp_f32 v20, v20\n"
"v_mfma_f32_16x16x32_bf16 a[8:11], a[180:183], v[32:35], a[8:11]\n"
"ds_read_b128 a[156:159], v235 offset:53248\n"
"v_cvt_pk_bf16_f32 v53, v18, v19\n"
"v_mfma_f32_16x16x32_bf16 a[40:43], a[180:183], v[40:43], a[40:43]\n"
"s_add_u32 m0, s60, 32768\n"
"v_exp_f32 v21, v21\n"
"v_mfma_f32_16x16x32_bf16 a[40:43], a[176:179], v[96:99], a[40:43]\n"
"buffer_load_dwordx4 v244, s[36:39], s61 offen offset:0 lds\n"
"v_mfma_f32_16x16x32_bf16 a[72:75], a[176:179], v[104:107], a[72:75]\n"
"v_exp_f32 v22, v22\n"
"v_mfma_f32_16x16x32_bf16 a[72:75], a[180:183], v[48:51], a[72:75]\n"
"ds_read_b128 a[152:155], v234 offset:53248\n"
"v_cvt_pk_bf16_f32 v54, v20, v21\n"
"v_mfma_f32_16x16x32_bf16 a[104:107], a[180:183], v[56:59], a[104:107]\n"
"v_exp_f32 v23, v23\n"
"v_mfma_f32_16x16x32_bf16 a[104:107], a[176:179], v[112:115], a[104:107]\n"
"buffer_load_dwordx4 v245, s[36:39], s61 offen offset:1024 lds\n"
"v_mfma_f32_16x16x32_bf16 a[108:111], a[184:187], v[112:115], a[108:111]\n"
"v_exp_f32 v24, v24\n"
"v_mfma_f32_16x16x32_bf16 a[108:111], a[188:191], v[56:59], a[108:111]\n"
"ds_read_b128 a[148:151], v233 offset:53248\n"
"v_cvt_pk_bf16_f32 v55, v22, v23\n"
"v_mfma_f32_16x16x32_bf16 a[76:79], a[188:191], v[48:51], a[76:79]\n"
"v_exp_f32 v25, v25\n"
"v_mfma_f32_16x16x32_bf16 a[76:79], a[184:187], v[104:107], a[76:79]\n"
"buffer_load_dwordx4 v246, s[36:39], s61 offen offset:2048 lds\n"
"v_mfma_f32_16x16x32_bf16 a[44:47], a[184:187], v[96:99], a[44:47]\n"
"v_exp_f32 v26, v26\n"
"v_mfma_f32_16x16x32_bf16 a[44:47], a[188:191], v[40:43], a[44:47]\n"
"ds_read_b128 a[144:147], v232 offset:53248\n"
"v_cvt_pk_bf16_f32 v60, v24, v25\n"
"v_mfma_f32_16x16x32_bf16 a[12:15], a[188:191], v[32:35], a[12:15]\n"
"s_waitcnt lgkmcnt(4)\n"
"v_exp_f32 v27, v27\n"
"v_mfma_f32_16x16x32_bf16 a[12:15], a[184:187], v[88:91], a[12:15]\n"
"buffer_load_dwordx4 v247, s[36:39], s61 offen offset:3072 lds\n"
"v_mfma_f32_16x16x32_bf16 v[0:3], a[128:131], v[128:131], v[64:67]\n"
"s_add_u32 s61, s61, 16384\n"
"v_exp_f32 v28, v28\n"
"v_mfma_f32_16x16x32_bf16 v[0:3], a[132:135], v[132:135], v[0:3]\n"
"ds_read_b64_tr_b16 a[160:161], v240 offset:32768\n"
"v_cvt_pk_bf16_f32 v61, v26, v27\n"
"v_mfma_f32_16x16x32_bf16 v[0:3], a[136:139], v[136:139], v[0:3]\n"
"s_waitcnt lgkmcnt(3)\n"
"v_exp_f32 v29, v29\n"
"v_mfma_f32_16x16x32_bf16 v[0:3], a[140:143], v[140:143], v[0:3]\n"
"ds_read_b64_tr_b16 a[162:163], v240 offset:36864\n"
"v_mfma_f32_16x16x32_bf16 v[4:7], a[156:159], v[140:143], v[64:67]\n"
"s_waitcnt lgkmcnt(2)\n"
"v_exp_f32 v30, v30\n"
"v_mfma_f32_16x16x32_bf16 v[4:7], a[152:155], v[136:139], v[4:7]\n"
"ds_read_b64_tr_b16 a[164:165], v240 offset:40960\n"
"v_cvt_pk_bf16_f32 v62, v28, v29\n"
"v_mfma_f32_16x16x32_bf16 v[4:7], a[148:151], v[132:135], v[4:7]\n"
"s_add_u32 m0, s60, 131072\n"
"v_exp_f32 v31, v31\n"
"v_mfma_f32_16x16x32_bf16 v[4:7], a[144:147], v[128:131], v[4:7]\n"
"ds_read_b64_tr_b16 a[166:167], v240 offset:45056\n"
"v_smfmac_f32_16x16x64_bf16 a[192:195], a[208:211], v[32:39], v250\n"
"buffer_load_dwordx4 v204, s[48:51], 0 offen lds\n"
"v_mfma_f32_16x16x32_bf16 v[12:15], a[144:147], v[144:147], v[68:71]\n"
"v_exp_f32 v0, v0\n"
"v_mfma_f32_16x16x32_bf16 v[12:15], a[148:151], v[148:151], v[12:15]\n"
"ds_read_b64_tr_b16 a[168:169], v241 offset:32768\n"
"v_cvt_pk_bf16_f32 v63, v30, v31\n"
"v_mfma_f32_16x16x32_bf16 v[12:15], a[152:155], v[152:155], v[12:15]\n"
"v_exp_f32 v1, v1\n"
"v_mfma_f32_16x16x32_bf16 v[12:15], a[156:159], v[156:159], v[12:15]\n"
"ds_read_b64_tr_b16 a[170:171], v241 offset:36864\n"
"v_mfma_f32_16x16x32_bf16 v[8:11], a[140:143], v[156:159], v[68:71]\n"
"v_exp_f32 v2, v2\n"
"v_mfma_f32_16x16x32_bf16 v[8:11], a[136:139], v[152:155], v[8:11]\n"
"ds_read_b64_tr_b16 a[172:173], v241 offset:40960\n"
"v_cvt_pk_bf16_f32 v84, v0, v1\n"
"v_mfma_f32_16x16x32_bf16 v[8:11], a[132:135], v[148:151], v[8:11]\n"
"v_exp_f32 v3, v3\n"
"v_mfma_f32_16x16x32_bf16 v[8:11], a[128:131], v[144:147], v[8:11]\n"
"ds_read_b64_tr_b16 a[174:175], v241 offset:45056\n"
"v_smfmac_f32_16x16x64_bf16 a[196:199], a[208:211], v[40:47], v250\n"
"v_mfma_f32_16x16x32_bf16 v[16:19], a[128:131], v[160:163], v[72:75]\n"
"v_exp_f32 v4, v4\n"
"v_mfma_f32_16x16x32_bf16 v[16:19], a[132:135], v[164:167], v[16:19]\n"
"ds_read_b64_tr_b16 a[176:177], v242 offset:32768\n"
"v_cvt_pk_bf16_f32 v85, v2, v3\n"
"v_mfma_f32_16x16x32_bf16 v[16:19], a[136:139], v[168:171], v[16:19]\n"
"v_exp_f32 v5, v5\n"
"v_mfma_f32_16x16x32_bf16 v[16:19], a[140:143], v[172:175], v[16:19]\n"
"ds_read_b64_tr_b16 a[178:179], v242 offset:36864\n"
"v_mfma_f32_16x16x32_bf16 v[20:23], a[156:159], v[172:175], v[72:75]\n"
"v_exp_f32 v6, v6\n"
"v_mfma_f32_16x16x32_bf16 v[20:23], a[152:155], v[168:171], v[20:23]\n"
"ds_read_b64_tr_b16 a[180:181], v242 offset:40960\n"
"v_cvt_pk_bf16_f32 v86, v4, v5\n"
"v_mfma_f32_16x16x32_bf16 v[20:23], a[148:151], v[164:167], v[20:23]\n"
"v_exp_f32 v7, v7\n"
"v_mfma_f32_16x16x32_bf16 v[20:23], a[144:147], v[160:163], v[20:23]\n"
"ds_read_b64_tr_b16 a[182:183], v242 offset:45056\n"
"v_smfmac_f32_16x16x64_bf16 a[200:203], a[208:211], v[48:55], v250\n"
"v_mfma_f32_16x16x32_bf16 v[28:31], a[144:147], v[176:179], v[76:79]\n"
"v_exp_f32 v8, v8\n"
"v_mfma_f32_16x16x32_bf16 v[28:31], a[148:151], v[180:183], v[28:31]\n"
"ds_read_b64_tr_b16 a[184:185], v243 offset:32768\n"
"v_cvt_pk_bf16_f32 v87, v6, v7\n"
"v_mfma_f32_16x16x32_bf16 v[28:31], a[152:155], v[184:187], v[28:31]\n"
"v_exp_f32 v9, v9\n"
"v_mfma_f32_16x16x32_bf16 v[28:31], a[156:159], v[188:191], v[28:31]\n"
"ds_read_b64_tr_b16 a[186:187], v243 offset:36864\n"
"v_mfma_f32_16x16x32_bf16 v[24:27], a[140:143], v[188:191], v[76:79]\n"
"v_exp_f32 v10, v10\n"
"v_mfma_f32_16x16x32_bf16 v[24:27], a[136:139], v[184:187], v[24:27]\n"
"ds_read_b64_tr_b16 a[188:189], v243 offset:40960\n"
"v_cvt_pk_bf16_f32 v92, v8, v9\n"
"v_mfma_f32_16x16x32_bf16 v[24:27], a[132:135], v[180:183], v[24:27]\n"
"s_waitcnt lgkmcnt(7)\n"
"v_exp_f32 v11, v11\n"
"v_mfma_f32_16x16x32_bf16 v[24:27], a[128:131], v[176:179], v[24:27]\n"
"ds_read_b64_tr_b16 a[190:191], v243 offset:45056\n"
"v_smfmac_f32_16x16x64_bf16 a[204:207], a[208:211], v[56:63], v250\n"
"v_mfma_f32_16x16x32_bf16 a[16:19], a[160:163], v[32:35], a[16:19]\n"
"v_exp_f32 v12, v12\n"
"v_mfma_f32_16x16x32_bf16 a[16:19], a[164:167], v[36:39], a[16:19]\n"
"ds_read_b128 a[128:131], v232 offset:57344\n"
"v_cvt_pk_bf16_f32 v93, v10, v11\n"
"v_mfma_f32_16x16x32_bf16 a[48:51], a[164:167], v[44:47], a[48:51]\n"
"v_exp_f32 v13, v13\n"
"v_mfma_f32_16x16x32_bf16 a[48:51], a[160:163], v[40:43], a[48:51]\n"
"ds_read_b128 a[132:135], v233 offset:57344\n"
"v_mfma_f32_16x16x32_bf16 a[80:83], a[160:163], v[48:51], a[80:83]\n"
"v_exp_f32 v14, v14\n"
"v_mfma_f32_16x16x32_bf16 a[80:83], a[164:167], v[52:55], a[80:83]\n"
"ds_read_b128 a[136:139], v234 offset:57344\n"
"v_cvt_pk_bf16_f32 v94, v12, v13\n"
"v_mfma_f32_16x16x32_bf16 a[112:115], a[164:167], v[60:63], a[112:115]\n"
"v_exp_f32 v15, v15\n"
"v_mfma_f32_16x16x32_bf16 a[112:115], a[160:163], v[56:59], a[112:115]\n"
"ds_read_b128 a[140:143], v235 offset:57344\n"
"v_mfma_f32_16x16x32_bf16 a[116:119], a[168:171], v[56:59], a[116:119]\n"
"v_exp_f32 v16, v16\n"
"v_mfma_f32_16x16x32_bf16 a[116:119], a[172:175], v[60:63], a[116:119]\n"
"ds_read_b128 a[144:147], v232 offset:61440\n"
"v_cvt_pk_bf16_f32 v95, v14, v15\n"
"v_mfma_f32_16x16x32_bf16 a[84:87], a[172:175], v[52:55], a[84:87]\n"
"v_exp_f32 v17, v17\n"
"v_mfma_f32_16x16x32_bf16 a[84:87], a[168:171], v[48:51], a[84:87]\n"
"v_mfma_f32_16x16x32_bf16 a[52:55], a[168:171], v[40:43], a[52:55]\n"
"v_exp_f32 v18, v18\n"
"v_mfma_f32_16x16x32_bf16 a[52:55], a[172:175], v[44:47], a[52:55]\n"
"ds_read_b128 a[148:151], v233 offset:61440\n"
"v_cvt_pk_bf16_f32 v100, v16, v17\n"
"v_mfma_f32_16x16x32_bf16 a[20:23], a[172:175], v[36:39], a[20:23]\n"
"s_waitcnt lgkmcnt(6)\n"
"v_exp_f32 v19, v19\n"
"v_mfma_f32_16x16x32_bf16 a[20:23], a[168:171], v[32:35], a[20:23]\n"
"v_mfma_f32_16x16x32_bf16 a[24:27], a[176:179], v[32:35], a[24:27]\n"
"v_exp_f32 v20, v20\n"
"v_mfma_f32_16x16x32_bf16 a[24:27], a[180:183], v[36:39], a[24:27]\n"
"ds_read_b128 a[152:155], v234 offset:61440\n"
"v_cvt_pk_bf16_f32 v101, v18, v19\n"
"v_mfma_f32_16x16x32_bf16 a[56:59], a[180:183], v[44:47], a[56:59]\n"
"v_exp_f32 v21, v21\n"
"v_mfma_f32_16x16x32_bf16 a[56:59], a[176:179], v[40:43], a[56:59]\n"
"v_mfma_f32_16x16x32_bf16 a[88:91], a[176:179], v[48:51], a[88:91]\n"
"v_exp_f32 v22, v22\n"
"v_mfma_f32_16x16x32_bf16 a[88:91], a[180:183], v[52:55], a[88:91]\n"
"ds_read_b128 a[156:159], v235 offset:61440\n"
"v_cvt_pk_bf16_f32 v102, v20, v21\n"
"v_mfma_f32_16x16x32_bf16 a[120:123], a[180:183], v[60:63], a[120:123]\n"
"v_exp_f32 v23, v23\n"
"v_mfma_f32_16x16x32_bf16 a[120:123], a[176:179], v[56:59], a[120:123]\n"
"v_mfma_f32_16x16x32_bf16 a[124:127], a[184:187], v[56:59], a[124:127]\n"
"v_exp_f32 v24, v24\n"
"v_mfma_f32_16x16x32_bf16 a[124:127], a[188:191], v[60:63], a[124:127]\n"
"v_cvt_pk_bf16_f32 v103, v22, v23\n"
"v_mfma_f32_16x16x32_bf16 a[92:95], a[188:191], v[52:55], a[92:95]\n"
"v_exp_f32 v25, v25\n"
"v_mfma_f32_16x16x32_bf16 a[92:95], a[184:187], v[48:51], a[92:95]\n"
"v_mfma_f32_16x16x32_bf16 a[60:63], a[184:187], v[40:43], a[60:63]\n"
"s_add_u32 s62, s62, 16384\n"
"v_exp_f32 v26, v26\n"
"v_mfma_f32_16x16x32_bf16 a[60:63], a[188:191], v[44:47], a[60:63]\n"
"v_cvt_pk_bf16_f32 v108, v24, v25\n"
"v_mfma_f32_16x16x32_bf16 a[28:31], a[188:191], v[36:39], a[28:31]\n"
"s_waitcnt vmcnt(13) lgkmcnt(0)\n"
"v_exp_f32 v27, v27\n"
"v_mfma_f32_16x16x32_bf16 a[28:31], a[184:187], v[32:35], a[28:31]\n"
"s_barrier\n"
"v_mfma_f32_16x16x32_bf16 v[0:3], a[128:131], v[128:131], v[64:67]\n"
"v_exp_f32 v28, v28\n"
"v_mfma_f32_16x16x32_bf16 v[0:3], a[132:135], v[132:135], v[0:3]\n"
"ds_read_b64_tr_b16 a[160:161], v236 offset:40960\n"
"v_cvt_pk_bf16_f32 v109, v26, v27\n"
"v_mfma_f32_16x16x32_bf16 v[0:3], a[136:139], v[136:139], v[0:3]\n"
"v_exp_f32 v29, v29\n"
"v_mfma_f32_16x16x32_bf16 v[0:3], a[140:143], v[140:143], v[0:3]\n"
"ds_read_b64_tr_b16 a[162:163], v236 offset:45056\n"
"v_mfma_f32_16x16x32_bf16 v[4:7], a[156:159], v[140:143], v[64:67]\n"
"s_mov_b32 s65, s58\n"
"v_exp_f32 v30, v30\n"
"v_mfma_f32_16x16x32_bf16 v[4:7], a[152:155], v[136:139], v[4:7]\n"
"ds_read_b64_tr_b16 a[164:165], v236 offset:49152\n"
"v_cvt_pk_bf16_f32 v110, v28, v29\n"
"v_mfma_f32_16x16x32_bf16 v[4:7], a[148:151], v[132:135], v[4:7]\n"
"v_exp_f32 v31, v31\n"
"v_mfma_f32_16x16x32_bf16 v[4:7], a[144:147], v[128:131], v[4:7]\n"
"ds_read_b64_tr_b16 a[166:167], v236 offset:53248\n"
"v_mfma_f32_16x16x32_bf16 v[12:15], a[144:147], v[144:147], v[68:71]\n"
"v_exp_f32 v0, v0\n"
"v_mfma_f32_16x16x32_bf16 v[12:15], a[148:151], v[148:151], v[12:15]\n"
"ds_read_b64_tr_b16 a[168:169], v237 offset:40960\n"
"v_cvt_pk_bf16_f32 v111, v30, v31\n"
"v_mfma_f32_16x16x32_bf16 v[12:15], a[152:155], v[152:155], v[12:15]\n"
"v_exp_f32 v1, v1\n"
"v_mfma_f32_16x16x32_bf16 v[12:15], a[156:159], v[156:159], v[12:15]\n"
"ds_read_b64_tr_b16 a[170:171], v237 offset:45056\n"
"v_mfma_f32_16x16x32_bf16 v[8:11], a[140:143], v[156:159], v[68:71]\n"
"v_exp_f32 v2, v2\n"
"v_mfma_f32_16x16x32_bf16 v[8:11], a[136:139], v[152:155], v[8:11]\n"
"ds_read_b64_tr_b16 a[172:173], v237 offset:49152\n"
"v_cvt_pk_bf16_f32 v88, v0, v1\n"
"v_mfma_f32_16x16x32_bf16 v[8:11], a[132:135], v[148:151], v[8:11]\n"
"v_exp_f32 v3, v3\n"
"v_mfma_f32_16x16x32_bf16 v[8:11], a[128:131], v[144:147], v[8:11]\n"
"ds_read_b64_tr_b16 a[174:175], v237 offset:53248\n"
"v_mfma_f32_16x16x32_bf16 v[16:19], a[128:131], v[160:163], v[72:75]\n"
"v_exp_f32 v4, v4\n"
"v_mfma_f32_16x16x32_bf16 v[16:19], a[132:135], v[164:167], v[16:19]\n"
"ds_read_b64_tr_b16 a[176:177], v238 offset:40960\n"
"v_cvt_pk_bf16_f32 v89, v2, v3\n"
"v_mfma_f32_16x16x32_bf16 v[16:19], a[136:139], v[168:171], v[16:19]\n"
"v_exp_f32 v5, v5\n"
"v_mfma_f32_16x16x32_bf16 v[16:19], a[140:143], v[172:175], v[16:19]\n"
"ds_read_b64_tr_b16 a[178:179], v238 offset:45056\n"
"v_mfma_f32_16x16x32_bf16 v[20:23], a[156:159], v[172:175], v[72:75]\n"
"v_exp_f32 v6, v6\n"
"v_mfma_f32_16x16x32_bf16 v[20:23], a[152:155], v[168:171], v[20:23]\n"
"ds_read_b64_tr_b16 a[180:181], v238 offset:49152\n"
"v_cvt_pk_bf16_f32 v90, v4, v5\n"
"v_mfma_f32_16x16x32_bf16 v[20:23], a[148:151], v[164:167], v[20:23]\n"
"v_exp_f32 v7, v7\n"
"v_mfma_f32_16x16x32_bf16 v[20:23], a[144:147], v[160:163], v[20:23]\n"
"ds_read_b64_tr_b16 a[182:183], v238 offset:53248\n"
"v_mfma_f32_16x16x32_bf16 v[28:31], a[144:147], v[176:179], v[76:79]\n"
"v_exp_f32 v8, v8\n"
"v_mfma_f32_16x16x32_bf16 v[28:31], a[148:151], v[180:183], v[28:31]\n"
"ds_read_b64_tr_b16 a[184:185], v239 offset:40960\n"
"v_cvt_pk_bf16_f32 v91, v6, v7\n"
"v_mfma_f32_16x16x32_bf16 v[28:31], a[152:155], v[184:187], v[28:31]\n"
"v_exp_f32 v9, v9\n"
"v_mfma_f32_16x16x32_bf16 v[28:31], a[156:159], v[188:191], v[28:31]\n"
"ds_read_b64_tr_b16 a[186:187], v239 offset:45056\n"
"v_mfma_f32_16x16x32_bf16 v[24:27], a[140:143], v[188:191], v[76:79]\n"
"v_exp_f32 v10, v10\n"
"v_mfma_f32_16x16x32_bf16 v[24:27], a[136:139], v[184:187], v[24:27]\n"
"ds_read_b64_tr_b16 a[188:189], v239 offset:49152\n"
"v_cvt_pk_bf16_f32 v96, v8, v9\n"
"v_mfma_f32_16x16x32_bf16 v[24:27], a[132:135], v[180:183], v[24:27]\n"
"s_waitcnt lgkmcnt(7)\n"
"v_exp_f32 v11, v11\n"
"v_mfma_f32_16x16x32_bf16 v[24:27], a[128:131], v[176:179], v[24:27]\n"
"ds_read_b64_tr_b16 a[190:191], v239 offset:53248\n"
"v_mfma_f32_16x16x32_bf16 a[0:3], a[160:163], v[36:39], a[0:3]\n"
"v_exp_f32 v12, v12\n"
"v_mfma_f32_16x16x32_bf16 a[0:3], a[164:167], v[84:87], a[0:3]\n"
"ds_read_b128 a[128:131], v232 offset:0\n"
"v_cvt_pk_bf16_f32 v97, v10, v11\n"
"v_mfma_f32_16x16x32_bf16 a[32:35], a[164:167], v[92:95], a[32:35]\n"
"s_add_u32 m0, s60, 81920\n"
"v_exp_f32 v13, v13\n"
"v_mfma_f32_16x16x32_bf16 a[32:35], a[160:163], v[44:47], a[32:35]\n"
"buffer_load_dwordx4 v248, s[40:43], s62 offen offset:0 lds\n"
"v_mfma_f32_16x16x32_bf16 a[64:67], a[160:163], v[52:55], a[64:67]\n"
"v_exp_f32 v14, v14\n"
"v_mfma_f32_16x16x32_bf16 a[64:67], a[164:167], v[100:103], a[64:67]\n"
"ds_read_b128 a[132:135], v233 offset:0\n"
"v_cvt_pk_bf16_f32 v98, v12, v13\n"
"v_mfma_f32_16x16x32_bf16 a[96:99], a[164:167], v[108:111], a[96:99]\n"
"v_exp_f32 v15, v15\n"
"v_mfma_f32_16x16x32_bf16 a[96:99], a[160:163], v[60:63], a[96:99]\n"
"buffer_load_dwordx4 v249, s[40:43], s62 offen offset:1024 lds\n"
"v_mfma_f32_16x16x32_bf16 a[100:103], a[168:171], v[60:63], a[100:103]\n"
"v_exp_f32 v16, v16\n"
"v_mfma_f32_16x16x32_bf16 a[100:103], a[172:175], v[108:111], a[100:103]\n"
"ds_read_b128 a[136:139], v234 offset:0\n"
"v_cvt_pk_bf16_f32 v99, v14, v15\n"
"v_mfma_f32_16x16x32_bf16 a[68:71], a[172:175], v[100:103], a[68:71]\n"
"v_exp_f32 v17, v17\n"
"v_mfma_f32_16x16x32_bf16 a[68:71], a[168:171], v[52:55], a[68:71]\n"
"buffer_load_dwordx4 v248, s[40:43], s62 offen offset:2048 lds\n"
"v_mfma_f32_16x16x32_bf16 a[36:39], a[168:171], v[44:47], a[36:39]\n"
"v_exp_f32 v18, v18\n"
"v_mfma_f32_16x16x32_bf16 a[36:39], a[172:175], v[92:95], a[36:39]\n"
"ds_read_b128 a[140:143], v235 offset:0\n"
"v_cvt_pk_bf16_f32 v104, v16, v17\n"
"v_mfma_f32_16x16x32_bf16 a[4:7], a[172:175], v[84:87], a[4:7]\n"
"L_w8_%=:\n"
"s_cbranch_execz L_wd2_%=\n"
"s_waitcnt lgkmcnt(4)\n"
"v_exp_f32 v19, v19\n"
"v_mfma_f32_16x16x32_bf16 a[4:7], a[168:171], v[36:39], a[4:7]\n"
"buffer_load_dwordx4 v249, s[40:43], s62 offen offset:3072 lds\n"
"v_mfma_f32_16x16x32_bf16 a[8:11], a[176:179], v[36:39], a[8:11]\n"
"v_exp_f32 v20, v20\n"
"v_mfma_f32_16x16x32_bf16 a[8:11], a[180:183], v[84:87], a[8:11]\n"
"ds_read_b128 a[156:159], v235 offset:4096\n"
"v_cvt_pk_bf16_f32 v105, v18, v19\n"
"v_mfma_f32_16x16x32_bf16 a[40:43], a[180:183], v[92:95], a[40:43]\n"
"s_add_u32 m0, s60, 49152\n"
"v_exp_f32 v21, v21\n"
"v_mfma_f32_16x16x32_bf16 a[40:43], a[176:179], v[44:47], a[40:43]\n"
"buffer_load_dwordx4 v244, s[36:39], s61 offen offset:0 lds\n"
"v_mfma_f32_16x16x32_bf16 a[72:75], a[176:179], v[52:55], a[72:75]\n"
"v_exp_f32 v22, v22\n"
"v_mfma_f32_16x16x32_bf16 a[72:75], a[180:183], v[100:103], a[72:75]\n"
"ds_read_b128 a[152:155], v234 offset:4096\n"
"v_cvt_pk_bf16_f32 v106, v20, v21\n"
"v_mfma_f32_16x16x32_bf16 a[104:107], a[180:183], v[108:111], a[104:107]\n"
"v_exp_f32 v23, v23\n"
"v_mfma_f32_16x16x32_bf16 a[104:107], a[176:179], v[60:63], a[104:107]\n"
"buffer_load_dwordx4 v245, s[36:39], s61 offen offset:1024 lds\n"
"v_mfma_f32_16x16x32_bf16 a[108:111], a[184:187], v[60:63], a[108:111]\n"
"v_exp_f32 v24, v24\n"
"v_mfma_f32_16x16x32_bf16 a[108:111], a[188:191], v[108:111], a[108:111]\n"
"ds_read_b128 a[148:151], v233 offset:4096\n"
"v_cvt_pk_bf16_f32 v107, v22, v23\n"
"v_mfma_f32_16x16x32_bf16 a[76:79], a[188:191], v[100:103], a[76:79]\n"
"v_exp_f32 v25, v25\n"
"v_mfma_f32_16x16x32_bf16 a[76:79], a[184:187], v[52:55], a[76:79]\n"
"buffer_load_dwordx4 v246, s[36:39], s61 offen offset:2048 lds\n"
"v_mfma_f32_16x16x32_bf16 a[44:47], a[184:187], v[44:47], a[44:47]\n"
"v_exp_f32 v26, v26\n"
"v_mfma_f32_16x16x32_bf16 a[44:47], a[188:191], v[92:95], a[44:47]\n"
"ds_read_b128 a[144:147], v232 offset:4096\n"
"v_cvt_pk_bf16_f32 v112, v24, v25\n"
"v_mfma_f32_16x16x32_bf16 a[12:15], a[188:191], v[84:87], a[12:15]\n"
"s_waitcnt lgkmcnt(4)\n"
"v_exp_f32 v27, v27\n"
"v_mfma_f32_16x16x32_bf16 a[12:15], a[184:187], v[36:39], a[12:15]\n"
"buffer_load_dwordx4 v247, s[36:39], s61 offen offset:3072 lds\n"
"v_mfma_f32_16x16x32_bf16 v[0:3], a[128:131], v[128:131], v[64:67]\n"
"s_add_u32 s61, s61, 16384\n"
"v_exp_f32 v28, v28\n"
"v_mfma_f32_16x16x32_bf16 v[0:3], a[132:135], v[132:135], v[0:3]\n"
"ds_read_b64_tr_b16 a[160:161], v240 offset:49152\n"
"v_cvt_pk_bf16_f32 v113, v26, v27\n"
"v_mfma_f32_16x16x32_bf16 v[0:3], a[136:139], v[136:139], v[0:3]\n"
"s_waitcnt lgkmcnt(3)\n"
"v_exp_f32 v29, v29\n"
"v_mfma_f32_16x16x32_bf16 v[0:3], a[140:143], v[140:143], v[0:3]\n"
"ds_read_b64_tr_b16 a[162:163], v240 offset:53248\n"
"v_mfma_f32_16x16x32_bf16 v[4:7], a[156:159], v[140:143], v[64:67]\n"
"s_waitcnt lgkmcnt(2)\n"
"v_exp_f32 v30, v30\n"
"v_mfma_f32_16x16x32_bf16 v[4:7], a[152:155], v[136:139], v[4:7]\n"
"ds_read_b64_tr_b16 a[164:165], v240 offset:57344\n"
"v_cvt_pk_bf16_f32 v114, v28, v29\n"
"v_mfma_f32_16x16x32_bf16 v[4:7], a[148:151], v[132:135], v[4:7]\n"
"v_exp_f32 v31, v31\n"
"v_mfma_f32_16x16x32_bf16 v[4:7], a[144:147], v[128:131], v[4:7]\n"
"ds_read_b64_tr_b16 a[166:167], v240 offset:61440\n"
"v_smfmac_f32_16x16x64_bf16 a[192:195], a[208:211], v[84:91], v250\n"
"v_mfma_f32_16x16x32_bf16 v[12:15], a[144:147], v[144:147], v[68:71]\n"
"v_exp_f32 v0, v0\n"
"v_mfma_f32_16x16x32_bf16 v[12:15], a[148:151], v[148:151], v[12:15]\n"
"ds_read_b64_tr_b16 a[168:169], v241 offset:49152\n"
"v_cvt_pk_bf16_f32 v115, v30, v31\n"
"v_mfma_f32_16x16x32_bf16 v[12:15], a[152:155], v[152:155], v[12:15]\n"
"v_exp_f32 v1, v1\n"
"v_mfma_f32_16x16x32_bf16 v[12:15], a[156:159], v[156:159], v[12:15]\n"
"ds_read_b64_tr_b16 a[170:171], v241 offset:53248\n"
"v_mfma_f32_16x16x32_bf16 v[8:11], a[140:143], v[156:159], v[68:71]\n"
"v_exp_f32 v2, v2\n"
"v_mfma_f32_16x16x32_bf16 v[8:11], a[136:139], v[152:155], v[8:11]\n"
"ds_read_b64_tr_b16 a[172:173], v241 offset:57344\n"
"v_cvt_pk_bf16_f32 v32, v0, v1\n"
"v_mfma_f32_16x16x32_bf16 v[8:11], a[132:135], v[148:151], v[8:11]\n"
"v_exp_f32 v3, v3\n"
"v_mfma_f32_16x16x32_bf16 v[8:11], a[128:131], v[144:147], v[8:11]\n"
"ds_read_b64_tr_b16 a[174:175], v241 offset:61440\n"
"v_smfmac_f32_16x16x64_bf16 a[196:199], a[208:211], v[92:99], v250\n"
"v_mfma_f32_16x16x32_bf16 v[16:19], a[128:131], v[160:163], v[72:75]\n"
"v_exp_f32 v4, v4\n"
"v_mfma_f32_16x16x32_bf16 v[16:19], a[132:135], v[164:167], v[16:19]\n"
"ds_read_b64_tr_b16 a[176:177], v242 offset:49152\n"
"v_cvt_pk_bf16_f32 v33, v2, v3\n"
"v_mfma_f32_16x16x32_bf16 v[16:19], a[136:139], v[168:171], v[16:19]\n"
"v_exp_f32 v5, v5\n"
"v_mfma_f32_16x16x32_bf16 v[16:19], a[140:143], v[172:175], v[16:19]\n"
"ds_read_b64_tr_b16 a[178:179], v242 offset:53248\n"
"v_mfma_f32_16x16x32_bf16 v[20:23], a[156:159], v[172:175], v[72:75]\n"
"v_exp_f32 v6, v6\n"
"v_mfma_f32_16x16x32_bf16 v[20:23], a[152:155], v[168:171], v[20:23]\n"
"ds_read_b64_tr_b16 a[180:181], v242 offset:57344\n"
"v_cvt_pk_bf16_f32 v34, v4, v5\n"
"v_mfma_f32_16x16x32_bf16 v[20:23], a[148:151], v[164:167], v[20:23]\n"
"v_exp_f32 v7, v7\n"
"v_mfma_f32_16x16x32_bf16 v[20:23], a[144:147], v[160:163], v[20:23]\n"
"ds_read_b64_tr_b16 a[182:183], v242 offset:61440\n"
"v_smfmac_f32_16x16x64_bf16 a[200:203], a[208:211], v[100:107], v250\n"
"v_mfma_f32_16x16x32_bf16 v[28:31], a[144:147], v[176:179], v[76:79]\n"
"v_exp_f32 v8, v8\n"
"v_mfma_f32_16x16x32_bf16 v[28:31], a[148:151], v[180:183], v[28:31]\n"
"ds_read_b64_tr_b16 a[184:185], v243 offset:49152\n"
"v_cvt_pk_bf16_f32 v35, v6, v7\n"
"v_mfma_f32_16x16x32_bf16 v[28:31], a[152:155], v[184:187], v[28:31]\n"
"v_exp_f32 v9, v9\n"
"v_mfma_f32_16x16x32_bf16 v[28:31], a[156:159], v[188:191], v[28:31]\n"
"ds_read_b64_tr_b16 a[186:187], v243 offset:53248\n"
"v_mfma_f32_16x16x32_bf16 v[24:27], a[140:143], v[188:191], v[76:79]\n"
"v_exp_f32 v10, v10\n"
"v_mfma_f32_16x16x32_bf16 v[24:27], a[136:139], v[184:187], v[24:27]\n"
"ds_read_b64_tr_b16 a[188:189], v243 offset:57344\n"
"v_cvt_pk_bf16_f32 v40, v8, v9\n"
"v_mfma_f32_16x16x32_bf16 v[24:27], a[132:135], v[180:183], v[24:27]\n"
"s_waitcnt lgkmcnt(7)\n"
"v_exp_f32 v11, v11\n"
"v_mfma_f32_16x16x32_bf16 v[24:27], a[128:131], v[176:179], v[24:27]\n"
"ds_read_b64_tr_b16 a[190:191], v243 offset:61440\n"
"v_smfmac_f32_16x16x64_bf16 a[204:207], a[208:211], v[108:115], v250\n"
"v_mfma_f32_16x16x32_bf16 a[16:19], a[160:163], v[84:87], a[16:19]\n"
"v_exp_f32 v12, v12\n"
"v_mfma_f32_16x16x32_bf16 a[16:19], a[164:167], v[88:91], a[16:19]\n"
"ds_read_b128 a[128:131], v232 offset:8192\n"
"v_cvt_pk_bf16_f32 v41, v10, v11\n"
"v_mfma_f32_16x16x32_bf16 a[48:51], a[164:167], v[96:99], a[48:51]\n"
"s_load_dwordx8 s[52:59], %0, s59\n"
"v_exp_f32 v13, v13\n"
"v_mfma_f32_16x16x32_bf16 a[48:51], a[160:163], v[92:95], a[48:51]\n"
"ds_read_b128 a[132:135], v233 offset:8192\n"
"v_mfma_f32_16x16x32_bf16 a[80:83], a[160:163], v[100:103], a[80:83]\n"
"v_exp_f32 v14, v14\n"
"v_mfma_f32_16x16x32_bf16 a[80:83], a[164:167], v[104:107], a[80:83]\n"
"ds_read_b128 a[136:139], v234 offset:8192\n"
"v_cvt_pk_bf16_f32 v42, v12, v13\n"
"v_mfma_f32_16x16x32_bf16 a[112:115], a[164:167], v[112:115], a[112:115]\n"
"v_exp_f32 v15, v15\n"
"v_mfma_f32_16x16x32_bf16 a[112:115], a[160:163], v[108:111], a[112:115]\n"
"ds_read_b128 a[140:143], v235 offset:8192\n"
"v_mfma_f32_16x16x32_bf16 a[116:119], a[168:171], v[108:111], a[116:119]\n"
"v_exp_f32 v16, v16\n"
"v_mfma_f32_16x16x32_bf16 a[116:119], a[172:175], v[112:115], a[116:119]\n"
"ds_read_b128 a[144:147], v232 offset:12288\n"
"v_cvt_pk_bf16_f32 v43, v14, v15\n"
"v_mfma_f32_16x16x32_bf16 a[84:87], a[172:175], v[104:107], a[84:87]\n"
"s_waitcnt vmcnt(8)\n"
"v_exp_f32 v17, v17\n"
"v_mfma_f32_16x16x32_bf16 a[84:87], a[168:171], v[100:103], a[84:87]\n"
"ds_read_b64_tr_b16 v[200:201], v194 offset:0\n"
"ds_read_b64_tr_b16 v[202:203], v194 offset:512\n"
"v_mfma_f32_16x16x32_bf16 a[52:55], a[168:171], v[92:95], a[52:55]\n"
"v_exp_f32 v18, v18\n"
"v_mfma_f32_16x16x32_bf16 a[52:55], a[172:175], v[96:99], a[52:55]\n"
"ds_read_b128 a[148:151], v233 offset:12288\n"
"v_cvt_pk_bf16_f32 v48, v16, v17\n"
"v_mfma_f32_16x16x32_bf16 a[20:23], a[172:175], v[88:91], a[20:23]\n"
"s_waitcnt lgkmcnt(8)\n"
"v_exp_f32 v19, v19\n"
"v_mfma_f32_16x16x32_bf16 a[20:23], a[168:171], v[84:87], a[20:23]\n"
"v_mfma_f32_16x16x32_bf16 a[24:27], a[176:179], v[84:87], a[24:27]\n"
"v_exp_f32 v20, v20\n"
"v_mfma_f32_16x16x32_bf16 a[24:27], a[180:183], v[88:91], a[24:27]\n"
"ds_read_b128 a[152:155], v234 offset:12288\n"
"v_cvt_pk_bf16_f32 v49, v18, v19\n"
"v_mfma_f32_16x16x32_bf16 a[56:59], a[180:183], v[96:99], a[56:59]\n"
"v_exp_f32 v21, v21\n"
"v_mfma_f32_16x16x32_bf16 a[56:59], a[176:179], v[92:95], a[56:59]\n"
"v_mfma_f32_16x16x32_bf16 a[88:91], a[176:179], v[100:103], a[88:91]\n"
"v_exp_f32 v22, v22\n"
"v_mfma_f32_16x16x32_bf16 a[88:91], a[180:183], v[104:107], a[88:91]\n"
"ds_read_b128 a[156:159], v235 offset:12288\n"
"v_cvt_pk_bf16_f32 v50, v20, v21\n"
"v_mfma_f32_16x16x32_bf16 a[120:123], a[180:183], v[112:115], a[120:123]\n"
"s_waitcnt lgkmcnt(3)\n"
"v_exp_f32 v23, v23\n"
"v_mfma_f32_16x16x32_bf16 a[120:123], a[176:179], v[108:111], a[120:123]\n"
"buffer_store_dwordx4 v[200:203], v228, s[44:47], s65 offen sc1\n"
"v_mfma_f32_16x16x32_bf16 a[124:127], a[184:187], v[108:111], a[124:127]\n"
"v_exp_f32 v24, v24\n"
"v_mfma_f32_16x16x32_bf16 a[124:127], a[188:191], v[112:115], a[124:127]\n"
"v_cvt_pk_bf16_f32 v51, v22, v23\n"
"v_mfma_f32_16x16x32_bf16 a[92:95], a[188:191], v[104:107], a[92:95]\n"
"v_exp_f32 v25, v25\n"
"v_mfma_f32_16x16x32_bf16 a[92:95], a[184:187], v[100:103], a[92:95]\n"
"v_mfma_f32_16x16x32_bf16 a[60:63], a[184:187], v[92:95], a[60:63]\n"
"s_add_u32 s62, s62, 16384\n"
"v_exp_f32 v26, v26\n"
"v_mfma_f32_16x16x32_bf16 a[60:63], a[188:191], v[96:99], a[60:63]\n"
"s_sub_u32 s63, s63, 1\n"
"v_cvt_pk_bf16_f32 v56, v24, v25\n"
"v_mfma_f32_16x16x32_bf16 a[28:31], a[188:191], v[88:91], a[28:31]\n"
"s_waitcnt vmcnt(13) lgkmcnt(0)\n"
"v_exp_f32 v27, v27\n"
"v_mfma_f32_16x16x32_bf16 a[28:31], a[184:187], v[84:87], a[28:31]\n"
"s_cmp_lg_u32 s63, 0\n"
"s_barrier\n"
"s_cbranch_scc1 L_loop_%=\n"
"v_and_b32 v224, 0xf0, v228\n"
"v_lshlrev_b32 v224, 3, v224\n"
"v_add_u32 v224, 626688, v224\n"
"v_mfma_f32_16x16x32_bf16 v[0:3], a[128:131], v[128:131], v[64:67]\n"
"v_exp_f32 v28, v28\n"
"v_mfma_f32_16x16x32_bf16 v[0:3], a[132:135], v[132:135], v[0:3]\n"
"ds_read_b64_tr_b16 a[160:161], v236 offset:0\n"
"v_cvt_pk_bf16_f32 v57, v26, v27\n"
"v_mfma_f32_16x16x32_bf16 v[0:3], a[136:139], v[136:139], v[0:3]\n"
"v_exp_f32 v29, v29\n"
"v_mfma_f32_16x16x32_bf16 v[0:3], a[140:143], v[140:143], v[0:3]\n"
"ds_read_b64_tr_b16 a[162:163], v236 offset:4096\n"
"global_load_dword v225, v224, %0 sc1\n"
"v_mfma_f32_16x16x32_bf16 v[4:7], a[156:159], v[140:143], v[64:67]\n"
"s_lshl_b32 s58, %12, 2\n"
"v_exp_f32 v30, v30\n"
"v_mfma_f32_16x16x32_bf16 v[4:7], a[152:155], v[136:139], v[4:7]\n"
"ds_read_b64_tr_b16 a[164:165], v237 offset:0\n"
"v_cvt_pk_bf16_f32 v58, v28, v29\n"
"v_mfma_f32_16x16x32_bf16 v[4:7], a[148:151], v[132:135], v[4:7]\n"
"s_add_u32 s58, s58, %17\n"
"v_exp_f32 v31, v31\n"
"v_mfma_f32_16x16x32_bf16 v[4:7], a[144:147], v[128:131], v[4:7]\n"
"ds_read_b64_tr_b16 a[212:213], v236 offset:57344\n"
"ds_read_b64_tr_b16 a[166:167], v237 offset:4096\n"
"v_mfma_f32_16x16x32_bf16 v[12:15], a[144:147], v[144:147], v[68:71]\n"
"s_mul_i32 s58, s58, 163840\n"
"v_exp_f32 v0, v0\n"
"v_mfma_f32_16x16x32_bf16 v[12:15], a[148:151], v[148:151], v[12:15]\n"
"ds_read_b64_tr_b16 a[168:169], v238 offset:0\n"
"v_cvt_pk_bf16_f32 v59, v30, v31\n"
"v_mfma_f32_16x16x32_bf16 v[12:15], a[152:155], v[152:155], v[12:15]\n"
"s_add_u32 s58, s58, 25165824\n"
"v_exp_f32 v1, v1\n"
"v_mfma_f32_16x16x32_bf16 v[12:15], a[156:159], v[156:159], v[12:15]\n"
"ds_read_b64_tr_b16 a[214:215], v236 offset:61440\n"
"ds_read_b64_tr_b16 a[170:171], v238 offset:4096\n"
"v_mfma_f32_16x16x32_bf16 v[8:11], a[140:143], v[156:159], v[68:71]\n"
"s_and_b32 s57, %11, 1\n"
"v_exp_f32 v2, v2\n"
"v_mfma_f32_16x16x32_bf16 v[8:11], a[136:139], v[152:155], v[8:11]\n"
"ds_read_b64_tr_b16 a[172:173], v239 offset:0\n"
"v_cvt_pk_bf16_f32 v36, v0, v1\n"
"v_mfma_f32_16x16x32_bf16 v[8:11], a[132:135], v[148:151], v[8:11]\n"
"s_mul_i32 s57, s57, 81920\n"
"v_exp_f32 v3, v3\n"
"v_mfma_f32_16x16x32_bf16 v[8:11], a[128:131], v[144:147], v[8:11]\n"
"ds_read_b64_tr_b16 a[216:217], v237 offset:57344\n"
"ds_read_b64_tr_b16 a[174:175], v239 offset:4096\n"
"v_mfma_f32_16x16x32_bf16 v[16:19], a[128:131], v[160:163], v[72:75]\n"
"s_add_u32 s58, s58, s57\n"
"v_exp_f32 v4, v4\n"
"v_mfma_f32_16x16x32_bf16 v[16:19], a[132:135], v[164:167], v[16:19]\n"
"ds_read_b64_tr_b16 a[176:177], v240 offset:0\n"
"v_cvt_pk_bf16_f32 v37, v2, v3\n"
"v_mfma_f32_16x16x32_bf16 v[16:19], a[136:139], v[168:171], v[16:19]\n"
"s_min_u32 s54, %18, 215\n"
"v_exp_f32 v5, v5\n"
"v_mfma_f32_16x16x32_bf16 v[16:19], a[140:143], v[172:175], v[16:19]\n"
"ds_read_b64_tr_b16 a[218:219], v237 offset:61440\n"
"ds_read_b64_tr_b16 a[178:179], v240 offset:4096\n"
"v_mfma_f32_16x16x32_bf16 v[20:23], a[156:159], v[172:175], v[72:75]\n"
"s_mul_i32 s54, s54, 21504\n"
"v_exp_f32 v6, v6\n"
"v_mfma_f32_16x16x32_bf16 v[20:23], a[152:155], v[168:171], v[20:23]\n"
"ds_read_b64_tr_b16 a[180:181], v241 offset:0\n"
"v_cvt_pk_bf16_f32 v38, v4, v5\n"
"v_mfma_f32_16x16x32_bf16 v[20:23], a[148:151], v[164:167], v[20:23]\n"
"s_add_u32 s54, s54, 31457280\n"
"v_exp_f32 v7, v7\n"
"v_mfma_f32_16x16x32_bf16 v[20:23], a[144:147], v[160:163], v[20:23]\n"
"ds_read_b64_tr_b16 a[220:221], v238 offset:57344\n"
"ds_read_b64_tr_b16 a[182:183], v241 offset:4096\n"
"v_mfma_f32_16x16x32_bf16 v[28:31], a[144:147], v[176:179], v[76:79]\n"
"s_mul_i32 s55, %17, 5120\n"
"v_exp_f32 v8, v8\n"
"v_mfma_f32_16x16x32_bf16 v[28:31], a[148:151], v[180:183], v[28:31]\n"
"ds_read_b64_tr_b16 a[184:185], v242 offset:0\n"
"v_cvt_pk_bf16_f32 v39, v6, v7\n"
"v_mfma_f32_16x16x32_bf16 v[28:31], a[152:155], v[184:187], v[28:31]\n"
"s_add_u32 s55, s55, s54\n"
"v_exp_f32 v9, v9\n"
"v_mfma_f32_16x16x32_bf16 v[28:31], a[156:159], v[188:191], v[28:31]\n"
"ds_read_b64_tr_b16 a[222:223], v238 offset:61440\n"
"ds_read_b64_tr_b16 a[186:187], v242 offset:4096\n"
"v_mfma_f32_16x16x32_bf16 v[24:27], a[140:143], v[188:191], v[76:79]\n"
"s_mul_i32 s56, %17, 320\n"
"v_exp_f32 v10, v10\n"
"v_mfma_f32_16x16x32_bf16 v[24:27], a[136:139], v[184:187], v[24:27]\n"
"ds_read_b64_tr_b16 a[224:225], v239 offset:57344\n"
"ds_read_b64_tr_b16 a[188:189], v243 offset:0\n"
"v_cvt_pk_bf16_f32 v44, v8, v9\n"
"v_mfma_f32_16x16x32_bf16 v[24:27], a[132:135], v[180:183], v[24:27]\n"
"s_waitcnt lgkmcnt(11)\n"
"v_exp_f32 v11, v11\n"
"v_mfma_f32_16x16x32_bf16 v[24:27], a[128:131], v[176:179], v[24:27]\n"
"ds_read_b64_tr_b16 a[226:227], v239 offset:61440\n"
"ds_read_b64_tr_b16 a[190:191], v243 offset:4096\n"
"v_mfma_f32_16x16x32_bf16 a[0:3], a[160:163], v[32:35], a[0:3]\n"
"s_add_u32 s56, s56, 20480\n"
"v_exp_f32 v12, v12\n"
"v_mfma_f32_16x16x32_bf16 a[32:35], a[160:163], v[40:43], a[32:35]\n"
"ds_read_b128 a[128:131], v232 offset:16384\n"
"v_cvt_pk_bf16_f32 v45, v10, v11\n"
"v_mfma_f32_16x16x32_bf16 a[64:67], a[160:163], v[48:51], a[64:67]\n"
"s_add_u32 m0, s60, 98304\n"
"v_exp_f32 v13, v13\n"
"v_mfma_f32_16x16x32_bf16 a[96:99], a[160:163], v[56:59], a[96:99]\n"
"buffer_load_dwordx4 v248, s[40:43], s62 offen offset:0 lds\n"
"v_mfma_f32_16x16x32_bf16 a[100:103], a[164:167], v[56:59], a[100:103]\n"
"s_add_u32 s54, s54, 20480\n"
"v_exp_f32 v14, v14\n"
"v_mfma_f32_16x16x32_bf16 a[68:71], a[164:167], v[48:51], a[68:71]\n"
"ds_read_b128 a[132:135], v233 offset:16384\n"
"v_cvt_pk_bf16_f32 v46, v12, v13\n"
"v_mfma_f32_16x16x32_bf16 a[36:39], a[164:167], v[40:43], a[36:39]\n"
"s_mul_i32 s52, %12, 320\n"
"v_exp_f32 v15, v15\n"
"v_mfma_f32_16x16x32_bf16 a[4:7], a[164:167], v[32:35], a[4:7]\n"
"buffer_load_dwordx4 v249, s[40:43], s62 offen offset:1024 lds\n"
"v_mfma_f32_16x16x32_bf16 a[8:11], a[168:171], v[32:35], a[8:11]\n"
"s_add_u32 s52, s52, s60\n"
"v_exp_f32 v16, v16\n"
"v_mfma_f32_16x16x32_bf16 a[40:43], a[168:171], v[40:43], a[40:43]\n"
"ds_read_b128 a[136:139], v234 offset:16384\n"
"v_cvt_pk_bf16_f32 v47, v14, v15\n"
"v_mfma_f32_16x16x32_bf16 a[72:75], a[168:171], v[48:51], a[72:75]\n"
"s_add_u32 s52, s52, 4096\n"
"v_exp_f32 v17, v17\n"
"v_mfma_f32_16x16x32_bf16 a[104:107], a[168:171], v[56:59], a[104:107]\n"
"buffer_load_dwordx4 v248, s[40:43], s62 offen offset:2048 lds\n"
"v_mfma_f32_16x16x32_bf16 a[108:111], a[172:175], v[56:59], a[108:111]\n"
"s_lshl_b32 s64, %17, 2\n"
"v_exp_f32 v18, v18\n"
"v_mfma_f32_16x16x32_bf16 a[76:79], a[172:175], v[48:51], a[76:79]\n"
"ds_read_b128 a[140:143], v235 offset:16384\n"
"v_cvt_pk_bf16_f32 v52, v16, v17\n"
"v_mfma_f32_16x16x32_bf16 a[44:47], a[172:175], v[40:43], a[44:47]\n"
"s_waitcnt lgkmcnt(4)\n"
"v_exp_f32 v19, v19\n"
"v_mfma_f32_16x16x32_bf16 a[12:15], a[172:175], v[32:35], a[12:15]\n"
"buffer_load_dwordx4 v249, s[40:43], s62 offen offset:3072 lds\n"
"v_mfma_f32_16x16x32_bf16 a[16:19], a[176:179], v[32:35], a[16:19]\n"
"s_barrier\n"
"v_exp_f32 v20, v20\n"
"v_mfma_f32_16x16x32_bf16 a[48:51], a[176:179], v[40:43], a[48:51]\n"
"ds_read_b128 a[156:159], v235 offset:20480\n"
"v_cvt_pk_bf16_f32 v53, v18, v19\n"
"v_mfma_f32_16x16x32_bf16 a[0:3], a[212:215], v[88:91], a[0:3]\n"
"s_add_u32 s62, s62, 16384\n"
"v_bfe_u32 v253, v228, 4, 4\n"
"v_mfma_f32_16x16x32_bf16 a[32:35], a[212:215], v[96:99], a[32:35]\n"
"v_subrev_u32 v253, s64, v253\n"
"v_mfma_f32_16x16x32_bf16 a[64:67], a[212:215], v[104:107], a[64:67]\n"
"v_lshrrev_b32 v254, 8, v228\n"
"v_mfma_f32_16x16x32_bf16 a[96:99], a[212:215], v[112:115], a[96:99]\n"
"v_mul_u32_u24 v252, 80, v253\n"
"v_mfma_f32_16x16x32_bf16 a[100:103], a[216:219], v[112:115], a[100:103]\n"
"v_add_lshl_u32 v252, v252, v254, 4\n"
"v_mfma_f32_16x16x32_bf16 a[68:71], a[216:219], v[104:107], a[68:71]\n"
"v_lshrrev_b32 v253, 1, v253\n"
"v_mfma_f32_16x16x32_bf16 a[36:39], a[216:219], v[96:99], a[36:39]\n"
"v_min_u32 v253, 1, v253\n"
"v_mfma_f32_16x16x32_bf16 a[4:7], a[216:219], v[88:91], a[4:7]\n"
"v_lshl_or_b32 v252, v253, 27, v252\n"
"v_mfma_f32_16x16x32_bf16 a[8:11], a[220:223], v[88:91], a[8:11]\n"
"v_and_b32 v252, 0xfffffff, v252\n"
"v_mfma_f32_16x16x32_bf16 a[40:43], a[220:223], v[96:99], a[40:43]\n"
"v_cmp_eq_u32 vcc, %17, v254\n"
"v_mfma_f32_16x16x32_bf16 a[72:75], a[220:223], v[104:107], a[72:75]\n"
"v_mov_b32 v253, 134217728\n"
"v_mfma_f32_16x16x32_bf16 a[104:107], a[220:223], v[112:115], a[104:107]\n"
"s_nop 0\n"
"v_mfma_f32_16x16x32_bf16 a[108:111], a[224:227], v[112:115], a[108:111]\n"
"v_cndmask_b32 v253, v253, v228, vcc\n"
"v_mfma_f32_16x16x32_bf16 a[76:79], a[224:227], v[104:107], a[76:79]\n"
"v_mfma_f32_16x16x32_bf16 a[44:47], a[224:227], v[96:99], a[44:47]\n"
"v_mfma_f32_16x16x32_bf16 a[12:15], a[224:227], v[88:91], a[12:15]\n"
"v_mfma_f32_16x16x32_bf16 a[80:83], a[176:179], v[48:51], a[80:83]\n"
"s_add_u32 m0, s60, 114688\n"
"v_exp_f32 v21, v21\n"
"v_mfma_f32_16x16x32_bf16 a[112:115], a[176:179], v[56:59], a[112:115]\n"
"buffer_load_dwordx4 v248, s[40:43], s62 offen offset:0 lds\n"
"v_mfma_f32_16x16x32_bf16 a[116:119], a[180:183], v[56:59], a[116:119]\n"
"v_exp_f32 v22, v22\n"
"v_mfma_f32_16x16x32_bf16 a[84:87], a[180:183], v[48:51], a[84:87]\n"
"ds_read_b128 a[152:155], v234 offset:20480\n"
"v_cvt_pk_bf16_f32 v54, v20, v21\n"
"v_mfma_f32_16x16x32_bf16 a[52:55], a[180:183], v[40:43], a[52:55]\n"
"v_exp_f32 v23, v23\n"
"v_mfma_f32_16x16x32_bf16 a[20:23], a[180:183], v[32:35], a[20:23]\n"
"buffer_load_dwordx4 v249, s[40:43], s62 offen offset:1024 lds\n"
"v_mfma_f32_16x16x32_bf16 a[24:27], a[184:187], v[32:35], a[24:27]\n"
"v_exp_f32 v24, v24\n"
"v_mfma_f32_16x16x32_bf16 a[56:59], a[184:187], v[40:43], a[56:59]\n"
"ds_read_b128 a[148:151], v233 offset:20480\n"
"v_cvt_pk_bf16_f32 v55, v22, v23\n"
"v_mfma_f32_16x16x32_bf16 a[88:91], a[184:187], v[48:51], a[88:91]\n"
"v_exp_f32 v25, v25\n"
"v_mfma_f32_16x16x32_bf16 a[120:123], a[184:187], v[56:59], a[120:123]\n"
"buffer_load_dwordx4 v248, s[40:43], s62 offen offset:2048 lds\n"
"v_mfma_f32_16x16x32_bf16 a[124:127], a[188:191], v[56:59], a[124:127]\n"
"v_exp_f32 v26, v26\n"
"v_mfma_f32_16x16x32_bf16 a[92:95], a[188:191], v[48:51], a[92:95]\n"
"ds_read_b128 a[144:147], v232 offset:20480\n"
"v_cvt_pk_bf16_f32 v60, v24, v25\n"
"v_mfma_f32_16x16x32_bf16 a[60:63], a[188:191], v[40:43], a[60:63]\n"
"s_waitcnt lgkmcnt(4)\n"
"v_exp_f32 v27, v27\n"
"v_mfma_f32_16x16x32_bf16 a[28:31], a[188:191], v[32:35], a[28:31]\n"
"buffer_load_dwordx4 v249, s[40:43], s62 offen offset:3072 lds\n"
"s_waitcnt vmcnt(8)\n"
"s_cmp_lt_u32 %18, 216\n"
"s_cselect_b32 s54, s54, 134217728\n"
"v_cmp_gt_u32 vcc, 64, v225\n"
"s_cbranch_vccnz L_ps3_%=\n"
"L_pd3_%=:\n"
"v_mfma_f32_16x16x32_bf16 v[0:3], a[128:131], v[128:131], v[64:67]\n"
"v_exp_f32 v28, v28\n"
"v_mfma_f32_16x16x32_bf16 v[0:3], a[132:135], v[132:135], v[0:3]\n"
"ds_read_b64_tr_b16 a[160:161], v236 offset:8192\n"
"v_cvt_pk_bf16_f32 v61, v26, v27\n"
"v_mfma_f32_16x16x32_bf16 v[0:3], a[136:139], v[136:139], v[0:3]\n"
"s_waitcnt lgkmcnt(3)\n"
"v_exp_f32 v29, v29\n"
"v_mfma_f32_16x16x32_bf16 v[0:3], a[140:143], v[140:143], v[0:3]\n"
"ds_read_b64_tr_b16 a[162:163], v236 offset:12288\n"
"buffer_load_dwordx4 v[216:219], v228, s[44:47], s55 offen offset:0 sc1\n"
"v_mfma_f32_16x16x32_bf16 v[4:7], a[156:159], v[140:143], v[64:67]\n"
"s_waitcnt lgkmcnt(2)\n"
"v_exp_f32 v30, v30\n"
"v_mfma_f32_16x16x32_bf16 v[4:7], a[152:155], v[136:139], v[4:7]\n"
"ds_read_b64_tr_b16 a[164:165], v237 offset:8192\n"
"v_cvt_pk_bf16_f32 v62, v28, v29\n"
"v_mfma_f32_16x16x32_bf16 v[4:7], a[148:151], v[132:135], v[4:7]\n"
"v_exp_f32 v31, v31\n"
"v_mfma_f32_16x16x32_bf16 v[4:7], a[144:147], v[128:131], v[4:7]\n"
"ds_read_b64_tr_b16 a[166:167], v237 offset:12288\n"
"buffer_load_dwordx4 a[224:227], v252, s[44:47], s56 offen offset:0 sc1\n"
"v_smfmac_f32_16x16x64_bf16 a[192:195], a[208:211], v[32:39], v250\n"
"buffer_load_dwordx4 v[220:223], v228, s[44:47], s55 offen offset:1024 sc1\n"
"v_mfma_f32_16x16x32_bf16 v[12:15], a[144:147], v[144:147], v[68:71]\n"
"v_exp_f32 v0, v0\n"
"v_mfma_f32_16x16x32_bf16 v[12:15], a[148:151], v[148:151], v[12:15]\n"
"ds_read_b64_tr_b16 a[168:169], v238 offset:8192\n"
"v_cvt_pk_bf16_f32 v63, v30, v31\n"
"v_mfma_f32_16x16x32_bf16 v[12:15], a[152:155], v[152:155], v[12:15]\n"
"v_exp_f32 v1, v1\n"
"v_mfma_f32_16x16x32_bf16 v[12:15], a[156:159], v[156:159], v[12:15]\n"
"ds_read_b64_tr_b16 a[170:171], v238 offset:12288\n"
"v_mfma_f32_16x16x32_bf16 v[8:11], a[140:143], v[156:159], v[68:71]\n"
"v_exp_f32 v2, v2\n"
"v_mfma_f32_16x16x32_bf16 v[8:11], a[136:139], v[152:155], v[8:11]\n"
"ds_read_b64_tr_b16 a[172:173], v239 offset:8192\n"
"v_cvt_pk_bf16_f32 v32, v0, v1\n"
"v_mfma_f32_16x16x32_bf16 v[8:11], a[132:135], v[148:151], v[8:11]\n"
"v_exp_f32 v3, v3\n"
"v_mfma_f32_16x16x32_bf16 v[8:11], a[128:131], v[144:147], v[8:11]\n"
"ds_read_b64_tr_b16 a[174:175], v239 offset:12288\n"
"v_smfmac_f32_16x16x64_bf16 a[196:199], a[208:211], v[40:47], v250\n"
"buffer_load_dwordx4 a[228:231], v252, s[44:47], s56 offen offset:64 sc1\n"
"v_mfma_f32_16x16x32_bf16 v[16:19], a[128:131], v[160:163], v[72:75]\n"
"v_exp_f32 v4, v4\n"
"v_mfma_f32_16x16x32_bf16 v[16:19], a[132:135], v[164:167], v[16:19]\n"
"ds_read_b64_tr_b16 a[176:177], v240 offset:8192\n"
"v_cvt_pk_bf16_f32 v33, v2, v3\n"
"v_mfma_f32_16x16x32_bf16 v[16:19], a[136:139], v[168:171], v[16:19]\n"
"v_exp_f32 v5, v5\n"
"v_mfma_f32_16x16x32_bf16 v[16:19], a[140:143], v[172:175], v[16:19]\n"
"ds_read_b64_tr_b16 a[178:179], v240 offset:12288\n"
"v_mfma_f32_16x16x32_bf16 v[20:23], a[156:159], v[172:175], v[72:75]\n"
"v_exp_f32 v6, v6\n"
"v_mfma_f32_16x16x32_bf16 v[20:23], a[152:155], v[168:171], v[20:23]\n"
"ds_read_b64_tr_b16 a[180:181], v241 offset:8192\n"
"v_cvt_pk_bf16_f32 v34, v4, v5\n"
"v_mfma_f32_16x16x32_bf16 v[20:23], a[148:151], v[164:167], v[20:23]\n"
"v_exp_f32 v7, v7\n"
"v_mfma_f32_16x16x32_bf16 v[20:23], a[144:147], v[160:163], v[20:23]\n"
"ds_read_b64_tr_b16 a[182:183], v241 offset:12288\n"
"v_smfmac_f32_16x16x64_bf16 a[200:203], a[208:211], v[48:55], v250\n"
"buffer_load_dwordx4 a[212:215], v228, s[44:47], s55 offen offset:2048 sc1\n"
"v_mfma_f32_16x16x32_bf16 v[28:31], a[144:147], v[176:179], v[76:79]\n"
"v_exp_f32 v8, v8\n"
"v_mfma_f32_16x16x32_bf16 v[28:31], a[148:151], v[180:183], v[28:31]\n"
"ds_read_b64_tr_b16 a[184:185], v242 offset:8192\n"
"v_cvt_pk_bf16_f32 v35, v6, v7\n"
"v_mfma_f32_16x16x32_bf16 v[28:31], a[152:155], v[184:187], v[28:31]\n"
"v_exp_f32 v9, v9\n"
"v_mfma_f32_16x16x32_bf16 v[28:31], a[156:159], v[188:191], v[28:31]\n"
"ds_read_b64_tr_b16 a[186:187], v242 offset:12288\n"
"v_mfma_f32_16x16x32_bf16 v[24:27], a[140:143], v[188:191], v[76:79]\n"
"v_exp_f32 v10, v10\n"
"v_mfma_f32_16x16x32_bf16 v[24:27], a[136:139], v[184:187], v[24:27]\n"
"ds_read_b64_tr_b16 a[188:189], v243 offset:8192\n"
"v_cvt_pk_bf16_f32 v40, v8, v9\n"
"v_mfma_f32_16x16x32_bf16 v[24:27], a[132:135], v[180:183], v[24:27]\n"
"s_waitcnt lgkmcnt(7)\n"
"v_exp_f32 v11, v11\n"
"v_mfma_f32_16x16x32_bf16 v[24:27], a[128:131], v[176:179], v[24:27]\n"
"ds_read_b64_tr_b16 a[190:191], v243 offset:12288\n"
"v_smfmac_f32_16x16x64_bf16 a[204:207], a[208:211], v[56:63], v250\n"
"buffer_load_dwordx4 a[232:235], v252, s[44:47], s56 offen offset:128 sc1\n"
"v_mfma_f32_16x16x32_bf16 a[0:3], a[160:163], v[36:39], a[0:3]\n"
"v_exp_f32 v12, v12\n"
"v_mfma_f32_16x16x32_bf16 a[32:35], a[160:163], v[44:47], a[32:35]\n"
"ds_read_b128 a[128:131], v232 offset:24576\n"
"v_cvt_pk_bf16_f32 v41, v10, v11\n"
"v_mfma_f32_16x16x32_bf16 a[64:67], a[160:163], v[52:55], a[64:67]\n"
"v_exp_f32 v13, v13\n"
"v_mfma_f32_16x16x32_bf16 a[96:99], a[160:163], v[60:63], a[96:99]\n"
"ds_read_b128 a[132:135], v233 offset:24576\n"
"v_mfma_f32_16x16x32_bf16 a[100:103], a[164:167], v[60:63], a[100:103]\n"
"v_exp_f32 v14, v14\n"
"v_mfma_f32_16x16x32_bf16 a[68:71], a[164:167], v[52:55], a[68:71]\n"
"ds_read_b128 a[136:139], v234 offset:24576\n"
"v_cvt_pk_bf16_f32 v42, v12, v13\n"
"v_mfma_f32_16x16x32_bf16 a[36:39], a[164:167], v[44:47], a[36:39]\n"
"v_exp_f32 v15, v15\n"
"v_mfma_f32_16x16x32_bf16 a[4:7], a[164:167], v[36:39], a[4:7]\n"
"ds_read_b128 a[140:143], v235 offset:24576\n"
"v_mfma_f32_16x16x32_bf16 a[8:11], a[168:171], v[36:39], a[8:11]\n"
"v_exp_f32 v16, v16\n"
"v_mfma_f32_16x16x32_bf16 a[40:43], a[168:171], v[44:47], a[40:43]\n"
"ds_read_b128 a[144:147], v232 offset:28672\n"
"v_cvt_pk_bf16_f32 v43, v14, v15\n"
"v_mfma_f32_16x16x32_bf16 a[72:75], a[168:171], v[52:55], a[72:75]\n"
"v_exp_f32 v17, v17\n"
"v_mfma_f32_16x16x32_bf16 a[104:107], a[168:171], v[60:63], a[104:107]\n"
"buffer_load_dwordx4 a[216:219], v228, s[44:47], s55 offen offset:3072 sc1\n"
"v_mfma_f32_16x16x32_bf16 a[108:111], a[172:175], v[60:63], a[108:111]\n"
"v_exp_f32 v18, v18\n"
"v_mfma_f32_16x16x32_bf16 a[76:79], a[172:175], v[52:55], a[76:79]\n"
"ds_read_b128 a[148:151], v233 offset:28672\n"
"v_cvt_pk_bf16_f32 v48, v16, v17\n"
"v_mfma_f32_16x16x32_bf16 a[44:47], a[172:175], v[44:47], a[44:47]\n"
"s_waitcnt lgkmcnt(6)\n"
"v_exp_f32 v19, v19\n"
"v_mfma_f32_16x16x32_bf16 a[12:15], a[172:175], v[36:39], a[12:15]\n"
"buffer_load_dwordx4 a[236:239], v252, s[44:47], s56 offen offset:192 sc1\n"
"v_mfma_f32_16x16x32_bf16 a[16:19], a[176:179], v[36:39], a[16:19]\n"
"v_exp_f32 v20, v20\n"
"v_mfma_f32_16x16x32_bf16 a[48:51], a[176:179], v[44:47], a[48:51]\n"
"ds_read_b128 a[152:155], v234 offset:28672\n"
"v_cvt_pk_bf16_f32 v49, v18, v19\n"
"v_mfma_f32_16x16x32_bf16 a[80:83], a[176:179], v[52:55], a[80:83]\n"
"s_add_u32 s55, s55, 4096\n"
"v_exp_f32 v21, v21\n"
"v_mfma_f32_16x16x32_bf16 a[112:115], a[176:179], v[60:63], a[112:115]\n"
"buffer_load_dwordx4 a[220:223], v228, s[44:47], s55 offen offset:0 sc1\n"
"v_mfma_f32_16x16x32_bf16 a[116:119], a[180:183], v[60:63], a[116:119]\n"
"v_exp_f32 v22, v22\n"
"v_mfma_f32_16x16x32_bf16 a[84:87], a[180:183], v[52:55], a[84:87]\n"
"ds_read_b128 a[156:159], v235 offset:28672\n"
"v_cvt_pk_bf16_f32 v50, v20, v21\n"
"v_mfma_f32_16x16x32_bf16 a[52:55], a[180:183], v[44:47], a[52:55]\n"
"v_exp_f32 v23, v23\n"
"v_mfma_f32_16x16x32_bf16 a[20:23], a[180:183], v[36:39], a[20:23]\n"
"buffer_load_dwordx4 a[240:243], v252, s[44:47], s56 offen offset:256 sc1\n"
"v_mfma_f32_16x16x32_bf16 a[24:27], a[184:187], v[36:39], a[24:27]\n"
"v_exp_f32 v24, v24\n"
"v_mfma_f32_16x16x32_bf16 a[56:59], a[184:187], v[44:47], a[56:59]\n"
"v_cvt_pk_bf16_f32 v51, v22, v23\n"
"v_mfma_f32_16x16x32_bf16 a[88:91], a[184:187], v[52:55], a[88:91]\n"
"s_add_u32 m0, s60, 150528\n"
"v_exp_f32 v25, v25\n"
"v_mfma_f32_16x16x32_bf16 a[120:123], a[184:187], v[60:63], a[120:123]\n"
"buffer_load_dwordx4 v228, s[44:47], s52 offen sc1 lds\n"
"v_mfma_f32_16x16x32_bf16 a[124:127], a[188:191], v[60:63], a[124:127]\n"
"s_add_u32 s62, s62, 16384\n"
"v_exp_f32 v26, v26\n"
"v_mfma_f32_16x16x32_bf16 a[92:95], a[188:191], v[52:55], a[92:95]\n"
"v_cvt_pk_bf16_f32 v56, v24, v25\n"
"v_mfma_f32_16x16x32_bf16 a[60:63], a[188:191], v[44:47], a[60:63]\n"
"s_waitcnt lgkmcnt(0)\n"
"v_exp_f32 v27, v27\n"
"v_mfma_f32_16x16x32_bf16 a[28:31], a[188:191], v[36:39], a[28:31]\n"
"s_barrier\n"
"v_mfma_f32_16x16x32_bf16 v[0:3], a[128:131], v[128:131], v[64:67]\n"
"v_exp_f32 v28, v28\n"
"v_mfma_f32_16x16x32_bf16 v[0:3], a[132:135], v[132:135], v[0:3]\n"
"ds_read_b64_tr_b16 a[160:161], v236 offset:16384\n"
"v_cvt_pk_bf16_f32 v57, v26, v27\n"
"v_mfma_f32_16x16x32_bf16 v[0:3], a[136:139], v[136:139], v[0:3]\n"
"s_add_u32 s34, s58, 0\n"
"v_exp_f32 v29, v29\n"
"v_mfma_f32_16x16x32_bf16 v[0:3], a[140:143], v[140:143], v[0:3]\n"
"ds_read_b64_tr_b16 a[162:163], v236 offset:20480\n"
"buffer_load_dwordx4 v[80:83], v228, s[44:47], s34 offen offset:0 sc1\n"
"v_mfma_f32_16x16x32_bf16 v[4:7], a[156:159], v[140:143], v[64:67]\n"
"L_w9_%=:\n"
"s_cbranch_execz L_wd3_%=\n"
"s_mov_b64 s[48:49], %1\n"
"v_exp_f32 v30, v30\n"
"v_mfma_f32_16x16x32_bf16 v[4:7], a[152:155], v[136:139], v[4:7]\n"
"ds_read_b64_tr_b16 a[164:165], v237 offset:16384\n"
"v_cvt_pk_bf16_f32 v58, v28, v29\n"
"v_mfma_f32_16x16x32_bf16 v[4:7], a[148:151], v[132:135], v[4:7]\n"
"s_and_b32 s49, s49, 0xffff\n"
"v_exp_f32 v31, v31\n"
"v_mfma_f32_16x16x32_bf16 v[4:7], a[144:147], v[128:131], v[4:7]\n"
"ds_read_b64_tr_b16 a[166:167], v237 offset:20480\n"
"buffer_load_dwordx4 v[84:87], v228, s[44:47], s34 offen offset:1024 sc1\n"
"v_mfma_f32_16x16x32_bf16 v[12:15], a[144:147], v[144:147], v[68:71]\n"
"s_lshl_b32 s55, %11, 2\n"
"v_exp_f32 v0, v0\n"
"v_mfma_f32_16x16x32_bf16 v[12:15], a[148:151], v[148:151], v[12:15]\n"
"ds_read_b64_tr_b16 a[168:169], v238 offset:16384\n"
"v_cvt_pk_bf16_f32 v59, v30, v31\n"
"v_mfma_f32_16x16x32_bf16 v[12:15], a[152:155], v[152:155], v[12:15]\n"
"s_add_u32 s55, s55, %17\n"
"v_exp_f32 v1, v1\n"
"v_mfma_f32_16x16x32_bf16 v[12:15], a[156:159], v[156:159], v[12:15]\n"
"ds_read_b64_tr_b16 a[170:171], v238 offset:20480\n"
"buffer_load_dwordx4 v[88:91], v228, s[44:47], s34 offen offset:2048 sc1\n"
"v_mfma_f32_16x16x32_bf16 v[8:11], a[140:143], v[156:159], v[68:71]\n"
"s_mul_i32 s55, s55, 20480\n"
"v_exp_f32 v2, v2\n"
"v_mfma_f32_16x16x32_bf16 v[8:11], a[136:139], v[152:155], v[8:11]\n"
"ds_read_b64_tr_b16 a[172:173], v239 offset:16384\n"
"v_cvt_pk_bf16_f32 v36, v0, v1\n"
"v_mfma_f32_16x16x32_bf16 v[8:11], a[132:135], v[148:151], v[8:11]\n"
"s_mul_i32 s56, %12, 160\n"
"v_exp_f32 v3, v3\n"
"v_mfma_f32_16x16x32_bf16 v[8:11], a[128:131], v[144:147], v[8:11]\n"
"ds_read_b64_tr_b16 a[174:175], v239 offset:20480\n"
"buffer_load_dwordx4 v[92:95], v228, s[44:47], s34 offen offset:3072 sc1\n"
"v_mfma_f32_16x16x32_bf16 v[16:19], a[128:131], v[160:163], v[72:75]\n"
"s_add_u32 s55, s55, s56\n"
"v_exp_f32 v4, v4\n"
"v_mfma_f32_16x16x32_bf16 v[16:19], a[132:135], v[164:167], v[16:19]\n"
"ds_read_b64_tr_b16 a[176:177], v240 offset:16384\n"
"v_cvt_pk_bf16_f32 v37, v2, v3\n"
"v_mfma_f32_16x16x32_bf16 v[16:19], a[136:139], v[168:171], v[16:19]\n"
"s_add_u32 s34, s58, 4096\n"
"v_exp_f32 v5, v5\n"
"v_mfma_f32_16x16x32_bf16 v[16:19], a[140:143], v[172:175], v[16:19]\n"
"ds_read_b64_tr_b16 a[178:179], v240 offset:20480\n"
"buffer_load_dwordx4 v[96:99], v228, s[44:47], s34 offen offset:0 sc1\n"
"v_mfma_f32_16x16x32_bf16 v[20:23], a[156:159], v[172:175], v[72:75]\n"
"v_exp_f32 v6, v6\n"
"v_mfma_f32_16x16x32_bf16 v[20:23], a[152:155], v[168:171], v[20:23]\n"
"ds_read_b64_tr_b16 a[180:181], v241 offset:16384\n"
"v_cvt_pk_bf16_f32 v38, v4, v5\n"
"v_mfma_f32_16x16x32_bf16 v[20:23], a[148:151], v[164:167], v[20:23]\n"
"v_exp_f32 v7, v7\n"
"v_mfma_f32_16x16x32_bf16 v[20:23], a[144:147], v[160:163], v[20:23]\n"
"ds_read_b64_tr_b16 a[182:183], v241 offset:20480\n"
"buffer_load_dwordx4 v[100:103], v228, s[44:47], s34 offen offset:1024 sc1\n"
"v_mfma_f32_16x16x32_bf16 v[28:31], a[144:147], v[176:179], v[76:79]\n"
"v_exp_f32 v8, v8\n"
"v_mfma_f32_16x16x32_bf16 v[28:31], a[148:151], v[180:183], v[28:31]\n"
"ds_read_b64_tr_b16 a[184:185], v242 offset:16384\n"
"v_cvt_pk_bf16_f32 v39, v6, v7\n"
"v_mfma_f32_16x16x32_bf16 v[28:31], a[152:155], v[184:187], v[28:31]\n"
"v_exp_f32 v9, v9\n"
"v_mfma_f32_16x16x32_bf16 v[28:31], a[156:159], v[188:191], v[28:31]\n"
"ds_read_b64_tr_b16 a[186:187], v242 offset:20480\n"
"buffer_load_dwordx4 v[104:107], v228, s[44:47], s34 offen offset:2048 sc1\n"
"v_mfma_f32_16x16x32_bf16 v[24:27], a[140:143], v[188:191], v[76:79]\n"
"v_exp_f32 v10, v10\n"
"v_mfma_f32_16x16x32_bf16 v[24:27], a[136:139], v[184:187], v[24:27]\n"
"ds_read_b64_tr_b16 a[188:189], v243 offset:16384\n"
"v_cvt_pk_bf16_f32 v44, v8, v9\n"
"v_mfma_f32_16x16x32_bf16 v[24:27], a[132:135], v[180:183], v[24:27]\n"
"s_waitcnt lgkmcnt(7)\n"
"v_exp_f32 v11, v11\n"
"v_mfma_f32_16x16x32_bf16 v[24:27], a[128:131], v[176:179], v[24:27]\n"
"ds_read_b64_tr_b16 a[190:191], v243 offset:20480\n"
"buffer_load_dwordx4 v[108:111], v228, s[44:47], s34 offen offset:3072 sc1\n"
"v_mfma_f32_16x16x32_bf16 a[0:3], a[160:163], v[32:35], a[0:3]\n"
"v_exp_f32 v12, v12\n"
"s_waitcnt vmcnt(9)\n"
"v_mfma_f32_16x16x32_bf16 v[224:227], a[224:227], v[216:219], 0\n"
"v_mfma_f32_16x16x32_bf16 v[224:227], a[228:231], v[220:223], v[224:227]\n"
"v_mfma_f32_16x16x32_bf16 v[224:227], a[232:235], a[212:215], v[224:227]\n"
"v_mfma_f32_16x16x32_bf16 v[224:227], a[236:239], a[216:219], v[224:227]\n"
"v_mfma_f32_16x16x32_bf16 v[224:227], a[240:243], a[220:223], v[224:227]\n"
"v_mfma_f32_16x16x32_bf16 a[32:35], a[160:163], v[40:43], a[32:35]\n"
"ds_read_b128 a[128:131], v232 offset:32768\n"
"v_cvt_pk_bf16_f32 v45, v10, v11\n"
"v_mfma_f32_16x16x32_bf16 a[64:67], a[160:163], v[48:51], a[64:67]\n"
"s_add_u32 s34, s58, 8192\n"
"v_exp_f32 v13, v13\n"
"v_mfma_f32_16x16x32_bf16 a[96:99], a[160:163], v[56:59], a[96:99]\n"
"buffer_load_dwordx4 v[112:115], v228, s[44:47], s34 offen offset:0 sc1\n"
"v_mfma_f32_16x16x32_bf16 a[100:103], a[164:167], v[56:59], a[100:103]\n"
"v_exp_f32 v14, v14\n"
"v_mfma_f32_16x16x32_bf16 a[68:71], a[164:167], v[48:51], a[68:71]\n"
"ds_read_b128 a[132:135], v233 offset:32768\n"
"v_cvt_pk_bf16_f32 v46, v12, v13\n"
"v_mfma_f32_16x16x32_bf16 a[36:39], a[164:167], v[40:43], a[36:39]\n"
"v_exp_f32 v15, v15\n"
"v_mfma_f32_16x16x32_bf16 a[4:7], a[164:167], v[32:35], a[4:7]\n"
"buffer_load_dwordx4 v[116:119], v228, s[44:47], s34 offen offset:1024 sc1\n"
"v_mfma_f32_16x16x32_bf16 a[8:11], a[168:171], v[32:35], a[8:11]\n"
"v_exp_f32 v16, v16\n"
"v_cvt_pk_bf16_f32 v213, v224, v225\n"
"v_mov_b32 v215, 0\n"
"v_lshlrev_b32 v226, 16, v213\n"
"v_and_b32 v227, 0xffff0000, v213\n"
"v_sub_f32 v224, v224, v226\n"
"v_sub_f32 v225, v225, v227\n"
"v_cvt_pk_bf16_f32 v212, v226, v224\n"
"v_cvt_pk_bf16_f32 v214, v225, v227\n"
"buffer_store_dwordx4 v[212:215], v253, s[44:47], s54 offen sc1\n"
"v_bfe_u32 v252, v228, 4, 4\n"
"v_mul_u32_u24 v252, 1280, v252\n"
"v_lshrrev_b32 v254, 8, v228\n"
"v_lshl_add_u32 v252, v254, 3, v252\n"
"v_mfma_f32_16x16x32_bf16 a[40:43], a[168:171], v[40:43], a[40:43]\n"
"ds_read_b128 a[136:139], v234 offset:32768\n"
"v_cvt_pk_bf16_f32 v47, v14, v15\n"
"v_mfma_f32_16x16x32_bf16 a[72:75], a[168:171], v[48:51], a[72:75]\n"
"v_exp_f32 v17, v17\n"
"v_mfma_f32_16x16x32_bf16 a[104:107], a[168:171], v[56:59], a[104:107]\n"
"buffer_load_dwordx4 v[120:123], v228, s[44:47], s34 offen offset:2048 sc1\n"
"v_mfma_f32_16x16x32_bf16 a[108:111], a[172:175], v[56:59], a[108:111]\n"
"v_exp_f32 v18, v18\n"
"v_mfma_f32_16x16x32_bf16 a[76:79], a[172:175], v[48:51], a[76:79]\n"
"ds_read_b128 a[140:143], v235 offset:32768\n"
"v_cvt_pk_bf16_f32 v52, v16, v17\n"
"v_mfma_f32_16x16x32_bf16 a[44:47], a[172:175], v[40:43], a[44:47]\n"
"s_waitcnt lgkmcnt(4)\n"
"v_exp_f32 v19, v19\n"
"v_mfma_f32_16x16x32_bf16 a[12:15], a[172:175], v[32:35], a[12:15]\n"
"buffer_load_dwordx4 v[124:127], v228, s[44:47], s34 offen offset:3072 sc1\n"
"v_mfma_f32_16x16x32_bf16 a[16:19], a[176:179], v[32:35], a[16:19]\n"
"v_exp_f32 v20, v20\n"
"v_mfma_f32_16x16x32_bf16 a[48:51], a[176:179], v[40:43], a[48:51]\n"
"ds_read_b128 a[156:159], v235 offset:36864\n"
"v_cvt_pk_bf16_f32 v53, v18, v19\n"
"v_mfma_f32_16x16x32_bf16 a[80:83], a[176:179], v[48:51], a[80:83]\n"
"s_add_u32 s34, s58, 12288\n"
"v_exp_f32 v21, v21\n"
"v_mfma_f32_16x16x32_bf16 a[112:115], a[176:179], v[56:59], a[112:115]\n"
"buffer_load_dwordx4 v[192:195], v228, s[44:47], s34 offen offset:0 sc1\n"
"v_mfma_f32_16x16x32_bf16 a[116:119], a[180:183], v[56:59], a[116:119]\n"
"v_exp_f32 v22, v22\n"
"v_mfma_f32_16x16x32_bf16 a[84:87], a[180:183], v[48:51], a[84:87]\n"
"ds_read_b128 a[152:155], v234 offset:36864\n"
"v_cvt_pk_bf16_f32 v54, v20, v21\n"
"v_mfma_f32_16x16x32_bf16 a[52:55], a[180:183], v[40:43], a[52:55]\n"
"v_exp_f32 v23, v23\n"
"v_mfma_f32_16x16x32_bf16 a[20:23], a[180:183], v[32:35], a[20:23]\n"
"buffer_load_dwordx4 v[196:199], v228, s[44:47], s34 offen offset:1024 sc1\n"
"v_mfma_f32_16x16x32_bf16 a[24:27], a[184:187], v[32:35], a[24:27]\n"
"v_exp_f32 v24, v24\n"
"v_mfma_f32_16x16x32_bf16 a[56:59], a[184:187], v[40:43], a[56:59]\n"
"ds_read_b128 a[148:151], v233 offset:36864\n"
"v_cvt_pk_bf16_f32 v55, v22, v23\n"
"v_mfma_f32_16x16x32_bf16 a[88:91], a[184:187], v[48:51], a[88:91]\n"
"v_exp_f32 v25, v25\n"
"v_mfma_f32_16x16x32_bf16 a[120:123], a[184:187], v[56:59], a[120:123]\n"
"buffer_load_dwordx4 v[200:203], v228, s[44:47], s34 offen offset:2048 sc1\n"
"v_mfma_f32_16x16x32_bf16 a[124:127], a[188:191], v[56:59], a[124:127]\n"
"v_exp_f32 v26, v26\n"
"v_mfma_f32_16x16x32_bf16 a[92:95], a[188:191], v[48:51], a[92:95]\n"
"ds_read_b128 a[144:147], v232 offset:36864\n"
"v_cvt_pk_bf16_f32 v60, v24, v25\n"
"v_mfma_f32_16x16x32_bf16 a[60:63], a[188:191], v[40:43], a[60:63]\n"
"s_waitcnt lgkmcnt(4)\n"
"v_exp_f32 v27, v27\n"
"v_mfma_f32_16x16x32_bf16 a[28:31], a[188:191], v[32:35], a[28:31]\n"
"buffer_load_dwordx4 v[204:207], v228, s[44:47], s34 offen offset:3072 sc1\n"
"v_mfma_f32_16x16x32_bf16 v[0:3], a[128:131], v[128:131], v[64:67]\n"
"s_add_u32 s34, s58, 16384\n"
"v_exp_f32 v28, v28\n"
"v_mfma_f32_16x16x32_bf16 v[0:3], a[132:135], v[132:135], v[0:3]\n"
"ds_read_b64_tr_b16 a[160:161], v236 offset:24576\n"
"v_cvt_pk_bf16_f32 v61, v26, v27\n"
"v_mfma_f32_16x16x32_bf16 v[0:3], a[136:139], v[136:139], v[0:3]\n"
"s_waitcnt lgkmcnt(3)\n"
"v_exp_f32 v29, v29\n"
"v_mfma_f32_16x16x32_bf16 v[0:3], a[140:143], v[140:143], v[0:3]\n"
"ds_read_b64_tr_b16 a[162:163], v236 offset:28672\n"
"buffer_load_dwordx4 v[208:211], v228, s[44:47], s34 offen offset:0 sc1\n"
"v_mfma_f32_16x16x32_bf16 v[4:7], a[156:159], v[140:143], v[64:67]\n"
"s_waitcnt lgkmcnt(2)\n"
"v_exp_f32 v30, v30\n"
"v_mfma_f32_16x16x32_bf16 v[4:7], a[152:155], v[136:139], v[4:7]\n"
"ds_read_b64_tr_b16 a[164:165], v237 offset:24576\n"
"v_cvt_pk_bf16_f32 v62, v28, v29\n"
"v_mfma_f32_16x16x32_bf16 v[4:7], a[148:151], v[132:135], v[4:7]\n"
"v_exp_f32 v31, v31\n"
"v_mfma_f32_16x16x32_bf16 v[4:7], a[144:147], v[128:131], v[4:7]\n"
"ds_read_b64_tr_b16 a[166:167], v237 offset:28672\n"
"buffer_load_dwordx4 v[212:215], v228, s[44:47], s34 offen offset:1024 sc1\n"
"v_smfmac_f32_16x16x64_bf16 a[192:195], a[208:211], v[32:39], v250\n"
"buffer_load_dwordx4 v[216:219], v228, s[44:47], s34 offen offset:2048 sc1\n"
"v_mfma_f32_16x16x32_bf16 v[12:15], a[144:147], v[144:147], v[68:71]\n"
"v_exp_f32 v0, v0\n"
"v_mfma_f32_16x16x32_bf16 v[12:15], a[148:151], v[148:151], v[12:15]\n"
"ds_read_b64_tr_b16 a[168:169], v238 offset:24576\n"
"v_cvt_pk_bf16_f32 v63, v30, v31\n"
"v_mfma_f32_16x16x32_bf16 v[12:15], a[152:155], v[152:155], v[12:15]\n"
"v_exp_f32 v1, v1\n"
"v_mfma_f32_16x16x32_bf16 v[12:15], a[156:159], v[156:159], v[12:15]\n"
"ds_read_b64_tr_b16 a[170:171], v238 offset:28672\n"
"buffer_load_dwordx4 v[220:223], v228, s[44:47], s34 offen offset:3072 sc1\n"
"v_mfma_f32_16x16x32_bf16 v[8:11], a[140:143], v[156:159], v[68:71]\n"
"v_exp_f32 v2, v2\n"
"v_mfma_f32_16x16x32_bf16 v[8:11], a[136:139], v[152:155], v[8:11]\n"
"ds_read_b64_tr_b16 a[172:173], v239 offset:24576\n"
"v_cvt_pk_bf16_f32 v32, v0, v1\n"
"v_mfma_f32_16x16x32_bf16 v[8:11], a[132:135], v[148:151], v[8:11]\n"
"s_add_u32 s34, s58, 20480\n"
"v_exp_f32 v3, v3\n"
"v_mfma_f32_16x16x32_bf16 v[8:11], a[128:131], v[144:147], v[8:11]\n"
"ds_read_b64_tr_b16 a[174:175], v239 offset:28672\n"
"v_smfmac_f32_16x16x64_bf16 a[196:199], a[208:211], v[40:47], v250\n"
"buffer_load_dwordx4 a[212:215], v228, s[44:47], s34 offen offset:0 sc1\n"
"v_mfma_f32_16x16x32_bf16 v[16:19], a[128:131], v[160:163], v[72:75]\n"
"v_exp_f32 v4, v4\n"
"v_mfma_f32_16x16x32_bf16 v[16:19], a[132:135], v[164:167], v[16:19]\n"
"ds_read_b64_tr_b16 a[176:177], v240 offset:24576\n"
"v_cvt_pk_bf16_f32 v33, v2, v3\n"
"v_mfma_f32_16x16x32_bf16 v[16:19], a[136:139], v[168:171], v[16:19]\n"
"v_exp_f32 v5, v5\n"
"v_mfma_f32_16x16x32_bf16 v[16:19], a[140:143], v[172:175], v[16:19]\n"
"ds_read_b64_tr_b16 a[178:179], v240 offset:28672\n"
"v_mfma_f32_16x16x32_bf16 v[20:23], a[156:159], v[172:175], v[72:75]\n"
"v_exp_f32 v6, v6\n"
"v_mfma_f32_16x16x32_bf16 v[20:23], a[152:155], v[168:171], v[20:23]\n"
"ds_read_b64_tr_b16 a[180:181], v241 offset:24576\n"
"v_cvt_pk_bf16_f32 v34, v4, v5\n"
"v_mfma_f32_16x16x32_bf16 v[20:23], a[148:151], v[164:167], v[20:23]\n"
"v_exp_f32 v7, v7\n"
"v_mfma_f32_16x16x32_bf16 v[20:23], a[144:147], v[160:163], v[20:23]\n"
"ds_read_b64_tr_b16 a[182:183], v241 offset:28672\n"
"v_smfmac_f32_16x16x64_bf16 a[200:203], a[208:211], v[48:55], v250\n"
"buffer_load_dwordx4 a[216:219], v228, s[44:47], s34 offen offset:1024 sc1\n"
"v_mfma_f32_16x16x32_bf16 v[28:31], a[144:147], v[176:179], v[76:79]\n"
"v_exp_f32 v8, v8\n"
"v_mfma_f32_16x16x32_bf16 v[28:31], a[148:151], v[180:183], v[28:31]\n"
"ds_read_b64_tr_b16 a[184:185], v242 offset:24576\n"
"v_cvt_pk_bf16_f32 v35, v6, v7\n"
"v_mfma_f32_16x16x32_bf16 v[28:31], a[152:155], v[184:187], v[28:31]\n"
"v_exp_f32 v9, v9\n"
"v_mfma_f32_16x16x32_bf16 v[28:31], a[156:159], v[188:191], v[28:31]\n"
"ds_read_b64_tr_b16 a[186:187], v242 offset:28672\n"
"v_mfma_f32_16x16x32_bf16 v[24:27], a[140:143], v[188:191], v[76:79]\n"
"v_exp_f32 v10, v10\n"
"v_mfma_f32_16x16x32_bf16 v[24:27], a[136:139], v[184:187], v[24:27]\n"
"ds_read_b64_tr_b16 a[188:189], v243 offset:24576\n"
"v_cvt_pk_bf16_f32 v40, v8, v9\n"
"v_mfma_f32_16x16x32_bf16 v[24:27], a[132:135], v[180:183], v[24:27]\n"
"s_waitcnt lgkmcnt(7)\n"
"v_exp_f32 v11, v11\n"
"v_mfma_f32_16x16x32_bf16 v[24:27], a[128:131], v[176:179], v[24:27]\n"
"ds_read_b64_tr_b16 a[190:191], v243 offset:28672\n"
"v_smfmac_f32_16x16x64_bf16 a[204:207], a[208:211], v[56:63], v250\n"
"buffer_load_dwordx4 a[220:223], v228, s[44:47], s34 offen offset:2048 sc1\n"
"v_mfma_f32_16x16x32_bf16 a[0:3], a[160:163], v[36:39], a[0:3]\n"
"v_exp_f32 v12, v12\n"
"v_mfma_f32_16x16x32_bf16 a[32:35], a[160:163], v[44:47], a[32:35]\n"
"ds_read_b128 a[128:131], v232 offset:40960\n"
"v_cvt_pk_bf16_f32 v41, v10, v11\n"
"v_mfma_f32_16x16x32_bf16 a[64:67], a[160:163], v[52:55], a[64:67]\n"
"v_exp_f32 v13, v13\n"
"v_mfma_f32_16x16x32_bf16 a[96:99], a[160:163], v[60:63], a[96:99]\n"
"ds_read_b128 a[132:135], v233 offset:40960\n"
"v_mfma_f32_16x16x32_bf16 a[100:103], a[164:167], v[60:63], a[100:103]\n"
"v_exp_f32 v14, v14\n"
"v_mfma_f32_16x16x32_bf16 a[68:71], a[164:167], v[52:55], a[68:71]\n"
"ds_read_b128 a[136:139], v234 offset:40960\n"
"v_cvt_pk_bf16_f32 v42, v12, v13\n"
"v_mfma_f32_16x16x32_bf16 a[36:39], a[164:167], v[44:47], a[36:39]\n"
"v_exp_f32 v15, v15\n"
"v_mfma_f32_16x16x32_bf16 a[4:7], a[164:167], v[36:39], a[4:7]\n"
"ds_read_b128 a[140:143], v235 offset:40960\n"
"v_mfma_f32_16x16x32_bf16 a[8:11], a[168:171], v[36:39], a[8:11]\n"
"v_exp_f32 v16, v16\n"
"v_mfma_f32_16x16x32_bf16 a[40:43], a[168:171], v[44:47], a[40:43]\n"
"ds_read_b128 a[144:147], v232 offset:45056\n"
"v_cvt_pk_bf16_f32 v43, v14, v15\n"
"v_mfma_f32_16x16x32_bf16 a[72:75], a[168:171], v[52:55], a[72:75]\n"
"v_exp_f32 v17, v17\n"
"v_mfma_f32_16x16x32_bf16 a[104:107], a[168:171], v[60:63], a[104:107]\n"
"buffer_load_dwordx4 a[224:227], v228, s[44:47], s34 offen offset:3072 sc1\n"
"v_mfma_f32_16x16x32_bf16 a[108:111], a[172:175], v[60:63], a[108:111]\n"
"s_add_u32 s34, s58, 24576\n"
"v_exp_f32 v18, v18\n"
"v_mfma_f32_16x16x32_bf16 a[76:79], a[172:175], v[52:55], a[76:79]\n"
"ds_read_b128 a[148:151], v233 offset:45056\n"
"v_cvt_pk_bf16_f32 v48, v16, v17\n"
"v_mfma_f32_16x16x32_bf16 a[44:47], a[172:175], v[44:47], a[44:47]\n"
"s_waitcnt lgkmcnt(6)\n"
"v_exp_f32 v19, v19\n"
"v_mfma_f32_16x16x32_bf16 a[12:15], a[172:175], v[36:39], a[12:15]\n"
"buffer_load_dwordx4 a[228:231], v228, s[44:47], s34 offen offset:0 sc1\n"
"v_mfma_f32_16x16x32_bf16 a[16:19], a[176:179], v[36:39], a[16:19]\n"
"v_exp_f32 v20, v20\n"
"v_mfma_f32_16x16x32_bf16 a[48:51], a[176:179], v[44:47], a[48:51]\n"
"ds_read_b128 a[152:155], v234 offset:45056\n"
"v_cvt_pk_bf16_f32 v49, v18, v19\n"
"v_mfma_f32_16x16x32_bf16 a[80:83], a[176:179], v[52:55], a[80:83]\n"
"v_exp_f32 v21, v21\n"
"v_mfma_f32_16x16x32_bf16 a[112:115], a[176:179], v[60:63], a[112:115]\n"
"buffer_load_dwordx4 a[232:235], v228, s[44:47], s34 offen offset:1024 sc1\n"
"v_mfma_f32_16x16x32_bf16 a[116:119], a[180:183], v[60:63], a[116:119]\n"
"v_exp_f32 v22, v22\n"
"v_mfma_f32_16x16x32_bf16 a[84:87], a[180:183], v[52:55], a[84:87]\n"
"ds_read_b128 a[156:159], v235 offset:45056\n"
"v_cvt_pk_bf16_f32 v50, v20, v21\n"
"v_mfma_f32_16x16x32_bf16 a[52:55], a[180:183], v[44:47], a[52:55]\n"
"v_exp_f32 v23, v23\n"
"v_mfma_f32_16x16x32_bf16 a[20:23], a[180:183], v[36:39], a[20:23]\n"
"buffer_load_dwordx4 a[236:239], v228, s[44:47], s34 offen offset:2048 sc1\n"
"v_mfma_f32_16x16x32_bf16 a[24:27], a[184:187], v[36:39], a[24:27]\n"
"v_exp_f32 v24, v24\n"
"v_mfma_f32_16x16x32_bf16 a[56:59], a[184:187], v[44:47], a[56:59]\n"
"v_cvt_pk_bf16_f32 v51, v22, v23\n"
"v_mfma_f32_16x16x32_bf16 a[88:91], a[184:187], v[52:55], a[88:91]\n"
"v_exp_f32 v25, v25\n"
"v_mfma_f32_16x16x32_bf16 a[120:123], a[184:187], v[60:63], a[120:123]\n"
"buffer_load_dwordx4 a[240:243], v228, s[44:47], s34 offen offset:3072 sc1\n"
"v_mfma_f32_16x16x32_bf16 a[124:127], a[188:191], v[60:63], a[124:127]\n"
"v_exp_f32 v26, v26\n"
"v_mfma_f32_16x16x32_bf16 a[92:95], a[188:191], v[52:55], a[92:95]\n"
"v_cvt_pk_bf16_f32 v56, v24, v25\n"
"v_mfma_f32_16x16x32_bf16 a[60:63], a[188:191], v[44:47], a[60:63]\n"
"s_waitcnt vmcnt(28) lgkmcnt(0)\n"
"v_exp_f32 v27, v27\n"
"v_mfma_f32_16x16x32_bf16 a[28:31], a[188:191], v[36:39], a[28:31]\n"
"s_barrier\n"
"v_mfma_f32_16x16x32_bf16 v[0:3], a[128:131], v[128:131], v[64:67]\n"
"s_add_u32 s34, s58, 28672\n"
"v_exp_f32 v28, v28\n"
"v_mfma_f32_16x16x32_bf16 v[0:3], a[132:135], v[132:135], v[0:3]\n"
"ds_read_b64_tr_b16 a[160:161], v236 offset:32768\n"
"v_cvt_pk_bf16_f32 v57, v26, v27\n"
"v_mfma_f32_16x16x32_bf16 v[0:3], a[136:139], v[136:139], v[0:3]\n"
"s_add_u32 m0, s60, 131072\n"
"v_exp_f32 v29, v29\n"
"v_mfma_f32_16x16x32_bf16 v[0:3], a[140:143], v[140:143], v[0:3]\n"
"ds_read_b64_tr_b16 a[162:163], v236 offset:36864\n"
"buffer_load_dwordx4 v228, s[44:47], s34 offen offset:0 sc1 lds\n"
"v_mfma_f32_16x16x32_bf16 v[4:7], a[156:159], v[140:143], v[64:67]\n"
"v_exp_f32 v30, v30\n"
"v_mfma_f32_16x16x32_bf16 v[4:7], a[152:155], v[136:139], v[4:7]\n"
"ds_read_b64_tr_b16 a[164:165], v237 offset:32768\n"
"v_cvt_pk_bf16_f32 v58, v28, v29\n"
"v_mfma_f32_16x16x32_bf16 v[4:7], a[148:151], v[132:135], v[4:7]\n"
"v_exp_f32 v31, v31\n"
"v_mfma_f32_16x16x32_bf16 v[4:7], a[144:147], v[128:131], v[4:7]\n"
"ds_read_b64_tr_b16 a[166:167], v237 offset:36864\n"
"buffer_load_dwordx4 v228, s[44:47], s34 offen offset:1024 sc1 lds\n"
"v_mfma_f32_16x16x32_bf16 v[12:15], a[144:147], v[144:147], v[68:71]\n"
"v_exp_f32 v0, v0\n"
"v_mfma_f32_16x16x32_bf16 v[12:15], a[148:151], v[148:151], v[12:15]\n"
"ds_read_b64_tr_b16 a[168:169], v238 offset:32768\n"
"v_cvt_pk_bf16_f32 v59, v30, v31\n"
"v_mfma_f32_16x16x32_bf16 v[12:15], a[152:155], v[152:155], v[12:15]\n"
"v_exp_f32 v1, v1\n"
"v_mfma_f32_16x16x32_bf16 v[12:15], a[156:159], v[156:159], v[12:15]\n"
"ds_read_b64_tr_b16 a[170:171], v238 offset:36864\n"
"buffer_load_dwordx4 v228, s[44:47], s34 offen offset:2048 sc1 lds\n"
"v_mfma_f32_16x16x32_bf16 v[8:11], a[140:143], v[156:159], v[68:71]\n"
"v_exp_f32 v2, v2\n"
"v_mfma_f32_16x16x32_bf16 v[8:11], a[136:139], v[152:155], v[8:11]\n"
"ds_read_b64_tr_b16 a[172:173], v239 offset:32768\n"
"v_cvt_pk_bf16_f32 v36, v0, v1\n"
"v_mfma_f32_16x16x32_bf16 v[8:11], a[132:135], v[148:151], v[8:11]\n"
"v_exp_f32 v3, v3\n"
"v_mfma_f32_16x16x32_bf16 v[8:11], a[128:131], v[144:147], v[8:11]\n"
"ds_read_b64_tr_b16 a[174:175], v239 offset:36864\n"
"buffer_load_dwordx4 v228, s[44:47], s34 offen offset:3072 sc1 lds\n"
"v_mfma_f32_16x16x32_bf16 v[16:19], a[128:131], v[160:163], v[72:75]\n"
"s_add_u32 s34, s58, 32768\n"
"v_exp_f32 v4, v4\n"
"v_mfma_f32_16x16x32_bf16 v[16:19], a[132:135], v[164:167], v[16:19]\n"
"ds_read_b64_tr_b16 a[176:177], v240 offset:32768\n"
"v_cvt_pk_bf16_f32 v37, v2, v3\n"
"v_mfma_f32_16x16x32_bf16 v[16:19], a[136:139], v[168:171], v[16:19]\n"
"s_add_u32 m0, s60, 147456\n"
"v_exp_f32 v5, v5\n"
"v_mfma_f32_16x16x32_bf16 v[16:19], a[140:143], v[172:175], v[16:19]\n"
"ds_read_b64_tr_b16 a[178:179], v240 offset:36864\n"
"buffer_load_dwordx4 v228, s[44:47], s34 offen offset:0 sc1 lds\n"
"v_mfma_f32_16x16x32_bf16 v[20:23], a[156:159], v[172:175], v[72:75]\n"
"v_exp_f32 v6, v6\n"
"v_mfma_f32_16x16x32_bf16 v[20:23], a[152:155], v[168:171], v[20:23]\n"
"ds_read_b64_tr_b16 a[180:181], v241 offset:32768\n"
"v_cvt_pk_bf16_f32 v38, v4, v5\n"
"v_mfma_f32_16x16x32_bf16 v[20:23], a[148:151], v[164:167], v[20:23]\n"
"v_exp_f32 v7, v7\n"
"v_mfma_f32_16x16x32_bf16 v[20:23], a[144:147], v[160:163], v[20:23]\n"
"ds_read_b64_tr_b16 a[182:183], v241 offset:36864\n"
"buffer_load_dwordx4 v228, s[44:47], s34 offen offset:1024 sc1 lds\n"
"v_mfma_f32_16x16x32_bf16 v[28:31], a[144:147], v[176:179], v[76:79]\n"
"v_exp_f32 v8, v8\n"
"v_mfma_f32_16x16x32_bf16 v[28:31], a[148:151], v[180:183], v[28:31]\n"
"ds_read_b64_tr_b16 a[184:185], v242 offset:32768\n"
"v_cvt_pk_bf16_f32 v39, v6, v7\n"
"v_mfma_f32_16x16x32_bf16 v[28:31], a[152:155], v[184:187], v[28:31]\n"
"v_exp_f32 v9, v9\n"
"v_mfma_f32_16x16x32_bf16 v[28:31], a[156:159], v[188:191], v[28:31]\n"
"ds_read_b64_tr_b16 a[186:187], v242 offset:36864\n"
"buffer_load_dwordx4 v228, s[44:47], s34 offen offset:2048 sc1 lds\n"
"v_mfma_f32_16x16x32_bf16 v[24:27], a[140:143], v[188:191], v[76:79]\n"
"s_add_u32 m0, s60, 0\n"
"s_add_u32 s34, s58, 35840\n"
"v_exp_f32 v10, v10\n"
"v_mfma_f32_16x16x32_bf16 v[24:27], a[136:139], v[184:187], v[24:27]\n"
"ds_read_b64_tr_b16 a[188:189], v243 offset:32768\n"
"v_cvt_pk_bf16_f32 v44, v8, v9\n"
"v_mfma_f32_16x16x32_bf16 v[24:27], a[132:135], v[180:183], v[24:27]\n"
"s_waitcnt lgkmcnt(7)\n"
"v_exp_f32 v11, v11\n"
"v_mfma_f32_16x16x32_bf16 v[24:27], a[128:131], v[176:179], v[24:27]\n"
"ds_read_b64_tr_b16 a[190:191], v243 offset:36864\n"
"buffer_load_dwordx4 v228, s[44:47], s34 offen offset:0 sc1 lds\n"
"v_mfma_f32_16x16x32_bf16 a[0:3], a[160:163], v[32:35], a[0:3]\n"
"v_exp_f32 v12, v12\n"
"v_mfma_f32_16x16x32_bf16 a[32:35], a[160:163], v[40:43], a[32:35]\n"
"ds_read_b128 a[128:131], v232 offset:49152\n"
"v_cvt_pk_bf16_f32 v45, v10, v11\n"
"v_mfma_f32_16x16x32_bf16 a[64:67], a[160:163], v[48:51], a[64:67]\n"
"v_exp_f32 v13, v13\n"
"v_mfma_f32_16x16x32_bf16 a[96:99], a[160:163], v[56:59], a[96:99]\n"
"buffer_load_dwordx4 v228, s[44:47], s34 offen offset:1024 sc1 lds\n"
"v_mfma_f32_16x16x32_bf16 a[100:103], a[164:167], v[56:59], a[100:103]\n"
"v_exp_f32 v14, v14\n"
"v_mfma_f32_16x16x32_bf16 a[68:71], a[164:167], v[48:51], a[68:71]\n"
"ds_read_b128 a[132:135], v233 offset:49152\n"
"v_cvt_pk_bf16_f32 v46, v12, v13\n"
"v_mfma_f32_16x16x32_bf16 a[36:39], a[164:167], v[40:43], a[36:39]\n"
"v_exp_f32 v15, v15\n"
"v_mfma_f32_16x16x32_bf16 a[4:7], a[164:167], v[32:35], a[4:7]\n"
"buffer_load_dwordx4 v228, s[44:47], s34 offen offset:2048 sc1 lds\n"
"v_mfma_f32_16x16x32_bf16 a[8:11], a[168:171], v[32:35], a[8:11]\n"
"v_exp_f32 v16, v16\n"
"v_mfma_f32_16x16x32_bf16 a[40:43], a[168:171], v[40:43], a[40:43]\n"
"ds_read_b128 a[136:139], v234 offset:49152\n"
"v_cvt_pk_bf16_f32 v47, v14, v15\n"
"v_mfma_f32_16x16x32_bf16 a[72:75], a[168:171], v[48:51], a[72:75]\n"
"v_exp_f32 v17, v17\n"
"v_mfma_f32_16x16x32_bf16 a[104:107], a[168:171], v[56:59], a[104:107]\n"
"buffer_load_dwordx4 v228, s[44:47], s34 offen offset:3072 sc1 lds\n"
"v_mfma_f32_16x16x32_bf16 a[108:111], a[172:175], v[56:59], a[108:111]\n"
"s_add_u32 m0, s60, 16384\n"
"s_add_u32 s34, s58, 39936\n"
"v_exp_f32 v18, v18\n"
"v_mfma_f32_16x16x32_bf16 a[76:79], a[172:175], v[48:51], a[76:79]\n"
"ds_read_b128 a[140:143], v235 offset:49152\n"
"v_cvt_pk_bf16_f32 v52, v16, v17\n"
"v_mfma_f32_16x16x32_bf16 a[44:47], a[172:175], v[40:43], a[44:47]\n"
"s_waitcnt lgkmcnt(4)\n"
"v_exp_f32 v19, v19\n"
"v_mfma_f32_16x16x32_bf16 a[12:15], a[172:175], v[32:35], a[12:15]\n"
"buffer_load_dwordx4 v228, s[44:47], s34 offen offset:0 sc1 lds\n"
"v_mfma_f32_16x16x32_bf16 a[16:19], a[176:179], v[32:35], a[16:19]\n"
"v_exp_f32 v20, v20\n"
"v_mfma_f32_16x16x32_bf16 a[48:51], a[176:179], v[40:43], a[48:51]\n"
"ds_read_b128 a[156:159], v235 offset:53248\n"
"v_cvt_pk_bf16_f32 v53, v18, v19\n"
"v_mfma_f32_16x16x32_bf16 a[80:83], a[176:179], v[48:51], a[80:83]\n"
"v_exp_f32 v21, v21\n"
"v_mfma_f32_16x16x32_bf16 a[112:115], a[176:179], v[56:59], a[112:115]\n"
"buffer_load_dwordx4 v228, s[44:47], s34 offen offset:1024 sc1 lds\n"
"v_mfma_f32_16x16x32_bf16 a[116:119], a[180:183], v[56:59], a[116:119]\n"
"v_exp_f32 v22, v22\n"
"v_mfma_f32_16x16x32_bf16 a[84:87], a[180:183], v[48:51], a[84:87]\n"
"ds_read_b128 a[152:155], v234 offset:53248\n"
"v_cvt_pk_bf16_f32 v54, v20, v21\n"
"v_mfma_f32_16x16x32_bf16 a[52:55], a[180:183], v[40:43], a[52:55]\n"
"v_exp_f32 v23, v23\n"
"v_mfma_f32_16x16x32_bf16 a[20:23], a[180:183], v[32:35], a[20:23]\n"
"buffer_load_dwordx4 v228, s[44:47], s34 offen offset:2048 sc1 lds\n"
"v_mfma_f32_16x16x32_bf16 a[24:27], a[184:187], v[32:35], a[24:27]\n"
"v_exp_f32 v24, v24\n"
"v_mfma_f32_16x16x32_bf16 a[56:59], a[184:187], v[40:43], a[56:59]\n"
"ds_read_b128 a[148:151], v233 offset:53248\n"
"v_cvt_pk_bf16_f32 v55, v22, v23\n"
"v_mfma_f32_16x16x32_bf16 a[88:91], a[184:187], v[48:51], a[88:91]\n"
"v_exp_f32 v25, v25\n"
"v_mfma_f32_16x16x32_bf16 a[120:123], a[184:187], v[56:59], a[120:123]\n"
"buffer_load_dwordx4 v228, s[44:47], s34 offen offset:3072 sc1 lds\n"
"v_mfma_f32_16x16x32_bf16 a[124:127], a[188:191], v[56:59], a[124:127]\n"
"s_add_u32 m0, s60, 65536\n"
"s_add_u32 s34, s58, 44032\n"
"v_exp_f32 v26, v26\n"
"v_mfma_f32_16x16x32_bf16 a[92:95], a[188:191], v[48:51], a[92:95]\n"
"ds_read_b128 a[144:147], v232 offset:53248\n"
"v_cvt_pk_bf16_f32 v60, v24, v25\n"
"v_mfma_f32_16x16x32_bf16 a[60:63], a[188:191], v[40:43], a[60:63]\n"
"s_waitcnt lgkmcnt(4)\n"
"v_exp_f32 v27, v27\n"
"v_mfma_f32_16x16x32_bf16 a[28:31], a[188:191], v[32:35], a[28:31]\n"
"buffer_load_dwordx4 v228, s[44:47], s34 offen offset:0 sc1 lds\n"
"v_mfma_f32_16x16x32_bf16 v[0:3], a[128:131], v[128:131], v[64:67]\n"
"v_exp_f32 v28, v28\n"
"v_mfma_f32_16x16x32_bf16 v[0:3], a[132:135], v[132:135], v[0:3]\n"
"ds_read_b64_tr_b16 a[160:161], v236 offset:40960\n"
"v_cvt_pk_bf16_f32 v61, v26, v27\n"
"v_mfma_f32_16x16x32_bf16 v[0:3], a[136:139], v[136:139], v[0:3]\n"
"s_waitcnt lgkmcnt(3)\n"
"v_exp_f32 v29, v29\n"
"v_mfma_f32_16x16x32_bf16 v[0:3], a[140:143], v[140:143], v[0:3]\n"
"ds_read_b64_tr_b16 a[162:163], v236 offset:45056\n"
"buffer_load_dwordx4 v228, s[44:47], s34 offen offset:1024 sc1 lds\n"
"v_mfma_f32_16x16x32_bf16 v[4:7], a[156:159], v[140:143], v[64:67]\n"
"s_waitcnt lgkmcnt(2)\n"
"v_exp_f32 v30, v30\n"
"v_mfma_f32_16x16x32_bf16 v[4:7], a[152:155], v[136:139], v[4:7]\n"
"ds_read_b64_tr_b16 a[164:165], v237 offset:40960\n"
"v_cvt_pk_bf16_f32 v62, v28, v29\n"
"v_mfma_f32_16x16x32_bf16 v[4:7], a[148:151], v[132:135], v[4:7]\n"
"v_exp_f32 v31, v31\n"
"v_mfma_f32_16x16x32_bf16 v[4:7], a[144:147], v[128:131], v[4:7]\n"
"ds_read_b64_tr_b16 a[166:167], v237 offset:45056\n"
"buffer_load_dwordx4 v228, s[44:47], s34 offen offset:2048 sc1 lds\n"
"v_smfmac_f32_16x16x64_bf16 a[192:195], a[208:211], v[32:39], v250\n"
"buffer_load_dwordx4 v228, s[44:47], s34 offen offset:3072 sc1 lds\n"
"v_mfma_f32_16x16x32_bf16 v[12:15], a[144:147], v[144:147], v[68:71]\n"
"v_exp_f32 v0, v0\n"
"v_mfma_f32_16x16x32_bf16 v[12:15], a[148:151], v[148:151], v[12:15]\n"
"ds_read_b64_tr_b16 a[168:169], v238 offset:40960\n"
"v_cvt_pk_bf16_f32 v63, v30, v31\n"
"v_mfma_f32_16x16x32_bf16 v[12:15], a[152:155], v[152:155], v[12:15]\n"
"v_exp_f32 v1, v1\n"
"v_mfma_f32_16x16x32_bf16 v[12:15], a[156:159], v[156:159], v[12:15]\n"
"ds_read_b64_tr_b16 a[170:171], v238 offset:45056\n"
"v_mfma_f32_16x16x32_bf16 v[8:11], a[140:143], v[156:159], v[68:71]\n"
"s_add_u32 s34, s58, 48128\n"
"v_exp_f32 v2, v2\n"
"v_mfma_f32_16x16x32_bf16 v[8:11], a[136:139], v[152:155], v[8:11]\n"
"ds_read_b64_tr_b16 a[172:173], v239 offset:40960\n"
"v_cvt_pk_bf16_f32 v32, v0, v1\n"
"v_mfma_f32_16x16x32_bf16 v[8:11], a[132:135], v[148:151], v[8:11]\n"
"s_add_u32 m0, s60, 32768\n"
"v_exp_f32 v3, v3\n"
"v_mfma_f32_16x16x32_bf16 v[8:11], a[128:131], v[144:147], v[8:11]\n"
"ds_read_b64_tr_b16 a[174:175], v239 offset:45056\n"
"v_smfmac_f32_16x16x64_bf16 a[196:199], a[208:211], v[40:47], v250\n"
"buffer_load_dwordx4 v228, s[44:47], s34 offen offset:0 sc1 lds\n"
"v_mfma_f32_16x16x32_bf16 v[16:19], a[128:131], v[160:163], v[72:75]\n"
"v_exp_f32 v4, v4\n"
"v_mfma_f32_16x16x32_bf16 v[16:19], a[132:135], v[164:167], v[16:19]\n"
"ds_read_b64_tr_b16 a[176:177], v240 offset:40960\n"
"v_cvt_pk_bf16_f32 v33, v2, v3\n"
"v_mfma_f32_16x16x32_bf16 v[16:19], a[136:139], v[168:171], v[16:19]\n"
"v_exp_f32 v5, v5\n"
"v_mfma_f32_16x16x32_bf16 v[16:19], a[140:143], v[172:175], v[16:19]\n"
"ds_read_b64_tr_b16 a[178:179], v240 offset:45056\n"
"v_mfma_f32_16x16x32_bf16 v[20:23], a[156:159], v[172:175], v[72:75]\n"
"v_exp_f32 v6, v6\n"
"v_mfma_f32_16x16x32_bf16 v[20:23], a[152:155], v[168:171], v[20:23]\n"
"ds_read_b64_tr_b16 a[180:181], v241 offset:40960\n"
"v_cvt_pk_bf16_f32 v34, v4, v5\n"
"v_mfma_f32_16x16x32_bf16 v[20:23], a[148:151], v[164:167], v[20:23]\n"
"v_exp_f32 v7, v7\n"
"v_mfma_f32_16x16x32_bf16 v[20:23], a[144:147], v[160:163], v[20:23]\n"
"ds_read_b64_tr_b16 a[182:183], v241 offset:45056\n"
"v_smfmac_f32_16x16x64_bf16 a[200:203], a[208:211], v[48:55], v250\n"
"buffer_load_dwordx4 v228, s[44:47], s34 offen offset:1024 sc1 lds\n"
"v_mfma_f32_16x16x32_bf16 v[28:31], a[144:147], v[176:179], v[76:79]\n"
"v_exp_f32 v8, v8\n"
"v_mfma_f32_16x16x32_bf16 v[28:31], a[148:151], v[180:183], v[28:31]\n"
"ds_read_b64_tr_b16 a[184:185], v242 offset:40960\n"
"v_cvt_pk_bf16_f32 v35, v6, v7\n"
"v_mfma_f32_16x16x32_bf16 v[28:31], a[152:155], v[184:187], v[28:31]\n"
"v_exp_f32 v9, v9\n"
"v_mfma_f32_16x16x32_bf16 v[28:31], a[156:159], v[188:191], v[28:31]\n"
"ds_read_b64_tr_b16 a[186:187], v242 offset:45056\n"
"v_mfma_f32_16x16x32_bf16 v[24:27], a[140:143], v[188:191], v[76:79]\n"
"v_exp_f32 v10, v10\n"
"v_mfma_f32_16x16x32_bf16 v[24:27], a[136:139], v[184:187], v[24:27]\n"
"L_w10_%=:\n"
"s_cbranch_execz L_wd3_%=\n"
"ds_read_b64_tr_b16 a[188:189], v243 offset:40960\n"
"v_cvt_pk_bf16_f32 v40, v8, v9\n"
"v_mfma_f32_16x16x32_bf16 v[24:27], a[132:135], v[180:183], v[24:27]\n"
"s_waitcnt lgkmcnt(7)\n"
"v_exp_f32 v11, v11\n"
"v_mfma_f32_16x16x32_bf16 v[24:27], a[128:131], v[176:179], v[24:27]\n"
"ds_read_b64_tr_b16 a[190:191], v243 offset:45056\n"
"v_smfmac_f32_16x16x64_bf16 a[204:207], a[208:211], v[56:63], v250\n"
"buffer_load_dwordx4 v228, s[44:47], s34 offen offset:2048 sc1 lds\n"
"v_mfma_f32_16x16x32_bf16 a[0:3], a[160:163], v[36:39], a[0:3]\n"
"v_exp_f32 v12, v12\n"
"v_mfma_f32_16x16x32_bf16 a[32:35], a[160:163], v[44:47], a[32:35]\n"
"ds_read_b128 a[128:131], v232 offset:57344\n"
"v_cvt_pk_bf16_f32 v41, v10, v11\n"
"v_mfma_f32_16x16x32_bf16 a[64:67], a[160:163], v[52:55], a[64:67]\n"
"v_exp_f32 v13, v13\n"
"v_mfma_f32_16x16x32_bf16 a[96:99], a[160:163], v[60:63], a[96:99]\n"
"ds_read_b128 a[132:135], v233 offset:57344\n"
"v_mfma_f32_16x16x32_bf16 a[100:103], a[164:167], v[60:63], a[100:103]\n"
"v_exp_f32 v14, v14\n"
"v_mfma_f32_16x16x32_bf16 a[68:71], a[164:167], v[52:55], a[68:71]\n"
"ds_read_b128 a[136:139], v234 offset:57344\n"
"v_cvt_pk_bf16_f32 v42, v12, v13\n"
"v_mfma_f32_16x16x32_bf16 a[36:39], a[164:167], v[44:47], a[36:39]\n"
"v_exp_f32 v15, v15\n"
"v_mfma_f32_16x16x32_bf16 a[4:7], a[164:167], v[36:39], a[4:7]\n"
"ds_read_b128 a[140:143], v235 offset:57344\n"
"v_mfma_f32_16x16x32_bf16 a[8:11], a[168:171], v[36:39], a[8:11]\n"
"v_exp_f32 v16, v16\n"
"v_mfma_f32_16x16x32_bf16 a[40:43], a[168:171], v[44:47], a[40:43]\n"
"ds_read_b128 a[144:147], v232 offset:61440\n"
"v_cvt_pk_bf16_f32 v43, v14, v15\n"
"v_mfma_f32_16x16x32_bf16 a[72:75], a[168:171], v[52:55], a[72:75]\n"
"v_exp_f32 v17, v17\n"
"v_mfma_f32_16x16x32_bf16 a[104:107], a[168:171], v[60:63], a[104:107]\n"
"buffer_load_dwordx4 v228, s[44:47], s34 offen offset:3072 sc1 lds\n"
"v_mfma_f32_16x16x32_bf16 a[108:111], a[172:175], v[60:63], a[108:111]\n"
"s_add_u32 m0, s60, 81920\n"
"s_add_u32 s34, s58, 52224\n"
"v_exp_f32 v18, v18\n"
"v_mfma_f32_16x16x32_bf16 a[76:79], a[172:175], v[52:55], a[76:79]\n"
"ds_read_b128 a[148:151], v233 offset:61440\n"
"v_cvt_pk_bf16_f32 v48, v16, v17\n"
"v_mfma_f32_16x16x32_bf16 a[44:47], a[172:175], v[44:47], a[44:47]\n"
"s_waitcnt lgkmcnt(6)\n"
"v_exp_f32 v19, v19\n"
"v_mfma_f32_16x16x32_bf16 a[12:15], a[172:175], v[36:39], a[12:15]\n"
"buffer_load_dwordx4 v228, s[44:47], s34 offen offset:0 sc1 lds\n"
"v_mfma_f32_16x16x32_bf16 a[16:19], a[176:179], v[36:39], a[16:19]\n"
"v_exp_f32 v20, v20\n"
"v_mfma_f32_16x16x32_bf16 a[48:51], a[176:179], v[44:47], a[48:51]\n"
"ds_read_b128 a[152:155], v234 offset:61440\n"
"v_cvt_pk_bf16_f32 v49, v18, v19\n"
"v_mfma_f32_16x16x32_bf16 a[80:83], a[176:179], v[52:55], a[80:83]\n"
"v_exp_f32 v21, v21\n"
"v_mfma_f32_16x16x32_bf16 a[112:115], a[176:179], v[60:63], a[112:115]\n"
"buffer_load_dwordx4 v228, s[44:47], s34 offen offset:1024 sc1 lds\n"
"v_mfma_f32_16x16x32_bf16 a[116:119], a[180:183], v[60:63], a[116:119]\n"
"v_exp_f32 v22, v22\n"
"v_mfma_f32_16x16x32_bf16 a[84:87], a[180:183], v[52:55], a[84:87]\n"
"ds_read_b128 a[156:159], v235 offset:61440\n"
"v_cvt_pk_bf16_f32 v50, v20, v21\n"
"v_mfma_f32_16x16x32_bf16 a[52:55], a[180:183], v[44:47], a[52:55]\n"
"v_exp_f32 v23, v23\n"
"v_mfma_f32_16x16x32_bf16 a[20:23], a[180:183], v[36:39], a[20:23]\n"
"buffer_load_dwordx4 v228, s[44:47], s34 offen offset:2048 sc1 lds\n"
"v_mfma_f32_16x16x32_bf16 a[24:27], a[184:187], v[36:39], a[24:27]\n"
"v_exp_f32 v24, v24\n"
"v_mfma_f32_16x16x32_bf16 a[56:59], a[184:187], v[44:47], a[56:59]\n"
"v_cvt_pk_bf16_f32 v51, v22, v23\n"
"v_mfma_f32_16x16x32_bf16 a[88:91], a[184:187], v[52:55], a[88:91]\n"
"v_exp_f32 v25, v25\n"
"v_mfma_f32_16x16x32_bf16 a[120:123], a[184:187], v[60:63], a[120:123]\n"
"buffer_load_dwordx4 v228, s[44:47], s34 offen offset:3072 sc1 lds\n"
"v_mfma_f32_16x16x32_bf16 a[124:127], a[188:191], v[60:63], a[124:127]\n"
"v_exp_f32 v26, v26\n"
"v_mfma_f32_16x16x32_bf16 a[92:95], a[188:191], v[52:55], a[92:95]\n"
"v_cvt_pk_bf16_f32 v56, v24, v25\n"
"v_mfma_f32_16x16x32_bf16 a[60:63], a[188:191], v[44:47], a[60:63]\n"
"s_waitcnt vmcnt(27) lgkmcnt(0)\n"
"v_exp_f32 v27, v27\n"
"v_mfma_f32_16x16x32_bf16 a[28:31], a[188:191], v[36:39], a[28:31]\n"
"s_barrier\n"
"v_mfma_f32_16x16x32_bf16 v[0:3], a[128:131], v[128:131], v[64:67]\n"
"s_add_u32 s34, s58, 56320\n"
"v_exp_f32 v28, v28\n"
"v_mfma_f32_16x16x32_bf16 v[0:3], a[132:135], v[132:135], v[0:3]\n"
"ds_read_b64_tr_b16 a[160:161], v236 offset:49152\n"
"v_cvt_pk_bf16_f32 v57, v26, v27\n"
"v_mfma_f32_16x16x32_bf16 v[0:3], a[136:139], v[136:139], v[0:3]\n"
"s_add_u32 m0, s60, 49152\n"
"v_exp_f32 v29, v29\n"
"v_mfma_f32_16x16x32_bf16 v[0:3], a[140:143], v[140:143], v[0:3]\n"
"ds_read_b64_tr_b16 a[162:163], v236 offset:53248\n"
"buffer_load_dwordx4 v228, s[44:47], s34 offen offset:0 sc1 lds\n"
"v_mfma_f32_16x16x32_bf16 v[4:7], a[156:159], v[140:143], v[64:67]\n"
"v_exp_f32 v30, v30\n"
"v_mfma_f32_16x16x32_bf16 v[4:7], a[152:155], v[136:139], v[4:7]\n"
"ds_read_b64_tr_b16 a[164:165], v237 offset:49152\n"
"v_cvt_pk_bf16_f32 v58, v28, v29\n"
"v_mfma_f32_16x16x32_bf16 v[4:7], a[148:151], v[132:135], v[4:7]\n"
"v_exp_f32 v31, v31\n"
"v_mfma_f32_16x16x32_bf16 v[4:7], a[144:147], v[128:131], v[4:7]\n"
"ds_read_b64_tr_b16 a[166:167], v237 offset:53248\n"
"buffer_load_dwordx4 v228, s[44:47], s34 offen offset:1024 sc1 lds\n"
"v_mfma_f32_16x16x32_bf16 v[12:15], a[144:147], v[144:147], v[68:71]\n"
"v_exp_f32 v0, v0\n"
"v_mfma_f32_16x16x32_bf16 v[12:15], a[148:151], v[148:151], v[12:15]\n"
"ds_read_b64_tr_b16 a[168:169], v238 offset:49152\n"
"v_cvt_pk_bf16_f32 v59, v30, v31\n"
"v_mfma_f32_16x16x32_bf16 v[12:15], a[152:155], v[152:155], v[12:15]\n"
"v_exp_f32 v1, v1\n"
"v_mfma_f32_16x16x32_bf16 v[12:15], a[156:159], v[156:159], v[12:15]\n"
"ds_read_b64_tr_b16 a[170:171], v238 offset:53248\n"
"buffer_load_dwordx4 v228, s[44:47], s34 offen offset:2048 sc1 lds\n"
"v_mfma_f32_16x16x32_bf16 v[8:11], a[140:143], v[156:159], v[68:71]\n"
"v_exp_f32 v2, v2\n"
"v_mfma_f32_16x16x32_bf16 v[8:11], a[136:139], v[152:155], v[8:11]\n"
"ds_read_b64_tr_b16 a[172:173], v239 offset:49152\n"
"v_cvt_pk_bf16_f32 v36, v0, v1\n"
"v_mfma_f32_16x16x32_bf16 v[8:11], a[132:135], v[148:151], v[8:11]\n"
"v_exp_f32 v3, v3\n"
"v_mfma_f32_16x16x32_bf16 v[8:11], a[128:131], v[144:147], v[8:11]\n"
"ds_read_b64_tr_b16 a[174:175], v239 offset:53248\n"
"buffer_load_dwordx4 v228, s[44:47], s34 offen offset:3072 sc1 lds\n"
"v_mfma_f32_16x16x32_bf16 v[16:19], a[128:131], v[160:163], v[72:75]\n"
"s_add_u32 s34, s58, 60416\n"
"v_exp_f32 v4, v4\n"
"v_mfma_f32_16x16x32_bf16 v[16:19], a[132:135], v[164:167], v[16:19]\n"
"ds_read_b64_tr_b16 a[176:177], v240 offset:49152\n"
"v_cvt_pk_bf16_f32 v37, v2, v3\n"
"v_mfma_f32_16x16x32_bf16 v[16:19], a[136:139], v[168:171], v[16:19]\n"
"s_add_u32 m0, s60, 98304\n"
"v_exp_f32 v5, v5\n"
"v_mfma_f32_16x16x32_bf16 v[16:19], a[140:143], v[172:175], v[16:19]\n"
"ds_read_b64_tr_b16 a[178:179], v240 offset:53248\n"
"buffer_load_dwordx4 v228, s[44:47], s34 offen offset:0 sc1 lds\n"
"v_mfma_f32_16x16x32_bf16 v[20:23], a[156:159], v[172:175], v[72:75]\n"
"v_exp_f32 v6, v6\n"
"v_mfma_f32_16x16x32_bf16 v[20:23], a[152:155], v[168:171], v[20:23]\n"
"ds_read_b64_tr_b16 a[180:181], v241 offset:49152\n"
"v_cvt_pk_bf16_f32 v38, v4, v5\n"
"v_mfma_f32_16x16x32_bf16 v[20:23], a[148:151], v[164:167], v[20:23]\n"
"v_exp_f32 v7, v7\n"
"v_mfma_f32_16x16x32_bf16 v[20:23], a[144:147], v[160:163], v[20:23]\n"
"ds_read_b64_tr_b16 a[182:183], v241 offset:53248\n"
"buffer_load_dwordx4 v228, s[44:47], s34 offen offset:1024 sc1 lds\n"
"v_mfma_f32_16x16x32_bf16 v[28:31], a[144:147], v[176:179], v[76:79]\n"
"v_exp_f32 v8, v8\n"
"v_mfma_f32_16x16x32_bf16 v[28:31], a[148:151], v[180:183], v[28:31]\n"
"ds_read_b64_tr_b16 a[184:185], v242 offset:49152\n"
"v_cvt_pk_bf16_f32 v39, v6, v7\n"
"v_mfma_f32_16x16x32_bf16 v[28:31], a[152:155], v[184:187], v[28:31]\n"
"v_exp_f32 v9, v9\n"
"v_mfma_f32_16x16x32_bf16 v[28:31], a[156:159], v[188:191], v[28:31]\n"
"ds_read_b64_tr_b16 a[186:187], v242 offset:53248\n"
"buffer_load_dwordx4 v228, s[44:47], s34 offen offset:2048 sc1 lds\n"
"v_mfma_f32_16x16x32_bf16 v[24:27], a[140:143], v[188:191], v[76:79]\n"
"v_exp_f32 v10, v10\n"
"v_mfma_f32_16x16x32_bf16 v[24:27], a[136:139], v[184:187], v[24:27]\n"
"ds_read_b64_tr_b16 a[188:189], v243 offset:49152\n"
"v_cvt_pk_bf16_f32 v44, v8, v9\n"
"v_mfma_f32_16x16x32_bf16 v[24:27], a[132:135], v[180:183], v[24:27]\n"
"s_waitcnt lgkmcnt(7)\n"
"v_exp_f32 v11, v11\n"
"v_mfma_f32_16x16x32_bf16 v[24:27], a[128:131], v[176:179], v[24:27]\n"
"ds_read_b64_tr_b16 a[190:191], v243 offset:53248\n"
"buffer_load_dwordx4 v228, s[44:47], s34 offen offset:3072 sc1 lds\n"
"v_mfma_f32_16x16x32_bf16 a[0:3], a[160:163], v[32:35], a[0:3]\n"
"v_exp_f32 v12, v12\n"
"v_mfma_f32_16x16x32_bf16 a[32:35], a[160:163], v[40:43], a[32:35]\n"
"ds_read_b64_tr_b16 a[128:129], v236 offset:57344\n"
"v_cvt_pk_bf16_f32 v45, v10, v11\n"
"v_mfma_f32_16x16x32_bf16 a[64:67], a[160:163], v[48:51], a[64:67]\n"
"s_add_u32 s34, s58, 61440\n"
"v_exp_f32 v13, v13\n"
"v_mfma_f32_16x16x32_bf16 a[96:99], a[160:163], v[56:59], a[96:99]\n"
"ds_read_b64_tr_b16 a[130:131], v236 offset:61440\n"
"buffer_load_dwordx4 v[64:67], v228, s[44:47], s34 offen offset:3072 sc1\n"
"v_mfma_f32_16x16x32_bf16 a[100:103], a[164:167], v[56:59], a[100:103]\n"
"v_exp_f32 v14, v14\n"
"v_mfma_f32_16x16x32_bf16 a[68:71], a[164:167], v[48:51], a[68:71]\n"
"ds_read_b64_tr_b16 a[132:133], v237 offset:57344\n"
"v_cvt_pk_bf16_f32 v46, v12, v13\n"
"v_mfma_f32_16x16x32_bf16 a[36:39], a[164:167], v[40:43], a[36:39]\n"
"s_add_u32 s34, s58, 65536\n"
"v_exp_f32 v15, v15\n"
"v_mfma_f32_16x16x32_bf16 a[4:7], a[164:167], v[32:35], a[4:7]\n"
"ds_read_b64_tr_b16 a[134:135], v237 offset:61440\n"
"buffer_load_dwordx4 v[68:71], v228, s[44:47], s34 offen offset:0 sc1\n"
"v_mfma_f32_16x16x32_bf16 a[8:11], a[168:171], v[32:35], a[8:11]\n"
"v_exp_f32 v16, v16\n"
"v_mfma_f32_16x16x32_bf16 a[40:43], a[168:171], v[40:43], a[40:43]\n"
"ds_read_b64_tr_b16 a[136:137], v238 offset:57344\n"
"v_cvt_pk_bf16_f32 v47, v14, v15\n"
"v_mfma_f32_16x16x32_bf16 a[72:75], a[168:171], v[48:51], a[72:75]\n"
"v_exp_f32 v17, v17\n"
"v_mfma_f32_16x16x32_bf16 a[104:107], a[168:171], v[56:59], a[104:107]\n"
"ds_read_b64_tr_b16 a[138:139], v238 offset:61440\n"
"buffer_load_dwordx4 v[72:75], v228, s[44:47], s34 offen offset:1024 sc1\n"
"v_mfma_f32_16x16x32_bf16 a[108:111], a[172:175], v[56:59], a[108:111]\n"
"v_exp_f32 v18, v18\n"
"v_mfma_f32_16x16x32_bf16 a[76:79], a[172:175], v[48:51], a[76:79]\n"
"ds_read_b64_tr_b16 a[140:141], v239 offset:57344\n"
"v_cvt_pk_bf16_f32 v52, v16, v17\n"
"v_mfma_f32_16x16x32_bf16 a[44:47], a[172:175], v[40:43], a[44:47]\n"
"s_waitcnt lgkmcnt(7)\n"
"v_exp_f32 v19, v19\n"
"v_mfma_f32_16x16x32_bf16 a[12:15], a[172:175], v[32:35], a[12:15]\n"
"ds_read_b64_tr_b16 a[142:143], v239 offset:61440\n"
"v_smfmac_f32_16x16x64_bf16 a[192:195], a[208:211], v[32:39], v250\n"
"buffer_load_dwordx4 v[76:79], v228, s[44:47], s34 offen offset:2048 sc1\n"
"v_mfma_f32_16x16x32_bf16 a[16:19], a[176:179], v[32:35], a[16:19]\n"
"v_exp_f32 v20, v20\n"
"v_mfma_f32_16x16x32_bf16 a[48:51], a[176:179], v[40:43], a[48:51]\n"
"ds_read_b64_tr_b16 a[144:145], v240 offset:57344\n"
"v_cvt_pk_bf16_f32 v53, v18, v19\n"
"v_mfma_f32_16x16x32_bf16 a[80:83], a[176:179], v[48:51], a[80:83]\n"
"v_exp_f32 v21, v21\n"
"v_mfma_f32_16x16x32_bf16 a[112:115], a[176:179], v[56:59], a[112:115]\n"
"ds_read_b64_tr_b16 a[146:147], v240 offset:61440\n"
"buffer_load_dwordx4 a[160:163], v228, s[44:47], s34 offen offset:3072 sc1\n"
"v_mfma_f32_16x16x32_bf16 a[116:119], a[180:183], v[56:59], a[116:119]\n"
"v_exp_f32 v22, v22\n"
"v_mfma_f32_16x16x32_bf16 a[84:87], a[180:183], v[48:51], a[84:87]\n"
"ds_read_b64_tr_b16 a[148:149], v241 offset:57344\n"
"v_cvt_pk_bf16_f32 v54, v20, v21\n"
"v_mfma_f32_16x16x32_bf16 a[52:55], a[180:183], v[40:43], a[52:55]\n"
"s_add_u32 s34, s58, 69632\n"
"v_exp_f32 v23, v23\n"
"v_mfma_f32_16x16x32_bf16 a[20:23], a[180:183], v[32:35], a[20:23]\n"
"ds_read_b64_tr_b16 a[150:151], v241 offset:61440\n"
"buffer_load_dwordx4 a[164:167], v228, s[44:47], s34 offen offset:0 sc1\n"
"v_mfma_f32_16x16x32_bf16 a[24:27], a[184:187], v[32:35], a[24:27]\n"
"v_exp_f32 v24, v24\n"
"v_mfma_f32_16x16x32_bf16 a[56:59], a[184:187], v[40:43], a[56:59]\n"
"ds_read_b64_tr_b16 a[152:153], v242 offset:57344\n"
"v_cvt_pk_bf16_f32 v55, v22, v23\n"
"v_mfma_f32_16x16x32_bf16 a[88:91], a[184:187], v[48:51], a[88:91]\n"
"v_exp_f32 v25, v25\n"
"v_mfma_f32_16x16x32_bf16 a[120:123], a[184:187], v[56:59], a[120:123]\n"
"ds_read_b64_tr_b16 a[154:155], v242 offset:61440\n"
"buffer_load_dwordx4 a[168:171], v228, s[44:47], s34 offen offset:1024 sc1\n"
"v_mfma_f32_16x16x32_bf16 a[124:127], a[188:191], v[56:59], a[124:127]\n"
"v_exp_f32 v26, v26\n"
"v_mfma_f32_16x16x32_bf16 a[92:95], a[188:191], v[48:51], a[92:95]\n"
"ds_read_b64_tr_b16 a[156:157], v243 offset:57344\n"
"v_cvt_pk_bf16_f32 v60, v24, v25\n"
"v_mfma_f32_16x16x32_bf16 a[60:63], a[188:191], v[40:43], a[60:63]\n"
"s_waitcnt lgkmcnt(7)\n"
"v_exp_f32 v27, v27\n"
"v_mfma_f32_16x16x32_bf16 a[28:31], a[188:191], v[32:35], a[28:31]\n"
"ds_read_b64_tr_b16 a[158:159], v243 offset:61440\n"
"v_smfmac_f32_16x16x64_bf16 a[196:199], a[208:211], v[40:47], v250\n"
"buffer_load_dwordx4 a[172:175], v228, s[44:47], s34 offen offset:2048 sc1\n"
"v_mfma_f32_16x16x32_bf16 a[0:3], a[128:131], v[36:39], a[0:3]\n"
"v_exp_f32 v28, v28\n"
"v_mfma_f32_16x16x32_bf16 a[4:7], a[132:135], v[36:39], a[4:7]\n"
"v_cvt_pk_bf16_f32 v61, v26, v27\n"
"v_mfma_f32_16x16x32_bf16 a[8:11], a[136:139], v[36:39], a[8:11]\n"
"s_waitcnt lgkmcnt(4)\n"
"v_exp_f32 v29, v29\n"
"v_mfma_f32_16x16x32_bf16 a[12:15], a[140:143], v[36:39], a[12:15]\n"
"v_smfmac_f32_16x16x64_bf16 a[200:203], a[208:211], v[48:55], v250\n"
"buffer_load_dwordx4 a[176:179], v228, s[44:47], s34 offen offset:3072 sc1\n"
"v_mfma_f32_16x16x32_bf16 a[16:19], a[144:147], v[36:39], a[16:19]\n"
"s_waitcnt lgkmcnt(0)\n"
"v_exp_f32 v30, v30\n"
"v_mfma_f32_16x16x32_bf16 a[20:23], a[148:151], v[36:39], a[20:23]\n"
"v_cvt_pk_bf16_f32 v62, v28, v29\n"
"v_mfma_f32_16x16x32_bf16 a[24:27], a[152:155], v[36:39], a[24:27]\n"
"s_add_u32 s34, s58, 73728\n"
"v_exp_f32 v31, v31\n"
"v_mfma_f32_16x16x32_bf16 a[28:31], a[156:159], v[36:39], a[28:31]\n"
"buffer_load_dwordx4 a[180:183], v228, s[44:47], s34 offen offset:0 sc1\n"
"v_mfma_f32_16x16x32_bf16 a[60:63], a[156:159], v[44:47], a[60:63]\n"
"v_mfma_f32_16x16x32_bf16 a[56:59], a[152:155], v[44:47], a[56:59]\n"
"v_cvt_pk_bf16_f32 v63, v30, v31\n"
"v_mfma_f32_16x16x32_bf16 a[52:55], a[148:151], v[44:47], a[52:55]\n"
"v_mfma_f32_16x16x32_bf16 a[48:51], a[144:147], v[44:47], a[48:51]\n"
"buffer_load_dwordx4 a[184:187], v228, s[44:47], s34 offen offset:1024 sc1\n"
"v_mfma_f32_16x16x32_bf16 a[44:47], a[140:143], v[44:47], a[44:47]\n"
"v_mfma_f32_16x16x32_bf16 a[40:43], a[136:139], v[44:47], a[40:43]\n"
"buffer_load_dwordx4 a[188:191], v228, s[44:47], s34 offen offset:2048 sc1\n"
"v_mfma_f32_16x16x32_bf16 a[36:39], a[132:135], v[44:47], a[36:39]\n"
"v_mfma_f32_16x16x32_bf16 a[32:35], a[128:131], v[44:47], a[32:35]\n"
"v_smfmac_f32_16x16x64_bf16 a[204:207], a[208:211], v[56:63], v250\n"
"v_mfma_f32_16x16x32_bf16 a[64:67], a[128:131], v[52:55], a[64:67]\n"
"v_mfma_f32_16x16x32_bf16 a[68:71], a[132:135], v[52:55], a[68:71]\n"
"v_mfma_f32_16x16x32_bf16 a[72:75], a[136:139], v[52:55], a[72:75]\n"
"v_mfma_f32_16x16x32_bf16 a[76:79], a[140:143], v[52:55], a[76:79]\n"
"v_accvgpr_read_b32 v12, a192\n"
"v_accvgpr_read_b32 v224, a193\n"
"v_mfma_f32_16x16x32_bf16 a[80:83], a[144:147], v[52:55], a[80:83]\n"
"v_accvgpr_read_b32 v13, a196\n"
"v_accvgpr_read_b32 v225, a197\n"
"v_mfma_f32_16x16x32_bf16 a[84:87], a[148:151], v[52:55], a[84:87]\n"
"v_accvgpr_read_b32 v14, a200\n"
"v_accvgpr_read_b32 v226, a201\n"
"v_mfma_f32_16x16x32_bf16 a[88:91], a[152:155], v[52:55], a[88:91]\n"
"v_accvgpr_read_b32 v15, a204\n"
"v_accvgpr_read_b32 v227, a205\n"
"v_mfma_f32_16x16x32_bf16 a[92:95], a[156:159], v[52:55], a[92:95]\n"
"v_add_f32 v224, v224, v12\n"
"v_add_f32 v225, v225, v13\n"
"v_mfma_f32_16x16x32_bf16 a[124:127], a[156:159], v[60:63], a[124:127]\n"
"v_add_f32 v226, v226, v14\n"
"v_add_f32 v227, v227, v15\n"
"v_mfma_f32_16x16x32_bf16 a[120:123], a[152:155], v[60:63], a[120:123]\n"
"v_rcp_f32 v12, v224\n"
"v_mfma_f32_16x16x32_bf16 a[116:119], a[148:151], v[60:63], a[116:119]\n"
"v_rcp_f32 v13, v225\n"
"v_mfma_f32_16x16x32_bf16 a[112:115], a[144:147], v[60:63], a[112:115]\n"
"v_rcp_f32 v14, v226\n"
"v_mfma_f32_16x16x32_bf16 a[108:111], a[140:143], v[60:63], a[108:111]\n"
"v_rcp_f32 v15, v227\n"
"v_mfma_f32_16x16x32_bf16 a[104:107], a[136:139], v[60:63], a[104:107]\n"
"v_accvgpr_read_b32 v0, a0\n"
"v_accvgpr_read_b32 v1, a1\n"
"v_mfma_f32_16x16x32_bf16 a[100:103], a[132:135], v[60:63], a[100:103]\n"
"v_accvgpr_read_b32 v2, a2\n"
"v_accvgpr_read_b32 v3, a3\n"
"v_mfma_f32_16x16x32_bf16 a[96:99], a[128:131], v[60:63], a[96:99]\n"
"v_accvgpr_read_b32 v4, a4\n"
"v_accvgpr_read_b32 v5, a5\n"
"s_waitcnt vmcnt(12)\n"
"v_accvgpr_read_b32 v6, a6\n"
"v_accvgpr_read_b32 v7, a7\n"
"v_pk_mul_f32 v[0:1], v[0:1], v[12:13] op_sel:[0,0] op_sel_hi:[1,0]\n"
"v_pk_mul_f32 v[2:3], v[2:3], v[12:13] op_sel:[0,0] op_sel_hi:[1,0]\n"
"v_pk_mul_f32 v[4:5], v[4:5], v[12:13] op_sel:[0,0] op_sel_hi:[1,0]\n"
"v_pk_mul_f32 v[6:7], v[6:7], v[12:13] op_sel:[0,0] op_sel_hi:[1,0]\n"
"v_cvt_pk_bf16_f32 v8, v0, v1\n"
"v_cvt_pk_bf16_f32 v9, v2, v3\n"
"v_cvt_pk_bf16_f32 v10, v4, v5\n"
"v_cvt_pk_bf16_f32 v11, v6, v7\n"
"s_add_u32 s64, %8, 0\n"
"buffer_store_dwordx4 v[8:11], v228, s[44:47], s64 offen offset:0 sc1\n"
"v_accvgpr_read_b32 v16, a32\n"
"v_accvgpr_read_b32 v17, a33\n"
"v_accvgpr_read_b32 v18, a34\n"
"v_accvgpr_read_b32 v19, a35\n"
"v_accvgpr_read_b32 v20, a36\n"
"v_accvgpr_read_b32 v21, a37\n"
"v_accvgpr_read_b32 v22, a38\n"
"v_accvgpr_read_b32 v23, a39\n"
"v_pk_mul_f32 v[16:17], v[16:17], v[12:13] op_sel:[0,1] op_sel_hi:[1,1]\n"
"v_pk_mul_f32 v[18:19], v[18:19], v[12:13] op_sel:[0,1] op_sel_hi:[1,1]\n"
"v_pk_mul_f32 v[20:21], v[20:21], v[12:13] op_sel:[0,1] op_sel_hi:[1,1]\n"
"v_pk_mul_f32 v[22:23], v[22:23], v[12:13] op_sel:[0,1] op_sel_hi:[1,1]\n"
"v_cvt_pk_bf16_f32 v24, v16, v17\n"
"v_cvt_pk_bf16_f32 v25, v18, v19\n"
"v_cvt_pk_bf16_f32 v26, v20, v21\n"
"v_cvt_pk_bf16_f32 v27, v22, v23\n"
"s_add_u32 s64, %8, 0\n"
"buffer_store_dwordx4 v[24:27], v228, s[44:47], s64 offen offset:1024 sc1\n"
"v_accvgpr_read_b32 v32, a64\n"
"v_accvgpr_read_b32 v33, a65\n"
"v_accvgpr_read_b32 v34, a66\n"
"v_accvgpr_read_b32 v35, a67\n"
"v_accvgpr_read_b32 v36, a68\n"
"v_accvgpr_read_b32 v37, a69\n"
"v_accvgpr_read_b32 v38, a70\n"
"v_accvgpr_read_b32 v39, a71\n"
"v_pk_mul_f32 v[32:33], v[32:33], v[14:15] op_sel:[0,0] op_sel_hi:[1,0]\n"
"v_pk_mul_f32 v[34:35], v[34:35], v[14:15] op_sel:[0,0] op_sel_hi:[1,0]\n"
"v_pk_mul_f32 v[36:37], v[36:37], v[14:15] op_sel:[0,0] op_sel_hi:[1,0]\n"
"v_pk_mul_f32 v[38:39], v[38:39], v[14:15] op_sel:[0,0] op_sel_hi:[1,0]\n"
"v_cvt_pk_bf16_f32 v40, v32, v33\n"
"v_cvt_pk_bf16_f32 v41, v34, v35\n"
"v_cvt_pk_bf16_f32 v42, v36, v37\n"
"v_cvt_pk_bf16_f32 v43, v38, v39\n"
"s_add_u32 s64, %8, 0\n"
"buffer_store_dwordx4 v[40:43], v228, s[44:47], s64 offen offset:2048 sc1\n"
"v_accvgpr_read_b32 v48, a96\n"
"v_accvgpr_read_b32 v49, a97\n"
"v_accvgpr_read_b32 v50, a98\n"
"v_accvgpr_read_b32 v51, a99\n"
"v_accvgpr_read_b32 v52, a100\n"
"v_accvgpr_read_b32 v53, a101\n"
"v_accvgpr_read_b32 v54, a102\n"
"v_accvgpr_read_b32 v55, a103\n"
"v_pk_mul_f32 v[48:49], v[48:49], v[14:15] op_sel:[0,1] op_sel_hi:[1,1]\n"
"v_pk_mul_f32 v[50:51], v[50:51], v[14:15] op_sel:[0,1] op_sel_hi:[1,1]\n"
"v_pk_mul_f32 v[52:53], v[52:53], v[14:15] op_sel:[0,1] op_sel_hi:[1,1]\n"
"v_pk_mul_f32 v[54:55], v[54:55], v[14:15] op_sel:[0,1] op_sel_hi:[1,1]\n"
"v_cvt_pk_bf16_f32 v56, v48, v49\n"
"v_cvt_pk_bf16_f32 v57, v50, v51\n"
"v_cvt_pk_bf16_f32 v58, v52, v53\n"
"v_cvt_pk_bf16_f32 v59, v54, v55\n"
"s_add_u32 s64, %8, 0\n"
"buffer_store_dwordx4 v[56:59], v228, s[44:47], s64 offen offset:3072 sc1\n"
"v_accvgpr_read_b32 v0, a8\n"
"v_accvgpr_read_b32 v1, a9\n"
"v_accvgpr_read_b32 v2, a10\n"
"v_accvgpr_read_b32 v3, a11\n"
"v_accvgpr_read_b32 v4, a12\n"
"v_accvgpr_read_b32 v5, a13\n"
"v_accvgpr_read_b32 v6, a14\n"
"v_accvgpr_read_b32 v7, a15\n"
"v_pk_mul_f32 v[0:1], v[0:1], v[12:13] op_sel:[0,0] op_sel_hi:[1,0]\n"
"v_pk_mul_f32 v[2:3], v[2:3], v[12:13] op_sel:[0,0] op_sel_hi:[1,0]\n"
"v_pk_mul_f32 v[4:5], v[4:5], v[12:13] op_sel:[0,0] op_sel_hi:[1,0]\n"
"v_pk_mul_f32 v[6:7], v[6:7], v[12:13] op_sel:[0,0] op_sel_hi:[1,0]\n"
"v_cvt_pk_bf16_f32 v8, v0, v1\n"
"v_cvt_pk_bf16_f32 v9, v2, v3\n"
"v_cvt_pk_bf16_f32 v10, v4, v5\n"
"v_cvt_pk_bf16_f32 v11, v6, v7\n"
"s_add_u32 s64, %8, 4096\n"
"buffer_store_dwordx4 v[8:11], v228, s[44:47], s64 offen offset:0 sc1\n"
"v_accvgpr_read_b32 v16, a40\n"
"v_accvgpr_read_b32 v17, a41\n"
"v_accvgpr_read_b32 v18, a42\n"
"v_accvgpr_read_b32 v19, a43\n"
"v_accvgpr_read_b32 v20, a44\n"
"v_accvgpr_read_b32 v21, a45\n"
"v_accvgpr_read_b32 v22, a46\n"
"v_accvgpr_read_b32 v23, a47\n"
"v_pk_mul_f32 v[16:17], v[16:17], v[12:13] op_sel:[0,1] op_sel_hi:[1,1]\n"
"v_pk_mul_f32 v[18:19], v[18:19], v[12:13] op_sel:[0,1] op_sel_hi:[1,1]\n"
"v_pk_mul_f32 v[20:21], v[20:21], v[12:13] op_sel:[0,1] op_sel_hi:[1,1]\n"
"v_pk_mul_f32 v[22:23], v[22:23], v[12:13] op_sel:[0,1] op_sel_hi:[1,1]\n"
"v_cvt_pk_bf16_f32 v24, v16, v17\n"
"v_cvt_pk_bf16_f32 v25, v18, v19\n"
"v_cvt_pk_bf16_f32 v26, v20, v21\n"
"v_cvt_pk_bf16_f32 v27, v22, v23\n"
"s_add_u32 s64, %8, 4096\n"
"buffer_store_dwordx4 v[24:27], v228, s[44:47], s64 offen offset:1024 sc1\n"
"v_accvgpr_read_b32 v32, a72\n"
"v_accvgpr_read_b32 v33, a73\n"
"v_accvgpr_read_b32 v34, a74\n"
"v_accvgpr_read_b32 v35, a75\n"
"v_accvgpr_read_b32 v36, a76\n"
"v_accvgpr_read_b32 v37, a77\n"
"v_accvgpr_read_b32 v38, a78\n"
"v_accvgpr_read_b32 v39, a79\n"
"v_pk_mul_f32 v[32:33], v[32:33], v[14:15] op_sel:[0,0] op_sel_hi:[1,0]\n"
"v_pk_mul_f32 v[34:35], v[34:35], v[14:15] op_sel:[0,0] op_sel_hi:[1,0]\n"
"v_pk_mul_f32 v[36:37], v[36:37], v[14:15] op_sel:[0,0] op_sel_hi:[1,0]\n"
"v_pk_mul_f32 v[38:39], v[38:39], v[14:15] op_sel:[0,0] op_sel_hi:[1,0]\n"
"v_cvt_pk_bf16_f32 v40, v32, v33\n"
"v_cvt_pk_bf16_f32 v41, v34, v35\n"
"v_cvt_pk_bf16_f32 v42, v36, v37\n"
"v_cvt_pk_bf16_f32 v43, v38, v39\n"
"s_add_u32 s64, %8, 4096\n"
"buffer_store_dwordx4 v[40:43], v228, s[44:47], s64 offen offset:2048 sc1\n"
"v_accvgpr_read_b32 v48, a104\n"
"v_accvgpr_read_b32 v49, a105\n"
"v_accvgpr_read_b32 v50, a106\n"
"v_accvgpr_read_b32 v51, a107\n"
"v_accvgpr_read_b32 v52, a108\n"
"v_accvgpr_read_b32 v53, a109\n"
"v_accvgpr_read_b32 v54, a110\n"
"v_accvgpr_read_b32 v55, a111\n"
"v_pk_mul_f32 v[48:49], v[48:49], v[14:15] op_sel:[0,1] op_sel_hi:[1,1]\n"
"v_pk_mul_f32 v[50:51], v[50:51], v[14:15] op_sel:[0,1] op_sel_hi:[1,1]\n"
"v_pk_mul_f32 v[52:53], v[52:53], v[14:15] op_sel:[0,1] op_sel_hi:[1,1]\n"
"v_pk_mul_f32 v[54:55], v[54:55], v[14:15] op_sel:[0,1] op_sel_hi:[1,1]\n"
"v_cvt_pk_bf16_f32 v56, v48, v49\n"
"v_cvt_pk_bf16_f32 v57, v50, v51\n"
"v_cvt_pk_bf16_f32 v58, v52, v53\n"
"v_cvt_pk_bf16_f32 v59, v54, v55\n"
"s_add_u32 s64, %8, 4096\n"
"buffer_store_dwordx4 v[56:59], v228, s[44:47], s64 offen offset:3072 sc1\n"
"v_accvgpr_read_b32 v0, a16\n"
"v_accvgpr_read_b32 v1, a17\n"
"v_accvgpr_read_b32 v2, a18\n"
"v_accvgpr_read_b32 v3, a19\n"
"v_accvgpr_read_b32 v4, a20\n"
"v_accvgpr_read_b32 v5, a21\n"
"v_accvgpr_read_b32 v6, a22\n"
"v_accvgpr_read_b32 v7, a23\n"
"v_pk_mul_f32 v[0:1], v[0:1], v[12:13] op_sel:[0,0] op_sel_hi:[1,0]\n"
"v_pk_mul_f32 v[2:3], v[2:3], v[12:13] op_sel:[0,0] op_sel_hi:[1,0]\n"
"v_pk_mul_f32 v[4:5], v[4:5], v[12:13] op_sel:[0,0] op_sel_hi:[1,0]\n"
"v_pk_mul_f32 v[6:7], v[6:7], v[12:13] op_sel:[0,0] op_sel_hi:[1,0]\n"
"v_cvt_pk_bf16_f32 v8, v0, v1\n"
"v_cvt_pk_bf16_f32 v9, v2, v3\n"
"v_cvt_pk_bf16_f32 v10, v4, v5\n"
"v_cvt_pk_bf16_f32 v11, v6, v7\n"
"s_add_u32 s64, %8, 8192\n"
"buffer_store_dwordx4 v[8:11], v228, s[44:47], s64 offen offset:0 sc1\n"
"v_accvgpr_read_b32 v16, a48\n"
"v_accvgpr_read_b32 v17, a49\n"
"v_accvgpr_read_b32 v18, a50\n"
"v_accvgpr_read_b32 v19, a51\n"
"v_accvgpr_read_b32 v20, a52\n"
"v_accvgpr_read_b32 v21, a53\n"
"v_accvgpr_read_b32 v22, a54\n"
"v_accvgpr_read_b32 v23, a55\n"
"v_pk_mul_f32 v[16:17], v[16:17], v[12:13] op_sel:[0,1] op_sel_hi:[1,1]\n"
"v_pk_mul_f32 v[18:19], v[18:19], v[12:13] op_sel:[0,1] op_sel_hi:[1,1]\n"
"v_pk_mul_f32 v[20:21], v[20:21], v[12:13] op_sel:[0,1] op_sel_hi:[1,1]\n"
"v_pk_mul_f32 v[22:23], v[22:23], v[12:13] op_sel:[0,1] op_sel_hi:[1,1]\n"
"v_cvt_pk_bf16_f32 v24, v16, v17\n"
"v_cvt_pk_bf16_f32 v25, v18, v19\n"
"v_cvt_pk_bf16_f32 v26, v20, v21\n"
"v_cvt_pk_bf16_f32 v27, v22, v23\n"
"s_add_u32 s64, %8, 8192\n"
"buffer_store_dwordx4 v[24:27], v228, s[44:47], s64 offen offset:1024 sc1\n"
"v_add_f32 v28, v224, v225\n"
"v_add_f32 v29, v226, v227\n"
"v_add_f32 v28, v28, v29\n"
"s_mov_b32 s64, 1669332992\n"
"v_cmp_nle_f32_e64 vcc, v28, s64\n"
"s_lshr_b32 s65, s60, 10\n"
"s_add_u32 s65, s65, 150528\n"
"v_mov_b32 v29, s65\n"
"v_mov_b32 v30, 150528\n"
"s_nop 1\n"
"s_cmp_lg_u64 vcc, 0\n"
"s_cselect_b32 s64, 1, 0\n"
"v_mov_b32 v28, s64\n"
"ds_write_b32 v29, v28\n"
"v_accvgpr_read_b32 v32, a80\n"
"v_accvgpr_read_b32 v33, a81\n"
"v_accvgpr_read_b32 v34, a82\n"
"v_accvgpr_read_b32 v35, a83\n"
"v_accvgpr_read_b32 v36, a84\n"
"v_accvgpr_read_b32 v37, a85\n"
"v_accvgpr_read_b32 v38, a86\n"
"v_accvgpr_read_b32 v39, a87\n"
"v_pk_mul_f32 v[32:33], v[32:33], v[14:15] op_sel:[0,0] op_sel_hi:[1,0]\n"
"v_pk_mul_f32 v[34:35], v[34:35], v[14:15] op_sel:[0,0] op_sel_hi:[1,0]\n"
"v_pk_mul_f32 v[36:37], v[36:37], v[14:15] op_sel:[0,0] op_sel_hi:[1,0]\n"
"v_pk_mul_f32 v[38:39], v[38:39], v[14:15] op_sel:[0,0] op_sel_hi:[1,0]\n"
"v_cvt_pk_bf16_f32 v40, v32, v33\n"
"v_cvt_pk_bf16_f32 v41, v34, v35\n"
"v_cvt_pk_bf16_f32 v42, v36, v37\n"
"v_cvt_pk_bf16_f32 v43, v38, v39\n"
"s_add_u32 s64, %8, 8192\n"
"buffer_store_dwordx4 v[40:43], v228, s[44:47], s64 offen offset:2048 sc1\n"
"v_accvgpr_read_b32 v48, a112\n"
"v_accvgpr_read_b32 v49, a113\n"
"v_accvgpr_read_b32 v50, a114\n"
"v_accvgpr_read_b32 v51, a115\n"
"v_accvgpr_read_b32 v52, a116\n"
"v_accvgpr_read_b32 v53, a117\n"
"v_accvgpr_read_b32 v54, a118\n"
"v_accvgpr_read_b32 v55, a119\n"
"v_pk_mul_f32 v[48:49], v[48:49], v[14:15] op_sel:[0,1] op_sel_hi:[1,1]\n"
"v_pk_mul_f32 v[50:51], v[50:51], v[14:15] op_sel:[0,1] op_sel_hi:[1,1]\n"
"v_pk_mul_f32 v[52:53], v[52:53], v[14:15] op_sel:[0,1] op_sel_hi:[1,1]\n"
"v_pk_mul_f32 v[54:55], v[54:55], v[14:15] op_sel:[0,1] op_sel_hi:[1,1]\n"
"v_cvt_pk_bf16_f32 v56, v48, v49\n"
"v_cvt_pk_bf16_f32 v57, v50, v51\n"
"v_cvt_pk_bf16_f32 v58, v52, v53\n"
"v_cvt_pk_bf16_f32 v59, v54, v55\n"
"s_add_u32 s64, %8, 8192\n"
"buffer_store_dwordx4 v[56:59], v228, s[44:47], s64 offen offset:3072 sc1\n"
"v_accvgpr_read_b32 v0, a24\n"
"v_accvgpr_read_b32 v1, a25\n"
"v_accvgpr_read_b32 v2, a26\n"
"v_accvgpr_read_b32 v3, a27\n"
"v_accvgpr_read_b32 v4, a28\n"
"v_accvgpr_read_b32 v5, a29\n"
"v_accvgpr_read_b32 v6, a30\n"
"v_accvgpr_read_b32 v7, a31\n"
"L_w11_%=:\n"
"s_cbranch_execz L_wd3_%=\n"
"v_pk_mul_f32 v[0:1], v[0:1], v[12:13] op_sel:[0,0] op_sel_hi:[1,0]\n"
"v_pk_mul_f32 v[2:3], v[2:3], v[12:13] op_sel:[0,0] op_sel_hi:[1,0]\n"
"v_pk_mul_f32 v[4:5], v[4:5], v[12:13] op_sel:[0,0] op_sel_hi:[1,0]\n"
"v_pk_mul_f32 v[6:7], v[6:7], v[12:13] op_sel:[0,0] op_sel_hi:[1,0]\n"
"v_cvt_pk_bf16_f32 v8, v0, v1\n"
"v_cvt_pk_bf16_f32 v9, v2, v3\n"
"v_cvt_pk_bf16_f32 v10, v4, v5\n"
"v_cvt_pk_bf16_f32 v11, v6, v7\n"
"s_add_u32 s64, %8, 12288\n"
"buffer_store_dwordx4 v[8:11], v228, s[44:47], s64 offen offset:0 sc1\n"
"v_accvgpr_read_b32 v16, a56\n"
"v_accvgpr_read_b32 v17, a57\n"
"v_accvgpr_read_b32 v18, a58\n"
"v_accvgpr_read_b32 v19, a59\n"
"v_accvgpr_read_b32 v20, a60\n"
"v_accvgpr_read_b32 v21, a61\n"
"v_accvgpr_read_b32 v22, a62\n"
"v_accvgpr_read_b32 v23, a63\n"
"v_pk_mul_f32 v[16:17], v[16:17], v[12:13] op_sel:[0,1] op_sel_hi:[1,1]\n"
"v_pk_mul_f32 v[18:19], v[18:19], v[12:13] op_sel:[0,1] op_sel_hi:[1,1]\n"
"v_pk_mul_f32 v[20:21], v[20:21], v[12:13] op_sel:[0,1] op_sel_hi:[1,1]\n"
"v_pk_mul_f32 v[22:23], v[22:23], v[12:13] op_sel:[0,1] op_sel_hi:[1,1]\n"
"v_cvt_pk_bf16_f32 v24, v16, v17\n"
"v_cvt_pk_bf16_f32 v25, v18, v19\n"
"v_cvt_pk_bf16_f32 v26, v20, v21\n"
"v_cvt_pk_bf16_f32 v27, v22, v23\n"
"s_add_u32 s64, %8, 12288\n"
"buffer_store_dwordx4 v[24:27], v228, s[44:47], s64 offen offset:1024 sc1\n"
"v_accvgpr_read_b32 v32, a88\n"
"v_accvgpr_read_b32 v33, a89\n"
"v_accvgpr_read_b32 v34, a90\n"
"v_accvgpr_read_b32 v35, a91\n"
"v_accvgpr_read_b32 v36, a92\n"
"v_accvgpr_read_b32 v37, a93\n"
"v_accvgpr_read_b32 v38, a94\n"
"v_accvgpr_read_b32 v39, a95\n"
"v_pk_mul_f32 v[32:33], v[32:33], v[14:15] op_sel:[0,0] op_sel_hi:[1,0]\n"
"v_pk_mul_f32 v[34:35], v[34:35], v[14:15] op_sel:[0,0] op_sel_hi:[1,0]\n"
"v_pk_mul_f32 v[36:37], v[36:37], v[14:15] op_sel:[0,0] op_sel_hi:[1,0]\n"
"v_pk_mul_f32 v[38:39], v[38:39], v[14:15] op_sel:[0,0] op_sel_hi:[1,0]\n"
"v_cvt_pk_bf16_f32 v40, v32, v33\n"
"v_cvt_pk_bf16_f32 v41, v34, v35\n"
"v_cvt_pk_bf16_f32 v42, v36, v37\n"
"v_cvt_pk_bf16_f32 v43, v38, v39\n"
"s_add_u32 s64, %8, 12288\n"
"buffer_store_dwordx4 v[40:43], v228, s[44:47], s64 offen offset:2048 sc1\n"
"v_accvgpr_read_b32 v48, a120\n"
"v_accvgpr_read_b32 v49, a121\n"
"v_accvgpr_read_b32 v50, a122\n"
"v_accvgpr_read_b32 v51, a123\n"
"v_accvgpr_read_b32 v52, a124\n"
"v_accvgpr_read_b32 v53, a125\n"
"v_accvgpr_read_b32 v54, a126\n"
"v_accvgpr_read_b32 v55, a127\n"
"v_pk_mul_f32 v[48:49], v[48:49], v[14:15] op_sel:[0,1] op_sel_hi:[1,1]\n"
"v_pk_mul_f32 v[50:51], v[50:51], v[14:15] op_sel:[0,1] op_sel_hi:[1,1]\n"
"v_pk_mul_f32 v[52:53], v[52:53], v[14:15] op_sel:[0,1] op_sel_hi:[1,1]\n"
"v_pk_mul_f32 v[54:55], v[54:55], v[14:15] op_sel:[0,1] op_sel_hi:[1,1]\n"
"v_cvt_pk_bf16_f32 v56, v48, v49\n"
"v_cvt_pk_bf16_f32 v57, v50, v51\n"
"v_cvt_pk_bf16_f32 v58, v52, v53\n"
"v_cvt_pk_bf16_f32 v59, v54, v55\n"
"s_add_u32 s64, %8, 12288\n"
"buffer_store_dwordx4 v[56:59], v228, s[44:47], s64 offen offset:3072 sc1\n"
"s_waitcnt lgkmcnt(0)\n"
"s_barrier\n"
"ds_read_b128 v[44:47], v30\n"
"s_waitcnt lgkmcnt(0)\n"
"v_or_b32 v44, v44, v45\n"
"v_or3_b32 v44, v44, v46, v47\n"
"s_nop 0\n"
"v_readfirstlane_b32 s64, v44\n"
"s_nop 3\n"
"s_andn2_b32 s64, s64, s66\n"
"s_cbranch_scc1 L_fb_%=\n"
"v_lshrrev_b32 v250, 8, v228\n"
"v_lshlrev_b32 v250, 4, v250\n"
"v_mov_b32 v249, v252\n"
"v_accvgpr_read_b32 v236, a244\n"
"v_accvgpr_read_b32 v237, a245\n"
"v_accvgpr_read_b32 v238, a246\n"
"v_accvgpr_read_b32 v239, a247\n"
"v_accvgpr_read_b32 v240, a248\n"
"v_accvgpr_read_b32 v241, a249\n"
"v_accvgpr_read_b32 v242, a250\n"
"v_accvgpr_read_b32 v243, a251\n"
"v_accvgpr_read_b32 v244, a252\n"
"v_accvgpr_read_b32 v245, a253\n"
"s_xor_b32 s64, %17, 0\n"
"s_lshl_b32 s64, s64, 10\n"
"v_add_u32 v232, s64, v228\n"
"s_xor_b32 s64, %17, 1\n"
"s_lshl_b32 s64, s64, 10\n"
"v_add_u32 v233, s64, v228\n"
"s_xor_b32 s64, %17, 2\n"
"s_lshl_b32 s64, s64, 10\n"
"v_add_u32 v234, s64, v228\n"
"s_xor_b32 s64, %17, 3\n"
"s_lshl_b32 s64, s64, 10\n"
"v_add_u32 v235, s64, v228\n"
"v_add_u32 v246, s60, v228\n"
"v_add_u32 v247, 65536, v246\n"
"v_add_u32 v248, 131072, v246\n"
"v_add_u32 v252, 131072, v250\n"
"ds_read_b128 v[20:23], v248 offset:0\n"
"ds_read_b128 v[24:27], v248 offset:1024\n"
"ds_read_b128 v[28:31], v248 offset:2048\n"
"ds_read_b128 v[32:35], v248 offset:3072\n"
"ds_read_b128 v[36:39], v248 offset:16384\n"
"ds_read_b128 v[40:43], v248 offset:17408\n"
"ds_read_b128 v[128:131], v248 offset:18432\n"
"ds_read_b128 v[132:135], v246 offset:0\n"
"ds_read_b128 v[136:139], v246 offset:1024\n"
"ds_read_b128 v[140:143], v246 offset:2048\n"
"ds_read_b128 v[144:147], v246 offset:3072\n"
"ds_read_b128 v[148:151], v246 offset:16384\n"
"ds_read_b128 v[152:155], v246 offset:17408\n"
"ds_read_b128 v[156:159], v246 offset:18432\n"
"ds_read_b128 v[160:163], v246 offset:19456\n"
"ds_read_b128 v[164:167], v247 offset:0\n"
"ds_read_b128 v[168:171], v247 offset:1024\n"
"ds_read_b128 v[172:175], v247 offset:2048\n"
"ds_read_b128 v[176:179], v247 offset:3072\n"
"ds_read_b128 v[180:183], v246 offset:32768\n"
"ds_read_b128 v[184:187], v246 offset:33792\n"
"ds_read_b128 v[188:191], v246 offset:34816\n"
"ds_read_b128 v[224:227], v246 offset:35840\n"
"ds_read_b128 a[128:131], v247 offset:16384\n"
"ds_read_b128 a[132:135], v247 offset:17408\n"
"ds_read_b128 a[136:139], v247 offset:18432\n"
"ds_read_b128 a[140:143], v247 offset:19456\n"
"ds_read_b128 a[144:147], v246 offset:49152\n"
"ds_read_b128 a[148:151], v246 offset:50176\n"
"ds_read_b128 a[152:155], v246 offset:51200\n"
"ds_read_b128 a[156:159], v246 offset:52224\n"
"ds_read_b128 a[192:195], v247 offset:32768\n"
"ds_read_b128 a[196:199], v247 offset:33792\n"
"ds_read_b128 a[200:203], v247 offset:34816\n"
"ds_read_b128 a[204:207], v247 offset:35840\n"
"s_load_dwordx2 s[40:41], %16, 0x38\n"
"s_mov_b32 s42, 2621440\n"
"s_lshl_b32 s61, %11, 19\n"
"s_and_b32 s64, %11, 1\n"
"s_lshl_b32 s64, s64, 18\n"
"s_add_u32 s61, s61, s64\n"
"s_add_u32 s61, s61, s60\n"
"s_add_u32 s55, s61, 4194304\n"
"s_xor_b32 s61, s55, 524288\n"
"s_add_u32 s35, s61, 16384\n"
"s_waitcnt lgkmcnt(0)\n"
"s_and_b32 s41, s41, 0xffff\n"
"s_xor_b32 s64, %17, 1\n"
"s_mul_i32 s64, s64, 15360\n"
"s_add_u32 s64, s64, 86016\n"
"v_add_u32 v246, s64, v228\n"
"s_xor_b32 s64, %17, 2\n"
"s_mul_i32 s64, s64, 15360\n"
"s_add_u32 s64, s64, 91136\n"
"v_add_u32 v247, s64, v228\n"
"s_xor_b32 s64, %17, 3\n"
"s_mul_i32 s64, s64, 15360\n"
"s_add_u32 s64, s64, 96256\n"
"v_add_u32 v248, s64, v228\n"
"s_mul_i32 s64, %17, 15360\n"
"s_add_u32 s64, s64, 86016\n"
"v_add_u32 v251, s64, v228\n"
"s_mul_i32 s62, %15, 43008\n"
"s_lshr_b32 s64, %17, 1\n"
"s_mul_i32 s64, s64, 21504\n"
"s_add_u32 s62, s62, s64\n"
"s_add_u32 s57, s62, 31477760\n"
"s_and_b32 s64, %17, 1\n"
"s_lshl_b32 s64, s64, 10\n"
"s_add_u32 s62, s62, s64\n"
"s_add_u32 s62, s62, 31457280\n"
"s_cmp_eq_u32 %14, 15\n"
"s_cselect_b32 s59, 0, 258048\n"
"s_add_u32 s63, s62, s59\n"
"s_sub_u32 s63, s63, 2048\n"
"s_mul_i32 s56, %17, 7168\n"
"s_barrier\n"
"s_lshl_b32 s64, %10, 10\n"
"s_add_u32 s64, s64, 524288\n"
"v_lshl_add_u32 v0, v228, 3, s64\n"
"s_lshl_b32 s64, %18, 2\n"
"s_add_u32 s64, s64, %17\n"
"s_and_b32 s64, s64, 15\n"
"s_lshl_b32 s64, s64, 7\n"
"s_add_u32 s64, s64, 628736\n"
"v_writelane_b32 v0, s64, 8\n"
"v_mov_b32 v1, 1\n"
"s_lshl_b32 s64, %11, 3\n"
"s_add_u32 s64, s64, %12\n"
"s_lshl_b32 s64, s64, 7\n"
"s_add_u32 s64, s64, 524288\n"
"v_mov_b32 v2, s64\n"
"s_mov_b64 exec, 0x1ff\n"
"s_waitcnt vmcnt(0)\n"
"global_atomic_add v0, v1, %0\n"
"s_mov_b64 exec, -1\n"
"buffer_load_dwordx4 a[208:211], v228, s[44:47], s34 offen offset:3072 sc1\n"
"s_add_u32 s34, s58, 77824\n"
"buffer_load_dwordx4 a[60:63], v228, s[44:47], s34 offen offset:0 sc1\n"
"buffer_load_dwordx4 a[244:247], v228, s[44:47], s34 offen offset:1024 sc1\n"
"buffer_load_dwordx4 a[248:251], v228, s[44:47], s34 offen offset:2048 sc1\n"
"buffer_load_dwordx4 a[252:255], v228, s[44:47], s34 offen offset:3072 sc1\n"
"s_add_u32 m0, s56, 0\n"
"s_add_u32 s34, s62, 0\n"
"buffer_load_dwordx4 v228, s[44:47], s34 offen offset:0 sc1 lds\n"
"s_add_u32 s34, s62, 41984\n"
"buffer_load_dwordx4 v228, s[44:47], s34 offen offset:1024 sc1 lds\n"
"s_add_u32 s34, s62, 83968\n"
"buffer_load_dwordx4 v228, s[44:47], s34 offen offset:2048 sc1 lds\n"
"s_add_u32 s34, s62, 125952\n"
"buffer_load_dwordx4 v228, s[44:47], s34 offen offset:3072 sc1 lds\n"
"s_add_u32 m0, s56, 4096\n"
"s_add_u32 s34, s62, 172032\n"
"buffer_load_dwordx4 v228, s[44:47], s34 offen offset:0 sc1 lds\n"
"s_add_u32 s34, s62, 214016\n"
"buffer_load_dwordx4 v228, s[44:47], s34 offen offset:1024 sc1 lds\n"
"s_add_u32 s34, s63, 0\n"
"buffer_load_dwordx4 v228, s[44:47], s34 offen offset:2048 sc1 lds\n"
"s_add_u32 m0, s56, 28672\n"
"s_add_u32 s34, s62, 2048\n"
"buffer_load_dwordx4 v228, s[44:47], s34 offen offset:0 sc1 lds\n"
"s_add_u32 s34, s62, 44032\n"
"buffer_load_dwordx4 v228, s[44:47], s34 offen offset:1024 sc1 lds\n"
"s_add_u32 s34, s62, 86016\n"
"buffer_load_dwordx4 v228, s[44:47], s34 offen offset:2048 sc1 lds\n"
"s_add_u32 s34, s62, 128000\n"
"buffer_load_dwordx4 v228, s[44:47], s34 offen offset:3072 sc1 lds\n"
"s_add_u32 m0, s56, 32768\n"
"s_add_u32 s34, s62, 174080\n"
"buffer_load_dwordx4 v228, s[44:47], s34 offen offset:0 sc1 lds\n"
"s_cmp_lg_u32 s60, 0\n"
"s_cbranch_scc1 L_ob_%=\n"
"v_mov_b32 v4, 0\n"
"s_mov_b64 exec, 1\n"
"s_mov_b32 s65, 200000\n"
"L_pl4_%=:\n"
"global_atomic_add v3, v2, v4, %0 sc0\n"
"s_waitcnt vmcnt(0)\n"
"v_cmp_gt_u32 vcc, 32, v3\n"
"s_cbranch_vccz L_pd4_%=\n"
"s_sleep 1\n"
"s_sub_u32 s65, s65, 1\n"
"s_cbranch_scc0 L_pl4_%=\n"
"L_pd4_%=:\n"
"s_mov_b64 exec, -1\n"
"s_branch L_ob2_%=\n"
"L_ob_%=:\n"
"s_mov_b64 exec, 0\n"
"s_cmp_eq_u32 s60, 4096\n"
"s_cbranch_scc1 L_w13_%=\n"
"s_cmp_eq_u32 s60, 8192\n"
"s_cbranch_scc1 L_w14_%=\n"
"s_branch L_w15_%=\n"
"L_wd4_%=:\n"
"s_mov_b64 exec, -1\n"
"L_ob2_%=:\n"
"s_barrier\n"
"buffer_load_dwordx4 a[64:67], v232, s[44:47], s61 offen offset:0 sc1\n"
"buffer_load_dwordx4 a[80:83], v232, s[44:47], s35 offen offset:0 sc1\n"
"buffer_load_dwordx4 a[68:71], v233, s[44:47], s61 offen offset:0 sc1\n"
"buffer_load_dwordx4 a[84:87], v233, s[44:47], s35 offen offset:0 sc1\n"
"buffer_load_dwordx4 a[72:75], v234, s[44:47], s61 offen offset:0 sc1\n"
"buffer_load_dwordx4 a[88:91], v234, s[44:47], s35 offen offset:0 sc1\n"
"buffer_load_dwordx4 a[76:79], v235, s[44:47], s61 offen offset:0 sc1\n"
"buffer_load_dwordx4 a[92:95], v235, s[44:47], s35 offen offset:0 sc1\n"
"s_add_u32 s61, s61, 32768\n"
"s_add_u32 s35, s61, 16384\n"
"s_and_b32 s64, %11, 1\n"
"s_mul_i32 s64, s64, 163840\n"
"s_add_u32 s52, %9, 81920\n"
"s_sub_u32 s52, s52, s64\n"
"s_lshl_b32 s58, %12, 2\n"
"s_add_u32 s58, s58, %17\n"
"s_mul_i32 s58, s58, 71680\n"
"s_add_u32 s58, s58, 37748736\n"
"v_lshrrev_b32 v231, 1, v228\n"
"s_waitcnt vmcnt(6)\n"
"v_mfma_f32_16x16x32_bf16 v[44:47], v[80:83], a[64:67], 0\n"
"v_mfma_f32_16x16x32_bf16 v[44:47], v[100:103], a[80:83], v[44:47]\n"
"v_mfma_f32_16x16x32_bf16 v[48:51], v[104:107], a[80:83], 0\n"
"buffer_load_dwordx4 a[96:99], v232, s[44:47], s61 offen offset:0 sc1\n"
"v_mfma_f32_16x16x32_bf16 v[48:51], v[84:87], a[64:67], v[48:51]\n"
"v_mfma_f32_16x16x32_bf16 v[52:55], v[88:91], a[64:67], 0\n"
"buffer_load_dwordx4 a[112:115], v232, s[44:47], s35 offen offset:0 sc1\n"
"v_mfma_f32_16x16x32_bf16 v[52:55], v[108:111], a[80:83], v[52:55]\n"
"v_mfma_f32_16x16x32_bf16 v[56:59], v[112:115], a[80:83], 0\n"
"v_mfma_f32_16x16x32_bf16 v[56:59], v[92:95], a[64:67], v[56:59]\n"
"buffer_load_dwordx4 a[100:103], v233, s[44:47], s61 offen offset:0 sc1\n"
"v_mfma_f32_16x16x32_bf16 v[60:63], v[96:99], a[64:67], 0\n"
"v_mfma_f32_16x16x32_bf16 v[60:63], v[116:119], a[80:83], v[60:63]\n"
"buffer_load_dwordx4 a[116:119], v233, s[44:47], s35 offen offset:0 sc1\n"
"s_waitcnt vmcnt(8)\n"
"v_mfma_f32_16x16x32_bf16 a[16:19], v[116:119], a[84:87], 0\n"
"v_mfma_f32_16x16x32_bf16 a[16:19], v[96:99], a[68:71], a[16:19]\n"
"v_mfma_f32_16x16x32_bf16 a[12:15], v[92:95], a[68:71], 0\n"
"buffer_load_dwordx4 a[104:107], v234, s[44:47], s61 offen offset:0 sc1\n"
"v_mfma_f32_16x16x32_bf16 a[12:15], v[112:115], a[84:87], a[12:15]\n"
"v_mfma_f32_16x16x32_bf16 a[8:11], v[108:111], a[84:87], 0\n"
"buffer_load_dwordx4 a[120:123], v234, s[44:47], s35 offen offset:0 sc1\n"
"v_mfma_f32_16x16x32_bf16 a[8:11], v[88:91], a[68:71], a[8:11]\n"
"v_mfma_f32_16x16x32_bf16 a[4:7], v[84:87], a[68:71], 0\n"
"v_mfma_f32_16x16x32_bf16 a[4:7], v[104:107], a[84:87], a[4:7]\n"
"buffer_load_dwordx4 a[108:111], v235, s[44:47], s61 offen offset:0 sc1\n"
"v_mfma_f32_16x16x32_bf16 a[0:3], v[100:103], a[84:87], 0\n"
"v_mfma_f32_16x16x32_bf16 a[0:3], v[80:83], a[68:71], a[0:3]\n"
"buffer_load_dwordx4 a[124:127], v235, s[44:47], s35 offen offset:0 sc1\n"
"s_waitcnt vmcnt(10)\n"
"v_mfma_f32_16x16x32_bf16 a[20:23], v[80:83], a[72:75], 0\n"
"s_add_u32 s61, s61, 32768\n"
"s_add_u32 s35, s61, 16384\n"
"v_mfma_f32_16x16x32_bf16 a[20:23], v[100:103], a[88:91], a[20:23]\n"
"v_mfma_f32_16x16x32_bf16 a[24:27], v[104:107], a[88:91], 0\n"
"v_mfma_f32_16x16x32_bf16 a[24:27], v[84:87], a[72:75], a[24:27]\n"
"v_mfma_f32_16x16x32_bf16 a[28:31], v[88:91], a[72:75], 0\n"
"buffer_load_dwordx4 a[64:67], v232, s[44:47], s61 offen offset:0 sc1\n"
"v_mfma_f32_16x16x32_bf16 a[28:31], v[108:111], a[88:91], a[28:31]\n"
"v_mfma_f32_16x16x32_bf16 a[32:35], v[112:115], a[88:91], 0\n"
"v_mfma_f32_16x16x32_bf16 a[32:35], v[92:95], a[72:75], a[32:35]\n"
"v_mfma_f32_16x16x32_bf16 a[36:39], v[96:99], a[72:75], 0\n"
"v_mfma_f32_16x16x32_bf16 a[36:39], v[116:119], a[88:91], a[36:39]\n"
"buffer_load_dwordx4 a[80:83], v232, s[44:47], s35 offen offset:0 sc1\n"
"s_waitcnt vmcnt(10)\n"
"v_mfma_f32_16x16x32_bf16 a[56:59], v[116:119], a[92:95], 0\n"
"v_mfma_f32_16x16x32_bf16 a[56:59], v[96:99], a[76:79], a[56:59]\n"
"v_mfma_f32_16x16x32_bf16 a[52:55], v[92:95], a[76:79], 0\n"
"v_mfma_f32_16x16x32_bf16 a[52:55], v[112:115], a[92:95], a[52:55]\n"
"v_mfma_f32_16x16x32_bf16 a[48:51], v[108:111], a[92:95], 0\n"
"buffer_load_dwordx4 a[68:71], v233, s[44:47], s61 offen offset:0 sc1\n"
"v_mfma_f32_16x16x32_bf16 a[48:51], v[88:91], a[76:79], a[48:51]\n"
"v_mfma_f32_16x16x32_bf16 a[44:47], v[84:87], a[76:79], 0\n"
"v_mfma_f32_16x16x32_bf16 a[44:47], v[104:107], a[92:95], a[44:47]\n"
"v_mfma_f32_16x16x32_bf16 a[40:43], v[100:103], a[92:95], 0\n"
"v_mfma_f32_16x16x32_bf16 a[40:43], v[80:83], a[76:79], a[40:43]\n"
"buffer_load_dwordx4 a[84:87], v233, s[44:47], s35 offen offset:0 sc1\n"
"s_waitcnt vmcnt(10)\n"
"v_mfma_f32_16x16x32_bf16 v[44:47], v[120:123], a[96:99], v[44:47]\n"
"v_mfma_f32_16x16x32_bf16 v[44:47], v[204:207], a[112:115], v[44:47]\n"
"v_mfma_f32_16x16x32_bf16 v[48:51], v[208:211], a[112:115], v[48:51]\n"
"buffer_load_dwordx4 a[72:75], v234, s[44:47], s61 offen offset:0 sc1\n"
"v_mfma_f32_16x16x32_bf16 v[48:51], v[124:127], a[96:99], v[48:51]\n"
"v_mfma_f32_16x16x32_bf16 v[52:55], v[192:195], a[96:99], v[52:55]\n"
"buffer_load_dwordx4 a[88:91], v234, s[44:47], s35 offen offset:0 sc1\n"
"v_mfma_f32_16x16x32_bf16 v[52:55], v[212:215], a[112:115], v[52:55]\n"
"v_mfma_f32_16x16x32_bf16 v[56:59], v[216:219], a[112:115], v[56:59]\n"
"v_mfma_f32_16x16x32_bf16 v[56:59], v[196:199], a[96:99], v[56:59]\n"
"buffer_load_dwordx4 a[76:79], v235, s[44:47], s61 offen offset:0 sc1\n"
"v_mfma_f32_16x16x32_bf16 v[60:63], v[200:203], a[96:99], v[60:63]\n"
"v_mfma_f32_16x16x32_bf16 v[60:63], v[220:223], a[112:115], v[60:63]\n"
"buffer_load_dwordx4 a[92:95], v235, s[44:47], s35 offen offset:0 sc1\n"
"s_waitcnt vmcnt(12)\n"
"v_mfma_f32_16x16x32_bf16 a[16:19], v[220:223], a[116:119], a[16:19]\n"
"s_add_u32 s61, s61, 32768\n"
"s_add_u32 s35, s61, 16384\n"
"v_mfma_f32_16x16x32_bf16 a[16:19], v[200:203], a[100:103], a[16:19]\n"
"v_mfma_f32_16x16x32_bf16 a[12:15], v[196:199], a[100:103], a[12:15]\n"
"v_mfma_f32_16x16x32_bf16 a[12:15], v[216:219], a[116:119], a[12:15]\n"
"v_mfma_f32_16x16x32_bf16 a[8:11], v[212:215], a[116:119], a[8:11]\n"
"buffer_load_dwordx4 a[96:99], v232, s[44:47], s61 offen offset:0 sc1\n"
"v_mfma_f32_16x16x32_bf16 a[8:11], v[192:195], a[100:103], a[8:11]\n"
"v_mfma_f32_16x16x32_bf16 a[4:7], v[124:127], a[100:103], a[4:7]\n"
"v_mfma_f32_16x16x32_bf16 a[4:7], v[208:211], a[116:119], a[4:7]\n"
"v_mfma_f32_16x16x32_bf16 a[0:3], v[204:207], a[116:119], a[0:3]\n"
"v_mfma_f32_16x16x32_bf16 a[0:3], v[120:123], a[100:103], a[0:3]\n"
"buffer_load_dwordx4 a[112:115], v232, s[44:47], s35 offen offset:0 sc1\n"
"s_waitcnt vmcnt(12)\n"
"v_mfma_f32_16x16x32_bf16 a[20:23], v[120:123], a[104:107], a[20:23]\n"
"v_mfma_f32_16x16x32_bf16 a[20:23], v[204:207], a[120:123], a[20:23]\n"
"v_mfma_f32_16x16x32_bf16 a[24:27], v[208:211], a[120:123], a[24:27]\n"
"v_mfma_f32_16x16x32_bf16 a[24:27], v[124:127], a[104:107], a[24:27]\n"
"v_mfma_f32_16x16x32_bf16 a[28:31], v[192:195], a[104:107], a[28:31]\n"
"buffer_load_dwordx4 a[100:103], v233, s[44:47], s61 offen offset:0 sc1\n"
"v_mfma_f32_16x16x32_bf16 a[28:31], v[212:215], a[120:123], a[28:31]\n"
"v_mfma_f32_16x16x32_bf16 a[32:35], v[216:219], a[120:123], a[32:35]\n"
"v_mfma_f32_16x16x32_bf16 a[32:35], v[196:199], a[104:107], a[32:35]\n"
"v_mfma_f32_16x16x32_bf16 a[36:39], v[200:203], a[104:107], a[36:39]\n"
"v_mfma_f32_16x16x32_bf16 a[36:39], v[220:223], a[120:123], a[36:39]\n"
"buffer_load_dwordx4 a[116:119], v233, s[44:47], s35 offen offset:0 sc1\n"
"s_waitcnt vmcnt(12)\n"
"v_mfma_f32_16x16x32_bf16 a[56:59], v[220:223], a[124:127], a[56:59]\n"
"v_mfma_f32_16x16x32_bf16 a[56:59], v[200:203], a[108:111], a[56:59]\n"
"v_mfma_f32_16x16x32_bf16 a[52:55], v[196:199], a[108:111], a[52:55]\n"
"v_mfma_f32_16x16x32_bf16 a[52:55], v[216:219], a[124:127], a[52:55]\n"
"v_mfma_f32_16x16x32_bf16 a[48:51], v[212:215], a[124:127], a[48:51]\n"
"buffer_load_dwordx4 a[104:107], v234, s[44:47], s61 offen offset:0 sc1\n"
"v_mfma_f32_16x16x32_bf16 a[48:51], v[192:195], a[108:111], a[48:51]\n"
"v_mfma_f32_16x16x32_bf16 a[44:47], v[124:127], a[108:111], a[44:47]\n"
"v_mfma_f32_16x16x32_bf16 a[44:47], v[208:211], a[124:127], a[44:47]\n"
"v_mfma_f32_16x16x32_bf16 a[40:43], v[204:207], a[124:127], a[40:43]\n"
"v_mfma_f32_16x16x32_bf16 a[40:43], v[120:123], a[108:111], a[40:43]\n"
"buffer_load_dwordx4 a[120:123], v234, s[44:47], s35 offen offset:0 sc1\n"
"s_waitcnt vmcnt(12)\n"
"v_mfma_f32_16x16x32_bf16 v[44:47], a[212:215], a[64:67], v[44:47]\n"
"v_mfma_f32_16x16x32_bf16 v[44:47], a[232:235], a[80:83], v[44:47]\n"
"v_mfma_f32_16x16x32_bf16 v[48:51], a[236:239], a[80:83], v[48:51]\n"
"v_mfma_f32_16x16x32_bf16 v[48:51], a[216:219], a[64:67], v[48:51]\n"
"v_mfma_f32_16x16x32_bf16 v[52:55], a[220:223], a[64:67], v[52:55]\n"
"buffer_load_dwordx4 a[108:111], v235, s[44:47], s61 offen offset:0 sc1\n"
"v_mfma_f32_16x16x32_bf16 v[52:55], a[240:243], a[80:83], v[52:55]\n"
"v_mfma_f32_16x16x32_bf16 v[56:59], v[20:23], a[80:83], v[56:59]\n"
"v_mfma_f32_16x16x32_bf16 v[56:59], a[224:227], a[64:67], v[56:59]\n"
"v_mfma_f32_16x16x32_bf16 v[60:63], a[228:231], a[64:67], v[60:63]\n"
"v_mfma_f32_16x16x32_bf16 v[60:63], v[24:27], a[80:83], v[60:63]\n"
"buffer_load_dwordx4 a[124:127], v235, s[44:47], s35 offen offset:0 sc1\n"
"s_waitcnt vmcnt(12)\n"
"v_mfma_f32_16x16x32_bf16 a[16:19], v[24:27], a[84:87], a[16:19]\n"
"s_add_u32 s61, s61, 32768\n"
"s_add_u32 s35, s61, 16384\n"
"v_mfma_f32_16x16x32_bf16 a[16:19], a[228:231], a[68:71], a[16:19]\n"
"v_mfma_f32_16x16x32_bf16 a[12:15], a[224:227], a[68:71], a[12:15]\n"
"v_mfma_f32_16x16x32_bf16 a[12:15], v[20:23], a[84:87], a[12:15]\n"
"v_mfma_f32_16x16x32_bf16 a[8:11], a[240:243], a[84:87], a[8:11]\n"
"buffer_load_dwordx4 a[64:67], v232, s[44:47], s61 offen offset:0 sc1\n"
"v_mfma_f32_16x16x32_bf16 a[8:11], a[220:223], a[68:71], a[8:11]\n"
"v_mfma_f32_16x16x32_bf16 a[4:7], a[216:219], a[68:71], a[4:7]\n"
"v_mfma_f32_16x16x32_bf16 a[4:7], a[236:239], a[84:87], a[4:7]\n"
"v_mfma_f32_16x16x32_bf16 a[0:3], a[232:235], a[84:87], a[0:3]\n"
"v_mfma_f32_16x16x32_bf16 a[0:3], a[212:215], a[68:71], a[0:3]\n"
"buffer_load_dwordx4 a[80:83], v232, s[44:47], s35 offen offset:0 sc1\n"
"s_waitcnt vmcnt(12)\n"
"v_mfma_f32_16x16x32_bf16 a[20:23], a[212:215], a[72:75], a[20:23]\n"
"v_mfma_f32_16x16x32_bf16 a[20:23], a[232:235], a[88:91], a[20:23]\n"
"v_mfma_f32_16x16x32_bf16 a[24:27], a[236:239], a[88:91], a[24:27]\n"
"v_mfma_f32_16x16x32_bf16 a[24:27], a[216:219], a[72:75], a[24:27]\n"
"v_mfma_f32_16x16x32_bf16 a[28:31], a[220:223], a[72:75], a[28:31]\n"
"buffer_load_dwordx4 a[68:71], v233, s[44:47], s61 offen offset:0 sc1\n"
"v_mfma_f32_16x16x32_bf16 a[28:31], a[240:243], a[88:91], a[28:31]\n"
"v_mfma_f32_16x16x32_bf16 a[32:35], v[20:23], a[88:91], a[32:35]\n"
"v_mfma_f32_16x16x32_bf16 a[32:35], a[224:227], a[72:75], a[32:35]\n"
"v_mfma_f32_16x16x32_bf16 a[36:39], a[228:231], a[72:75], a[36:39]\n"
"v_mfma_f32_16x16x32_bf16 a[36:39], v[24:27], a[88:91], a[36:39]\n"
"buffer_load_dwordx4 a[84:87], v233, s[44:47], s35 offen offset:0 sc1\n"
"s_waitcnt vmcnt(12)\n"
"v_mfma_f32_16x16x32_bf16 a[56:59], v[24:27], a[92:95], a[56:59]\n"
"v_mfma_f32_16x16x32_bf16 a[56:59], a[228:231], a[76:79], a[56:59]\n"
"v_mfma_f32_16x16x32_bf16 a[52:55], a[224:227], a[76:79], a[52:55]\n"
"v_mfma_f32_16x16x32_bf16 a[52:55], v[20:23], a[92:95], a[52:55]\n"
"v_mfma_f32_16x16x32_bf16 a[48:51], a[240:243], a[92:95], a[48:51]\n"
"buffer_load_dwordx4 a[72:75], v234, s[44:47], s61 offen offset:0 sc1\n"
"v_mfma_f32_16x16x32_bf16 a[48:51], a[220:223], a[76:79], a[48:51]\n"
"v_mfma_f32_16x16x32_bf16 a[44:47], a[216:219], a[76:79], a[44:47]\n"
"v_mfma_f32_16x16x32_bf16 a[44:47], a[236:239], a[92:95], a[44:47]\n"
"v_mfma_f32_16x16x32_bf16 a[40:43], a[232:235], a[92:95], a[40:43]\n"
"v_mfma_f32_16x16x32_bf16 a[40:43], a[212:215], a[76:79], a[40:43]\n"
"buffer_load_dwordx4 a[88:91], v234, s[44:47], s35 offen offset:0 sc1\n"
"s_waitcnt vmcnt(12)\n"
"v_mfma_f32_16x16x32_bf16 v[44:47], v[28:31], a[96:99], v[44:47]\n"
"v_mfma_f32_16x16x32_bf16 v[44:47], v[132:135], a[112:115], v[44:47]\n"
"v_mfma_f32_16x16x32_bf16 v[48:51], v[136:139], a[112:115], v[48:51]\n"
"v_mfma_f32_16x16x32_bf16 v[48:51], v[32:35], a[96:99], v[48:51]\n"
"v_mfma_f32_16x16x32_bf16 v[52:55], v[36:39], a[96:99], v[52:55]\n"
"buffer_load_dwordx4 a[76:79], v235, s[44:47], s61 offen offset:0 sc1\n"
"v_mfma_f32_16x16x32_bf16 v[52:55], v[140:143], a[112:115], v[52:55]\n"
"v_mfma_f32_16x16x32_bf16 v[56:59], v[144:147], a[112:115], v[56:59]\n"
"v_mfma_f32_16x16x32_bf16 v[56:59], v[40:43], a[96:99], v[56:59]\n"
"v_mfma_f32_16x16x32_bf16 v[60:63], v[128:131], a[96:99], v[60:63]\n"
"v_mfma_f32_16x16x32_bf16 v[60:63], v[148:151], a[112:115], v[60:63]\n"
"buffer_load_dwordx4 a[92:95], v235, s[44:47], s35 offen offset:0 sc1\n"
"s_waitcnt vmcnt(12)\n"
"v_mfma_f32_16x16x32_bf16 a[16:19], v[148:151], a[116:119], a[16:19]\n"
"s_add_u32 s61, s61, 32768\n"
"s_add_u32 s35, s61, 16384\n"
"v_mfma_f32_16x16x32_bf16 a[16:19], v[128:131], a[100:103], a[16:19]\n"
"v_mfma_f32_16x16x32_bf16 a[12:15], v[40:43], a[100:103], a[12:15]\n"
"v_mfma_f32_16x16x32_bf16 a[12:15], v[144:147], a[116:119], a[12:15]\n"
"v_mfma_f32_16x16x32_bf16 a[8:11], v[140:143], a[116:119], a[8:11]\n"
"buffer_load_dwordx4 a[96:99], v232, s[44:47], s61 offen offset:0 sc1\n"
"v_mfma_f32_16x16x32_bf16 a[8:11], v[36:39], a[100:103], a[8:11]\n"
"v_mfma_f32_16x16x32_bf16 a[4:7], v[32:35], a[100:103], a[4:7]\n"
"v_mfma_f32_16x16x32_bf16 a[4:7], v[136:139], a[116:119], a[4:7]\n"
"v_mfma_f32_16x16x32_bf16 a[0:3], v[132:135], a[116:119], a[0:3]\n"
"v_mfma_f32_16x16x32_bf16 a[0:3], v[28:31], a[100:103], a[0:3]\n"
"buffer_load_dwordx4 a[112:115], v232, s[44:47], s35 offen offset:0 sc1\n"
"s_waitcnt vmcnt(12)\n"
"v_mfma_f32_16x16x32_bf16 a[20:23], v[28:31], a[104:107], a[20:23]\n"
"v_mfma_f32_16x16x32_bf16 a[20:23], v[132:135], a[120:123], a[20:23]\n"
"v_mfma_f32_16x16x32_bf16 a[24:27], v[136:139], a[120:123], a[24:27]\n"
"v_mfma_f32_16x16x32_bf16 a[24:27], v[32:35], a[104:107], a[24:27]\n"
"v_mfma_f32_16x16x32_bf16 a[28:31], v[36:39], a[104:107], a[28:31]\n"
"buffer_load_dwordx4 a[100:103], v233, s[44:47], s61 offen offset:0 sc1\n"
"v_mfma_f32_16x16x32_bf16 a[28:31], v[140:143], a[120:123], a[28:31]\n"
"v_mfma_f32_16x16x32_bf16 a[32:35], v[144:147], a[120:123], a[32:35]\n"
"v_mfma_f32_16x16x32_bf16 a[32:35], v[40:43], a[104:107], a[32:35]\n"
"v_mfma_f32_16x16x32_bf16 a[36:39], v[128:131], a[104:107], a[36:39]\n"
"v_mfma_f32_16x16x32_bf16 a[36:39], v[148:151], a[120:123], a[36:39]\n"
"buffer_load_dwordx4 a[116:119], v233, s[44:47], s35 offen offset:0 sc1\n"
"s_waitcnt vmcnt(12)\n"
"v_mfma_f32_16x16x32_bf16 a[56:59], v[148:151], a[124:127], a[56:59]\n"
"v_mfma_f32_16x16x32_bf16 a[56:59], v[128:131], a[108:111], a[56:59]\n"
"v_mfma_f32_16x16x32_bf16 a[52:55], v[40:43], a[108:111], a[52:55]\n"
"v_mfma_f32_16x16x32_bf16 a[52:55], v[144:147], a[124:127], a[52:55]\n"
"v_mfma_f32_16x16x32_bf16 a[48:51], v[140:143], a[124:127], a[48:51]\n"
"buffer_load_dwordx4 a[104:107], v234, s[44:47], s61 offen offset:0 sc1\n"
"v_mfma_f32_16x16x32_bf16 a[48:51], v[36:39], a[108:111], a[48:51]\n"
"v_mfma_f32_16x16x32_bf16 a[44:47], v[32:35], a[108:111], a[44:47]\n"
"v_mfma_f32_16x16x32_bf16 a[44:47], v[136:139], a[124:127], a[44:47]\n"
"v_mfma_f32_16x16x32_bf16 a[40:43], v[132:135], a[124:127], a[40:43]\n"
"v_mfma_f32_16x16x32_bf16 a[40:43], v[28:31], a[108:111], a[40:43]\n"
"buffer_load_dwordx4 a[120:123], v234, s[44:47], s35 offen offset:0 sc1\n"
"s_waitcnt vmcnt(12)\n"
"v_mfma_f32_16x16x32_bf16 v[44:47], v[152:155], a[64:67], v[44:47]\n"
"v_mfma_f32_16x16x32_bf16 v[44:47], v[172:175], a[80:83], v[44:47]\n"
"v_mfma_f32_16x16x32_bf16 v[48:51], v[176:179], a[80:83], v[48:51]\n"
"v_mfma_f32_16x16x32_bf16 v[48:51], v[156:159], a[64:67], v[48:51]\n"
"v_mfma_f32_16x16x32_bf16 v[52:55], v[160:163], a[64:67], v[52:55]\n"
"buffer_load_dwordx4 a[108:111], v235, s[44:47], s61 offen offset:0 sc1\n"
"v_mfma_f32_16x16x32_bf16 v[52:55], v[180:183], a[80:83], v[52:55]\n"
"v_mfma_f32_16x16x32_bf16 v[56:59], v[184:187], a[80:83], v[56:59]\n"
"v_mfma_f32_16x16x32_bf16 v[56:59], v[164:167], a[64:67], v[56:59]\n"
"v_mfma_f32_16x16x32_bf16 v[60:63], v[168:171], a[64:67], v[60:63]\n"
"v_mfma_f32_16x16x32_bf16 v[60:63], v[188:191], a[80:83], v[60:63]\n"
"buffer_load_dwordx4 a[124:127], v235, s[44:47], s35 offen offset:0 sc1\n"
"s_waitcnt vmcnt(12)\n"
"v_mfma_f32_16x16x32_bf16 a[16:19], v[188:191], a[84:87], a[16:19]\n"
"s_add_u32 s61, s61, 32768\n"
"s_add_u32 s35, s61, 16384\n"
"v_mfma_f32_16x16x32_bf16 a[16:19], v[168:171], a[68:71], a[16:19]\n"
"v_mfma_f32_16x16x32_bf16 a[12:15], v[164:167], a[68:71], a[12:15]\n"
"v_mfma_f32_16x16x32_bf16 a[12:15], v[184:187], a[84:87], a[12:15]\n"
"v_mfma_f32_16x16x32_bf16 a[8:11], v[180:183], a[84:87], a[8:11]\n"
"buffer_load_dwordx4 a[64:67], v232, s[44:47], s61 offen offset:0 sc1\n"
"v_mfma_f32_16x16x32_bf16 a[8:11], v[160:163], a[68:71], a[8:11]\n"
"v_mfma_f32_16x16x32_bf16 a[4:7], v[156:159], a[68:71], a[4:7]\n"
"v_mfma_f32_16x16x32_bf16 a[4:7], v[176:179], a[84:87], a[4:7]\n"
"v_mfma_f32_16x16x32_bf16 a[0:3], v[172:175], a[84:87], a[0:3]\n"
"v_mfma_f32_16x16x32_bf16 a[0:3], v[152:155], a[68:71], a[0:3]\n"
"buffer_load_dwordx4 a[80:83], v232, s[44:47], s35 offen offset:0 sc1\n"
"s_waitcnt vmcnt(12)\n"
"v_mfma_f32_16x16x32_bf16 a[20:23], v[152:155], a[72:75], a[20:23]\n"
"v_mfma_f32_16x16x32_bf16 a[20:23], v[172:175], a[88:91], a[20:23]\n"
"v_mfma_f32_16x16x32_bf16 a[24:27], v[176:179], a[88:91], a[24:27]\n"
"v_mfma_f32_16x16x32_bf16 a[24:27], v[156:159], a[72:75], a[24:27]\n"
"v_mfma_f32_16x16x32_bf16 a[28:31], v[160:163], a[72:75], a[28:31]\n"
"buffer_load_dwordx4 a[68:71], v233, s[44:47], s61 offen offset:0 sc1\n"
"v_mfma_f32_16x16x32_bf16 a[28:31], v[180:183], a[88:91], a[28:31]\n"
"v_mfma_f32_16x16x32_bf16 a[32:35], v[184:187], a[88:91], a[32:35]\n"
"v_mfma_f32_16x16x32_bf16 a[32:35], v[164:167], a[72:75], a[32:35]\n"
"v_mfma_f32_16x16x32_bf16 a[36:39], v[168:171], a[72:75], a[36:39]\n"
"v_mfma_f32_16x16x32_bf16 a[36:39], v[188:191], a[88:91], a[36:39]\n"
"buffer_load_dwordx4 a[84:87], v233, s[44:47], s35 offen offset:0 sc1\n"
"s_waitcnt vmcnt(12)\n"
"v_mfma_f32_16x16x32_bf16 a[56:59], v[188:191], a[92:95], a[56:59]\n"
"v_mfma_f32_16x16x32_bf16 a[56:59], v[168:171], a[76:79], a[56:59]\n"
"v_mfma_f32_16x16x32_bf16 a[52:55], v[164:167], a[76:79], a[52:55]\n"
"v_mfma_f32_16x16x32_bf16 a[52:55], v[184:187], a[92:95], a[52:55]\n"
"v_mfma_f32_16x16x32_bf16 a[48:51], v[180:183], a[92:95], a[48:51]\n"
"buffer_load_dwordx4 a[72:75], v234, s[44:47], s61 offen offset:0 sc1\n"
"v_mfma_f32_16x16x32_bf16 a[48:51], v[160:163], a[76:79], a[48:51]\n"
"v_mfma_f32_16x16x32_bf16 a[44:47], v[156:159], a[76:79], a[44:47]\n"
"v_mfma_f32_16x16x32_bf16 a[44:47], v[176:179], a[92:95], a[44:47]\n"
"v_mfma_f32_16x16x32_bf16 a[40:43], v[172:175], a[92:95], a[40:43]\n"
"v_mfma_f32_16x16x32_bf16 a[40:43], v[152:155], a[76:79], a[40:43]\n"
"buffer_load_dwordx4 a[88:91], v234, s[44:47], s35 offen offset:0 sc1\n"
"s_waitcnt vmcnt(12)\n"
"v_mfma_f32_16x16x32_bf16 v[44:47], v[224:227], a[96:99], v[44:47]\n"
"v_mfma_f32_16x16x32_bf16 v[44:47], a[144:147], a[112:115], v[44:47]\n"
"v_mfma_f32_16x16x32_bf16 v[48:51], a[148:151], a[112:115], v[48:51]\n"
"v_mfma_f32_16x16x32_bf16 v[48:51], a[128:131], a[96:99], v[48:51]\n"
"v_mfma_f32_16x16x32_bf16 v[52:55], a[132:135], a[96:99], v[52:55]\n"
"buffer_load_dwordx4 a[76:79], v235, s[44:47], s61 offen offset:0 sc1\n"
"v_mfma_f32_16x16x32_bf16 v[52:55], a[152:155], a[112:115], v[52:55]\n"
"v_mfma_f32_16x16x32_bf16 v[56:59], a[156:159], a[112:115], v[56:59]\n"
"L_w12_%=:\n"
"s_cbranch_execz L_wd3_%=\n"
"v_mfma_f32_16x16x32_bf16 v[56:59], a[136:139], a[96:99], v[56:59]\n"
"v_mfma_f32_16x16x32_bf16 v[60:63], a[140:143], a[96:99], v[60:63]\n"
"v_mfma_f32_16x16x32_bf16 v[60:63], a[192:195], a[112:115], v[60:63]\n"
"buffer_load_dwordx4 a[92:95], v235, s[44:47], s35 offen offset:0 sc1\n"
"s_waitcnt vmcnt(12)\n"
"v_mfma_f32_16x16x32_bf16 a[16:19], a[192:195], a[116:119], a[16:19]\n"
"s_add_u32 s61, s61, 32768\n"
"s_add_u32 s35, s61, 16384\n"
"v_mfma_f32_16x16x32_bf16 a[16:19], a[140:143], a[100:103], a[16:19]\n"
"v_mfma_f32_16x16x32_bf16 a[12:15], a[136:139], a[100:103], a[12:15]\n"
"v_mfma_f32_16x16x32_bf16 a[12:15], a[156:159], a[116:119], a[12:15]\n"
"v_mfma_f32_16x16x32_bf16 a[8:11], a[152:155], a[116:119], a[8:11]\n"
"buffer_load_dwordx4 a[96:99], v232, s[44:47], s61 offen offset:0 sc1\n"
"v_mfma_f32_16x16x32_bf16 a[8:11], a[132:135], a[100:103], a[8:11]\n"
"v_mfma_f32_16x16x32_bf16 a[4:7], a[128:131], a[100:103], a[4:7]\n"
"v_mfma_f32_16x16x32_bf16 a[4:7], a[148:151], a[116:119], a[4:7]\n"
"v_mfma_f32_16x16x32_bf16 a[0:3], a[144:147], a[116:119], a[0:3]\n"
"v_mfma_f32_16x16x32_bf16 a[0:3], v[224:227], a[100:103], a[0:3]\n"
"buffer_load_dwordx4 a[112:115], v232, s[44:47], s35 offen offset:0 sc1\n"
"s_waitcnt vmcnt(12)\n"
"v_mfma_f32_16x16x32_bf16 a[20:23], v[224:227], a[104:107], a[20:23]\n"
"v_mfma_f32_16x16x32_bf16 a[20:23], a[144:147], a[120:123], a[20:23]\n"
"v_mfma_f32_16x16x32_bf16 a[24:27], a[148:151], a[120:123], a[24:27]\n"
"v_mfma_f32_16x16x32_bf16 a[24:27], a[128:131], a[104:107], a[24:27]\n"
"v_mfma_f32_16x16x32_bf16 a[28:31], a[132:135], a[104:107], a[28:31]\n"
"buffer_load_dwordx4 a[100:103], v233, s[44:47], s61 offen offset:0 sc1\n"
"v_mfma_f32_16x16x32_bf16 a[28:31], a[152:155], a[120:123], a[28:31]\n"
"v_mfma_f32_16x16x32_bf16 a[32:35], a[156:159], a[120:123], a[32:35]\n"
"v_mfma_f32_16x16x32_bf16 a[32:35], a[136:139], a[104:107], a[32:35]\n"
"v_mfma_f32_16x16x32_bf16 a[36:39], a[140:143], a[104:107], a[36:39]\n"
"v_mfma_f32_16x16x32_bf16 a[36:39], a[192:195], a[120:123], a[36:39]\n"
"buffer_load_dwordx4 a[116:119], v233, s[44:47], s35 offen offset:0 sc1\n"
"s_waitcnt vmcnt(12)\n"
"v_mfma_f32_16x16x32_bf16 a[56:59], a[192:195], a[124:127], a[56:59]\n"
"v_mfma_f32_16x16x32_bf16 a[56:59], a[140:143], a[108:111], a[56:59]\n"
"v_mfma_f32_16x16x32_bf16 a[52:55], a[136:139], a[108:111], a[52:55]\n"
"v_mfma_f32_16x16x32_bf16 a[52:55], a[156:159], a[124:127], a[52:55]\n"
"v_mfma_f32_16x16x32_bf16 a[48:51], a[152:155], a[124:127], a[48:51]\n"
"buffer_load_dwordx4 a[104:107], v234, s[44:47], s61 offen offset:0 sc1\n"
"v_mfma_f32_16x16x32_bf16 a[48:51], a[132:135], a[108:111], a[48:51]\n"
"v_mfma_f32_16x16x32_bf16 a[44:47], a[128:131], a[108:111], a[44:47]\n"
"v_mfma_f32_16x16x32_bf16 a[44:47], a[148:151], a[124:127], a[44:47]\n"
"v_mfma_f32_16x16x32_bf16 a[40:43], a[144:147], a[124:127], a[40:43]\n"
"v_mfma_f32_16x16x32_bf16 a[40:43], v[224:227], a[108:111], a[40:43]\n"
"buffer_load_dwordx4 a[120:123], v234, s[44:47], s35 offen offset:0 sc1\n"
"s_waitcnt vmcnt(12)\n"
"v_mfma_f32_16x16x32_bf16 v[44:47], a[196:199], a[64:67], v[44:47]\n"
"v_mfma_f32_16x16x32_bf16 v[44:47], v[72:75], a[80:83], v[44:47]\n"
"v_mfma_f32_16x16x32_bf16 v[48:51], v[76:79], a[80:83], v[48:51]\n"
"v_mfma_f32_16x16x32_bf16 v[48:51], a[200:203], a[64:67], v[48:51]\n"
"v_mfma_f32_16x16x32_bf16 v[52:55], a[204:207], a[64:67], v[52:55]\n"
"buffer_load_dwordx4 a[108:111], v235, s[44:47], s61 offen offset:0 sc1\n"
"v_mfma_f32_16x16x32_bf16 v[52:55], a[160:163], a[80:83], v[52:55]\n"
"v_mfma_f32_16x16x32_bf16 v[56:59], a[164:167], a[80:83], v[56:59]\n"
"v_mfma_f32_16x16x32_bf16 v[56:59], v[64:67], a[64:67], v[56:59]\n"
"v_mfma_f32_16x16x32_bf16 v[60:63], v[68:71], a[64:67], v[60:63]\n"
"v_mfma_f32_16x16x32_bf16 v[60:63], a[168:171], a[80:83], v[60:63]\n"
"buffer_load_dwordx4 a[124:127], v235, s[44:47], s35 offen offset:0 sc1\n"
"s_waitcnt vmcnt(12)\n"
"v_mfma_f32_16x16x32_bf16 a[16:19], a[168:171], a[84:87], a[16:19]\n"
"s_mov_b32 s61, s55\n"
"s_add_u32 s35, s61, 16384\n"
"v_mfma_f32_16x16x32_bf16 a[16:19], v[68:71], a[68:71], a[16:19]\n"
"v_mfma_f32_16x16x32_bf16 a[12:15], v[64:67], a[68:71], a[12:15]\n"
"v_mfma_f32_16x16x32_bf16 a[12:15], a[164:167], a[84:87], a[12:15]\n"
"v_mfma_f32_16x16x32_bf16 a[8:11], a[160:163], a[84:87], a[8:11]\n"
"buffer_load_dwordx4 a[64:67], v232, s[44:47], s61 offen offset:0 sc1\n"
"v_mfma_f32_16x16x32_bf16 a[8:11], a[204:207], a[68:71], a[8:11]\n"
"v_mfma_f32_16x16x32_bf16 a[4:7], a[200:203], a[68:71], a[4:7]\n"
"v_mfma_f32_16x16x32_bf16 a[4:7], v[76:79], a[84:87], a[4:7]\n"
"v_mfma_f32_16x16x32_bf16 a[0:3], v[72:75], a[84:87], a[0:3]\n"
"v_mfma_f32_16x16x32_bf16 a[0:3], a[196:199], a[68:71], a[0:3]\n"
"buffer_load_dwordx4 a[80:83], v232, s[44:47], s35 offen offset:0 sc1\n"
"s_waitcnt vmcnt(12)\n"
"v_mfma_f32_16x16x32_bf16 a[20:23], a[196:199], a[72:75], a[20:23]\n"
"v_mfma_f32_16x16x32_bf16 a[20:23], v[72:75], a[88:91], a[20:23]\n"
"v_mfma_f32_16x16x32_bf16 a[24:27], v[76:79], a[88:91], a[24:27]\n"
"v_mfma_f32_16x16x32_bf16 a[24:27], a[200:203], a[72:75], a[24:27]\n"
"v_mfma_f32_16x16x32_bf16 a[28:31], a[204:207], a[72:75], a[28:31]\n"
"buffer_load_dwordx4 a[68:71], v233, s[44:47], s61 offen offset:0 sc1\n"
"v_mfma_f32_16x16x32_bf16 a[28:31], a[160:163], a[88:91], a[28:31]\n"
"v_mfma_f32_16x16x32_bf16 a[32:35], a[164:167], a[88:91], a[32:35]\n"
"v_mfma_f32_16x16x32_bf16 a[32:35], v[64:67], a[72:75], a[32:35]\n"
"v_mfma_f32_16x16x32_bf16 a[36:39], v[68:71], a[72:75], a[36:39]\n"
"v_mfma_f32_16x16x32_bf16 a[36:39], a[168:171], a[88:91], a[36:39]\n"
"buffer_load_dwordx4 a[84:87], v233, s[44:47], s35 offen offset:0 sc1\n"
"s_waitcnt vmcnt(12)\n"
"v_mfma_f32_16x16x32_bf16 a[56:59], a[168:171], a[92:95], a[56:59]\n"
"v_mfma_f32_16x16x32_bf16 a[56:59], v[68:71], a[76:79], a[56:59]\n"
"v_mfma_f32_16x16x32_bf16 a[52:55], v[64:67], a[76:79], a[52:55]\n"
"v_mfma_f32_16x16x32_bf16 a[52:55], a[164:167], a[92:95], a[52:55]\n"
"v_mfma_f32_16x16x32_bf16 a[48:51], a[160:163], a[92:95], a[48:51]\n"
"buffer_load_dwordx4 a[72:75], v234, s[44:47], s61 offen offset:0 sc1\n"
"v_mfma_f32_16x16x32_bf16 a[48:51], a[204:207], a[76:79], a[48:51]\n"
"v_mfma_f32_16x16x32_bf16 a[44:47], a[200:203], a[76:79], a[44:47]\n"
"v_mfma_f32_16x16x32_bf16 a[44:47], v[76:79], a[92:95], a[44:47]\n"
"v_mfma_f32_16x16x32_bf16 a[40:43], v[72:75], a[92:95], a[40:43]\n"
"v_mfma_f32_16x16x32_bf16 a[40:43], a[196:199], a[76:79], a[40:43]\n"
"buffer_load_dwordx4 a[88:91], v234, s[44:47], s35 offen offset:0 sc1\n"
"s_waitcnt vmcnt(10)\n"
"v_mfma_f32_16x16x32_bf16 a[0:3], a[172:175], a[100:103], a[0:3]\n"
"v_mfma_f32_16x16x32_bf16 a[0:3], a[208:211], a[116:119], a[0:3]\n"
"v_mfma_f32_16x16x32_bf16 a[4:7], a[60:63], a[116:119], a[4:7]\n"
"v_mfma_f32_16x16x32_bf16 a[4:7], a[176:179], a[100:103], a[4:7]\n"
"v_mfma_f32_16x16x32_bf16 a[8:11], a[180:183], a[100:103], a[8:11]\n"
"buffer_load_dwordx4 a[76:79], v235, s[44:47], s61 offen offset:0 sc1\n"
"v_mfma_f32_16x16x32_bf16 a[8:11], a[244:247], a[116:119], a[8:11]\n"
"v_mfma_f32_16x16x32_bf16 a[12:15], a[248:251], a[116:119], a[12:15]\n"
"s_add_u32 s34, s62, 216064\n"
"v_mfma_f32_16x16x32_bf16 a[12:15], a[184:187], a[100:103], a[12:15]\n"
"buffer_load_dwordx4 v228, s[44:47], s34 offen offset:1024 sc1 lds\n"
"v_mfma_f32_16x16x32_bf16 a[16:19], a[188:191], a[100:103], a[16:19]\n"
"v_mfma_f32_16x16x32_bf16 a[16:19], a[252:255], a[116:119], a[16:19]\n"
"buffer_load_dwordx4 a[92:95], v235, s[44:47], s35 offen offset:0 sc1\n"
"s_waitcnt vmcnt(11)\n"
"v_mfma_f32_16x16x32_bf16 a[36:39], a[252:255], a[120:123], a[36:39]\n"
"s_add_u32 s61, s61, 32768\n"
"s_add_u32 s35, s61, 16384\n"
"v_mfma_f32_16x16x32_bf16 a[36:39], a[188:191], a[104:107], a[36:39]\n"
"v_mfma_f32_16x16x32_bf16 a[32:35], a[184:187], a[104:107], a[32:35]\n"
"v_mfma_f32_16x16x32_bf16 a[32:35], a[248:251], a[120:123], a[32:35]\n"
"ds_write_b128 v246, a[0:3] offset:0\n"
"v_mfma_f32_16x16x32_bf16 a[28:31], a[244:247], a[120:123], a[28:31]\n"
"buffer_load_dwordx4 a[100:103], v233, s[44:47], s61 offen offset:0 sc1\n"
"v_mfma_f32_16x16x32_bf16 a[28:31], a[180:183], a[104:107], a[28:31]\n"
"v_mfma_f32_16x16x32_bf16 a[24:27], a[176:179], a[104:107], a[24:27]\n"
"ds_write_b128 v246, a[4:7] offset:1024\n"
"v_mfma_f32_16x16x32_bf16 a[24:27], a[60:63], a[120:123], a[24:27]\n"
"v_mfma_f32_16x16x32_bf16 a[20:23], a[208:211], a[120:123], a[20:23]\n"
"v_mfma_f32_16x16x32_bf16 a[20:23], a[172:175], a[104:107], a[20:23]\n"
"buffer_load_dwordx4 a[116:119], v233, s[44:47], s35 offen offset:0 sc1\n"
"s_waitcnt vmcnt(11)\n"
"v_mfma_f32_16x16x32_bf16 a[40:43], a[172:175], a[108:111], a[40:43]\n"
"ds_write_b128 v246, a[8:11] offset:2048\n"
"v_mfma_f32_16x16x32_bf16 a[40:43], a[208:211], a[124:127], a[40:43]\n"
"v_mfma_f32_16x16x32_bf16 a[44:47], a[60:63], a[124:127], a[44:47]\n"
"ds_write_b128 v246, a[12:15] offset:3072\n"
"v_mfma_f32_16x16x32_bf16 a[44:47], a[176:179], a[108:111], a[44:47]\n"
"v_mfma_f32_16x16x32_bf16 a[48:51], a[180:183], a[108:111], a[48:51]\n"
"buffer_load_dwordx4 a[104:107], v234, s[44:47], s61 offen offset:0 sc1\n"
"v_mfma_f32_16x16x32_bf16 a[48:51], a[244:247], a[124:127], a[48:51]\n"
"ds_write_b128 v246, a[16:19] offset:4096\n"
"v_mfma_f32_16x16x32_bf16 a[52:55], a[248:251], a[124:127], a[52:55]\n"
"s_add_u32 s34, s63, 2048\n"
"v_mfma_f32_16x16x32_bf16 a[52:55], a[184:187], a[108:111], a[52:55]\n"
"buffer_load_dwordx4 v228, s[44:47], s34 offen offset:2048 sc1 lds\n"
"v_mfma_f32_16x16x32_bf16 a[56:59], a[188:191], a[108:111], a[56:59]\n"
"ds_write_b128 v247, a[36:39] offset:4096\n"
"v_mfma_f32_16x16x32_bf16 a[56:59], a[252:255], a[124:127], a[56:59]\n"
"buffer_load_dwordx4 a[120:123], v234, s[44:47], s35 offen offset:0 sc1\n"
"s_waitcnt vmcnt(20)\n"
"v_mfma_f32_16x16x32_bf16 v[60:63], a[252:255], a[112:115], v[60:63]\n"
"v_mfma_f32_16x16x32_bf16 v[60:63], a[188:191], a[96:99], v[60:63]\n"
"ds_write_b128 v247, a[32:35] offset:3072\n"
"v_mfma_f32_16x16x32_bf16 v[56:59], a[184:187], a[96:99], v[56:59]\n"
"v_mfma_f32_16x16x32_bf16 v[56:59], a[248:251], a[112:115], v[56:59]\n"
"v_mfma_f32_16x16x32_bf16 v[52:55], a[244:247], a[112:115], v[52:55]\n"
"buffer_load_dwordx4 a[108:111], v235, s[44:47], s61 offen offset:0 sc1\n"
"v_mfma_f32_16x16x32_bf16 v[52:55], a[180:183], a[96:99], v[52:55]\n"
"ds_write_b128 v247, a[28:31] offset:2048\n"
"v_mfma_f32_16x16x32_bf16 v[48:51], a[176:179], a[96:99], v[48:51]\n"
"v_mfma_f32_16x16x32_bf16 v[48:51], a[60:63], a[112:115], v[48:51]\n"
"ds_write_b128 v247, a[24:27] offset:1024\n"
"v_mfma_f32_16x16x32_bf16 v[44:47], a[208:211], a[112:115], v[44:47]\n"
"v_mfma_f32_16x16x32_bf16 v[44:47], a[172:175], a[96:99], v[44:47]\n"
"buffer_load_dwordx4 a[124:127], v235, s[44:47], s35 offen offset:0 sc1\n"
"s_waitcnt vmcnt(14)\n"
"v_mfma_f32_16x16x32_bf16 v[0:3], v[80:83], a[64:67], 0\n"
"ds_write_b128 v247, a[20:23] offset:0\n"
"v_mfma_f32_16x16x32_bf16 v[0:3], v[100:103], a[80:83], v[0:3]\n"
"v_mfma_f32_16x16x32_bf16 v[4:7], v[104:107], a[80:83], 0\n"
"v_mfma_f32_16x16x32_bf16 v[4:7], v[84:87], a[64:67], v[4:7]\n"
"ds_write_b128 v248, a[40:43] offset:0\n"
"v_mfma_f32_16x16x32_bf16 v[8:11], v[88:91], a[64:67], 0\n"
"buffer_load_dwordx4 a[96:99], v232, s[44:47], s61 offen offset:0 sc1\n"
"v_mfma_f32_16x16x32_bf16 v[8:11], v[108:111], a[80:83], v[8:11]\n"
"v_mfma_f32_16x16x32_bf16 v[12:15], v[112:115], a[80:83], 0\n"
"ds_write_b128 v248, a[44:47] offset:1024\n"
"s_add_u32 m0, s56, 57344\n"
"s_add_u32 s34, s62, 4096\n"
"v_mfma_f32_16x16x32_bf16 v[12:15], v[92:95], a[64:67], v[12:15]\n"
"buffer_load_dwordx4 v228, s[44:47], s34 offen offset:0 sc1 lds\n"
"v_mfma_f32_16x16x32_bf16 v[16:19], v[96:99], a[64:67], 0\n"
"v_mfma_f32_16x16x32_bf16 v[16:19], v[116:119], a[80:83], v[16:19]\n"
"buffer_load_dwordx4 a[112:115], v232, s[44:47], s35 offen offset:0 sc1\n"
"s_waitcnt vmcnt(15)\n"
"v_mfma_f32_16x16x32_bf16 a[16:19], v[116:119], a[84:87], 0\n"
"s_add_u32 s61, s61, 32768\n"
"s_add_u32 s35, s61, 16384\n"
"ds_write_b128 v248, a[48:51] offset:2048\n"
"v_mfma_f32_16x16x32_bf16 a[16:19], v[96:99], a[68:71], a[16:19]\n"
"v_mfma_f32_16x16x32_bf16 a[12:15], v[92:95], a[68:71], 0\n"
"ds_write_b128 v248, a[52:55] offset:3072\n"
"v_mfma_f32_16x16x32_bf16 a[12:15], v[112:115], a[84:87], a[12:15]\n"
"v_mfma_f32_16x16x32_bf16 a[8:11], v[108:111], a[84:87], 0\n"
"buffer_load_dwordx4 a[64:67], v232, s[44:47], s61 offen offset:0 sc1\n"
"v_mfma_f32_16x16x32_bf16 a[8:11], v[88:91], a[68:71], a[8:11]\n"
"ds_write_b128 v248, a[56:59] offset:4096\n"
"v_mfma_f32_16x16x32_bf16 a[4:7], v[84:87], a[68:71], 0\n"
"s_add_u32 s34, s62, 46080\n"
"v_mfma_f32_16x16x32_bf16 a[4:7], v[104:107], a[84:87], a[4:7]\n"
"buffer_load_dwordx4 v228, s[44:47], s34 offen offset:1024 sc1 lds\n"
"v_mfma_f32_16x16x32_bf16 a[0:3], v[100:103], a[84:87], 0\n"
"v_mfma_f32_16x16x32_bf16 a[0:3], v[80:83], a[68:71], a[0:3]\n"
"buffer_load_dwordx4 a[80:83], v232, s[44:47], s35 offen offset:0 sc1\n"
"s_waitcnt vmcnt(16)\n"
"v_mfma_f32_16x16x32_bf16 a[20:23], v[80:83], a[72:75], 0\n"
"v_mfma_f32_16x16x32_bf16 a[20:23], v[100:103], a[88:91], a[20:23]\n"
"v_mfma_f32_16x16x32_bf16 a[24:27], v[104:107], a[88:91], 0\n"
"v_mfma_f32_16x16x32_bf16 a[24:27], v[84:87], a[72:75], a[24:27]\n"
"v_mfma_f32_16x16x32_bf16 a[28:31], v[88:91], a[72:75], 0\n"
"buffer_load_dwordx4 a[68:71], v233, s[44:47], s61 offen offset:0 sc1\n"
"v_mfma_f32_16x16x32_bf16 a[28:31], v[108:111], a[88:91], a[28:31]\n"
"v_mfma_f32_16x16x32_bf16 a[32:35], v[112:115], a[88:91], 0\n"
"s_add_u32 s34, s62, 88064\n"
"v_mfma_f32_16x16x32_bf16 a[32:35], v[92:95], a[72:75], a[32:35]\n"
"buffer_load_dwordx4 v228, s[44:47], s34 offen offset:2048 sc1 lds\n"
"v_mfma_f32_16x16x32_bf16 a[36:39], v[96:99], a[72:75], 0\n"
"v_mfma_f32_16x16x32_bf16 a[36:39], v[116:119], a[88:91], a[36:39]\n"
"buffer_load_dwordx4 a[84:87], v233, s[44:47], s35 offen offset:0 sc1\n"
"s_waitcnt vmcnt(16)\n"
"v_mfma_f32_16x16x32_bf16 a[56:59], v[116:119], a[92:95], 0\n"
"v_mfma_f32_16x16x32_bf16 a[56:59], v[96:99], a[76:79], a[56:59]\n"
"v_mfma_f32_16x16x32_bf16 a[52:55], v[92:95], a[76:79], 0\n"
"v_mfma_f32_16x16x32_bf16 a[52:55], v[112:115], a[92:95], a[52:55]\n"
"v_mfma_f32_16x16x32_bf16 a[48:51], v[108:111], a[92:95], 0\n"
"buffer_load_dwordx4 a[72:75], v234, s[44:47], s61 offen offset:0 sc1\n"
"v_mfma_f32_16x16x32_bf16 a[48:51], v[88:91], a[76:79], a[48:51]\n"
"v_mfma_f32_16x16x32_bf16 a[44:47], v[84:87], a[76:79], 0\n"
"v_mfma_f32_16x16x32_bf16 a[44:47], v[104:107], a[92:95], a[44:47]\n"
"v_mfma_f32_16x16x32_bf16 a[40:43], v[100:103], a[92:95], 0\n"
"v_mfma_f32_16x16x32_bf16 a[40:43], v[80:83], a[76:79], a[40:43]\n"
"buffer_load_dwordx4 a[88:91], v234, s[44:47], s35 offen offset:0 sc1\n"
"s_waitcnt lgkmcnt(0)\n"
"s_barrier\n"
"s_waitcnt vmcnt(8)\n"
"v_mfma_f32_16x16x32_bf16 v[0:3], v[120:123], a[96:99], v[0:3]\n"
"ds_read_b128 v[80:83], v251 offset:0\n"
"v_mfma_f32_16x16x32_bf16 v[0:3], v[204:207], a[112:115], v[0:3]\n"
"ds_read_b128 v[84:87], v251 offset:1024\n"
"v_mfma_f32_16x16x32_bf16 v[4:7], v[208:211], a[112:115], v[4:7]\n"
"ds_read_b128 v[88:91], v251 offset:2048\n"
"v_mfma_f32_16x16x32_bf16 v[4:7], v[124:127], a[96:99], v[4:7]\n"
"ds_read_b128 v[92:95], v251 offset:3072\n"
"v_mfma_f32_16x16x32_bf16 v[8:11], v[192:195], a[96:99], v[8:11]\n"
"buffer_load_dwordx4 a[76:79], v235, s[44:47], s61 offen offset:0 sc1\n"
"v_mfma_f32_16x16x32_bf16 v[8:11], v[212:215], a[112:115], v[8:11]\n"
"ds_read_b128 v[96:99], v251 offset:4096\n"
"v_mfma_f32_16x16x32_bf16 v[12:15], v[216:219], a[112:115], v[12:15]\n"
"ds_read_b128 v[100:103], v251 offset:5120\n"
"s_add_u32 s34, s62, 130048\n"
"v_mfma_f32_16x16x32_bf16 v[12:15], v[196:199], a[96:99], v[12:15]\n"
"buffer_load_dwordx4 v228, s[44:47], s34 offen offset:3072 sc1 lds\n"
"v_mfma_f32_16x16x32_bf16 v[16:19], v[200:203], a[96:99], v[16:19]\n"
"ds_read_b128 v[104:107], v251 offset:6144\n"
"v_mfma_f32_16x16x32_bf16 v[16:19], v[220:223], a[112:115], v[16:19]\n"
"buffer_load_dwordx4 a[92:95], v235, s[44:47], s35 offen offset:0 sc1\n"
"s_waitcnt vmcnt(19)\n"
"v_mfma_f32_16x16x32_bf16 a[16:19], v[220:223], a[116:119], a[16:19]\n"
"s_add_u32 s61, s61, 32768\n"
"s_add_u32 s35, s61, 16384\n"
"v_mfma_f32_16x16x32_bf16 a[16:19], v[200:203], a[100:103], a[16:19]\n"
"ds_read_b128 v[108:111], v251 offset:7168\n"
"v_mfma_f32_16x16x32_bf16 a[12:15], v[196:199], a[100:103], a[12:15]\n"
"ds_read_b128 v[112:115], v251 offset:8192\n"
"v_mfma_f32_16x16x32_bf16 a[12:15], v[216:219], a[116:119], a[12:15]\n"
"ds_read_b128 v[116:119], v251 offset:9216\n"
"v_mfma_f32_16x16x32_bf16 a[8:11], v[212:215], a[116:119], a[8:11]\n"
"buffer_load_dwordx4 a[96:99], v232, s[44:47], s61 offen offset:0 sc1\n"
"v_mfma_f32_16x16x32_bf16 a[8:11], v[192:195], a[100:103], a[8:11]\n"
"v_mfma_f32_16x16x32_bf16 a[4:7], v[124:127], a[100:103], a[4:7]\n"
"s_add_u32 m0, s56, 61440\n"
"s_add_u32 s34, s62, 176128\n"
"v_mfma_f32_16x16x32_bf16 a[4:7], v[208:211], a[116:119], a[4:7]\n"
"buffer_load_dwordx4 v228, s[44:47], s34 offen offset:0 sc1 lds\n"
"v_mfma_f32_16x16x32_bf16 a[0:3], v[204:207], a[116:119], a[0:3]\n"
"v_mfma_f32_16x16x32_bf16 a[0:3], v[120:123], a[100:103], a[0:3]\n"
"buffer_load_dwordx4 a[112:115], v232, s[44:47], s35 offen offset:0 sc1\n"
"s_waitcnt vmcnt(19)\n"
"v_mfma_f32_16x16x32_bf16 a[20:23], v[120:123], a[104:107], a[20:23]\n"
"s_waitcnt lgkmcnt(5)\n"
"v_add_f32 v44, v44, v80\n"
"v_add_f32 v45, v45, v81\n"
"v_mfma_f32_16x16x32_bf16 a[20:23], v[204:207], a[120:123], a[20:23]\n"
"v_add_f32 v46, v46, v82\n"
"v_add_f32 v47, v47, v83\n"
"v_mfma_f32_16x16x32_bf16 a[24:27], v[208:211], a[120:123], a[24:27]\n"
"v_add_f32 v48, v48, v84\n"
"v_add_f32 v49, v49, v85\n"
"v_mfma_f32_16x16x32_bf16 a[24:27], v[124:127], a[104:107], a[24:27]\n"
"v_add_f32 v50, v50, v86\n"
"v_add_f32 v51, v51, v87\n"
"v_mfma_f32_16x16x32_bf16 a[28:31], v[192:195], a[104:107], a[28:31]\n"
"buffer_load_dwordx4 a[100:103], v233, s[44:47], s61 offen offset:0 sc1\n"
"v_mfma_f32_16x16x32_bf16 a[28:31], v[212:215], a[120:123], a[28:31]\n"
"v_add_f32 v52, v52, v88\n"
"v_add_f32 v53, v53, v89\n"
"v_mfma_f32_16x16x32_bf16 a[32:35], v[216:219], a[120:123], a[32:35]\n"
"v_add_f32 v54, v54, v90\n"
"v_add_f32 v55, v55, v91\n"
"s_add_u32 s34, s62, 218112\n"
"v_mfma_f32_16x16x32_bf16 a[32:35], v[196:199], a[104:107], a[32:35]\n"
"buffer_load_dwordx4 v228, s[44:47], s34 offen offset:1024 sc1 lds\n"
"v_mfma_f32_16x16x32_bf16 a[36:39], v[200:203], a[104:107], a[36:39]\n"
"v_add_f32 v56, v56, v92\n"
"v_add_f32 v57, v57, v93\n"
"v_mfma_f32_16x16x32_bf16 a[36:39], v[220:223], a[120:123], a[36:39]\n"
"buffer_load_dwordx4 a[116:119], v233, s[44:47], s35 offen offset:0 sc1\n"
"s_waitcnt vmcnt(20)\n"
"v_mfma_f32_16x16x32_bf16 a[56:59], v[220:223], a[124:127], a[56:59]\n"
"v_add_f32 v58, v58, v94\n"
"v_add_f32 v59, v59, v95\n"
"v_mfma_f32_16x16x32_bf16 a[56:59], v[200:203], a[108:111], a[56:59]\n"
"v_add_f32 v60, v60, v96\n"
"v_add_f32 v61, v61, v97\n"
"v_mfma_f32_16x16x32_bf16 a[52:55], v[196:199], a[108:111], a[52:55]\n"
"v_add_f32 v62, v62, v98\n"
"v_add_f32 v63, v63, v99\n"
"v_mfma_f32_16x16x32_bf16 a[52:55], v[216:219], a[124:127], a[52:55]\n"
"ds_read_b128 v[80:83], v251 offset:10240\n"
"v_mfma_f32_16x16x32_bf16 a[48:51], v[212:215], a[124:127], a[48:51]\n"
"buffer_load_dwordx4 a[104:107], v234, s[44:47], s61 offen offset:0 sc1\n"
"v_mfma_f32_16x16x32_bf16 a[48:51], v[192:195], a[108:111], a[48:51]\n"
"ds_read_b128 v[84:87], v251 offset:11264\n"
"v_mfma_f32_16x16x32_bf16 a[44:47], v[124:127], a[108:111], a[44:47]\n"
"ds_read_b128 v[88:91], v251 offset:12288\n"
"v_mfma_f32_16x16x32_bf16 a[44:47], v[208:211], a[124:127], a[44:47]\n"
"ds_read_b128 v[92:95], v251 offset:13312\n"
"v_mfma_f32_16x16x32_bf16 a[40:43], v[204:207], a[124:127], a[40:43]\n"
"ds_read_b128 v[96:99], v251 offset:14336\n"
"v_mfma_f32_16x16x32_bf16 a[40:43], v[120:123], a[108:111], a[40:43]\n"
"buffer_load_dwordx4 a[120:123], v234, s[44:47], s35 offen offset:0 sc1\n"
"s_waitcnt vmcnt(16)\n"
"v_mfma_f32_16x16x32_bf16 v[0:3], a[212:215], a[64:67], v[0:3]\n"
"s_waitcnt lgkmcnt(5)\n"
"v_add_f32 v44, v44, v100\n"
"v_add_f32 v45, v45, v101\n"
"v_mfma_f32_16x16x32_bf16 v[0:3], a[232:235], a[80:83], v[0:3]\n"
"v_add_f32 v46, v46, v102\n"
"v_add_f32 v47, v47, v103\n"
"v_mfma_f32_16x16x32_bf16 v[4:7], a[236:239], a[80:83], v[4:7]\n"
"v_add_f32 v48, v48, v104\n"
"v_add_f32 v49, v49, v105\n"
"v_mfma_f32_16x16x32_bf16 v[4:7], a[216:219], a[64:67], v[4:7]\n"
"v_add_f32 v50, v50, v106\n"
"v_add_f32 v51, v51, v107\n"
"v_mfma_f32_16x16x32_bf16 v[8:11], a[220:223], a[64:67], v[8:11]\n"
"buffer_load_dwordx4 a[108:111], v235, s[44:47], s61 offen offset:0 sc1\n"
"v_mfma_f32_16x16x32_bf16 v[8:11], a[240:243], a[80:83], v[8:11]\n"
"v_add_f32 v52, v52, v108\n"
"v_add_f32 v53, v53, v109\n"
"v_mfma_f32_16x16x32_bf16 v[12:15], v[20:23], a[80:83], v[12:15]\n"
"v_add_f32 v54, v54, v110\n"
"v_add_f32 v55, v55, v111\n"
"s_add_u32 s34, s63, 4096\n"
"v_mfma_f32_16x16x32_bf16 v[12:15], a[224:227], a[64:67], v[12:15]\n"
"buffer_load_dwordx4 v228, s[44:47], s34 offen offset:2048 sc1 lds\n"
"v_mfma_f32_16x16x32_bf16 v[16:19], a[228:231], a[64:67], v[16:19]\n"
"v_add_f32 v56, v56, v112\n"
"v_add_f32 v57, v57, v113\n"
"v_mfma_f32_16x16x32_bf16 v[16:19], v[24:27], a[80:83], v[16:19]\n"
"buffer_load_dwordx4 a[124:127], v235, s[44:47], s35 offen offset:0 sc1\n"
"s_waitcnt vmcnt(16)\n"
"v_mfma_f32_16x16x32_bf16 a[16:19], v[24:27], a[84:87], a[16:19]\n"
"s_add_u32 s61, s61, 32768\n"
"s_add_u32 s35, s61, 16384\n"
"v_mfma_f32_16x16x32_bf16 a[16:19], a[228:231], a[68:71], a[16:19]\n"
"v_add_f32 v58, v58, v114\n"
"v_add_f32 v59, v59, v115\n"
"v_mfma_f32_16x16x32_bf16 a[12:15], a[224:227], a[68:71], a[12:15]\n"
"v_add_f32 v60, v60, v116\n"
"v_add_f32 v61, v61, v117\n"
"v_mfma_f32_16x16x32_bf16 a[12:15], v[20:23], a[84:87], a[12:15]\n"
"v_add_f32 v62, v62, v118\n"
"v_add_f32 v63, v63, v119\n"
"v_mfma_f32_16x16x32_bf16 a[8:11], a[240:243], a[84:87], a[8:11]\n"
"buffer_load_dwordx4 a[64:67], v232, s[44:47], s61 offen offset:0 sc1\n"
"v_mfma_f32_16x16x32_bf16 a[8:11], a[220:223], a[68:71], a[8:11]\n"
"s_waitcnt lgkmcnt(0)\n"
"v_add_f32 v44, v44, v80\n"
"v_add_f32 v45, v45, v81\n"
"v_mfma_f32_16x16x32_bf16 a[4:7], a[216:219], a[68:71], a[4:7]\n"
"v_add_f32 v46, v46, v82\n"
"v_add_f32 v47, v47, v83\n"
"v_mfma_f32_16x16x32_bf16 a[4:7], a[236:239], a[84:87], a[4:7]\n"
"v_add_f32 v48, v48, v84\n"
"v_add_f32 v49, v49, v85\n"
"v_mfma_f32_16x16x32_bf16 a[0:3], a[232:235], a[84:87], a[0:3]\n"
"v_add_f32 v50, v50, v86\n"
"v_add_f32 v51, v51, v87\n"
"v_mfma_f32_16x16x32_bf16 a[0:3], a[212:215], a[68:71], a[0:3]\n"
"buffer_load_dwordx4 a[80:83], v232, s[44:47], s35 offen offset:0 sc1\n"
"s_waitcnt vmcnt(16)\n"
"v_mfma_f32_16x16x32_bf16 a[20:23], a[212:215], a[72:75], a[20:23]\n"
"v_add_f32 v52, v52, v88\n"
"v_add_f32 v53, v53, v89\n"
"v_mfma_f32_16x16x32_bf16 a[20:23], a[232:235], a[88:91], a[20:23]\n"
"v_add_f32 v54, v54, v90\n"
"v_add_f32 v55, v55, v91\n"
"v_mfma_f32_16x16x32_bf16 a[24:27], a[236:239], a[88:91], a[24:27]\n"
"v_add_f32 v56, v56, v92\n"
"v_add_f32 v57, v57, v93\n"
"v_mfma_f32_16x16x32_bf16 a[24:27], a[216:219], a[72:75], a[24:27]\n"
"v_add_f32 v58, v58, v94\n"
"v_add_f32 v59, v59, v95\n"
"v_mfma_f32_16x16x32_bf16 a[28:31], a[220:223], a[72:75], a[28:31]\n"
"buffer_load_dwordx4 a[68:71], v233, s[44:47], s61 offen offset:0 sc1\n"
"v_mfma_f32_16x16x32_bf16 a[28:31], a[240:243], a[88:91], a[28:31]\n"
"v_add_f32 v60, v60, v96\n"
"v_add_f32 v61, v61, v97\n"
"v_mfma_f32_16x16x32_bf16 a[32:35], v[20:23], a[88:91], a[32:35]\n"
"v_add_f32 v62, v62, v98\n"
"v_add_f32 v63, v63, v99\n"
"v_mfma_f32_16x16x32_bf16 a[32:35], a[224:227], a[72:75], a[32:35]\n"
"v_cvt_pk_bf16_f32 v44, v44, v45\n"
"v_cvt_pk_bf16_f32 v45, v46, v47\n"
"v_mfma_f32_16x16x32_bf16 a[36:39], a[228:231], a[72:75], a[36:39]\n"
"v_cvt_pk_bf16_f32 v46, v48, v49\n"
"v_cvt_pk_bf16_f32 v47, v50, v51\n"
"v_mfma_f32_16x16x32_bf16 a[36:39], v[24:27], a[88:91], a[36:39]\n"
"buffer_load_dwordx4 a[84:87], v233, s[44:47], s35 offen offset:0 sc1\n"
"s_waitcnt vmcnt(15)\n"
"v_mfma_f32_16x16x32_bf16 a[56:59], v[24:27], a[92:95], a[56:59]\n"
"v_cvt_pk_bf16_f32 v48, v52, v53\n"
"v_cvt_pk_bf16_f32 v49, v54, v55\n"
"v_mfma_f32_16x16x32_bf16 a[56:59], a[228:231], a[76:79], a[56:59]\n"
"v_cvt_pk_bf16_f32 v50, v56, v57\n"
"v_cvt_pk_bf16_f32 v51, v58, v59\n"
"v_mfma_f32_16x16x32_bf16 a[52:55], a[224:227], a[76:79], a[52:55]\n"
"v_cvt_pk_bf16_f32 v52, v60, v61\n"
"v_cvt_pk_bf16_f32 v53, v62, v63\n"
"v_mfma_f32_16x16x32_bf16 a[52:55], v[20:23], a[92:95], a[52:55]\n"
"buffer_store_dwordx4 v[44:47], v228, s[44:47], s52 offen offset:0 sc1\n"
"v_mfma_f32_16x16x32_bf16 a[48:51], a[240:243], a[92:95], a[48:51]\n"
"buffer_load_dwordx4 a[72:75], v234, s[44:47], s61 offen offset:0 sc1\n"
"v_mfma_f32_16x16x32_bf16 a[48:51], a[220:223], a[76:79], a[48:51]\n"
"v_mfma_f32_16x16x32_bf16 a[44:47], a[216:219], a[76:79], a[44:47]\n"
"v_mfma_f32_16x16x32_bf16 a[44:47], a[236:239], a[92:95], a[44:47]\n"
"v_mfma_f32_16x16x32_bf16 a[40:43], a[232:235], a[92:95], a[40:43]\n"
"buffer_store_dwordx4 v[48:51], v228, s[44:47], s52 offen offset:1024 sc1\n"
"v_mfma_f32_16x16x32_bf16 a[40:43], a[212:215], a[76:79], a[40:43]\n"
"buffer_load_dwordx4 a[88:91], v234, s[44:47], s35 offen offset:0 sc1\n"
"s_waitcnt vmcnt(14)\n"
"v_mfma_f32_16x16x32_bf16 v[0:3], v[28:31], a[96:99], v[0:3]\n"
"v_mfma_f32_16x16x32_bf16 v[0:3], v[132:135], a[112:115], v[0:3]\n"
"v_mfma_f32_16x16x32_bf16 v[4:7], v[136:139], a[112:115], v[4:7]\n"
"v_mfma_f32_16x16x32_bf16 v[4:7], v[32:35], a[96:99], v[4:7]\n"
"buffer_store_dwordx2 v[52:53], v231, s[44:47], s52 offen offset:2048 sc1\n"
"v_mfma_f32_16x16x32_bf16 v[8:11], v[36:39], a[96:99], v[8:11]\n"
"buffer_load_dwordx4 a[76:79], v235, s[44:47], s61 offen offset:0 sc1\n"
"v_mfma_f32_16x16x32_bf16 v[8:11], v[140:143], a[112:115], v[8:11]\n"
"v_mfma_f32_16x16x32_bf16 v[12:15], v[144:147], a[112:115], v[12:15]\n"
"v_mfma_f32_16x16x32_bf16 v[12:15], v[40:43], a[96:99], v[12:15]\n"
"v_mfma_f32_16x16x32_bf16 v[16:19], v[128:131], a[96:99], v[16:19]\n"
"v_mfma_f32_16x16x32_bf16 v[16:19], v[148:151], a[112:115], v[16:19]\n"
"buffer_load_dwordx4 a[92:95], v235, s[44:47], s35 offen offset:0 sc1\n"
"s_waitcnt vmcnt(13)\n"
"v_mfma_f32_16x16x32_bf16 a[16:19], v[148:151], a[116:119], a[16:19]\n"
"s_add_u32 s61, s61, 32768\n"
"s_add_u32 s35, s61, 16384\n"
"v_mfma_f32_16x16x32_bf16 a[16:19], v[128:131], a[100:103], a[16:19]\n"
"v_mfma_f32_16x16x32_bf16 a[12:15], v[40:43], a[100:103], a[12:15]\n"
"v_mfma_f32_16x16x32_bf16 a[12:15], v[144:147], a[116:119], a[12:15]\n"
"v_mfma_f32_16x16x32_bf16 a[8:11], v[140:143], a[116:119], a[8:11]\n"
"buffer_load_dwordx4 a[96:99], v232, s[44:47], s61 offen offset:0 sc1\n"
"v_mfma_f32_16x16x32_bf16 a[8:11], v[36:39], a[100:103], a[8:11]\n"
"v_mfma_f32_16x16x32_bf16 a[4:7], v[32:35], a[100:103], a[4:7]\n"
"v_mfma_f32_16x16x32_bf16 a[4:7], v[136:139], a[116:119], a[4:7]\n"
"v_mfma_f32_16x16x32_bf16 a[0:3], v[132:135], a[116:119], a[0:3]\n"
"v_mfma_f32_16x16x32_bf16 a[0:3], v[28:31], a[100:103], a[0:3]\n"
"buffer_load_dwordx4 a[112:115], v232, s[44:47], s35 offen offset:0 sc1\n"
"s_waitcnt vmcnt(13)\n"
"v_mfma_f32_16x16x32_bf16 a[20:23], v[28:31], a[104:107], a[20:23]\n"
"v_mfma_f32_16x16x32_bf16 a[20:23], v[132:135], a[120:123], a[20:23]\n"
"v_mfma_f32_16x16x32_bf16 a[24:27], v[136:139], a[120:123], a[24:27]\n"
"v_mfma_f32_16x16x32_bf16 a[24:27], v[32:35], a[104:107], a[24:27]\n"
"v_mfma_f32_16x16x32_bf16 a[28:31], v[36:39], a[104:107], a[28:31]\n"
"buffer_load_dwordx4 a[100:103], v233, s[44:47], s61 offen offset:0 sc1\n"
"v_mfma_f32_16x16x32_bf16 a[28:31], v[140:143], a[120:123], a[28:31]\n"
"v_mfma_f32_16x16x32_bf16 a[32:35], v[144:147], a[120:123], a[32:35]\n"
"v_mfma_f32_16x16x32_bf16 a[32:35], v[40:43], a[104:107], a[32:35]\n"
"v_mfma_f32_16x16x32_bf16 a[36:39], v[128:131], a[104:107], a[36:39]\n"
"v_mfma_f32_16x16x32_bf16 a[36:39], v[148:151], a[120:123], a[36:39]\n"
"buffer_load_dwordx4 a[116:119], v233, s[44:47], s35 offen offset:0 sc1\n"
"s_waitcnt vmcnt(12)\n"
"v_mfma_f32_16x16x32_bf16 a[56:59], v[148:151], a[124:127], a[56:59]\n"
"v_mfma_f32_16x16x32_bf16 a[56:59], v[128:131], a[108:111], a[56:59]\n"
"v_mfma_f32_16x16x32_bf16 a[52:55], v[40:43], a[108:111], a[52:55]\n"
"v_mfma_f32_16x16x32_bf16 a[52:55], v[144:147], a[124:127], a[52:55]\n"
"v_mfma_f32_16x16x32_bf16 a[48:51], v[140:143], a[124:127], a[48:51]\n"
"buffer_load_dwordx4 a[104:107], v234, s[44:47], s61 offen offset:0 sc1\n"
"v_mfma_f32_16x16x32_bf16 a[48:51], v[36:39], a[108:111], a[48:51]\n"
"v_mfma_f32_16x16x32_bf16 a[44:47], v[32:35], a[108:111], a[44:47]\n"
"v_mfma_f32_16x16x32_bf16 a[44:47], v[136:139], a[124:127], a[44:47]\n"
"v_mfma_f32_16x16x32_bf16 a[40:43], v[132:135], a[124:127], a[40:43]\n"
"v_mfma_f32_16x16x32_bf16 a[40:43], v[28:31], a[108:111], a[40:43]\n"
"buffer_load_dwordx4 a[120:123], v234, s[44:47], s35 offen offset:0 sc1\n"
"s_waitcnt vmcnt(12)\n"
"v_mfma_f32_16x16x32_bf16 v[0:3], v[152:155], a[64:67], v[0:3]\n"
"v_mfma_f32_16x16x32_bf16 v[0:3], v[172:175], a[80:83], v[0:3]\n"
"v_mfma_f32_16x16x32_bf16 v[4:7], v[176:179], a[80:83], v[4:7]\n"
"v_mfma_f32_16x16x32_bf16 v[4:7], v[156:159], a[64:67], v[4:7]\n"
"v_mfma_f32_16x16x32_bf16 v[8:11], v[160:163], a[64:67], v[8:11]\n"
"buffer_load_dwordx4 a[108:111], v235, s[44:47], s61 offen offset:0 sc1\n"
"v_mfma_f32_16x16x32_bf16 v[8:11], v[180:183], a[80:83], v[8:11]\n"
"v_mfma_f32_16x16x32_bf16 v[12:15], v[184:187], a[80:83], v[12:15]\n"
"v_mfma_f32_16x16x32_bf16 v[12:15], v[164:167], a[64:67], v[12:15]\n"
"v_mfma_f32_16x16x32_bf16 v[16:19], v[168:171], a[64:67], v[16:19]\n"
"v_mfma_f32_16x16x32_bf16 v[16:19], v[188:191], a[80:83], v[16:19]\n"
"buffer_load_dwordx4 a[124:127], v235, s[44:47], s35 offen offset:0 sc1\n"
"s_waitcnt vmcnt(12)\n"
"v_mfma_f32_16x16x32_bf16 a[16:19], v[188:191], a[84:87], a[16:19]\n"
"s_add_u32 s61, s61, 32768\n"
"s_add_u32 s35, s61, 16384\n"
"v_mfma_f32_16x16x32_bf16 a[16:19], v[168:171], a[68:71], a[16:19]\n"
"v_mfma_f32_16x16x32_bf16 a[12:15], v[164:167], a[68:71], a[12:15]\n"
"v_mfma_f32_16x16x32_bf16 a[12:15], v[184:187], a[84:87], a[12:15]\n"
"v_mfma_f32_16x16x32_bf16 a[8:11], v[180:183], a[84:87], a[8:11]\n"
"buffer_load_dwordx4 a[64:67], v232, s[44:47], s61 offen offset:0 sc1\n"
"v_mfma_f32_16x16x32_bf16 a[8:11], v[160:163], a[68:71], a[8:11]\n"
"v_mfma_f32_16x16x32_bf16 a[4:7], v[156:159], a[68:71], a[4:7]\n"
"v_mfma_f32_16x16x32_bf16 a[4:7], v[176:179], a[84:87], a[4:7]\n"
"v_mfma_f32_16x16x32_bf16 a[0:3], v[172:175], a[84:87], a[0:3]\n"
"v_mfma_f32_16x16x32_bf16 a[0:3], v[152:155], a[68:71], a[0:3]\n"
"buffer_load_dwordx4 a[80:83], v232, s[44:47], s35 offen offset:0 sc1\n"
"s_waitcnt vmcnt(12)\n"
"v_mfma_f32_16x16x32_bf16 a[20:23], v[152:155], a[72:75], a[20:23]\n"
"v_mfma_f32_16x16x32_bf16 a[20:23], v[172:175], a[88:91], a[20:23]\n"
"v_mfma_f32_16x16x32_bf16 a[24:27], v[176:179], a[88:91], a[24:27]\n"
"v_mfma_f32_16x16x32_bf16 a[24:27], v[156:159], a[72:75], a[24:27]\n"
"v_mfma_f32_16x16x32_bf16 a[28:31], v[160:163], a[72:75], a[28:31]\n"
"buffer_load_dwordx4 a[68:71], v233, s[44:47], s61 offen offset:0 sc1\n"
"v_mfma_f32_16x16x32_bf16 a[28:31], v[180:183], a[88:91], a[28:31]\n"
"v_mfma_f32_16x16x32_bf16 a[32:35], v[184:187], a[88:91], a[32:35]\n"
"v_mfma_f32_16x16x32_bf16 a[32:35], v[164:167], a[72:75], a[32:35]\n"
"v_mfma_f32_16x16x32_bf16 a[36:39], v[168:171], a[72:75], a[36:39]\n"
"v_mfma_f32_16x16x32_bf16 a[36:39], v[188:191], a[88:91], a[36:39]\n"
"buffer_load_dwordx4 a[84:87], v233, s[44:47], s35 offen offset:0 sc1\n"
"s_waitcnt vmcnt(12)\n"
"v_mfma_f32_16x16x32_bf16 a[56:59], v[188:191], a[92:95], a[56:59]\n"
"v_mfma_f32_16x16x32_bf16 a[56:59], v[168:171], a[76:79], a[56:59]\n"
"v_mfma_f32_16x16x32_bf16 a[52:55], v[164:167], a[76:79], a[52:55]\n"
"v_mfma_f32_16x16x32_bf16 a[52:55], v[184:187], a[92:95], a[52:55]\n"
"v_mfma_f32_16x16x32_bf16 a[48:51], v[180:183], a[92:95], a[48:51]\n"
"buffer_load_dwordx4 a[72:75], v234, s[44:47], s61 offen offset:0 sc1\n"
"v_mfma_f32_16x16x32_bf16 a[48:51], v[160:163], a[76:79], a[48:51]\n"
"v_mfma_f32_16x16x32_bf16 a[44:47], v[156:159], a[76:79], a[44:47]\n"
"v_mfma_f32_16x16x32_bf16 a[44:47], v[176:179], a[92:95], a[44:47]\n"
"v_mfma_f32_16x16x32_bf16 a[40:43], v[172:175], a[92:95], a[40:43]\n"
"v_mfma_f32_16x16x32_bf16 a[40:43], v[152:155], a[76:79], a[40:43]\n"
"buffer_load_dwordx4 a[88:91], v234, s[44:47], s35 offen offset:0 sc1\n"
"s_waitcnt lgkmcnt(0)\n"
"s_barrier\n"
"s_waitcnt vmcnt(12)\n"
"v_mfma_f32_16x16x32_bf16 v[0:3], v[224:227], a[96:99], v[0:3]\n"
"ds_read_b128 v[20:23], v252 offset:23552\n"
"v_mfma_f32_16x16x32_bf16 v[0:3], a[144:147], a[112:115], v[0:3]\n"
"ds_read_b128 v[24:27], v252 offset:23616\n"
"v_mfma_f32_16x16x32_bf16 v[4:7], a[148:151], a[112:115], v[4:7]\n"
"ds_read_b128 v[28:31], v252 offset:23680\n"
"v_mfma_f32_16x16x32_bf16 v[4:7], a[128:131], a[96:99], v[4:7]\n"
"L_w13_%=:\n"
"s_cbranch_execz L_wd4_%=\n"
"ds_read_b128 v[32:35], v252 offset:23744\n"
"v_mfma_f32_16x16x32_bf16 v[8:11], a[132:135], a[96:99], v[8:11]\n"
"buffer_load_dwordx4 a[76:79], v235, s[44:47], s61 offen offset:0 sc1\n"
"v_mfma_f32_16x16x32_bf16 v[8:11], a[152:155], a[112:115], v[8:11]\n"
"ds_read_b128 v[36:39], v252 offset:23808\n"
"v_mfma_f32_16x16x32_bf16 v[12:15], a[156:159], a[112:115], v[12:15]\n"
"ds_read_b128 v[40:43], v252 offset:31744\n"
"v_mfma_f32_16x16x32_bf16 v[12:15], a[136:139], a[96:99], v[12:15]\n"
"buffer_load_dwordx4 v[204:207], v228, s[44:47], %9 offen offset:0 sc1\n"
"v_mfma_f32_16x16x32_bf16 v[16:19], a[140:143], a[96:99], v[16:19]\n"
"ds_read_b128 v[44:47], v252 offset:31808\n"
"v_mfma_f32_16x16x32_bf16 v[16:19], a[192:195], a[112:115], v[16:19]\n"
"buffer_load_dwordx4 a[92:95], v235, s[44:47], s35 offen offset:0 sc1\n"
"s_waitcnt vmcnt(13)\n"
"v_mfma_f32_16x16x32_bf16 a[16:19], a[192:195], a[116:119], a[16:19]\n"
"s_add_u32 s61, s61, 32768\n"
"s_add_u32 s35, s61, 16384\n"
"v_mfma_f32_16x16x32_bf16 a[16:19], a[140:143], a[100:103], a[16:19]\n"
"ds_read_b128 v[48:51], v252 offset:31872\n"
"v_mfma_f32_16x16x32_bf16 a[12:15], a[136:139], a[100:103], a[12:15]\n"
"ds_read_b128 v[52:55], v252 offset:31936\n"
"v_mfma_f32_16x16x32_bf16 a[12:15], a[156:159], a[116:119], a[12:15]\n"
"ds_read_b128 v[56:59], v252 offset:32000\n"
"v_mfma_f32_16x16x32_bf16 a[8:11], a[152:155], a[116:119], a[8:11]\n"
"buffer_load_dwordx4 a[96:99], v232, s[44:47], s61 offen offset:0 sc1\n"
"v_mfma_f32_16x16x32_bf16 a[8:11], a[132:135], a[100:103], a[8:11]\n"
"v_lshlrev_b32 v140, 16, v236\n"
"v_and_b32 v141, 0xffff0000, v236\n"
"v_mfma_f32_16x16x32_bf16 a[4:7], a[128:131], a[100:103], a[4:7]\n"
"v_lshlrev_b32 v142, 16, v237\n"
"v_and_b32 v143, 0xffff0000, v237\n"
"v_mfma_f32_16x16x32_bf16 a[4:7], a[148:151], a[116:119], a[4:7]\n"
"buffer_load_dwordx4 v[208:211], v228, s[44:47], %9 offen offset:1024 sc1\n"
"v_mfma_f32_16x16x32_bf16 a[0:3], a[144:147], a[116:119], a[0:3]\n"
"v_lshlrev_b32 v144, 16, v238\n"
"v_and_b32 v145, 0xffff0000, v238\n"
"v_mfma_f32_16x16x32_bf16 a[0:3], v[224:227], a[100:103], a[0:3]\n"
"buffer_load_dwordx4 a[112:115], v232, s[44:47], s35 offen offset:0 sc1\n"
"s_waitcnt vmcnt(14)\n"
"v_mfma_f32_16x16x32_bf16 a[20:23], v[224:227], a[104:107], a[20:23]\n"
"v_lshlrev_b32 v146, 16, v239\n"
"v_and_b32 v147, 0xffff0000, v239\n"
"v_mfma_f32_16x16x32_bf16 a[20:23], a[144:147], a[120:123], a[20:23]\n"
"v_lshlrev_b32 v148, 16, v240\n"
"v_and_b32 v149, 0xffff0000, v240\n"
"v_mfma_f32_16x16x32_bf16 a[24:27], a[148:151], a[120:123], a[24:27]\n"
"v_lshlrev_b32 v150, 16, v241\n"
"v_and_b32 v151, 0xffff0000, v241\n"
"v_mfma_f32_16x16x32_bf16 a[24:27], a[128:131], a[104:107], a[24:27]\n"
"v_lshlrev_b32 v152, 16, v242\n"
"v_and_b32 v153, 0xffff0000, v242\n"
"v_mfma_f32_16x16x32_bf16 a[28:31], a[132:135], a[104:107], a[28:31]\n"
"buffer_load_dwordx4 a[100:103], v233, s[44:47], s61 offen offset:0 sc1\n"
"v_mfma_f32_16x16x32_bf16 a[28:31], a[152:155], a[120:123], a[28:31]\n"
"v_lshlrev_b32 v154, 16, v243\n"
"v_and_b32 v155, 0xffff0000, v243\n"
"v_mfma_f32_16x16x32_bf16 a[32:35], a[156:159], a[120:123], a[32:35]\n"
"v_lshlrev_b32 v156, 16, v244\n"
"v_and_b32 v157, 0xffff0000, v244\n"
"v_mfma_f32_16x16x32_bf16 a[32:35], a[136:139], a[104:107], a[32:35]\n"
"buffer_load_dwordx2 v[212:213], v231, s[44:47], %9 offen offset:2048 sc1\n"
"v_mfma_f32_16x16x32_bf16 a[36:39], a[140:143], a[104:107], a[36:39]\n"
"v_lshlrev_b32 v158, 16, v245\n"
"v_and_b32 v159, 0xffff0000, v245\n"
"v_mfma_f32_16x16x32_bf16 a[36:39], a[192:195], a[120:123], a[36:39]\n"
"buffer_load_dwordx4 a[116:119], v233, s[44:47], s35 offen offset:0 sc1\n"
"s_waitcnt vmcnt(15)\n"
"v_mfma_f32_16x16x32_bf16 a[56:59], a[192:195], a[124:127], a[56:59]\n"
"s_lshl_b32 s55, %11, 2\n"
"s_add_u32 s55, s55, %17\n"
"v_mfma_f32_16x16x32_bf16 a[56:59], a[140:143], a[108:111], a[56:59]\n"
"s_lshl_b32 s67, s55, 10\n"
"s_mul_i32 s55, s55, 20480\n"
"v_mfma_f32_16x16x32_bf16 a[52:55], a[136:139], a[108:111], a[52:55]\n"
"s_add_u32 s55, s55, 46137344\n"
"s_lshl_b32 s52, %12, 11\n"
"v_mfma_f32_16x16x32_bf16 a[52:55], a[156:159], a[124:127], a[52:55]\n"
"s_add_u32 s52, s52, s55\n"
"s_lshl_b32 s64, %12, 9\n"
"v_mfma_f32_16x16x32_bf16 a[48:51], a[152:155], a[124:127], a[48:51]\n"
"buffer_load_dwordx4 a[104:107], v234, s[44:47], s61 offen offset:0 sc1\n"
"v_mfma_f32_16x16x32_bf16 a[48:51], a[132:135], a[108:111], a[48:51]\n"
"s_add_u32 s55, s55, s64\n"
"s_add_u32 s55, s55, 16384\n"
"v_mfma_f32_16x16x32_bf16 a[44:47], a[128:131], a[108:111], a[44:47]\n"
"s_lshl_b32 s64, %12, 7\n"
"s_add_u32 s67, s67, s64\n"
"v_mfma_f32_16x16x32_bf16 a[44:47], a[148:151], a[124:127], a[44:47]\n"
"s_add_u32 s67, s67, 65536\n"
"v_mfma_f32_16x16x32_bf16 a[40:43], a[144:147], a[124:127], a[40:43]\n"
"v_and_b32 v230, 0xf0, v228\n"
"v_lshrrev_b32 v254, 1, v230\n"
"v_mfma_f32_16x16x32_bf16 a[40:43], v[224:227], a[108:111], a[40:43]\n"
"buffer_load_dwordx4 a[120:123], v234, s[44:47], s35 offen offset:0 sc1\n"
"s_waitcnt vmcnt(15)\n"
"v_mfma_f32_16x16x32_bf16 v[0:3], a[196:199], a[64:67], v[0:3]\n"
"v_bfe_u32 v253, v228, 8, 1\n"
"v_lshl_or_b32 v230, v253, 3, v230\n"
"v_mfma_f32_16x16x32_bf16 v[0:3], v[72:75], a[80:83], v[0:3]\n"
"v_lshrrev_b32 v253, 9, v228\n"
"v_lshl_or_b32 v230, v253, 8, v230\n"
"s_add_u32 s54, s58, 0\n"
"v_mfma_f32_16x16x32_bf16 v[4:7], v[76:79], a[80:83], v[4:7]\n"
"buffer_load_dwordx4 a[212:215], v228, s[44:47], s54 offen offset:0 sc1\n"
"v_mfma_f32_16x16x32_bf16 v[4:7], a[200:203], a[64:67], v[4:7]\n"
"v_mfma_f32_16x16x32_bf16 v[8:11], a[204:207], a[64:67], v[8:11]\n"
"buffer_load_dwordx4 a[108:111], v235, s[44:47], s61 offen offset:0 sc1\n"
"v_mfma_f32_16x16x32_bf16 v[8:11], a[160:163], a[80:83], v[8:11]\n"
"v_mfma_f32_16x16x32_bf16 v[12:15], a[164:167], a[80:83], v[12:15]\n"
"buffer_load_dwordx4 a[216:219], v228, s[44:47], s54 offen offset:1024 sc1\n"
"v_mfma_f32_16x16x32_bf16 v[12:15], v[64:67], a[64:67], v[12:15]\n"
"v_mfma_f32_16x16x32_bf16 v[16:19], v[68:71], a[64:67], v[16:19]\n"
"v_mfma_f32_16x16x32_bf16 v[16:19], a[168:171], a[80:83], v[16:19]\n"
"buffer_load_dwordx4 a[124:127], v235, s[44:47], s35 offen offset:0 sc1\n"
"s_waitcnt vmcnt(17)\n"
"v_mfma_f32_16x16x32_bf16 a[16:19], a[168:171], a[84:87], a[16:19]\n"
"v_mfma_f32_16x16x32_bf16 a[16:19], v[68:71], a[68:71], a[16:19]\n"
"buffer_load_dwordx4 a[220:223], v228, s[44:47], s54 offen offset:2048 sc1\n"
"v_mfma_f32_16x16x32_bf16 a[12:15], v[64:67], a[68:71], a[12:15]\n"
"v_mfma_f32_16x16x32_bf16 a[12:15], a[164:167], a[84:87], a[12:15]\n"
"v_mfma_f32_16x16x32_bf16 a[8:11], a[160:163], a[84:87], a[8:11]\n"
"v_mfma_f32_16x16x32_bf16 a[8:11], a[204:207], a[68:71], a[8:11]\n"
"v_mfma_f32_16x16x32_bf16 a[4:7], a[200:203], a[68:71], a[4:7]\n"
"buffer_load_dwordx4 a[224:227], v228, s[44:47], s54 offen offset:3072 sc1\n"
"v_mfma_f32_16x16x32_bf16 a[4:7], v[76:79], a[84:87], a[4:7]\n"
"v_mfma_f32_16x16x32_bf16 a[0:3], v[72:75], a[84:87], a[0:3]\n"
"v_mfma_f32_16x16x32_bf16 a[0:3], a[196:199], a[68:71], a[0:3]\n"
"s_waitcnt vmcnt(17)\n"
"v_mfma_f32_16x16x32_bf16 a[20:23], a[196:199], a[72:75], a[20:23]\n"
"s_add_u32 s54, s58, 4096\n"
"v_mfma_f32_16x16x32_bf16 a[20:23], v[72:75], a[88:91], a[20:23]\n"
"buffer_load_dwordx4 a[228:231], v228, s[44:47], s54 offen offset:0 sc1\n"
"v_mfma_f32_16x16x32_bf16 a[24:27], v[76:79], a[88:91], a[24:27]\n"
"v_mfma_f32_16x16x32_bf16 a[24:27], a[200:203], a[72:75], a[24:27]\n"
"v_mfma_f32_16x16x32_bf16 a[28:31], a[204:207], a[72:75], a[28:31]\n"
"v_mfma_f32_16x16x32_bf16 a[28:31], a[160:163], a[88:91], a[28:31]\n"
"buffer_load_dwordx4 a[232:235], v228, s[44:47], s54 offen offset:1024 sc1\n"
"v_mfma_f32_16x16x32_bf16 a[32:35], a[164:167], a[88:91], a[32:35]\n"
"v_mfma_f32_16x16x32_bf16 a[32:35], v[64:67], a[72:75], a[32:35]\n"
"v_mfma_f32_16x16x32_bf16 a[36:39], v[68:71], a[72:75], a[36:39]\n"
"v_mfma_f32_16x16x32_bf16 a[36:39], a[168:171], a[88:91], a[36:39]\n"
"s_waitcnt vmcnt(16)\n"
"v_mfma_f32_16x16x32_bf16 a[56:59], a[168:171], a[92:95], a[56:59]\n"
"buffer_load_dwordx4 a[236:239], v228, s[44:47], s54 offen offset:2048 sc1\n"
"v_mfma_f32_16x16x32_bf16 a[56:59], v[68:71], a[76:79], a[56:59]\n"
"v_mfma_f32_16x16x32_bf16 a[52:55], v[64:67], a[76:79], a[52:55]\n"
"v_mfma_f32_16x16x32_bf16 a[52:55], a[164:167], a[92:95], a[52:55]\n"
"v_mfma_f32_16x16x32_bf16 a[48:51], a[160:163], a[92:95], a[48:51]\n"
"v_mfma_f32_16x16x32_bf16 a[48:51], a[204:207], a[76:79], a[48:51]\n"
"buffer_load_dwordx4 a[240:243], v228, s[44:47], s54 offen offset:3072 sc1\n"
"v_mfma_f32_16x16x32_bf16 a[44:47], a[200:203], a[76:79], a[44:47]\n"
"v_mfma_f32_16x16x32_bf16 a[44:47], v[76:79], a[92:95], a[44:47]\n"
"v_mfma_f32_16x16x32_bf16 a[40:43], v[72:75], a[92:95], a[40:43]\n"
"v_mfma_f32_16x16x32_bf16 a[40:43], a[196:199], a[76:79], a[40:43]\n"
"s_waitcnt lgkmcnt(0)\n"
"s_waitcnt vmcnt(12)\n"
"v_mfma_f32_16x16x32_bf16 a[0:3], a[172:175], a[100:103], a[0:3]\n"
"s_waitcnt vmcnt(13)\n"
"v_max3_u32 v253, v204, v205, v206\n"
"v_max3_u32 v253, v253, v207, v208\n"
"v_mfma_f32_16x16x32_bf16 a[0:3], a[208:211], a[116:119], a[0:3]\n"
"v_max3_u32 v253, v253, v209, v210\n"
"v_max3_u32 v253, v253, v211, v212\n"
"s_add_u32 s54, s58, 8192\n"
"v_mfma_f32_16x16x32_bf16 a[4:7], a[60:63], a[116:119], a[4:7]\n"
"buffer_load_dwordx4 a[128:131], v228, s[44:47], s54 offen offset:0 sc1\n"
"v_mfma_f32_16x16x32_bf16 a[4:7], a[176:179], a[100:103], a[4:7]\n"
"v_max3_u32 v253, v253, v213, v213\n"
"v_cmp_eq_u32 vcc, -1, v253\n"
"v_mfma_f32_16x16x32_bf16 a[8:11], a[180:183], a[100:103], a[8:11]\n"
"s_cbranch_vccnz L_exs_%=\n"
"L_exd_%=:\n"
"v_lshlrev_b32 v160, 16, v204\n"
"v_and_b32 v161, 0xffff0000, v204\n"
"v_mfma_f32_16x16x32_bf16 a[8:11], a[244:247], a[116:119], a[8:11]\n"
"v_lshlrev_b32 v162, 16, v205\n"
"v_and_b32 v163, 0xffff0000, v205\n"
"v_mfma_f32_16x16x32_bf16 a[12:15], a[248:251], a[116:119], a[12:15]\n"
"buffer_load_dwordx4 a[132:135], v228, s[44:47], s54 offen offset:1024 sc1\n"
"v_mfma_f32_16x16x32_bf16 a[12:15], a[184:187], a[100:103], a[12:15]\n"
"v_lshlrev_b32 v164, 16, v206\n"
"v_and_b32 v165, 0xffff0000, v206\n"
"v_mfma_f32_16x16x32_bf16 a[16:19], a[188:191], a[100:103], a[16:19]\n"
"v_lshlrev_b32 v166, 16, v207\n"
"v_and_b32 v167, 0xffff0000, v207\n"
"v_mfma_f32_16x16x32_bf16 a[16:19], a[252:255], a[116:119], a[16:19]\n"
"s_waitcnt vmcnt(12)\n"
"v_mfma_f32_16x16x32_bf16 a[36:39], a[252:255], a[120:123], a[36:39]\n"
"v_lshlrev_b32 v168, 16, v208\n"
"v_and_b32 v169, 0xffff0000, v208\n"
"v_mfma_f32_16x16x32_bf16 a[36:39], a[188:191], a[104:107], a[36:39]\n"
"buffer_load_dwordx4 a[136:139], v228, s[44:47], s54 offen offset:2048 sc1\n"
"v_mfma_f32_16x16x32_bf16 a[32:35], a[184:187], a[104:107], a[32:35]\n"
"v_lshlrev_b32 v170, 16, v209\n"
"v_and_b32 v171, 0xffff0000, v209\n"
"v_mfma_f32_16x16x32_bf16 a[32:35], a[248:251], a[120:123], a[32:35]\n"
"ds_write_b128 v246, a[0:3] offset:0\n"
"v_mfma_f32_16x16x32_bf16 a[28:31], a[244:247], a[120:123], a[28:31]\n"
"v_lshlrev_b32 v172, 16, v210\n"
"v_and_b32 v173, 0xffff0000, v210\n"
"v_mfma_f32_16x16x32_bf16 a[28:31], a[180:183], a[104:107], a[28:31]\n"
"ds_write_b128 v246, a[4:7] offset:1024\n"
"v_mfma_f32_16x16x32_bf16 a[24:27], a[176:179], a[104:107], a[24:27]\n"
"buffer_load_dwordx4 a[140:143], v228, s[44:47], s54 offen offset:3072 sc1\n"
"v_mfma_f32_16x16x32_bf16 a[24:27], a[60:63], a[120:123], a[24:27]\n"
"ds_write_b128 v246, a[8:11] offset:2048\n"
"v_mfma_f32_16x16x32_bf16 a[20:23], a[208:211], a[120:123], a[20:23]\n"
"v_lshlrev_b32 v174, 16, v211\n"
"v_and_b32 v175, 0xffff0000, v211\n"
"v_mfma_f32_16x16x32_bf16 a[20:23], a[172:175], a[104:107], a[20:23]\n"
"ds_write_b128 v246, a[12:15] offset:3072\n"
"s_waitcnt vmcnt(10)\n"
"v_mfma_f32_16x16x32_bf16 a[40:43], a[172:175], a[108:111], a[40:43]\n"
"v_lshlrev_b32 v176, 16, v212\n"
"v_and_b32 v177, 0xffff0000, v212\n"
"s_add_u32 s54, s58, 12288\n"
"v_mfma_f32_16x16x32_bf16 a[40:43], a[208:211], a[124:127], a[40:43]\n"
"buffer_load_dwordx4 a[144:147], v228, s[44:47], s54 offen offset:0 sc1\n"
"v_mfma_f32_16x16x32_bf16 a[44:47], a[60:63], a[124:127], a[44:47]\n"
"ds_write_b128 v246, a[16:19] offset:4096\n"
"v_mfma_f32_16x16x32_bf16 a[44:47], a[176:179], a[108:111], a[44:47]\n"
"ds_write_b128 v247, a[36:39] offset:4096\n"
"v_mfma_f32_16x16x32_bf16 a[48:51], a[180:183], a[108:111], a[48:51]\n"
"v_lshlrev_b32 v178, 16, v213\n"
"v_and_b32 v179, 0xffff0000, v213\n"
"v_mfma_f32_16x16x32_bf16 a[48:51], a[244:247], a[124:127], a[48:51]\n"
"buffer_load_dwordx4 a[148:151], v228, s[44:47], s54 offen offset:1024 sc1\n"
"v_mfma_f32_16x16x32_bf16 a[52:55], a[248:251], a[124:127], a[52:55]\n"
"ds_write_b128 v247, a[32:35] offset:3072\n"
"v_mfma_f32_16x16x32_bf16 a[52:55], a[184:187], a[108:111], a[52:55]\n"
"ds_write_b128 v247, a[28:31] offset:2048\n"
"v_mfma_f32_16x16x32_bf16 a[56:59], a[188:191], a[108:111], a[56:59]\n"
"v_fmac_f32 v140, v20, v160\n"
"v_fmac_f32 v141, v21, v161\n"
"v_mfma_f32_16x16x32_bf16 a[56:59], a[252:255], a[124:127], a[56:59]\n"
"ds_write_b128 v247, a[24:27] offset:1024\n"
"s_waitcnt vmcnt(21)\n"
"v_mfma_f32_16x16x32_bf16 v[16:19], a[252:255], a[112:115], v[16:19]\n"
"buffer_load_dwordx4 a[152:155], v228, s[44:47], s54 offen offset:2048 sc1\n"
"v_mfma_f32_16x16x32_bf16 v[16:19], a[188:191], a[96:99], v[16:19]\n"
"ds_write_b128 v247, a[20:23] offset:0\n"
"v_mfma_f32_16x16x32_bf16 v[12:15], a[184:187], a[96:99], v[12:15]\n"
"v_fmac_f32 v142, v22, v162\n"
"v_fmac_f32 v143, v23, v163\n"
"v_mfma_f32_16x16x32_bf16 v[12:15], a[248:251], a[112:115], v[12:15]\n"
"ds_write_b128 v248, a[40:43] offset:0\n"
"v_mfma_f32_16x16x32_bf16 v[8:11], a[244:247], a[112:115], v[8:11]\n"
"v_fmac_f32 v144, v24, v164\n"
"v_fmac_f32 v145, v25, v165\n"
"v_mfma_f32_16x16x32_bf16 v[8:11], a[180:183], a[96:99], v[8:11]\n"
"buffer_load_dwordx4 a[156:159], v228, s[44:47], s54 offen offset:3072 sc1\n"
"v_mfma_f32_16x16x32_bf16 v[4:7], a[176:179], a[96:99], v[4:7]\n"
"ds_write_b128 v248, a[44:47] offset:1024\n"
"v_mfma_f32_16x16x32_bf16 v[4:7], a[60:63], a[112:115], v[4:7]\n"
"ds_write_b128 v248, a[48:51] offset:2048\n"
"v_mfma_f32_16x16x32_bf16 v[0:3], a[208:211], a[112:115], v[0:3]\n"
"v_fmac_f32 v146, v26, v166\n"
"v_fmac_f32 v147, v27, v167\n"
"v_mfma_f32_16x16x32_bf16 v[0:3], a[172:175], a[96:99], v[0:3]\n"
"ds_write_b128 v248, a[52:55] offset:3072\n"
"ds_write_b128 v248, a[56:59] offset:4096\n"
"v_fmac_f32 v148, v28, v168\n"
"v_fmac_f32 v149, v29, v169\n"
"v_fmac_f32 v150, v30, v170\n"
"v_fmac_f32 v151, v31, v171\n"
"v_fmac_f32 v152, v32, v172\n"
"v_fmac_f32 v153, v33, v173\n"
"v_fmac_f32 v154, v34, v174\n"
"v_fmac_f32 v155, v35, v175\n"
"v_fmac_f32 v156, v36, v176\n"
"v_fmac_f32 v157, v37, v177\n"
"v_fmac_f32 v158, v38, v178\n"
"v_fmac_f32 v159, v39, v179\n"
"s_waitcnt lgkmcnt(0)\n"
"s_barrier\n"
"ds_read_b128 v[80:83], v251 offset:0\n"
"ds_read_b128 v[84:87], v251 offset:1024\n"
"ds_read_b128 v[88:91], v251 offset:2048\n"
"ds_read_b128 v[92:95], v251 offset:3072\n"
"ds_read_b128 v[96:99], v251 offset:4096\n"
"ds_read_b128 v[100:103], v251 offset:5120\n"
"ds_read_b128 v[104:107], v251 offset:6144\n"
"ds_read_b128 v[108:111], v251 offset:7168\n"
"ds_read_b128 v[112:115], v251 offset:8192\n"
"ds_read_b128 v[116:119], v251 offset:9216\n"
"ds_read_b128 v[120:123], v251 offset:10240\n"
"ds_read_b128 v[124:127], v251 offset:11264\n"
"ds_read_b128 v[128:131], v251 offset:12288\n"
"ds_read_b128 v[132:135], v251 offset:13312\n"
"ds_read_b128 v[136:139], v251 offset:14336\n"
"s_waitcnt lgkmcnt(10)\n"
"v_pk_add_f32 v[0:1], v[0:1], v[80:81]\n"
"s_add_u32 s54, s58, 16384\n"
"buffer_load_dwordx4 a[160:163], v228, s[44:47], s54 offen offset:0 sc1\n"
"v_pk_add_f32 v[2:3], v[2:3], v[82:83]\n"
"v_pk_add_f32 v[4:5], v[4:5], v[84:85]\n"
"v_pk_add_f32 v[6:7], v[6:7], v[86:87]\n"
"v_pk_add_f32 v[8:9], v[8:9], v[88:89]\n"
"v_pk_add_f32 v[10:11], v[10:11], v[90:91]\n"
"v_pk_add_f32 v[12:13], v[12:13], v[92:93]\n"
"v_pk_add_f32 v[14:15], v[14:15], v[94:95]\n"
"buffer_load_dwordx4 a[164:167], v228, s[44:47], s54 offen offset:1024 sc1\n"
"v_pk_add_f32 v[16:17], v[16:17], v[96:97]\n"
"v_pk_add_f32 v[18:19], v[18:19], v[98:99]\n"
"s_waitcnt lgkmcnt(5)\n"
"v_pk_add_f32 v[0:1], v[0:1], v[100:101]\n"
"v_pk_add_f32 v[2:3], v[2:3], v[102:103]\n"
"v_pk_add_f32 v[4:5], v[4:5], v[104:105]\n"
"v_pk_add_f32 v[6:7], v[6:7], v[106:107]\n"
"buffer_load_dwordx4 a[168:171], v228, s[44:47], s54 offen offset:2048 sc1\n"
"v_pk_add_f32 v[8:9], v[8:9], v[108:109]\n"
"v_pk_add_f32 v[10:11], v[10:11], v[110:111]\n"
"v_pk_add_f32 v[12:13], v[12:13], v[112:113]\n"
"v_pk_add_f32 v[14:15], v[14:15], v[114:115]\n"
"v_pk_add_f32 v[16:17], v[16:17], v[116:117]\n"
"v_pk_add_f32 v[18:19], v[18:19], v[118:119]\n"
"s_waitcnt lgkmcnt(0)\n"
"v_pk_add_f32 v[0:1], v[0:1], v[120:121]\n"
"buffer_load_dwordx4 a[172:175], v228, s[44:47], s54 offen offset:3072 sc1\n"
"v_pk_add_f32 v[2:3], v[2:3], v[122:123]\n"
"v_pk_add_f32 v[4:5], v[4:5], v[124:125]\n"
"v_pk_add_f32 v[6:7], v[6:7], v[126:127]\n"
"v_pk_add_f32 v[8:9], v[8:9], v[128:129]\n"
"v_pk_add_f32 v[10:11], v[10:11], v[130:131]\n"
"v_pk_add_f32 v[12:13], v[12:13], v[132:133]\n"
"v_pk_add_f32 v[14:15], v[14:15], v[134:135]\n"
"v_pk_add_f32 v[16:17], v[16:17], v[136:137]\n"
"v_pk_add_f32 v[18:19], v[18:19], v[138:139]\n"
"v_pk_fma_f32 v[140:141], v[20:21], v[0:1], v[140:141]\n"
"v_pk_fma_f32 v[142:143], v[22:23], v[2:3], v[142:143]\n"
"v_pk_fma_f32 v[144:145], v[24:25], v[4:5], v[144:145]\n"
"v_pk_fma_f32 v[146:147], v[26:27], v[6:7], v[146:147]\n"
"v_pk_fma_f32 v[148:149], v[28:29], v[8:9], v[148:149]\n"
"v_pk_fma_f32 v[150:151], v[30:31], v[10:11], v[150:151]\n"
"v_pk_fma_f32 v[152:153], v[32:33], v[12:13], v[152:153]\n"
"v_pk_fma_f32 v[154:155], v[34:35], v[14:15], v[154:155]\n"
"v_pk_fma_f32 v[156:157], v[36:37], v[16:17], v[156:157]\n"
"v_pk_fma_f32 v[158:159], v[38:39], v[18:19], v[158:159]\n"
"v_pk_mul_f32 v[40:41], v[40:41], v[140:141]\n"
"v_pk_mul_f32 v[42:43], v[42:43], v[142:143]\n"
"v_pk_mul_f32 v[44:45], v[44:45], v[144:145]\n"
"v_pk_mul_f32 v[46:47], v[46:47], v[146:147]\n"
"v_pk_mul_f32 v[48:49], v[48:49], v[148:149]\n"
"v_pk_mul_f32 v[50:51], v[50:51], v[150:151]\n"
"v_pk_mul_f32 v[52:53], v[52:53], v[152:153]\n"
"v_pk_mul_f32 v[54:55], v[54:55], v[154:155]\n"
"v_pk_mul_f32 v[56:57], v[56:57], v[156:157]\n"
"v_pk_mul_f32 v[58:59], v[58:59], v[158:159]\n"
"v_cvt_pk_bf16_f32 v0, v40, v41\n"
"v_cvt_pk_bf16_f32 v1, v42, v43\n"
"v_cvt_pk_bf16_f32 v2, v44, v45\n"
"v_cvt_pk_bf16_f32 v3, v46, v47\n"
"v_cvt_pk_bf16_f32 v4, v48, v49\n"
"v_cvt_pk_bf16_f32 v5, v50, v51\n"
"v_cvt_pk_bf16_f32 v6, v52, v53\n"
"v_cvt_pk_bf16_f32 v7, v54, v55\n"
"v_cvt_pk_bf16_f32 v8, v56, v57\n"
"v_cvt_pk_bf16_f32 v9, v58, v59\n"
"buffer_store_dwordx4 v[0:3], v228, s[44:47], s52 offen sc1\n"
"buffer_store_dwordx4 v[4:7], v228, s[44:47], s52 offen offset:1024 sc1\n"
"buffer_store_dwordx2 v[8:9], v230, s[44:47], s55 offen sc1\n"
"v_pk_add_f32 v[12:13], v[140:141], v[142:143]\n"
"v_pk_add_f32 v[14:15], v[144:145], v[146:147]\n"
"v_pk_add_f32 v[12:13], v[12:13], v[148:149]\n"
"v_pk_add_f32 v[14:15], v[14:15], v[150:151]\n"
"v_pk_add_f32 v[12:13], v[12:13], v[152:153]\n"
"v_pk_add_f32 v[14:15], v[14:15], v[154:155]\n"
"v_pk_add_f32 v[12:13], v[12:13], v[156:157]\n"
"v_pk_add_f32 v[14:15], v[14:15], v[158:159]\n"
"v_pk_add_f32 v[12:13], v[12:13], v[14:15]\n"
"v_add_f32 v12, v12, v13\n"
"v_mov_b32 v14, v12\n"
"v_cvt_pk_bf16_f32 v236, v140, v141\n"
"v_cvt_pk_bf16_f32 v237, v142, v143\n"
"v_permlane16_swap_b32 v12, v14\n"
"v_add_f32 v12, v12, v14\n"
"v_mov_b32 v14, v12\n"
"v_cvt_pk_bf16_f32 v238, v144, v145\n"
"v_cvt_pk_bf16_f32 v239, v146, v147\n"
"v_permlane32_swap_b32 v12, v14\n"
"v_add_f32 v12, v12, v14\n"
"v_mul_f32 v12, 0x3c4ccccd, v12\n"
"v_pk_add_f32 v[80:81], v[140:141], v[12:13] op_sel_hi:[1,0] neg_lo:[0,1] neg_hi:[0,1]\n"
"v_pk_add_f32 v[82:83], v[142:143], v[12:13] op_sel_hi:[1,0] neg_lo:[0,1] neg_hi:[0,1]\n"
"v_pk_add_f32 v[84:85], v[144:145], v[12:13] op_sel_hi:[1,0] neg_lo:[0,1] neg_hi:[0,1]\n"
"v_pk_add_f32 v[86:87], v[146:147], v[12:13] op_sel_hi:[1,0] neg_lo:[0,1] neg_hi:[0,1]\n"
"v_pk_add_f32 v[88:89], v[148:149], v[12:13] op_sel_hi:[1,0] neg_lo:[0,1] neg_hi:[0,1]\n"
"v_pk_add_f32 v[90:91], v[150:151], v[12:13] op_sel_hi:[1,0] neg_lo:[0,1] neg_hi:[0,1]\n"
"v_pk_add_f32 v[92:93], v[152:153], v[12:13] op_sel_hi:[1,0] neg_lo:[0,1] neg_hi:[0,1]\n"
"v_pk_add_f32 v[94:95], v[154:155], v[12:13] op_sel_hi:[1,0] neg_lo:[0,1] neg_hi:[0,1]\n"
"v_pk_add_f32 v[96:97], v[156:157], v[12:13] op_sel_hi:[1,0] neg_lo:[0,1] neg_hi:[0,1]\n"
"v_pk_add_f32 v[98:99], v[158:159], v[12:13] op_sel_hi:[1,0] neg_lo:[0,1] neg_hi:[0,1]\n"
"v_pk_mul_f32 v[14:15], v[80:81], v[80:81]\n"
"v_pk_mul_f32 v[16:17], v[82:83], v[82:83]\n"
"v_pk_fma_f32 v[14:15], v[84:85], v[84:85], v[14:15]\n"
"v_pk_fma_f32 v[16:17], v[86:87], v[86:87], v[16:17]\n"
"v_pk_fma_f32 v[14:15], v[88:89], v[88:89], v[14:15]\n"
"v_pk_fma_f32 v[16:17], v[90:91], v[90:91], v[16:17]\n"
"v_pk_fma_f32 v[14:15], v[92:93], v[92:93], v[14:15]\n"
"v_pk_fma_f32 v[16:17], v[94:95], v[94:95], v[16:17]\n"
"v_pk_fma_f32 v[14:15], v[96:97], v[96:97], v[14:15]\n"
"v_pk_fma_f32 v[16:17], v[98:99], v[98:99], v[16:17]\n"
"v_pk_add_f32 v[14:15], v[14:15], v[16:17]\n"
"v_add_f32 v13, v14, v15\n"
"v_mov_b32 v14, v13\n"
"v_cvt_pk_bf16_f32 v240, v148, v149\n"
"v_cvt_pk_bf16_f32 v241, v150, v151\n"
"v_permlane16_swap_b32 v13, v14\n"
"v_add_f32 v13, v13, v14\n"
"v_mov_b32 v14, v13\n"
"v_cvt_pk_bf16_f32 v242, v152, v153\n"
"v_cvt_pk_bf16_f32 v243, v154, v155\n"
"v_permlane32_swap_b32 v13, v14\n"
"v_add_f32 v13, v13, v14\n"
"s_mov_b64 exec, 0xffff\n"
"buffer_store_dwordx2 v[12:13], v254, s[44:47], s67 offen sc1\n"
"s_mov_b64 exec, -1\n"
"v_cvt_pk_bf16_f32 v244, v156, v157\n"
"v_cvt_pk_bf16_f32 v245, v158, v159\n"
"v_and_b32 v18, 0xf0, v228\n"
"v_lshlrev_b32 v18, 3, v18\n"
"v_add_u32 v18, 628736, v18\n"
"global_load_dword v19, v18, %0 sc1\n"
"s_lshr_b32 s64, %11, 1\n"
"s_lshl_b32 s64, s64, 11\n"
"s_add_u32 s64, s64, 557056\n"
"v_lshl_add_u32 v16, v228, 3, s64\n"
"v_mov_b32 v17, 1\n"
"s_and_b32 s64, %17, 1\n"
"s_cselect_b32 s59, s59, 129024\n"
"s_add_u32 s59, s59, s57\n"
"s_sub_u32 s59, s59, 3072\n"
"s_mul_i32 s65, s64, 129024\n"
"s_add_u32 s57, s57, s65\n"
"s_mul_i32 s64, s64, 3072\n"
"s_lshr_b32 s65, %17, 1\n"
"s_mul_i32 s65, s65, 7168\n"
"s_add_u32 s64, s64, s65\n"
"s_add_u32 m0, s64, 143360\n"
"s_nop 0\n"
"s_lshl_b32 s35, %13, 3\n"
"s_lshl_b32 s64, %17, 1\n"
"s_add_u32 s35, s35, s64\n"
"s_mul_i32 s61, s35, 20480\n"
"s_add_u32 s61, s61, 46137344\n"
"s_lshl_b32 s64, %13, 4\n"
"s_add_u32 s64, s64, %14\n"
"s_lshl_b32 s64, s64, 7\n"
"s_add_u32 s64, s64, 557056\n"
"v_mov_b32 v2, s64\n"
"v_add_u32 v229, 20480, v228\n"
"v_mov_b32 v246, v228\n"
"v_add_u32 v247, 65536, v228\n"
"v_add_u32 v248, 131072, v228\n"
"v_lshl_add_u32 v231, v250, 4, v254\n"
"s_lshl_b32 s64, s35, 10\n"
"s_add_u32 s64, s64, 65536\n"
"s_cmp_lg_u32 s60, 0\n"
"s_waitcnt vmcnt(0)\n"
"s_barrier\n"
"s_cbranch_scc1 L_xs_%=\n"
"s_mov_b64 exec, 0xffff\n"
"global_atomic_add v16, v17, %0\n"
"s_mov_b64 exec, -1\n"
"L_xs_%=:\n"
"v_cmp_gt_u32 vcc, 64, v19\n"
"s_cbranch_vccnz L_ps5_%=\n"
"L_pd5_%=:\n"
"buffer_load_dwordx4 v228, s[44:47], s57 offen offset:0 sc1 lds\n"
"s_add_u32 s57, s57, 41984\n"
"buffer_load_dwordx4 v228, s[44:47], s57 offen offset:1024 sc1 lds\n"
"s_add_u32 s57, s57, 41984\n"
"buffer_load_dwordx4 v228, s[44:47], s57 offen offset:2048 sc1 lds\n"
"buffer_load_dwordx4 v228, s[44:47], s59 offen offset:3072 sc1 lds\n"
"s_add_u32 m0, s56, 86016\n"
"s_add_u32 s34, s62, 6144\n"
"buffer_load_dwordx4 v228, s[44:47], s34 offen offset:0 sc1 lds\n"
"s_add_u32 s34, s62, 48128\n"
"buffer_load_dwordx4 v228, s[44:47], s34 offen offset:1024 sc1 lds\n"
"s_add_u32 s34, s62, 90112\n"
"buffer_load_dwordx4 v228, s[44:47], s34 offen offset:2048 sc1 lds\n"
"ds_read_b128 v[160:163], v246 offset:0\n"
"ds_read_b128 v[164:167], v246 offset:7168\n"
"ds_read_b128 v[168:171], v246 offset:1024\n"
"ds_read_b128 v[172:175], v246 offset:8192\n"
"ds_read_b128 v[176:179], v246 offset:2048\n"
"ds_read_b128 v[180:183], v246 offset:9216\n"
"s_cmp_lg_u32 s60, 0\n"
"s_cbranch_scc1 L_xb_%=\n"
"v_mov_b32 v4, 0\n"
"s_mov_b64 exec, 1\n"
"s_mov_b32 s65, 200000\n"
"L_pl6_%=:\n"
"global_atomic_add v3, v2, v4, %0 sc0\n"
"s_waitcnt vmcnt(0)\n"
"v_cmp_gt_u32 vcc, 16, v3\n"
"s_cbranch_vccz L_pd6_%=\n"
"s_sleep 1\n"
"s_sub_u32 s65, s65, 1\n"
"s_cbranch_scc0 L_pl6_%=\n"
"L_pd6_%=:\n"
"s_mov_b64 exec, -1\n"
"s_branch L_xb2_%=\n"
"L_xb_%=:\n"
"s_mov_b64 exec, 0\n"
"s_cmp_eq_u32 s60, 4096\n"
"s_cbranch_scc1 L_w16_%=\n"
"s_cmp_eq_u32 s60, 8192\n"
"s_cbranch_scc1 L_w17_%=\n"
"s_branch L_wd5_%=\n"
"L_wd5_%=:\n"
"s_mov_b64 exec, -1\n"
"s_waitcnt vmcnt(0)\n"
"L_xb2_%=:\n"
"s_barrier\n"
"buffer_load_dwordx4 v[112:115], v228, s[44:47], s61 offen offset:0 sc1\n"
"buffer_load_dwordx4 v[116:119], v229, s[44:47], s61 offen offset:0 sc1\n"
"buffer_load_dwordx4 v[120:123], v228, s[44:47], s61 offen offset:1024 sc1\n"
"buffer_load_dwordx4 v[124:127], v229, s[44:47], s61 offen offset:1024 sc1\n"
"s_waitcnt vmcnt(0) lgkmcnt(4)\n"
"v_mfma_f32_16x16x32_bf16 v[0:3], v[160:163], v[112:115], 0\n"
"ds_read_b128 v[184:187], v246 offset:3072\n"
"v_mfma_f32_16x16x32_bf16 v[0:3], v[164:167], v[120:123], v[0:3]\n"
"buffer_load_dwordx4 v[128:131], v228, s[44:47], s61 offen offset:2048 sc1\n"
"v_mfma_f32_16x16x32_bf16 v[4:7], v[164:167], v[124:127], 0\n"
"ds_read_b128 v[188:191], v246 offset:10240\n"
"v_mfma_f32_16x16x32_bf16 v[4:7], v[160:163], v[116:119], v[4:7]\n"
"s_waitcnt lgkmcnt(4)\n"
"v_mfma_f32_16x16x32_bf16 v[12:15], v[168:171], v[116:119], 0\n"
"ds_read_b128 v[160:163], v246 offset:4096\n"
"v_mfma_f32_16x16x32_bf16 v[12:15], v[172:175], v[124:127], v[12:15]\n"
"buffer_load_dwordx4 v[132:135], v229, s[44:47], s61 offen offset:2048 sc1\n"
"v_mfma_f32_16x16x32_bf16 v[8:11], v[172:175], v[120:123], 0\n"
"ds_read_b128 v[164:167], v246 offset:11264\n"
"v_mfma_f32_16x16x32_bf16 v[8:11], v[168:171], v[112:115], v[8:11]\n"
"s_waitcnt lgkmcnt(4)\n"
"v_mfma_f32_16x16x32_bf16 v[16:19], v[176:179], v[112:115], 0\n"
"ds_read_b128 v[168:171], v246 offset:5120\n"
"v_mfma_f32_16x16x32_bf16 v[16:19], v[180:183], v[120:123], v[16:19]\n"
"buffer_load_dwordx4 v[136:139], v228, s[44:47], s61 offen offset:3072 sc1\n"
"v_mfma_f32_16x16x32_bf16 v[20:23], v[180:183], v[124:127], 0\n"
"ds_read_b128 v[172:175], v246 offset:12288\n"
"v_mfma_f32_16x16x32_bf16 v[20:23], v[176:179], v[116:119], v[20:23]\n"
"s_waitcnt lgkmcnt(4)\n"
"v_mfma_f32_16x16x32_bf16 v[28:31], v[184:187], v[116:119], 0\n"
"ds_read_b128 v[176:179], v246 offset:6144\n"
"v_mfma_f32_16x16x32_bf16 v[28:31], v[188:191], v[124:127], v[28:31]\n"
"buffer_load_dwordx4 v[140:143], v229, s[44:47], s61 offen offset:3072 sc1\n"
"v_mfma_f32_16x16x32_bf16 v[24:27], v[188:191], v[120:123], 0\n"
"ds_read_b128 v[180:183], v246 offset:13312\n"
"v_mfma_f32_16x16x32_bf16 v[24:27], v[184:187], v[112:115], v[24:27]\n"
"s_add_u32 s61, s61, 4096\n"
"s_waitcnt lgkmcnt(4)\n"
"v_mfma_f32_16x16x32_bf16 v[32:35], v[160:163], v[112:115], 0\n"
"ds_read_b128 v[184:187], v246 offset:14336\n"
"v_mfma_f32_16x16x32_bf16 v[32:35], v[164:167], v[120:123], v[32:35]\n"
"buffer_load_dwordx4 v[144:147], v228, s[44:47], s61 offen offset:0 sc1\n"
"v_mfma_f32_16x16x32_bf16 v[36:39], v[164:167], v[124:127], 0\n"
"ds_read_b128 v[188:191], v246 offset:21504\n"
"v_mfma_f32_16x16x32_bf16 v[36:39], v[160:163], v[116:119], v[36:39]\n"
"s_waitcnt lgkmcnt(4)\n"
"v_mfma_f32_16x16x32_bf16 v[44:47], v[168:171], v[116:119], 0\n"
"ds_read_b128 v[160:163], v246 offset:15360\n"
"v_mfma_f32_16x16x32_bf16 v[44:47], v[172:175], v[124:127], v[44:47]\n"
"buffer_load_dwordx4 v[148:151], v229, s[44:47], s61 offen offset:0 sc1\n"
"v_mfma_f32_16x16x32_bf16 v[40:43], v[172:175], v[120:123], 0\n"
"ds_read_b128 v[164:167], v246 offset:22528\n"
"v_mfma_f32_16x16x32_bf16 v[40:43], v[168:171], v[112:115], v[40:43]\n"
"s_waitcnt lgkmcnt(4)\n"
"v_mfma_f32_16x16x32_bf16 v[48:51], v[176:179], v[112:115], 0\n"
"ds_read_b128 v[168:171], v246 offset:16384\n"
"v_mfma_f32_16x16x32_bf16 v[48:51], v[180:183], v[120:123], v[48:51]\n"
"buffer_load_dwordx4 v[152:155], v228, s[44:47], s61 offen offset:1024 sc1\n"
"v_mfma_f32_16x16x32_bf16 v[52:55], v[180:183], v[124:127], 0\n"
"ds_read_b128 v[172:175], v246 offset:23552\n"
"v_mfma_f32_16x16x32_bf16 v[52:55], v[176:179], v[116:119], v[52:55]\n"
"s_waitcnt lgkmcnt(4)\n"
"v_mfma_f32_16x16x32_bf16 v[60:63], v[184:187], v[116:119], 0\n"
"ds_read_b128 v[176:179], v246 offset:17408\n"
"v_mfma_f32_16x16x32_bf16 v[60:63], v[188:191], v[124:127], v[60:63]\n"
"buffer_load_dwordx4 v[156:159], v229, s[44:47], s61 offen offset:1024 sc1\n"
"v_mfma_f32_16x16x32_bf16 v[56:59], v[188:191], v[120:123], 0\n"
"ds_read_b128 v[180:183], v246 offset:24576\n"
"v_mfma_f32_16x16x32_bf16 v[56:59], v[184:187], v[112:115], v[56:59]\n"
"s_waitcnt lgkmcnt(4)\n"
"v_mfma_f32_16x16x32_bf16 v[64:67], v[160:163], v[112:115], 0\n"
"ds_read_b128 v[184:187], v246 offset:18432\n"
"v_mfma_f32_16x16x32_bf16 v[64:67], v[164:167], v[120:123], v[64:67]\n"
"buffer_load_dwordx2 v[216:217], v231, s[44:47], s64 offen offset:0 sc1\n"
"v_mfma_f32_16x16x32_bf16 v[68:71], v[164:167], v[124:127], 0\n"
"L_w14_%=:\n"
"s_cbranch_execz L_wd4_%=\n"
"ds_read_b128 v[188:191], v246 offset:25600\n"
"v_mfma_f32_16x16x32_bf16 v[68:71], v[160:163], v[116:119], v[68:71]\n"
"s_waitcnt lgkmcnt(4)\n"
"v_mfma_f32_16x16x32_bf16 v[76:79], v[168:171], v[116:119], 0\n"
"ds_read_b128 v[160:163], v246 offset:19456\n"
"v_mfma_f32_16x16x32_bf16 v[76:79], v[172:175], v[124:127], v[76:79]\n"
"buffer_load_dwordx2 v[218:219], v231, s[44:47], s64 offen offset:128 sc1\n"
"v_mfma_f32_16x16x32_bf16 v[72:75], v[172:175], v[120:123], 0\n"
"ds_read_b128 v[164:167], v246 offset:26624\n"
"v_mfma_f32_16x16x32_bf16 v[72:75], v[168:171], v[112:115], v[72:75]\n"
"s_waitcnt lgkmcnt(4)\n"
"v_mfma_f32_16x16x32_bf16 v[80:83], v[176:179], v[112:115], 0\n"
"ds_read_b128 v[168:171], v246 offset:20480\n"
"v_mfma_f32_16x16x32_bf16 v[80:83], v[180:183], v[120:123], v[80:83]\n"
"buffer_load_dwordx2 v[220:221], v231, s[44:47], s64 offen offset:1024 sc1\n"
"v_mfma_f32_16x16x32_bf16 v[84:87], v[180:183], v[124:127], 0\n"
"ds_read_b128 v[172:175], v246 offset:27648\n"
"v_mfma_f32_16x16x32_bf16 v[84:87], v[176:179], v[116:119], v[84:87]\n"
"s_waitcnt lgkmcnt(4)\n"
"v_mfma_f32_16x16x32_bf16 v[92:95], v[184:187], v[116:119], 0\n"
"ds_read_b128 v[176:179], v246 offset:28672\n"
"v_mfma_f32_16x16x32_bf16 v[92:95], v[188:191], v[124:127], v[92:95]\n"
"buffer_load_dwordx2 v[222:223], v231, s[44:47], s64 offen offset:1152 sc1\n"
"v_mfma_f32_16x16x32_bf16 v[88:91], v[188:191], v[120:123], 0\n"
"ds_read_b128 v[180:183], v246 offset:35840\n"
"v_mfma_f32_16x16x32_bf16 v[88:91], v[184:187], v[112:115], v[88:91]\n"
"s_add_u32 s34, s62, 132096\n"
"s_waitcnt lgkmcnt(4)\n"
"v_mfma_f32_16x16x32_bf16 v[96:99], v[160:163], v[112:115], 0\n"
"ds_read_b128 v[184:187], v246 offset:29696\n"
"v_mfma_f32_16x16x32_bf16 v[96:99], v[164:167], v[120:123], v[96:99]\n"
"buffer_load_dwordx4 v228, s[44:47], s34 offen offset:3072 sc1 lds\n"
"v_mfma_f32_16x16x32_bf16 v[100:103], v[164:167], v[124:127], 0\n"
"ds_read_b128 v[188:191], v246 offset:36864\n"
"v_mfma_f32_16x16x32_bf16 v[100:103], v[160:163], v[116:119], v[100:103]\n"
"s_add_u32 m0, s56, 90112\n"
"s_add_u32 s34, s62, 178176\n"
"s_waitcnt lgkmcnt(4)\n"
"v_mfma_f32_16x16x32_bf16 v[108:111], v[168:171], v[116:119], 0\n"
"ds_read_b128 v[160:163], v246 offset:30720\n"
"v_mfma_f32_16x16x32_bf16 v[108:111], v[172:175], v[124:127], v[108:111]\n"
"buffer_load_dwordx4 v228, s[44:47], s34 offen offset:0 sc1 lds\n"
"v_mfma_f32_16x16x32_bf16 v[104:107], v[172:175], v[120:123], 0\n"
"ds_read_b128 v[164:167], v246 offset:37888\n"
"v_mfma_f32_16x16x32_bf16 v[104:107], v[168:171], v[112:115], v[104:107]\n"
"s_waitcnt vmcnt(10) lgkmcnt(4)\n"
"v_mfma_f32_16x16x32_bf16 v[0:3], v[176:179], v[128:131], v[0:3]\n"
"ds_read_b128 v[168:171], v246 offset:31744\n"
"v_mfma_f32_16x16x32_bf16 v[0:3], v[180:183], v[136:139], v[0:3]\n"
"buffer_load_dwordx4 v[112:115], v228, s[44:47], s61 offen offset:2048 sc1\n"
"v_mfma_f32_16x16x32_bf16 v[4:7], v[180:183], v[140:143], v[4:7]\n"
"ds_read_b128 v[172:175], v246 offset:38912\n"
"v_mfma_f32_16x16x32_bf16 v[4:7], v[176:179], v[132:135], v[4:7]\n"
"s_waitcnt lgkmcnt(4)\n"
"v_mfma_f32_16x16x32_bf16 v[12:15], v[184:187], v[132:135], v[12:15]\n"
"ds_read_b128 v[176:179], v246 offset:32768\n"
"v_mfma_f32_16x16x32_bf16 v[12:15], v[188:191], v[140:143], v[12:15]\n"
"buffer_load_dwordx4 v[116:119], v229, s[44:47], s61 offen offset:2048 sc1\n"
"v_mfma_f32_16x16x32_bf16 v[8:11], v[188:191], v[136:139], v[8:11]\n"
"ds_read_b128 v[180:183], v246 offset:39936\n"
"v_mfma_f32_16x16x32_bf16 v[8:11], v[184:187], v[128:131], v[8:11]\n"
"s_add_u32 s34, s62, 220160\n"
"s_waitcnt lgkmcnt(4)\n"
"v_mfma_f32_16x16x32_bf16 v[16:19], v[160:163], v[128:131], v[16:19]\n"
"ds_read_b128 v[184:187], v246 offset:33792\n"
"v_mfma_f32_16x16x32_bf16 v[16:19], v[164:167], v[136:139], v[16:19]\n"
"buffer_load_dwordx4 v228, s[44:47], s34 offen offset:1024 sc1 lds\n"
"v_mfma_f32_16x16x32_bf16 v[20:23], v[164:167], v[140:143], v[20:23]\n"
"ds_read_b128 v[188:191], v246 offset:40960\n"
"v_mfma_f32_16x16x32_bf16 v[20:23], v[160:163], v[132:135], v[20:23]\n"
"s_add_u32 s34, s63, 6144\n"
"s_waitcnt lgkmcnt(4)\n"
"v_mfma_f32_16x16x32_bf16 v[28:31], v[168:171], v[132:135], v[28:31]\n"
"ds_read_b128 v[160:163], v246 offset:34816\n"
"v_mfma_f32_16x16x32_bf16 v[28:31], v[172:175], v[140:143], v[28:31]\n"
"buffer_load_dwordx4 v228, s[44:47], s34 offen offset:2048 sc1 lds\n"
"v_mfma_f32_16x16x32_bf16 v[24:27], v[172:175], v[136:139], v[24:27]\n"
"ds_read_b128 v[164:167], v246 offset:41984\n"
"v_mfma_f32_16x16x32_bf16 v[24:27], v[168:171], v[128:131], v[24:27]\n"
"s_add_u32 m0, s56, 114688\n"
"s_add_u32 s34, s62, 8192\n"
"s_waitcnt lgkmcnt(4)\n"
"v_mfma_f32_16x16x32_bf16 v[32:35], v[176:179], v[128:131], v[32:35]\n"
"ds_read_b128 v[168:171], v246 offset:43008\n"
"v_mfma_f32_16x16x32_bf16 v[32:35], v[180:183], v[136:139], v[32:35]\n"
"buffer_load_dwordx4 v228, s[44:47], s34 offen offset:0 sc1 lds\n"
"v_mfma_f32_16x16x32_bf16 v[36:39], v[180:183], v[140:143], v[36:39]\n"
"ds_read_b128 v[172:175], v246 offset:50176\n"
"v_mfma_f32_16x16x32_bf16 v[36:39], v[176:179], v[132:135], v[36:39]\n"
"s_add_u32 s34, s62, 50176\n"
"s_waitcnt lgkmcnt(4)\n"
"v_mfma_f32_16x16x32_bf16 v[44:47], v[184:187], v[132:135], v[44:47]\n"
"ds_read_b128 v[176:179], v246 offset:44032\n"
"v_mfma_f32_16x16x32_bf16 v[44:47], v[188:191], v[140:143], v[44:47]\n"
"buffer_load_dwordx4 v228, s[44:47], s34 offen offset:1024 sc1 lds\n"
"v_mfma_f32_16x16x32_bf16 v[40:43], v[188:191], v[136:139], v[40:43]\n"
"ds_read_b128 v[180:183], v246 offset:51200\n"
"v_mfma_f32_16x16x32_bf16 v[40:43], v[184:187], v[128:131], v[40:43]\n"
"s_waitcnt lgkmcnt(4)\n"
"v_mfma_f32_16x16x32_bf16 v[48:51], v[160:163], v[128:131], v[48:51]\n"
"ds_read_b128 v[184:187], v246 offset:45056\n"
"v_mfma_f32_16x16x32_bf16 v[48:51], v[164:167], v[136:139], v[48:51]\n"
"v_mfma_f32_16x16x32_bf16 v[52:55], v[164:167], v[140:143], v[52:55]\n"
"ds_read_b128 v[188:191], v246 offset:52224\n"
"v_mfma_f32_16x16x32_bf16 v[52:55], v[160:163], v[132:135], v[52:55]\n"
"s_waitcnt lgkmcnt(4)\n"
"v_mfma_f32_16x16x32_bf16 v[60:63], v[168:171], v[132:135], v[60:63]\n"
"ds_read_b128 v[160:163], v246 offset:46080\n"
"v_mfma_f32_16x16x32_bf16 v[60:63], v[172:175], v[140:143], v[60:63]\n"
"buffer_load_dwordx4 v[120:123], v228, s[44:47], s61 offen offset:3072 sc1\n"
"v_mfma_f32_16x16x32_bf16 v[56:59], v[172:175], v[136:139], v[56:59]\n"
"ds_read_b128 v[164:167], v246 offset:53248\n"
"v_mfma_f32_16x16x32_bf16 v[56:59], v[168:171], v[128:131], v[56:59]\n"
"s_waitcnt lgkmcnt(4)\n"
"v_mfma_f32_16x16x32_bf16 v[64:67], v[176:179], v[128:131], v[64:67]\n"
"ds_read_b128 v[168:171], v246 offset:47104\n"
"v_mfma_f32_16x16x32_bf16 v[64:67], v[180:183], v[136:139], v[64:67]\n"
"buffer_load_dwordx4 v[124:127], v229, s[44:47], s61 offen offset:3072 sc1\n"
"v_mfma_f32_16x16x32_bf16 v[68:71], v[180:183], v[140:143], v[68:71]\n"
"ds_read_b128 v[172:175], v246 offset:54272\n"
"v_mfma_f32_16x16x32_bf16 v[68:71], v[176:179], v[132:135], v[68:71]\n"
"s_add_u32 s34, s62, 92160\n"
"s_waitcnt lgkmcnt(4)\n"
"v_mfma_f32_16x16x32_bf16 v[76:79], v[184:187], v[132:135], v[76:79]\n"
"ds_read_b128 v[176:179], v246 offset:48128\n"
"v_mfma_f32_16x16x32_bf16 v[76:79], v[188:191], v[140:143], v[76:79]\n"
"buffer_load_dwordx4 v228, s[44:47], s34 offen offset:2048 sc1 lds\n"
"v_mfma_f32_16x16x32_bf16 v[72:75], v[188:191], v[136:139], v[72:75]\n"
"ds_read_b128 v[180:183], v246 offset:55296\n"
"v_mfma_f32_16x16x32_bf16 v[72:75], v[184:187], v[128:131], v[72:75]\n"
"s_add_u32 s34, s62, 134144\n"
"s_waitcnt lgkmcnt(4)\n"
"v_mfma_f32_16x16x32_bf16 v[80:83], v[160:163], v[128:131], v[80:83]\n"
"ds_read_b128 v[184:187], v246 offset:49152\n"
"v_mfma_f32_16x16x32_bf16 v[80:83], v[164:167], v[136:139], v[80:83]\n"
"buffer_load_dwordx4 v228, s[44:47], s34 offen offset:3072 sc1 lds\n"
"v_mfma_f32_16x16x32_bf16 v[84:87], v[164:167], v[140:143], v[84:87]\n"
"ds_read_b128 v[188:191], v246 offset:56320\n"
"v_mfma_f32_16x16x32_bf16 v[84:87], v[160:163], v[132:135], v[84:87]\n"
"s_add_u32 m0, s56, 118784\n"
"s_add_u32 s34, s62, 180224\n"
"s_waitcnt lgkmcnt(4)\n"
"v_mfma_f32_16x16x32_bf16 v[92:95], v[168:171], v[132:135], v[92:95]\n"
"s_barrier\n"
"ds_read_b128 v[160:163], v246 offset:57344\n"
"v_mfma_f32_16x16x32_bf16 v[92:95], v[172:175], v[140:143], v[92:95]\n"
"buffer_load_dwordx4 v228, s[44:47], s34 offen offset:0 sc1 lds\n"
"v_mfma_f32_16x16x32_bf16 v[88:91], v[172:175], v[136:139], v[88:91]\n"
"ds_read_b128 v[164:167], v246 offset:64512\n"
"v_mfma_f32_16x16x32_bf16 v[88:91], v[168:171], v[128:131], v[88:91]\n"
"s_add_u32 s34, s62, 222208\n"
"s_waitcnt lgkmcnt(4)\n"
"v_mfma_f32_16x16x32_bf16 v[96:99], v[176:179], v[128:131], v[96:99]\n"
"ds_read_b128 v[168:171], v246 offset:58368\n"
"v_mfma_f32_16x16x32_bf16 v[96:99], v[180:183], v[136:139], v[96:99]\n"
"buffer_load_dwordx4 v228, s[44:47], s34 offen offset:1024 sc1 lds\n"
"v_mfma_f32_16x16x32_bf16 v[100:103], v[180:183], v[140:143], v[100:103]\n"
"ds_read_b128 v[172:175], v247 offset:0\n"
"v_mfma_f32_16x16x32_bf16 v[100:103], v[176:179], v[132:135], v[100:103]\n"
"s_waitcnt lgkmcnt(4)\n"
"v_mfma_f32_16x16x32_bf16 v[108:111], v[184:187], v[132:135], v[108:111]\n"
"ds_read_b128 v[176:179], v246 offset:59392\n"
"v_mfma_f32_16x16x32_bf16 v[108:111], v[188:191], v[140:143], v[108:111]\n"
"v_mfma_f32_16x16x32_bf16 v[104:107], v[188:191], v[136:139], v[104:107]\n"
"ds_read_b128 v[180:183], v247 offset:1024\n"
"v_mfma_f32_16x16x32_bf16 v[104:107], v[184:187], v[128:131], v[104:107]\n"
"s_add_u32 s61, s61, 4096\n"
"s_waitcnt vmcnt(18) lgkmcnt(4)\n"
"v_mfma_f32_16x16x32_bf16 v[0:3], v[160:163], v[144:147], v[0:3]\n"
"ds_read_b128 v[184:187], v246 offset:60416\n"
"v_mfma_f32_16x16x32_bf16 v[0:3], v[164:167], v[152:155], v[0:3]\n"
"buffer_load_dwordx4 v[128:131], v228, s[44:47], s61 offen offset:0 sc1\n"
"v_mfma_f32_16x16x32_bf16 v[4:7], v[164:167], v[156:159], v[4:7]\n"
"ds_read_b128 v[188:191], v247 offset:2048\n"
"v_mfma_f32_16x16x32_bf16 v[4:7], v[160:163], v[148:151], v[4:7]\n"
"s_waitcnt lgkmcnt(4)\n"
"v_mfma_f32_16x16x32_bf16 v[12:15], v[168:171], v[148:151], v[12:15]\n"
"ds_read_b128 v[160:163], v246 offset:61440\n"
"v_mfma_f32_16x16x32_bf16 v[12:15], v[172:175], v[156:159], v[12:15]\n"
"buffer_load_dwordx4 v[132:135], v229, s[44:47], s61 offen offset:0 sc1\n"
"v_mfma_f32_16x16x32_bf16 v[8:11], v[172:175], v[152:155], v[8:11]\n"
"ds_read_b128 v[164:167], v247 offset:3072\n"
"v_mfma_f32_16x16x32_bf16 v[8:11], v[168:171], v[144:147], v[8:11]\n"
"s_add_u32 s34, s63, 8192\n"
"s_waitcnt lgkmcnt(4)\n"
"v_mfma_f32_16x16x32_bf16 v[16:19], v[176:179], v[144:147], v[16:19]\n"
"ds_read_b128 v[168:171], v246 offset:62464\n"
"v_mfma_f32_16x16x32_bf16 v[16:19], v[180:183], v[152:155], v[16:19]\n"
"buffer_load_dwordx4 v228, s[44:47], s34 offen offset:2048 sc1 lds\n"
"v_mfma_f32_16x16x32_bf16 v[20:23], v[180:183], v[156:159], v[20:23]\n"
"ds_read_b128 v[172:175], v247 offset:4096\n"
"v_mfma_f32_16x16x32_bf16 v[20:23], v[176:179], v[148:151], v[20:23]\n"
"s_add_u32 m0, s56, 0\n"
"s_add_u32 s34, s62, 10240\n"
"s_waitcnt lgkmcnt(4)\n"
"v_mfma_f32_16x16x32_bf16 v[28:31], v[184:187], v[148:151], v[28:31]\n"
"ds_read_b128 v[176:179], v246 offset:63488\n"
"v_mfma_f32_16x16x32_bf16 v[28:31], v[188:191], v[156:159], v[28:31]\n"
"buffer_load_dwordx4 v228, s[44:47], s34 offen offset:0 sc1 lds\n"
"v_mfma_f32_16x16x32_bf16 v[24:27], v[188:191], v[152:155], v[24:27]\n"
"ds_read_b128 v[180:183], v247 offset:5120\n"
"v_mfma_f32_16x16x32_bf16 v[24:27], v[184:187], v[144:147], v[24:27]\n"
"s_add_u32 s34, s62, 52224\n"
"s_waitcnt lgkmcnt(4)\n"
"v_mfma_f32_16x16x32_bf16 v[32:35], v[160:163], v[144:147], v[32:35]\n"
"ds_read_b128 v[184:187], v247 offset:6144\n"
"v_mfma_f32_16x16x32_bf16 v[32:35], v[164:167], v[152:155], v[32:35]\n"
"buffer_load_dwordx4 v228, s[44:47], s34 offen offset:1024 sc1 lds\n"
"v_mfma_f32_16x16x32_bf16 v[36:39], v[164:167], v[156:159], v[36:39]\n"
"ds_read_b128 v[188:191], v247 offset:13312\n"
"v_mfma_f32_16x16x32_bf16 v[36:39], v[160:163], v[148:151], v[36:39]\n"
"s_add_u32 s34, s62, 94208\n"
"s_waitcnt lgkmcnt(4)\n"
"v_mfma_f32_16x16x32_bf16 v[44:47], v[168:171], v[148:151], v[44:47]\n"
"ds_read_b128 v[160:163], v247 offset:7168\n"
"v_mfma_f32_16x16x32_bf16 v[44:47], v[172:175], v[156:159], v[44:47]\n"
"buffer_load_dwordx4 v228, s[44:47], s34 offen offset:2048 sc1 lds\n"
"v_mfma_f32_16x16x32_bf16 v[40:43], v[172:175], v[152:155], v[40:43]\n"
"ds_read_b128 v[164:167], v247 offset:14336\n"
"v_mfma_f32_16x16x32_bf16 v[40:43], v[168:171], v[144:147], v[40:43]\n"
"s_waitcnt lgkmcnt(4)\n"
"v_mfma_f32_16x16x32_bf16 v[48:51], v[176:179], v[144:147], v[48:51]\n"
"ds_read_b128 v[168:171], v247 offset:8192\n"
"v_mfma_f32_16x16x32_bf16 v[48:51], v[180:183], v[152:155], v[48:51]\n"
"v_mfma_f32_16x16x32_bf16 v[52:55], v[180:183], v[156:159], v[52:55]\n"
"ds_read_b128 v[172:175], v247 offset:15360\n"
"v_mfma_f32_16x16x32_bf16 v[52:55], v[176:179], v[148:151], v[52:55]\n"
"s_waitcnt lgkmcnt(4)\n"
"v_mfma_f32_16x16x32_bf16 v[60:63], v[184:187], v[148:151], v[60:63]\n"
"ds_read_b128 v[176:179], v247 offset:9216\n"
"v_mfma_f32_16x16x32_bf16 v[60:63], v[188:191], v[156:159], v[60:63]\n"
"buffer_load_dwordx4 v[136:139], v228, s[44:47], s61 offen offset:1024 sc1\n"
"v_mfma_f32_16x16x32_bf16 v[56:59], v[188:191], v[152:155], v[56:59]\n"
"ds_read_b128 v[180:183], v247 offset:16384\n"
"v_mfma_f32_16x16x32_bf16 v[56:59], v[184:187], v[144:147], v[56:59]\n"
"s_waitcnt lgkmcnt(4)\n"
"v_mfma_f32_16x16x32_bf16 v[64:67], v[160:163], v[144:147], v[64:67]\n"
"ds_read_b128 v[184:187], v247 offset:10240\n"
"v_mfma_f32_16x16x32_bf16 v[64:67], v[164:167], v[152:155], v[64:67]\n"
"buffer_load_dwordx4 v[140:143], v229, s[44:47], s61 offen offset:1024 sc1\n"
"v_mfma_f32_16x16x32_bf16 v[68:71], v[164:167], v[156:159], v[68:71]\n"
"ds_read_b128 v[188:191], v247 offset:17408\n"
"v_mfma_f32_16x16x32_bf16 v[68:71], v[160:163], v[148:151], v[68:71]\n"
"s_add_u32 s34, s62, 136192\n"
"s_waitcnt lgkmcnt(4)\n"
"v_mfma_f32_16x16x32_bf16 v[76:79], v[168:171], v[148:151], v[76:79]\n"
"ds_read_b128 v[160:163], v247 offset:11264\n"
"v_mfma_f32_16x16x32_bf16 v[76:79], v[172:175], v[156:159], v[76:79]\n"
"buffer_load_dwordx4 v228, s[44:47], s34 offen offset:3072 sc1 lds\n"
"v_mfma_f32_16x16x32_bf16 v[72:75], v[172:175], v[152:155], v[72:75]\n"
"ds_read_b128 v[164:167], v247 offset:18432\n"
"v_mfma_f32_16x16x32_bf16 v[72:75], v[168:171], v[144:147], v[72:75]\n"
"s_add_u32 m0, s56, 4096\n"
"s_add_u32 s34, s62, 182272\n"
"s_waitcnt lgkmcnt(4)\n"
"v_mfma_f32_16x16x32_bf16 v[80:83], v[176:179], v[144:147], v[80:83]\n"
"ds_read_b128 v[168:171], v247 offset:12288\n"
"v_mfma_f32_16x16x32_bf16 v[80:83], v[180:183], v[152:155], v[80:83]\n"
"buffer_load_dwordx4 v228, s[44:47], s34 offen offset:0 sc1 lds\n"
"v_mfma_f32_16x16x32_bf16 v[84:87], v[180:183], v[156:159], v[84:87]\n"
"ds_read_b128 v[172:175], v247 offset:19456\n"
"v_mfma_f32_16x16x32_bf16 v[84:87], v[176:179], v[148:151], v[84:87]\n"
"s_add_u32 s34, s62, 224256\n"
"s_waitcnt vmcnt(18) lgkmcnt(4)\n"
"v_mfma_f32_16x16x32_bf16 v[92:95], v[184:187], v[148:151], v[92:95]\n"
"s_barrier\n"
"ds_read_b128 v[176:179], v247 offset:20480\n"
"v_mfma_f32_16x16x32_bf16 v[92:95], v[188:191], v[156:159], v[92:95]\n"
"buffer_load_dwordx4 v228, s[44:47], s34 offen offset:1024 sc1 lds\n"
"v_mfma_f32_16x16x32_bf16 v[88:91], v[188:191], v[152:155], v[88:91]\n"
"ds_read_b128 v[180:183], v247 offset:27648\n"
"v_mfma_f32_16x16x32_bf16 v[88:91], v[184:187], v[144:147], v[88:91]\n"
"s_add_u32 s34, s63, 10240\n"
"s_waitcnt lgkmcnt(4)\n"
"v_mfma_f32_16x16x32_bf16 v[96:99], v[160:163], v[144:147], v[96:99]\n"
"ds_read_b128 v[184:187], v247 offset:21504\n"
"v_mfma_f32_16x16x32_bf16 v[96:99], v[164:167], v[152:155], v[96:99]\n"
"buffer_load_dwordx4 v228, s[44:47], s34 offen offset:2048 sc1 lds\n"
"v_mfma_f32_16x16x32_bf16 v[100:103], v[164:167], v[156:159], v[100:103]\n"
"ds_read_b128 v[188:191], v247 offset:28672\n"
"v_mfma_f32_16x16x32_bf16 v[100:103], v[160:163], v[148:151], v[100:103]\n"
"s_waitcnt lgkmcnt(4)\n"
"v_mfma_f32_16x16x32_bf16 v[108:111], v[168:171], v[148:151], v[108:111]\n"
"ds_read_b128 v[160:163], v247 offset:22528\n"
"v_mfma_f32_16x16x32_bf16 v[108:111], v[172:175], v[156:159], v[108:111]\n"
"v_mfma_f32_16x16x32_bf16 v[104:107], v[172:175], v[152:155], v[104:107]\n"
"ds_read_b128 v[164:167], v247 offset:29696\n"
"v_mfma_f32_16x16x32_bf16 v[104:107], v[168:171], v[144:147], v[104:107]\n"
"s_waitcnt vmcnt(16) lgkmcnt(4)\n"
"v_mfma_f32_16x16x32_bf16 v[0:3], v[176:179], v[112:115], v[0:3]\n"
"ds_read_b128 v[168:171], v247 offset:23552\n"
"v_add_f32 v224, v216, v218\n"
"v_mfma_f32_16x16x32_bf16 v[0:3], v[180:183], v[120:123], v[0:3]\n"
"buffer_load_dwordx4 v[144:147], v228, s[44:47], s61 offen offset:2048 sc1\n"
"v_mfma_f32_16x16x32_bf16 v[4:7], v[180:183], v[124:127], v[4:7]\n"
"ds_read_b128 v[172:175], v247 offset:30720\n"
"v_mov_b32 v225, v224\n"
"v_mfma_f32_16x16x32_bf16 v[4:7], v[176:179], v[116:119], v[4:7]\n"
"s_waitcnt lgkmcnt(4)\n"
"v_mfma_f32_16x16x32_bf16 v[12:15], v[184:187], v[116:119], v[12:15]\n"
"ds_read_b128 v[176:179], v247 offset:24576\n"
"v_permlane16_swap_b32 v224, v225\n"
"v_mfma_f32_16x16x32_bf16 v[12:15], v[188:191], v[124:127], v[12:15]\n"
"buffer_load_dwordx4 v[148:151], v229, s[44:47], s61 offen offset:2048 sc1\n"
"v_mfma_f32_16x16x32_bf16 v[8:11], v[188:191], v[120:123], v[8:11]\n"
"ds_read_b128 v[180:183], v247 offset:31744\n"
"v_add_f32 v224, v224, v225\n"
"v_mfma_f32_16x16x32_bf16 v[8:11], v[184:187], v[112:115], v[8:11]\n"
"s_add_u32 m0, s56, 28672\n"
"s_add_u32 s34, s62, 12288\n"
"s_waitcnt lgkmcnt(4)\n"
"v_mfma_f32_16x16x32_bf16 v[16:19], v[160:163], v[112:115], v[16:19]\n"
"ds_read_b128 v[184:187], v247 offset:25600\n"
"v_mov_b32 v225, v224\n"
"v_mfma_f32_16x16x32_bf16 v[16:19], v[164:167], v[120:123], v[16:19]\n"
"buffer_load_dwordx4 v228, s[44:47], s34 offen offset:0 sc1 lds\n"
"v_mfma_f32_16x16x32_bf16 v[20:23], v[164:167], v[124:127], v[20:23]\n"
"ds_read_b128 v[188:191], v247 offset:32768\n"
"v_permlane32_swap_b32 v224, v225\n"
"v_mfma_f32_16x16x32_bf16 v[20:23], v[160:163], v[116:119], v[20:23]\n"
"s_add_u32 s34, s62, 54272\n"
"s_waitcnt lgkmcnt(4)\n"
"v_mfma_f32_16x16x32_bf16 v[28:31], v[168:171], v[116:119], v[28:31]\n"
"ds_read_b128 v[160:163], v247 offset:26624\n"
"v_add_f32 v224, v224, v225\n"
"v_mfma_f32_16x16x32_bf16 v[28:31], v[172:175], v[124:127], v[28:31]\n"
"buffer_load_dwordx4 v228, s[44:47], s34 offen offset:1024 sc1 lds\n"
"v_mfma_f32_16x16x32_bf16 v[24:27], v[172:175], v[120:123], v[24:27]\n"
"ds_read_b128 v[164:167], v247 offset:33792\n"
"v_mul_f32 v224, 0x3e000000, v224\n"
"v_mfma_f32_16x16x32_bf16 v[24:27], v[168:171], v[112:115], v[24:27]\n"
"s_add_u32 s34, s62, 96256\n"
"s_waitcnt lgkmcnt(4)\n"
"v_mfma_f32_16x16x32_bf16 v[32:35], v[176:179], v[112:115], v[32:35]\n"
"ds_read_b128 v[168:171], v247 offset:34816\n"
"v_sub_f32 v216, v216, v224\n"
"v_mfma_f32_16x16x32_bf16 v[32:35], v[180:183], v[120:123], v[32:35]\n"
"buffer_load_dwordx4 v228, s[44:47], s34 offen offset:2048 sc1 lds\n"
"v_mfma_f32_16x16x32_bf16 v[36:39], v[180:183], v[124:127], v[36:39]\n"
"ds_read_b128 v[172:175], v247 offset:41984\n"
"v_sub_f32 v218, v218, v224\n"
"v_mfma_f32_16x16x32_bf16 v[36:39], v[176:179], v[116:119], v[36:39]\n"
"s_add_u32 s34, s62, 138240\n"
"s_waitcnt lgkmcnt(4)\n"
"v_mfma_f32_16x16x32_bf16 v[44:47], v[184:187], v[116:119], v[44:47]\n"
"ds_read_b128 v[176:179], v247 offset:35840\n"
"v_add_f32 v217, v217, v219\n"
"v_mfma_f32_16x16x32_bf16 v[44:47], v[188:191], v[124:127], v[44:47]\n"
"buffer_load_dwordx4 v228, s[44:47], s34 offen offset:3072 sc1 lds\n"
"v_mfma_f32_16x16x32_bf16 v[40:43], v[188:191], v[120:123], v[40:43]\n"
"ds_read_b128 v[180:183], v247 offset:43008\n"
"v_mul_f32 v216, v216, v216\n"
"v_mfma_f32_16x16x32_bf16 v[40:43], v[184:187], v[112:115], v[40:43]\n"
"s_waitcnt lgkmcnt(4)\n"
"v_mfma_f32_16x16x32_bf16 v[48:51], v[160:163], v[112:115], v[48:51]\n"
"ds_read_b128 v[184:187], v247 offset:36864\n"
"v_fmac_f32 v216, v218, v218\n"
"v_mfma_f32_16x16x32_bf16 v[48:51], v[164:167], v[120:123], v[48:51]\n"
"v_mfma_f32_16x16x32_bf16 v[52:55], v[164:167], v[124:127], v[52:55]\n"
"ds_read_b128 v[188:191], v247 offset:44032\n"
"v_fmac_f32 v217, 0x42a00000, v216\n"
"v_mfma_f32_16x16x32_bf16 v[52:55], v[160:163], v[116:119], v[52:55]\n"
"s_waitcnt lgkmcnt(4)\n"
"v_mfma_f32_16x16x32_bf16 v[60:63], v[168:171], v[116:119], v[60:63]\n"
"ds_read_b128 v[160:163], v247 offset:37888\n"
"v_mov_b32 v225, v217\n"
"v_mfma_f32_16x16x32_bf16 v[60:63], v[172:175], v[124:127], v[60:63]\n"
"buffer_load_dwordx4 v[152:155], v228, s[44:47], s61 offen offset:3072 sc1\n"
"v_mfma_f32_16x16x32_bf16 v[56:59], v[172:175], v[120:123], v[56:59]\n"
"ds_read_b128 v[164:167], v247 offset:45056\n"
"v_permlane16_swap_b32 v217, v225\n"
"v_mfma_f32_16x16x32_bf16 v[56:59], v[168:171], v[112:115], v[56:59]\n"
"s_waitcnt lgkmcnt(4)\n"
"v_mfma_f32_16x16x32_bf16 v[64:67], v[176:179], v[112:115], v[64:67]\n"
"ds_read_b128 v[168:171], v247 offset:38912\n"
"v_add_f32 v217, v217, v225\n"
"v_mfma_f32_16x16x32_bf16 v[64:67], v[180:183], v[120:123], v[64:67]\n"
"buffer_load_dwordx4 v[156:159], v229, s[44:47], s61 offen offset:3072 sc1\n"
"v_mfma_f32_16x16x32_bf16 v[68:71], v[180:183], v[124:127], v[68:71]\n"
"ds_read_b128 v[172:175], v247 offset:46080\n"
"v_mov_b32 v225, v217\n"
"v_mfma_f32_16x16x32_bf16 v[68:71], v[176:179], v[116:119], v[68:71]\n"
"s_add_u32 m0, s56, 32768\n"
"s_add_u32 s34, s62, 184320\n"
"s_waitcnt lgkmcnt(4)\n"
"v_mfma_f32_16x16x32_bf16 v[76:79], v[184:187], v[116:119], v[76:79]\n"
"ds_read_b128 v[176:179], v247 offset:39936\n"
"v_permlane32_swap_b32 v217, v225\n"
"v_mfma_f32_16x16x32_bf16 v[76:79], v[188:191], v[124:127], v[76:79]\n"
"buffer_load_dwordx4 v228, s[44:47], s34 offen offset:0 sc1 lds\n"
"v_mfma_f32_16x16x32_bf16 v[72:75], v[188:191], v[120:123], v[72:75]\n"
"ds_read_b128 v[180:183], v247 offset:47104\n"
"v_add_f32 v217, v217, v225\n"
"v_mfma_f32_16x16x32_bf16 v[72:75], v[184:187], v[112:115], v[72:75]\n"
"s_add_u32 s34, s62, 226304\n"
"s_waitcnt lgkmcnt(4)\n"
"v_mfma_f32_16x16x32_bf16 v[80:83], v[160:163], v[112:115], v[80:83]\n"
"ds_read_b128 v[184:187], v247 offset:40960\n"
"v_mov_b32 v216, v224\n"
"v_mfma_f32_16x16x32_bf16 v[80:83], v[164:167], v[120:123], v[80:83]\n"
"buffer_load_dwordx4 v228, s[44:47], s34 offen offset:1024 sc1 lds\n"
"v_mfma_f32_16x16x32_bf16 v[84:87], v[164:167], v[124:127], v[84:87]\n"
"ds_read_b128 v[188:191], v247 offset:48128\n"
"v_mov_b32 v225, 0x3727c5ac\n"
"v_mfma_f32_16x16x32_bf16 v[84:87], v[160:163], v[116:119], v[84:87]\n"
"s_add_u32 s34, s63, 12288\n"
"s_waitcnt vmcnt(19) lgkmcnt(4)\n"
"v_mfma_f32_16x16x32_bf16 v[92:95], v[168:171], v[116:119], v[92:95]\n"
"s_barrier\n"
"ds_read_b128 v[160:163], v247 offset:49152\n"
"v_mfma_f32_16x16x32_bf16 v[92:95], v[172:175], v[124:127], v[92:95]\n"
"buffer_load_dwordx4 v228, s[44:47], s34 offen offset:2048 sc1 lds\n"
"v_mfma_f32_16x16x32_bf16 v[88:91], v[172:175], v[120:123], v[88:91]\n"
"ds_read_b128 v[164:167], v247 offset:56320\n"
"v_fmac_f32 v225, 0x3acccccd, v217\n"
"v_mfma_f32_16x16x32_bf16 v[88:91], v[168:171], v[112:115], v[88:91]\n"
"s_add_u32 m0, s56, 57344\n"
"s_add_u32 s34, s62, 14336\n"
"s_waitcnt lgkmcnt(4)\n"
"v_mfma_f32_16x16x32_bf16 v[96:99], v[176:179], v[112:115], v[96:99]\n"
"ds_read_b128 v[168:171], v247 offset:50176\n"
"v_mov_b32 v217, v225\n"
"v_mfma_f32_16x16x32_bf16 v[96:99], v[180:183], v[120:123], v[96:99]\n"
"buffer_load_dwordx4 v228, s[44:47], s34 offen offset:0 sc1 lds\n"
"v_mfma_f32_16x16x32_bf16 v[100:103], v[180:183], v[124:127], v[100:103]\n"
"ds_read_b128 v[172:175], v247 offset:57344\n"
"v_sqrt_f32 v217, v217\n"
"v_mfma_f32_16x16x32_bf16 v[100:103], v[176:179], v[116:119], v[100:103]\n"
"s_waitcnt lgkmcnt(4)\n"
"v_mfma_f32_16x16x32_bf16 v[108:111], v[184:187], v[116:119], v[108:111]\n"
"ds_read_b128 v[176:179], v247 offset:51200\n"
"v_sub_f32 v216, 0, v216\n"
"v_mfma_f32_16x16x32_bf16 v[108:111], v[188:191], v[124:127], v[108:111]\n"
"v_mfma_f32_16x16x32_bf16 v[104:107], v[188:191], v[120:123], v[104:107]\n"
"ds_read_b128 v[180:183], v247 offset:58368\n"
"v_cvt_pk_bf16_f32 v192, v216, v216\n"
"v_mfma_f32_16x16x32_bf16 v[104:107], v[184:187], v[112:115], v[104:107]\n"
"s_add_u32 s61, s61, 4096\n"
"s_waitcnt vmcnt(16) lgkmcnt(4)\n"
"v_mfma_f32_16x16x32_bf16 v[0:3], v[160:163], v[128:131], v[0:3]\n"
"ds_read_b128 v[184:187], v247 offset:52224\n"
"v_cvt_pk_bf16_f32 v194, v217, v217\n"
"v_mfma_f32_16x16x32_bf16 v[0:3], v[164:167], v[136:139], v[0:3]\n"
"buffer_load_dwordx4 v[112:115], v228, s[44:47], s61 offen offset:0 sc1\n"
"v_mfma_f32_16x16x32_bf16 v[4:7], v[164:167], v[140:143], v[4:7]\n"
"ds_read_b128 v[188:191], v247 offset:59392\n"
"v_and_b32 v218, 0xffff0000, v192\n"
"v_mfma_f32_16x16x32_bf16 v[4:7], v[160:163], v[132:135], v[4:7]\n"
"s_waitcnt lgkmcnt(4)\n"
"v_mfma_f32_16x16x32_bf16 v[12:15], v[168:171], v[132:135], v[12:15]\n"
"ds_read_b128 v[160:163], v247 offset:53248\n"
"v_and_b32 v219, 0xffff0000, v194\n"
"v_mfma_f32_16x16x32_bf16 v[12:15], v[172:175], v[140:143], v[12:15]\n"
"buffer_load_dwordx4 v[116:119], v229, s[44:47], s61 offen offset:0 sc1\n"
"v_mfma_f32_16x16x32_bf16 v[8:11], v[172:175], v[136:139], v[8:11]\n"
"ds_read_b128 v[164:167], v247 offset:60416\n"
"v_sub_f32 v216, v216, v218\n"
"v_mfma_f32_16x16x32_bf16 v[8:11], v[168:171], v[128:131], v[8:11]\n"
"s_add_u32 s34, s62, 56320\n"
"s_waitcnt lgkmcnt(4)\n"
"v_mfma_f32_16x16x32_bf16 v[16:19], v[176:179], v[128:131], v[16:19]\n"
"ds_read_b128 v[168:171], v247 offset:54272\n"
"v_sub_f32 v217, v217, v219\n"
"v_mfma_f32_16x16x32_bf16 v[16:19], v[180:183], v[136:139], v[16:19]\n"
"buffer_load_dwordx4 v228, s[44:47], s34 offen offset:1024 sc1 lds\n"
"v_mfma_f32_16x16x32_bf16 v[20:23], v[180:183], v[140:143], v[20:23]\n"
"ds_read_b128 v[172:175], v247 offset:61440\n"
"v_cvt_pk_bf16_f32 v193, v216, v219\n"
"v_mfma_f32_16x16x32_bf16 v[20:23], v[176:179], v[132:135], v[20:23]\n"
"s_add_u32 s34, s62, 98304\n"
"s_waitcnt lgkmcnt(4)\n"
"v_mfma_f32_16x16x32_bf16 v[28:31], v[184:187], v[132:135], v[28:31]\n"
"ds_read_b128 v[176:179], v247 offset:55296\n"
"v_cvt_pk_bf16_f32 v194, v219, v217\n"
"v_mfma_f32_16x16x32_bf16 v[28:31], v[188:191], v[140:143], v[28:31]\n"
"buffer_load_dwordx4 v228, s[44:47], s34 offen offset:2048 sc1 lds\n"
"v_mfma_f32_16x16x32_bf16 v[24:27], v[188:191], v[136:139], v[24:27]\n"
"ds_read_b128 v[180:183], v247 offset:62464\n"
"v_mov_b32 v195, 0\n"
"v_mfma_f32_16x16x32_bf16 v[24:27], v[184:187], v[128:131], v[24:27]\n"
"s_add_u32 s34, s62, 140288\n"
"s_waitcnt lgkmcnt(4)\n"
"v_mfma_f32_16x16x32_bf16 v[32:35], v[160:163], v[128:131], v[32:35]\n"
"ds_read_b128 v[184:187], v247 offset:63488\n"
"v_add_f32 v224, v220, v222\n"
"v_mfma_f32_16x16x32_bf16 v[32:35], v[164:167], v[136:139], v[32:35]\n"
"buffer_load_dwordx4 v228, s[44:47], s34 offen offset:3072 sc1 lds\n"
"v_mfma_f32_16x16x32_bf16 v[36:39], v[164:167], v[140:143], v[36:39]\n"
"ds_read_b128 v[188:191], v248 offset:5120\n"
"v_mov_b32 v225, v224\n"
"v_mfma_f32_16x16x32_bf16 v[36:39], v[160:163], v[132:135], v[36:39]\n"
"s_add_u32 m0, s56, 61440\n"
"s_add_u32 s34, s62, 186368\n"
"s_waitcnt lgkmcnt(4)\n"
"v_mfma_f32_16x16x32_bf16 v[44:47], v[168:171], v[132:135], v[44:47]\n"
"ds_read_b128 v[160:163], v247 offset:64512\n"
"v_permlane16_swap_b32 v224, v225\n"
"v_mfma_f32_16x16x32_bf16 v[44:47], v[172:175], v[140:143], v[44:47]\n"
"buffer_load_dwordx4 v228, s[44:47], s34 offen offset:0 sc1 lds\n"
"v_mfma_f32_16x16x32_bf16 v[40:43], v[172:175], v[136:139], v[40:43]\n"
"ds_read_b128 v[164:167], v248 offset:6144\n"
"v_add_f32 v224, v224, v225\n"
"v_mfma_f32_16x16x32_bf16 v[40:43], v[168:171], v[128:131], v[40:43]\n"
"s_waitcnt lgkmcnt(4)\n"
"v_mfma_f32_16x16x32_bf16 v[48:51], v[176:179], v[128:131], v[48:51]\n"
"ds_read_b128 v[168:171], v248 offset:0\n"
"v_mov_b32 v225, v224\n"
"v_mfma_f32_16x16x32_bf16 v[48:51], v[180:183], v[136:139], v[48:51]\n"
"v_mfma_f32_16x16x32_bf16 v[52:55], v[180:183], v[140:143], v[52:55]\n"
"ds_read_b128 v[172:175], v248 offset:7168\n"
"v_permlane32_swap_b32 v224, v225\n"
"v_mfma_f32_16x16x32_bf16 v[52:55], v[176:179], v[132:135], v[52:55]\n"
"s_waitcnt lgkmcnt(4)\n"
"v_mfma_f32_16x16x32_bf16 v[60:63], v[184:187], v[132:135], v[60:63]\n"
"ds_read_b128 v[176:179], v248 offset:1024\n"
"v_add_f32 v224, v224, v225\n"
"v_mfma_f32_16x16x32_bf16 v[60:63], v[188:191], v[140:143], v[60:63]\n"
"buffer_load_dwordx4 v[120:123], v228, s[44:47], s61 offen offset:1024 sc1\n"
"v_mfma_f32_16x16x32_bf16 v[56:59], v[188:191], v[136:139], v[56:59]\n"
"ds_read_b128 v[180:183], v248 offset:8192\n"
"v_mul_f32 v224, 0x3e000000, v224\n"
"v_mfma_f32_16x16x32_bf16 v[56:59], v[184:187], v[128:131], v[56:59]\n"
"s_waitcnt lgkmcnt(4)\n"
"v_mfma_f32_16x16x32_bf16 v[64:67], v[160:163], v[128:131], v[64:67]\n"
"ds_read_b128 v[184:187], v248 offset:2048\n"
"v_sub_f32 v220, v220, v224\n"
"v_mfma_f32_16x16x32_bf16 v[64:67], v[164:167], v[136:139], v[64:67]\n"
"buffer_load_dwordx4 v[124:127], v229, s[44:47], s61 offen offset:1024 sc1\n"
"v_mfma_f32_16x16x32_bf16 v[68:71], v[164:167], v[140:143], v[68:71]\n"
"ds_read_b128 v[188:191], v248 offset:9216\n"
"v_sub_f32 v222, v222, v224\n"
"v_mfma_f32_16x16x32_bf16 v[68:71], v[160:163], v[132:135], v[68:71]\n"
"s_add_u32 s34, s62, 228352\n"
"s_waitcnt lgkmcnt(4)\n"
"v_mfma_f32_16x16x32_bf16 v[76:79], v[168:171], v[132:135], v[76:79]\n"
"ds_read_b128 v[160:163], v248 offset:3072\n"
"v_add_f32 v221, v221, v223\n"
"v_mfma_f32_16x16x32_bf16 v[76:79], v[172:175], v[140:143], v[76:79]\n"
"buffer_load_dwordx4 v228, s[44:47], s34 offen offset:1024 sc1 lds\n"
"v_mfma_f32_16x16x32_bf16 v[72:75], v[172:175], v[136:139], v[72:75]\n"
"ds_read_b128 v[164:167], v248 offset:10240\n"
"v_mul_f32 v220, v220, v220\n"
"v_mfma_f32_16x16x32_bf16 v[72:75], v[168:171], v[128:131], v[72:75]\n"
"s_add_u32 s34, s63, 14336\n"
"s_waitcnt lgkmcnt(4)\n"
"v_mfma_f32_16x16x32_bf16 v[80:83], v[176:179], v[128:131], v[80:83]\n"
"ds_read_b128 v[168:171], v248 offset:4096\n"
"v_fmac_f32 v220, v222, v222\n"
"v_mfma_f32_16x16x32_bf16 v[80:83], v[180:183], v[136:139], v[80:83]\n"
"buffer_load_dwordx4 v228, s[44:47], s34 offen offset:2048 sc1 lds\n"
"v_mfma_f32_16x16x32_bf16 v[84:87], v[180:183], v[140:143], v[84:87]\n"
"ds_read_b128 v[172:175], v248 offset:11264\n"
"v_fmac_f32 v221, 0x42a00000, v220\n"
"v_mfma_f32_16x16x32_bf16 v[84:87], v[176:179], v[132:135], v[84:87]\n"
"s_add_u32 s54, s58, 20480\n"
"s_waitcnt vmcnt(22) lgkmcnt(4)\n"
"v_mfma_f32_16x16x32_bf16 v[92:95], v[184:187], v[132:135], v[92:95]\n"
"s_barrier\n"
"ds_read_b128 v[176:179], v246 offset:0\n"
"v_mfma_f32_16x16x32_bf16 v[92:95], v[188:191], v[140:143], v[92:95]\n"
"buffer_load_dwordx4 a[176:179], v228, s[44:47], s54 offen offset:0 sc1\n"
"v_mfma_f32_16x16x32_bf16 v[88:91], v[188:191], v[136:139], v[88:91]\n"
"L_w15_%=:\n"
"s_cbranch_execz L_wd4_%=\n"
"ds_read_b128 v[180:183], v246 offset:7168\n"
"v_mov_b32 v225, v221\n"
"v_mfma_f32_16x16x32_bf16 v[88:91], v[184:187], v[128:131], v[88:91]\n"
"s_add_u32 m0, s56, 86016\n"
"s_add_u32 s34, s62, 16384\n"
"s_waitcnt lgkmcnt(4)\n"
"v_mfma_f32_16x16x32_bf16 v[96:99], v[160:163], v[128:131], v[96:99]\n"
"ds_read_b128 v[184:187], v246 offset:1024\n"
"v_permlane16_swap_b32 v221, v225\n"
"v_mfma_f32_16x16x32_bf16 v[96:99], v[164:167], v[136:139], v[96:99]\n"
"buffer_load_dwordx4 v228, s[44:47], s34 offen offset:0 sc1 lds\n"
"v_mfma_f32_16x16x32_bf16 v[100:103], v[164:167], v[140:143], v[100:103]\n"
"ds_read_b128 v[188:191], v246 offset:8192\n"
"v_add_f32 v221, v221, v225\n"
"v_mfma_f32_16x16x32_bf16 v[100:103], v[160:163], v[132:135], v[100:103]\n"
"s_waitcnt lgkmcnt(4)\n"
"v_mfma_f32_16x16x32_bf16 v[108:111], v[168:171], v[132:135], v[108:111]\n"
"ds_read_b128 v[160:163], v246 offset:2048\n"
"v_mov_b32 v225, v221\n"
"v_mfma_f32_16x16x32_bf16 v[108:111], v[172:175], v[140:143], v[108:111]\n"
"v_mfma_f32_16x16x32_bf16 v[104:107], v[172:175], v[136:139], v[104:107]\n"
"ds_read_b128 v[164:167], v246 offset:9216\n"
"v_permlane32_swap_b32 v221, v225\n"
"v_mfma_f32_16x16x32_bf16 v[104:107], v[168:171], v[128:131], v[104:107]\n"
"s_waitcnt vmcnt(16) lgkmcnt(4)\n"
"v_mfma_f32_16x16x32_bf16 v[0:3], v[176:179], v[144:147], v[0:3]\n"
"ds_read_b128 v[168:171], v246 offset:3072\n"
"v_add_f32 v221, v221, v225\n"
"v_mfma_f32_16x16x32_bf16 v[0:3], v[180:183], v[152:155], v[0:3]\n"
"buffer_load_dwordx4 v[128:131], v228, s[44:47], s61 offen offset:2048 sc1\n"
"v_mfma_f32_16x16x32_bf16 v[4:7], v[180:183], v[156:159], v[4:7]\n"
"ds_read_b128 v[172:175], v246 offset:10240\n"
"v_mov_b32 v220, v224\n"
"v_mfma_f32_16x16x32_bf16 v[4:7], v[176:179], v[148:151], v[4:7]\n"
"s_waitcnt lgkmcnt(4)\n"
"v_mfma_f32_16x16x32_bf16 v[12:15], v[184:187], v[148:151], v[12:15]\n"
"ds_read_b128 v[176:179], v246 offset:4096\n"
"v_mov_b32 v225, 0x3727c5ac\n"
"v_mfma_f32_16x16x32_bf16 v[12:15], v[188:191], v[156:159], v[12:15]\n"
"buffer_load_dwordx4 v[132:135], v229, s[44:47], s61 offen offset:2048 sc1\n"
"v_mfma_f32_16x16x32_bf16 v[8:11], v[188:191], v[152:155], v[8:11]\n"
"ds_read_b128 v[180:183], v246 offset:11264\n"
"v_fmac_f32 v225, 0x3acccccd, v221\n"
"v_mfma_f32_16x16x32_bf16 v[8:11], v[184:187], v[144:147], v[8:11]\n"
"s_add_u32 s34, s62, 58368\n"
"s_waitcnt lgkmcnt(4)\n"
"v_mfma_f32_16x16x32_bf16 v[16:19], v[160:163], v[144:147], v[16:19]\n"
"ds_read_b128 v[184:187], v246 offset:5120\n"
"v_mov_b32 v221, v225\n"
"v_mfma_f32_16x16x32_bf16 v[16:19], v[164:167], v[152:155], v[16:19]\n"
"buffer_load_dwordx4 v228, s[44:47], s34 offen offset:1024 sc1 lds\n"
"v_mfma_f32_16x16x32_bf16 v[20:23], v[164:167], v[156:159], v[20:23]\n"
"ds_read_b128 v[188:191], v246 offset:12288\n"
"v_sqrt_f32 v221, v221\n"
"v_mfma_f32_16x16x32_bf16 v[20:23], v[160:163], v[148:151], v[20:23]\n"
"s_add_u32 s34, s62, 100352\n"
"s_waitcnt lgkmcnt(4)\n"
"v_mfma_f32_16x16x32_bf16 v[28:31], v[168:171], v[148:151], v[28:31]\n"
"ds_read_b128 v[160:163], v246 offset:6144\n"
"v_sub_f32 v220, 0, v220\n"
"v_mfma_f32_16x16x32_bf16 v[28:31], v[172:175], v[156:159], v[28:31]\n"
"buffer_load_dwordx4 v228, s[44:47], s34 offen offset:2048 sc1 lds\n"
"v_mfma_f32_16x16x32_bf16 v[24:27], v[172:175], v[152:155], v[24:27]\n"
"ds_read_b128 v[164:167], v246 offset:13312\n"
"v_cvt_pk_bf16_f32 v196, v220, v220\n"
"v_mfma_f32_16x16x32_bf16 v[24:27], v[168:171], v[144:147], v[24:27]\n"
"s_add_u32 s34, s62, 142336\n"
"s_waitcnt lgkmcnt(4)\n"
"v_mfma_f32_16x16x32_bf16 v[32:35], v[176:179], v[144:147], v[32:35]\n"
"ds_read_b128 v[168:171], v246 offset:14336\n"
"v_cvt_pk_bf16_f32 v198, v221, v221\n"
"v_mfma_f32_16x16x32_bf16 v[32:35], v[180:183], v[152:155], v[32:35]\n"
"buffer_load_dwordx4 v228, s[44:47], s34 offen offset:3072 sc1 lds\n"
"v_mfma_f32_16x16x32_bf16 v[36:39], v[180:183], v[156:159], v[36:39]\n"
"ds_read_b128 v[172:175], v246 offset:21504\n"
"v_and_b32 v222, 0xffff0000, v196\n"
"v_mfma_f32_16x16x32_bf16 v[36:39], v[176:179], v[148:151], v[36:39]\n"
"s_add_u32 m0, s56, 90112\n"
"s_add_u32 s34, s62, 188416\n"
"s_waitcnt lgkmcnt(4)\n"
"v_mfma_f32_16x16x32_bf16 v[44:47], v[184:187], v[148:151], v[44:47]\n"
"ds_read_b128 v[176:179], v246 offset:15360\n"
"v_and_b32 v223, 0xffff0000, v198\n"
"v_mfma_f32_16x16x32_bf16 v[44:47], v[188:191], v[156:159], v[44:47]\n"
"buffer_load_dwordx4 v228, s[44:47], s34 offen offset:0 sc1 lds\n"
"v_mfma_f32_16x16x32_bf16 v[40:43], v[188:191], v[152:155], v[40:43]\n"
"ds_read_b128 v[180:183], v246 offset:22528\n"
"v_sub_f32 v220, v220, v222\n"
"v_mfma_f32_16x16x32_bf16 v[40:43], v[184:187], v[144:147], v[40:43]\n"
"s_waitcnt lgkmcnt(4)\n"
"v_mfma_f32_16x16x32_bf16 v[48:51], v[160:163], v[144:147], v[48:51]\n"
"ds_read_b128 v[184:187], v246 offset:16384\n"
"v_sub_f32 v221, v221, v223\n"
"v_mfma_f32_16x16x32_bf16 v[48:51], v[164:167], v[152:155], v[48:51]\n"
"v_mfma_f32_16x16x32_bf16 v[52:55], v[164:167], v[156:159], v[52:55]\n"
"ds_read_b128 v[188:191], v246 offset:23552\n"
"v_cvt_pk_bf16_f32 v197, v220, v223\n"
"v_mfma_f32_16x16x32_bf16 v[52:55], v[160:163], v[148:151], v[52:55]\n"
"s_waitcnt lgkmcnt(4)\n"
"v_mfma_f32_16x16x32_bf16 v[60:63], v[168:171], v[148:151], v[60:63]\n"
"ds_read_b128 v[160:163], v246 offset:17408\n"
"v_cvt_pk_bf16_f32 v198, v223, v221\n"
"v_mfma_f32_16x16x32_bf16 v[60:63], v[172:175], v[156:159], v[60:63]\n"
"buffer_load_dwordx4 v[136:139], v228, s[44:47], s61 offen offset:3072 sc1\n"
"v_mfma_f32_16x16x32_bf16 v[56:59], v[172:175], v[152:155], v[56:59]\n"
"ds_read_b128 v[164:167], v246 offset:24576\n"
"v_mov_b32 v199, 0\n"
"v_mfma_f32_16x16x32_bf16 v[56:59], v[168:171], v[144:147], v[56:59]\n"
"s_waitcnt lgkmcnt(4)\n"
"v_mfma_f32_16x16x32_bf16 v[64:67], v[176:179], v[144:147], v[64:67]\n"
"ds_read_b128 v[168:171], v246 offset:18432\n"
"v_mfma_f32_16x16x32_bf16 v[64:67], v[180:183], v[152:155], v[64:67]\n"
"buffer_load_dwordx4 v[140:143], v229, s[44:47], s61 offen offset:3072 sc1\n"
"v_mfma_f32_16x16x32_bf16 v[68:71], v[180:183], v[156:159], v[68:71]\n"
"ds_read_b128 v[172:175], v246 offset:25600\n"
"v_mfma_f32_16x16x32_bf16 v[68:71], v[176:179], v[148:151], v[68:71]\n"
"s_add_u32 s34, s62, 230400\n"
"s_waitcnt lgkmcnt(4)\n"
"v_mfma_f32_16x16x32_bf16 v[76:79], v[184:187], v[148:151], v[76:79]\n"
"ds_read_b128 v[176:179], v246 offset:19456\n"
"v_mfma_f32_16x16x32_bf16 v[76:79], v[188:191], v[156:159], v[76:79]\n"
"buffer_load_dwordx4 v228, s[44:47], s34 offen offset:1024 sc1 lds\n"
"v_mfma_f32_16x16x32_bf16 v[72:75], v[188:191], v[152:155], v[72:75]\n"
"ds_read_b128 v[180:183], v246 offset:26624\n"
"v_mfma_f32_16x16x32_bf16 v[72:75], v[184:187], v[144:147], v[72:75]\n"
"s_add_u32 s34, s63, 16384\n"
"s_waitcnt lgkmcnt(4)\n"
"v_mfma_f32_16x16x32_bf16 v[80:83], v[160:163], v[144:147], v[80:83]\n"
"ds_read_b128 v[184:187], v246 offset:20480\n"
"v_mfma_f32_16x16x32_bf16 v[80:83], v[164:167], v[152:155], v[80:83]\n"
"buffer_load_dwordx4 v228, s[44:47], s34 offen offset:2048 sc1 lds\n"
"v_mfma_f32_16x16x32_bf16 v[84:87], v[164:167], v[156:159], v[84:87]\n"
"ds_read_b128 v[188:191], v246 offset:27648\n"
"v_mfma_f32_16x16x32_bf16 v[84:87], v[160:163], v[148:151], v[84:87]\n"
"s_waitcnt vmcnt(23) lgkmcnt(4)\n"
"v_mfma_f32_16x16x32_bf16 v[92:95], v[168:171], v[148:151], v[92:95]\n"
"s_barrier\n"
"ds_read_b128 v[160:163], v246 offset:28672\n"
"v_mfma_f32_16x16x32_bf16 v[92:95], v[172:175], v[156:159], v[92:95]\n"
"buffer_load_dwordx4 a[180:183], v228, s[44:47], s54 offen offset:1024 sc1\n"
"v_mfma_f32_16x16x32_bf16 v[88:91], v[172:175], v[152:155], v[88:91]\n"
"ds_read_b128 v[164:167], v246 offset:35840\n"
"v_mfma_f32_16x16x32_bf16 v[88:91], v[168:171], v[144:147], v[88:91]\n"
"s_add_u32 m0, s56, 114688\n"
"s_add_u32 s34, s62, 18432\n"
"s_waitcnt lgkmcnt(4)\n"
"v_mfma_f32_16x16x32_bf16 v[96:99], v[176:179], v[144:147], v[96:99]\n"
"ds_read_b128 v[168:171], v246 offset:29696\n"
"v_mfma_f32_16x16x32_bf16 v[96:99], v[180:183], v[152:155], v[96:99]\n"
"buffer_load_dwordx4 v228, s[44:47], s34 offen offset:0 sc1 lds\n"
"v_mfma_f32_16x16x32_bf16 v[100:103], v[180:183], v[156:159], v[100:103]\n"
"ds_read_b128 v[172:175], v246 offset:36864\n"
"v_mfma_f32_16x16x32_bf16 v[100:103], v[176:179], v[148:151], v[100:103]\n"
"s_waitcnt lgkmcnt(4)\n"
"v_mfma_f32_16x16x32_bf16 v[108:111], v[184:187], v[148:151], v[108:111]\n"
"ds_read_b128 v[176:179], v246 offset:30720\n"
"v_mfma_f32_16x16x32_bf16 v[108:111], v[188:191], v[156:159], v[108:111]\n"
"v_mfma_f32_16x16x32_bf16 v[104:107], v[188:191], v[152:155], v[104:107]\n"
"ds_read_b128 v[180:183], v246 offset:37888\n"
"v_mfma_f32_16x16x32_bf16 v[104:107], v[184:187], v[144:147], v[104:107]\n"
"s_add_u32 s61, s61, 4096\n"
"s_waitcnt vmcnt(16) lgkmcnt(4)\n"
"v_mfma_f32_16x16x32_bf16 v[0:3], v[160:163], v[112:115], v[0:3]\n"
"ds_read_b128 v[184:187], v246 offset:31744\n"
"v_mfma_f32_16x16x32_bf16 v[0:3], v[164:167], v[120:123], v[0:3]\n"
"buffer_load_dwordx4 v[144:147], v228, s[44:47], s61 offen offset:0 sc1\n"
"v_mfma_f32_16x16x32_bf16 v[4:7], v[164:167], v[124:127], v[4:7]\n"
"ds_read_b128 v[188:191], v246 offset:38912\n"
"v_mfma_f32_16x16x32_bf16 v[4:7], v[160:163], v[116:119], v[4:7]\n"
"s_waitcnt lgkmcnt(4)\n"
"v_mfma_f32_16x16x32_bf16 v[12:15], v[168:171], v[116:119], v[12:15]\n"
"ds_read_b128 v[160:163], v246 offset:32768\n"
"v_mfma_f32_16x16x32_bf16 v[12:15], v[172:175], v[124:127], v[12:15]\n"
"buffer_load_dwordx4 v[148:151], v229, s[44:47], s61 offen offset:0 sc1\n"
"v_mfma_f32_16x16x32_bf16 v[8:11], v[172:175], v[120:123], v[8:11]\n"
"ds_read_b128 v[164:167], v246 offset:39936\n"
"v_mfma_f32_16x16x32_bf16 v[8:11], v[168:171], v[112:115], v[8:11]\n"
"s_add_u32 s34, s62, 60416\n"
"s_waitcnt lgkmcnt(4)\n"
"v_mfma_f32_16x16x32_bf16 v[16:19], v[176:179], v[112:115], v[16:19]\n"
"ds_read_b128 v[168:171], v246 offset:33792\n"
"v_mfma_f32_16x16x32_bf16 v[16:19], v[180:183], v[120:123], v[16:19]\n"
"buffer_load_dwordx4 v228, s[44:47], s34 offen offset:1024 sc1 lds\n"
"v_mfma_f32_16x16x32_bf16 v[20:23], v[180:183], v[124:127], v[20:23]\n"
"ds_read_b128 v[172:175], v246 offset:40960\n"
"v_mfma_f32_16x16x32_bf16 v[20:23], v[176:179], v[116:119], v[20:23]\n"
"s_add_u32 s34, s62, 102400\n"
"s_waitcnt lgkmcnt(4)\n"
"v_mfma_f32_16x16x32_bf16 v[28:31], v[184:187], v[116:119], v[28:31]\n"
"ds_read_b128 v[176:179], v246 offset:34816\n"
"v_mfma_f32_16x16x32_bf16 v[28:31], v[188:191], v[124:127], v[28:31]\n"
"buffer_load_dwordx4 v228, s[44:47], s34 offen offset:2048 sc1 lds\n"
"v_mfma_f32_16x16x32_bf16 v[24:27], v[188:191], v[120:123], v[24:27]\n"
"ds_read_b128 v[180:183], v246 offset:41984\n"
"v_mfma_f32_16x16x32_bf16 v[24:27], v[184:187], v[112:115], v[24:27]\n"
"s_add_u32 s34, s62, 144384\n"
"s_waitcnt lgkmcnt(4)\n"
"v_mfma_f32_16x16x32_bf16 v[32:35], v[160:163], v[112:115], v[32:35]\n"
"ds_read_b128 v[184:187], v246 offset:43008\n"
"v_mfma_f32_16x16x32_bf16 v[32:35], v[164:167], v[120:123], v[32:35]\n"
"buffer_load_dwordx4 v228, s[44:47], s34 offen offset:3072 sc1 lds\n"
"v_mfma_f32_16x16x32_bf16 v[36:39], v[164:167], v[124:127], v[36:39]\n"
"ds_read_b128 v[188:191], v246 offset:50176\n"
"v_mfma_f32_16x16x32_bf16 v[36:39], v[160:163], v[116:119], v[36:39]\n"
"s_add_u32 m0, s56, 118784\n"
"s_add_u32 s34, s62, 190464\n"
"s_waitcnt lgkmcnt(4)\n"
"v_mfma_f32_16x16x32_bf16 v[44:47], v[168:171], v[116:119], v[44:47]\n"
"ds_read_b128 v[160:163], v246 offset:44032\n"
"v_mfma_f32_16x16x32_bf16 v[44:47], v[172:175], v[124:127], v[44:47]\n"
"buffer_load_dwordx4 v228, s[44:47], s34 offen offset:0 sc1 lds\n"
"v_mfma_f32_16x16x32_bf16 v[40:43], v[172:175], v[120:123], v[40:43]\n"
"ds_read_b128 v[164:167], v246 offset:51200\n"
"v_mfma_f32_16x16x32_bf16 v[40:43], v[168:171], v[112:115], v[40:43]\n"
"s_waitcnt lgkmcnt(4)\n"
"v_mfma_f32_16x16x32_bf16 v[48:51], v[176:179], v[112:115], v[48:51]\n"
"ds_read_b128 v[168:171], v246 offset:45056\n"
"v_mfma_f32_16x16x32_bf16 v[48:51], v[180:183], v[120:123], v[48:51]\n"
"v_mfma_f32_16x16x32_bf16 v[52:55], v[180:183], v[124:127], v[52:55]\n"
"ds_read_b128 v[172:175], v246 offset:52224\n"
"v_mfma_f32_16x16x32_bf16 v[52:55], v[176:179], v[116:119], v[52:55]\n"
"s_waitcnt lgkmcnt(4)\n"
"v_mfma_f32_16x16x32_bf16 v[60:63], v[184:187], v[116:119], v[60:63]\n"
"ds_read_b128 v[176:179], v246 offset:46080\n"
"v_mfma_f32_16x16x32_bf16 v[60:63], v[188:191], v[124:127], v[60:63]\n"
"buffer_load_dwordx4 v[152:155], v228, s[44:47], s61 offen offset:1024 sc1\n"
"v_mfma_f32_16x16x32_bf16 v[56:59], v[188:191], v[120:123], v[56:59]\n"
"ds_read_b128 v[180:183], v246 offset:53248\n"
"v_mfma_f32_16x16x32_bf16 v[56:59], v[184:187], v[112:115], v[56:59]\n"
"s_waitcnt lgkmcnt(4)\n"
"v_mfma_f32_16x16x32_bf16 v[64:67], v[160:163], v[112:115], v[64:67]\n"
"ds_read_b128 v[184:187], v246 offset:47104\n"
"v_mfma_f32_16x16x32_bf16 v[64:67], v[164:167], v[120:123], v[64:67]\n"
"buffer_load_dwordx4 v[156:159], v229, s[44:47], s61 offen offset:1024 sc1\n"
"v_mfma_f32_16x16x32_bf16 v[68:71], v[164:167], v[124:127], v[68:71]\n"
"ds_read_b128 v[188:191], v246 offset:54272\n"
"v_mfma_f32_16x16x32_bf16 v[68:71], v[160:163], v[116:119], v[68:71]\n"
"s_add_u32 s34, s62, 232448\n"
"s_waitcnt lgkmcnt(4)\n"
"v_mfma_f32_16x16x32_bf16 v[76:79], v[168:171], v[116:119], v[76:79]\n"
"ds_read_b128 v[160:163], v246 offset:48128\n"
"v_mfma_f32_16x16x32_bf16 v[76:79], v[172:175], v[124:127], v[76:79]\n"
"buffer_load_dwordx4 v228, s[44:47], s34 offen offset:1024 sc1 lds\n"
"v_mfma_f32_16x16x32_bf16 v[72:75], v[172:175], v[120:123], v[72:75]\n"
"ds_read_b128 v[164:167], v246 offset:55296\n"
"v_mfma_f32_16x16x32_bf16 v[72:75], v[168:171], v[112:115], v[72:75]\n"
"s_add_u32 s34, s63, 18432\n"
"s_waitcnt lgkmcnt(4)\n"
"v_mfma_f32_16x16x32_bf16 v[80:83], v[176:179], v[112:115], v[80:83]\n"
"ds_read_b128 v[168:171], v246 offset:49152\n"
"v_mfma_f32_16x16x32_bf16 v[80:83], v[180:183], v[120:123], v[80:83]\n"
"buffer_load_dwordx4 v228, s[44:47], s34 offen offset:2048 sc1 lds\n"
"v_mfma_f32_16x16x32_bf16 v[84:87], v[180:183], v[124:127], v[84:87]\n"
"ds_read_b128 v[172:175], v246 offset:56320\n"
"v_mfma_f32_16x16x32_bf16 v[84:87], v[176:179], v[116:119], v[84:87]\n"
"s_waitcnt vmcnt(24) lgkmcnt(4)\n"
"v_mfma_f32_16x16x32_bf16 v[92:95], v[184:187], v[116:119], v[92:95]\n"
"s_barrier\n"
"ds_read_b128 v[176:179], v246 offset:57344\n"
"v_mfma_f32_16x16x32_bf16 v[92:95], v[188:191], v[124:127], v[92:95]\n"
"buffer_load_dwordx4 a[184:187], v228, s[44:47], s54 offen offset:2048 sc1\n"
"v_mfma_f32_16x16x32_bf16 v[88:91], v[188:191], v[120:123], v[88:91]\n"
"ds_read_b128 v[180:183], v246 offset:64512\n"
"v_mfma_f32_16x16x32_bf16 v[88:91], v[184:187], v[112:115], v[88:91]\n"
"s_waitcnt lgkmcnt(4)\n"
"v_mfma_f32_16x16x32_bf16 v[96:99], v[160:163], v[112:115], v[96:99]\n"
"ds_read_b128 v[184:187], v246 offset:58368\n"
"v_mfma_f32_16x16x32_bf16 v[96:99], v[164:167], v[120:123], v[96:99]\n"
"buffer_load_dwordx4 a[188:191], v228, s[44:47], s54 offen offset:3072 sc1\n"
"v_mfma_f32_16x16x32_bf16 v[100:103], v[164:167], v[124:127], v[100:103]\n"
"ds_read_b128 v[188:191], v247 offset:0\n"
"v_mfma_f32_16x16x32_bf16 v[100:103], v[160:163], v[116:119], v[100:103]\n"
"s_waitcnt lgkmcnt(4)\n"
"v_mfma_f32_16x16x32_bf16 v[108:111], v[168:171], v[116:119], v[108:111]\n"
"ds_read_b128 v[160:163], v246 offset:59392\n"
"v_mfma_f32_16x16x32_bf16 v[108:111], v[172:175], v[124:127], v[108:111]\n"
"v_mfma_f32_16x16x32_bf16 v[104:107], v[172:175], v[120:123], v[104:107]\n"
"ds_read_b128 v[164:167], v247 offset:1024\n"
"v_mfma_f32_16x16x32_bf16 v[104:107], v[168:171], v[112:115], v[104:107]\n"
"s_waitcnt vmcnt(16) lgkmcnt(4)\n"
"v_mfma_f32_16x16x32_bf16 v[0:3], v[176:179], v[128:131], v[0:3]\n"
"ds_read_b128 v[168:171], v246 offset:60416\n"
"v_mfma_f32_16x16x32_bf16 v[0:3], v[180:183], v[136:139], v[0:3]\n"
"buffer_load_dwordx4 v[112:115], v228, s[44:47], s61 offen offset:2048 sc1\n"
"v_mfma_f32_16x16x32_bf16 v[4:7], v[180:183], v[140:143], v[4:7]\n"
"ds_read_b128 v[172:175], v247 offset:2048\n"
"v_mfma_f32_16x16x32_bf16 v[4:7], v[176:179], v[132:135], v[4:7]\n"
"s_waitcnt lgkmcnt(4)\n"
"v_mfma_f32_16x16x32_bf16 v[12:15], v[184:187], v[132:135], v[12:15]\n"
"ds_read_b128 v[176:179], v246 offset:61440\n"
"v_mfma_f32_16x16x32_bf16 v[12:15], v[188:191], v[140:143], v[12:15]\n"
"buffer_load_dwordx4 v[116:119], v229, s[44:47], s61 offen offset:2048 sc1\n"
"v_mfma_f32_16x16x32_bf16 v[8:11], v[188:191], v[136:139], v[8:11]\n"
"ds_read_b128 v[180:183], v247 offset:3072\n"
"v_mfma_f32_16x16x32_bf16 v[8:11], v[184:187], v[128:131], v[8:11]\n"
"s_add_u32 s54, s58, 24576\n"
"s_waitcnt lgkmcnt(4)\n"
"v_mfma_f32_16x16x32_bf16 v[16:19], v[160:163], v[128:131], v[16:19]\n"
"ds_read_b128 v[184:187], v246 offset:62464\n"
"v_mfma_f32_16x16x32_bf16 v[16:19], v[164:167], v[136:139], v[16:19]\n"
"buffer_load_dwordx4 a[192:195], v228, s[44:47], s54 offen offset:0 sc1\n"
"v_mfma_f32_16x16x32_bf16 v[20:23], v[164:167], v[140:143], v[20:23]\n"
"ds_read_b128 v[188:191], v247 offset:4096\n"
"v_mfma_f32_16x16x32_bf16 v[20:23], v[160:163], v[132:135], v[20:23]\n"
"s_waitcnt lgkmcnt(4)\n"
"v_mfma_f32_16x16x32_bf16 v[28:31], v[168:171], v[132:135], v[28:31]\n"
"ds_read_b128 v[160:163], v246 offset:63488\n"
"v_mfma_f32_16x16x32_bf16 v[28:31], v[172:175], v[140:143], v[28:31]\n"
"buffer_load_dwordx4 a[196:199], v228, s[44:47], s54 offen offset:1024 sc1\n"
"v_mfma_f32_16x16x32_bf16 v[24:27], v[172:175], v[136:139], v[24:27]\n"
"ds_read_b128 v[164:167], v247 offset:5120\n"
"v_mfma_f32_16x16x32_bf16 v[24:27], v[168:171], v[128:131], v[24:27]\n"
"s_waitcnt lgkmcnt(4)\n"
"v_mfma_f32_16x16x32_bf16 v[32:35], v[176:179], v[128:131], v[32:35]\n"
"ds_read_b128 v[168:171], v247 offset:6144\n"
"v_mfma_f32_16x16x32_bf16 v[32:35], v[180:183], v[136:139], v[32:35]\n"
"buffer_load_dwordx4 a[200:203], v228, s[44:47], s54 offen offset:2048 sc1\n"
"v_mfma_f32_16x16x32_bf16 v[36:39], v[180:183], v[140:143], v[36:39]\n"
"ds_read_b128 v[172:175], v247 offset:13312\n"
"v_mfma_f32_16x16x32_bf16 v[36:39], v[176:179], v[132:135], v[36:39]\n"
"s_waitcnt lgkmcnt(4)\n"
"v_mfma_f32_16x16x32_bf16 v[44:47], v[184:187], v[132:135], v[44:47]\n"
"ds_read_b128 v[176:179], v247 offset:7168\n"
"v_mfma_f32_16x16x32_bf16 v[44:47], v[188:191], v[140:143], v[44:47]\n"
"buffer_load_dwordx4 a[204:207], v228, s[44:47], s54 offen offset:3072 sc1\n"
"v_mfma_f32_16x16x32_bf16 v[40:43], v[188:191], v[136:139], v[40:43]\n"
"ds_read_b128 v[180:183], v247 offset:14336\n"
"v_mfma_f32_16x16x32_bf16 v[40:43], v[184:187], v[128:131], v[40:43]\n"
"s_waitcnt lgkmcnt(4)\n"
"v_mfma_f32_16x16x32_bf16 v[48:51], v[160:163], v[128:131], v[48:51]\n"
"ds_read_b128 v[184:187], v247 offset:8192\n"
"v_mfma_f32_16x16x32_bf16 v[48:51], v[164:167], v[136:139], v[48:51]\n"
"v_mfma_f32_16x16x32_bf16 v[52:55], v[164:167], v[140:143], v[52:55]\n"
"ds_read_b128 v[188:191], v247 offset:15360\n"
"v_mfma_f32_16x16x32_bf16 v[52:55], v[160:163], v[132:135], v[52:55]\n"
"s_waitcnt lgkmcnt(4)\n"
"v_mfma_f32_16x16x32_bf16 v[60:63], v[168:171], v[132:135], v[60:63]\n"
"ds_read_b128 v[160:163], v247 offset:9216\n"
"v_mfma_f32_16x16x32_bf16 v[60:63], v[172:175], v[140:143], v[60:63]\n"
"buffer_load_dwordx4 v[120:123], v228, s[44:47], s61 offen offset:3072 sc1\n"
"v_mfma_f32_16x16x32_bf16 v[56:59], v[172:175], v[136:139], v[56:59]\n"
"ds_read_b128 v[164:167], v247 offset:16384\n"
"v_mfma_f32_16x16x32_bf16 v[56:59], v[168:171], v[128:131], v[56:59]\n"
"s_waitcnt lgkmcnt(4)\n"
"v_mfma_f32_16x16x32_bf16 v[64:67], v[176:179], v[128:131], v[64:67]\n"
"ds_read_b128 v[168:171], v247 offset:10240\n"
"v_mfma_f32_16x16x32_bf16 v[64:67], v[180:183], v[136:139], v[64:67]\n"
"buffer_load_dwordx4 v[124:127], v229, s[44:47], s61 offen offset:3072 sc1\n"
"v_mfma_f32_16x16x32_bf16 v[68:71], v[180:183], v[140:143], v[68:71]\n"
"ds_read_b128 v[172:175], v247 offset:17408\n"
"v_mfma_f32_16x16x32_bf16 v[68:71], v[176:179], v[132:135], v[68:71]\n"
"s_add_u32 s54, s58, 28672\n"
"s_waitcnt lgkmcnt(4)\n"
"v_mfma_f32_16x16x32_bf16 v[76:79], v[184:187], v[132:135], v[76:79]\n"
"ds_read_b128 v[176:179], v247 offset:11264\n"
"v_mfma_f32_16x16x32_bf16 v[76:79], v[188:191], v[140:143], v[76:79]\n"
"buffer_load_dwordx4 a[208:211], v228, s[44:47], s54 offen offset:0 sc1\n"
"v_mfma_f32_16x16x32_bf16 v[72:75], v[188:191], v[136:139], v[72:75]\n"
"ds_read_b128 v[180:183], v247 offset:18432\n"
"v_mfma_f32_16x16x32_bf16 v[72:75], v[184:187], v[128:131], v[72:75]\n"
"s_waitcnt lgkmcnt(4)\n"
"v_mfma_f32_16x16x32_bf16 v[80:83], v[160:163], v[128:131], v[80:83]\n"
"ds_read_b128 v[184:187], v247 offset:12288\n"
"v_mfma_f32_16x16x32_bf16 v[80:83], v[164:167], v[136:139], v[80:83]\n"
"buffer_load_dwordx4 a[244:247], v228, s[44:47], s54 offen offset:1024 sc1\n"
"v_mfma_f32_16x16x32_bf16 v[84:87], v[164:167], v[140:143], v[84:87]\n"
"ds_read_b128 v[188:191], v247 offset:19456\n"
"v_mfma_f32_16x16x32_bf16 v[84:87], v[160:163], v[132:135], v[84:87]\n"
"s_waitcnt vmcnt(24) lgkmcnt(4)\n"
"v_mfma_f32_16x16x32_bf16 v[92:95], v[168:171], v[132:135], v[92:95]\n"
"s_barrier\n"
"ds_read_b128 v[160:163], v247 offset:20480\n"
"v_mfma_f32_16x16x32_bf16 v[92:95], v[172:175], v[140:143], v[92:95]\n"
"buffer_load_dwordx4 a[248:251], v228, s[44:47], s54 offen offset:2048 sc1\n"
"v_mfma_f32_16x16x32_bf16 v[88:91], v[172:175], v[136:139], v[88:91]\n"
"ds_read_b128 v[164:167], v247 offset:27648\n"
"v_mfma_f32_16x16x32_bf16 v[88:91], v[168:171], v[128:131], v[88:91]\n"
"s_waitcnt lgkmcnt(4)\n"
"v_mfma_f32_16x16x32_bf16 v[96:99], v[176:179], v[128:131], v[96:99]\n"
"ds_read_b128 v[168:171], v247 offset:21504\n"
"v_mfma_f32_16x16x32_bf16 v[96:99], v[180:183], v[136:139], v[96:99]\n"
"buffer_load_dwordx4 a[252:255], v228, s[44:47], s54 offen offset:3072 sc1\n"
"v_mfma_f32_16x16x32_bf16 v[100:103], v[180:183], v[140:143], v[100:103]\n"
"ds_read_b128 v[172:175], v247 offset:28672\n"
"v_mfma_f32_16x16x32_bf16 v[100:103], v[176:179], v[132:135], v[100:103]\n"
"s_waitcnt lgkmcnt(4)\n"
"v_mfma_f32_16x16x32_bf16 v[108:111], v[184:187], v[132:135], v[108:111]\n"
"ds_read_b128 v[176:179], v247 offset:22528\n"
"v_mfma_f32_16x16x32_bf16 v[108:111], v[188:191], v[140:143], v[108:111]\n"
"v_mfma_f32_16x16x32_bf16 v[104:107], v[188:191], v[136:139], v[104:107]\n"
"ds_read_b128 v[180:183], v247 offset:29696\n"
"v_mfma_f32_16x16x32_bf16 v[104:107], v[184:187], v[128:131], v[104:107]\n"
"s_add_u32 s54, s58, 32768\n"
"s_waitcnt vmcnt(16) lgkmcnt(4)\n"
"v_mfma_f32_16x16x32_bf16 v[0:3], v[160:163], v[144:147], v[0:3]\n"
"ds_read_b128 v[184:187], v247 offset:23552\n"
"v_mfma_f32_16x16x32_bf16 v[0:3], v[164:167], v[152:155], v[0:3]\n"
"buffer_load_dwordx4 a[60:63], v228, s[44:47], s54 offen offset:0 sc1\n"
"v_mfma_f32_16x16x32_bf16 v[4:7], v[164:167], v[156:159], v[4:7]\n"
"ds_read_b128 v[188:191], v247 offset:30720\n"
"v_mfma_f32_16x16x32_bf16 v[4:7], v[160:163], v[148:151], v[4:7]\n"
"s_waitcnt lgkmcnt(4)\n"
"v_mfma_f32_16x16x32_bf16 v[12:15], v[168:171], v[148:151], v[12:15]\n"
"ds_read_b128 v[160:163], v247 offset:24576\n"
"v_mfma_f32_16x16x32_bf16 v[12:15], v[172:175], v[156:159], v[12:15]\n"
"buffer_load_dwordx4 a[64:67], v228, s[44:47], s54 offen offset:1024 sc1\n"
"v_mfma_f32_16x16x32_bf16 v[8:11], v[172:175], v[152:155], v[8:11]\n"
"ds_read_b128 v[164:167], v247 offset:31744\n"
"v_mfma_f32_16x16x32_bf16 v[8:11], v[168:171], v[144:147], v[8:11]\n"
"s_waitcnt lgkmcnt(4)\n"
"v_mfma_f32_16x16x32_bf16 v[16:19], v[176:179], v[144:147], v[16:19]\n"
"ds_read_b128 v[168:171], v247 offset:25600\n"
"v_mfma_f32_16x16x32_bf16 v[16:19], v[180:183], v[152:155], v[16:19]\n"
"buffer_load_dwordx4 a[68:71], v228, s[44:47], s54 offen offset:2048 sc1\n"
"v_mfma_f32_16x16x32_bf16 v[20:23], v[180:183], v[156:159], v[20:23]\n"
"ds_read_b128 v[172:175], v247 offset:32768\n"
"v_mfma_f32_16x16x32_bf16 v[20:23], v[176:179], v[148:151], v[20:23]\n"
"s_waitcnt lgkmcnt(4)\n"
"v_mfma_f32_16x16x32_bf16 v[28:31], v[184:187], v[148:151], v[28:31]\n"
"ds_read_b128 v[176:179], v247 offset:26624\n"
"v_mfma_f32_16x16x32_bf16 v[28:31], v[188:191], v[156:159], v[28:31]\n"
"buffer_load_dwordx4 a[72:75], v228, s[44:47], s54 offen offset:3072 sc1\n"
"v_mfma_f32_16x16x32_bf16 v[24:27], v[188:191], v[152:155], v[24:27]\n"
"ds_read_b128 v[180:183], v247 offset:33792\n"
"v_mfma_f32_16x16x32_bf16 v[24:27], v[184:187], v[144:147], v[24:27]\n"
"s_add_u32 s54, s58, 36864\n"
"s_waitcnt lgkmcnt(4)\n"
"v_mfma_f32_16x16x32_bf16 v[32:35], v[160:163], v[144:147], v[32:35]\n"
"ds_read_b128 v[184:187], v247 offset:34816\n"
"v_mfma_f32_16x16x32_bf16 v[32:35], v[164:167], v[152:155], v[32:35]\n"
"buffer_load_dwordx4 a[76:79], v228, s[44:47], s54 offen offset:0 sc1\n"
"v_mfma_f32_16x16x32_bf16 v[36:39], v[164:167], v[156:159], v[36:39]\n"
"ds_read_b128 v[188:191], v247 offset:41984\n"
"v_mfma_f32_16x16x32_bf16 v[36:39], v[160:163], v[148:151], v[36:39]\n"
"s_waitcnt lgkmcnt(4)\n"
"v_mfma_f32_16x16x32_bf16 v[44:47], v[168:171], v[148:151], v[44:47]\n"
"ds_read_b128 v[160:163], v247 offset:35840\n"
"v_mfma_f32_16x16x32_bf16 v[44:47], v[172:175], v[156:159], v[44:47]\n"
"buffer_load_dwordx4 a[80:83], v228, s[44:47], s54 offen offset:1024 sc1\n"
"v_mfma_f32_16x16x32_bf16 v[40:43], v[172:175], v[152:155], v[40:43]\n"
"ds_read_b128 v[164:167], v247 offset:43008\n"
"v_mfma_f32_16x16x32_bf16 v[40:43], v[168:171], v[144:147], v[40:43]\n"
"s_waitcnt lgkmcnt(4)\n"
"v_mfma_f32_16x16x32_bf16 v[48:51], v[176:179], v[144:147], v[48:51]\n"
"ds_read_b128 v[168:171], v247 offset:36864\n"
"v_mfma_f32_16x16x32_bf16 v[48:51], v[180:183], v[152:155], v[48:51]\n"
"v_mfma_f32_16x16x32_bf16 v[52:55], v[180:183], v[156:159], v[52:55]\n"
"ds_read_b128 v[172:175], v247 offset:44032\n"
"v_mfma_f32_16x16x32_bf16 v[52:55], v[176:179], v[148:151], v[52:55]\n"
"s_waitcnt lgkmcnt(4)\n"
"v_mfma_f32_16x16x32_bf16 v[60:63], v[184:187], v[148:151], v[60:63]\n"
"ds_read_b128 v[176:179], v247 offset:37888\n"
"v_mfma_f32_16x16x32_bf16 v[60:63], v[188:191], v[156:159], v[60:63]\n"
"buffer_load_dwordx4 a[84:87], v228, s[44:47], s54 offen offset:2048 sc1\n"
"v_mfma_f32_16x16x32_bf16 v[56:59], v[188:191], v[152:155], v[56:59]\n"
"ds_read_b128 v[180:183], v247 offset:45056\n"
"v_mfma_f32_16x16x32_bf16 v[56:59], v[184:187], v[144:147], v[56:59]\n"
"s_waitcnt lgkmcnt(4)\n"
"v_mfma_f32_16x16x32_bf16 v[64:67], v[160:163], v[144:147], v[64:67]\n"
"ds_read_b128 v[184:187], v247 offset:38912\n"
"v_mfma_f32_16x16x32_bf16 v[64:67], v[164:167], v[152:155], v[64:67]\n"
"buffer_load_dwordx4 a[88:91], v228, s[44:47], s54 offen offset:3072 sc1\n"
"v_mfma_f32_16x16x32_bf16 v[68:71], v[164:167], v[156:159], v[68:71]\n"
"ds_read_b128 v[188:191], v247 offset:46080\n"
"v_mfma_f32_16x16x32_bf16 v[68:71], v[160:163], v[148:151], v[68:71]\n"
"s_add_u32 s54, s58, 40960\n"
"s_waitcnt lgkmcnt(4)\n"
"v_mfma_f32_16x16x32_bf16 v[76:79], v[168:171], v[148:151], v[76:79]\n"
"ds_read_b128 v[160:163], v247 offset:39936\n"
"v_mfma_f32_16x16x32_bf16 v[76:79], v[172:175], v[156:159], v[76:79]\n"
"buffer_load_dwordx4 a[92:95], v228, s[44:47], s54 offen offset:0 sc1\n"
"v_mfma_f32_16x16x32_bf16 v[72:75], v[172:175], v[152:155], v[72:75]\n"
"ds_read_b128 v[164:167], v247 offset:47104\n"
"v_mfma_f32_16x16x32_bf16 v[72:75], v[168:171], v[144:147], v[72:75]\n"
"s_waitcnt lgkmcnt(4)\n"
"v_mfma_f32_16x16x32_bf16 v[80:83], v[176:179], v[144:147], v[80:83]\n"
"ds_read_b128 v[168:171], v247 offset:40960\n"
"v_mfma_f32_16x16x32_bf16 v[80:83], v[180:183], v[152:155], v[80:83]\n"
"buffer_load_dwordx4 a[96:99], v228, s[44:47], s54 offen offset:1024 sc1\n"
"v_mfma_f32_16x16x32_bf16 v[84:87], v[180:183], v[156:159], v[84:87]\n"
"ds_read_b128 v[172:175], v247 offset:48128\n"
"v_mfma_f32_16x16x32_bf16 v[84:87], v[176:179], v[148:151], v[84:87]\n"
"s_waitcnt vmcnt(24) lgkmcnt(4)\n"
"v_mfma_f32_16x16x32_bf16 v[92:95], v[184:187], v[148:151], v[92:95]\n"
"s_barrier\n"
"ds_read_b128 v[176:179], v247 offset:49152\n"
"v_mfma_f32_16x16x32_bf16 v[92:95], v[188:191], v[156:159], v[92:95]\n"
"buffer_load_dwordx4 a[100:103], v228, s[44:47], s54 offen offset:2048 sc1\n"
"v_mfma_f32_16x16x32_bf16 v[88:91], v[188:191], v[152:155], v[88:91]\n"
"ds_read_b128 v[180:183], v247 offset:56320\n"
"v_mfma_f32_16x16x32_bf16 v[88:91], v[184:187], v[144:147], v[88:91]\n"
"s_waitcnt lgkmcnt(4)\n"
"v_mfma_f32_16x16x32_bf16 v[96:99], v[160:163], v[144:147], v[96:99]\n"
"ds_read_b128 v[184:187], v247 offset:50176\n"
"v_mfma_f32_16x16x32_bf16 v[96:99], v[164:167], v[152:155], v[96:99]\n"
"buffer_load_dwordx4 a[104:107], v228, s[44:47], s54 offen offset:3072 sc1\n"
"v_mfma_f32_16x16x32_bf16 v[100:103], v[164:167], v[156:159], v[100:103]\n"
"ds_read_b128 v[188:191], v247 offset:57344\n"
"v_mfma_f32_16x16x32_bf16 v[100:103], v[160:163], v[148:151], v[100:103]\n"
"s_waitcnt lgkmcnt(4)\n"
"v_mfma_f32_16x16x32_bf16 v[108:111], v[168:171], v[148:151], v[108:111]\n"
"ds_read_b128 v[160:163], v247 offset:51200\n"
"v_mfma_f32_16x16x32_bf16 v[108:111], v[172:175], v[156:159], v[108:111]\n"
"v_mfma_f32_16x16x32_bf16 v[104:107], v[172:175], v[152:155], v[104:107]\n"
"ds_read_b128 v[164:167], v247 offset:58368\n"
"v_mfma_f32_16x16x32_bf16 v[104:107], v[168:171], v[144:147], v[104:107]\n"
"s_add_u32 s54, s58, 45056\n"
"s_waitcnt vmcnt(16) lgkmcnt(4)\n"
"v_mfma_f32_16x16x32_bf16 v[0:3], v[176:179], v[112:115], v[0:3]\n"
"ds_read_b128 v[168:171], v247 offset:52224\n"
"v_mfma_f32_16x16x32_bf16 v[0:3], v[180:183], v[120:123], v[0:3]\n"
"buffer_load_dwordx4 a[108:111], v228, s[44:47], s54 offen offset:0 sc1\n"
"v_mfma_f32_16x16x32_bf16 v[4:7], v[180:183], v[124:127], v[4:7]\n"
"ds_read_b128 v[172:175], v247 offset:59392\n"
"v_mfma_f32_16x16x32_bf16 v[4:7], v[176:179], v[116:119], v[4:7]\n"
"s_waitcnt lgkmcnt(4)\n"
"v_mfma_f32_16x16x32_bf16 v[12:15], v[184:187], v[116:119], v[12:15]\n"
"ds_read_b128 v[176:179], v247 offset:53248\n"
"v_mfma_f32_16x16x32_bf16 v[12:15], v[188:191], v[124:127], v[12:15]\n"
"buffer_load_dwordx4 a[112:115], v228, s[44:47], s54 offen offset:1024 sc1\n"
"v_mfma_f32_16x16x32_bf16 v[8:11], v[188:191], v[120:123], v[8:11]\n"
"ds_read_b128 v[180:183], v247 offset:60416\n"
"v_mfma_f32_16x16x32_bf16 v[8:11], v[184:187], v[112:115], v[8:11]\n"
"s_waitcnt lgkmcnt(4)\n"
"v_mfma_f32_16x16x32_bf16 v[16:19], v[160:163], v[112:115], v[16:19]\n"
"ds_read_b128 v[184:187], v247 offset:54272\n"
"v_mfma_f32_16x16x32_bf16 v[16:19], v[164:167], v[120:123], v[16:19]\n"
"buffer_load_dwordx4 a[116:119], v228, s[44:47], s54 offen offset:2048 sc1\n"
"v_mfma_f32_16x16x32_bf16 v[20:23], v[164:167], v[124:127], v[20:23]\n"
"ds_read_b128 v[188:191], v247 offset:61440\n"
"v_mfma_f32_16x16x32_bf16 v[20:23], v[160:163], v[116:119], v[20:23]\n"
"s_waitcnt lgkmcnt(4)\n"
"v_mfma_f32_16x16x32_bf16 v[28:31], v[168:171], v[116:119], v[28:31]\n"
"ds_read_b128 v[160:163], v247 offset:55296\n"
"v_mfma_f32_16x16x32_bf16 v[28:31], v[172:175], v[124:127], v[28:31]\n"
"buffer_load_dwordx4 a[120:123], v228, s[44:47], s54 offen offset:3072 sc1\n"
"v_mfma_f32_16x16x32_bf16 v[24:27], v[172:175], v[120:123], v[24:27]\n"
"ds_read_b128 v[164:167], v247 offset:62464\n"
"v_mfma_f32_16x16x32_bf16 v[24:27], v[168:171], v[112:115], v[24:27]\n"
"s_add_u32 s54, s58, 49152\n"
"s_waitcnt lgkmcnt(4)\n"
"v_mfma_f32_16x16x32_bf16 v[32:35], v[176:179], v[112:115], v[32:35]\n"
"ds_read_b128 v[168:171], v247 offset:63488\n"
"v_mfma_f32_16x16x32_bf16 v[32:35], v[180:183], v[120:123], v[32:35]\n"
"buffer_load_dwordx4 a[124:127], v228, s[44:47], s54 offen offset:0 sc1\n"
"v_mfma_f32_16x16x32_bf16 v[36:39], v[180:183], v[124:127], v[36:39]\n"
"ds_read_b128 v[172:175], v248 offset:5120\n"
"v_mfma_f32_16x16x32_bf16 v[36:39], v[176:179], v[116:119], v[36:39]\n"
"s_waitcnt lgkmcnt(4)\n"
"v_mfma_f32_16x16x32_bf16 v[44:47], v[184:187], v[116:119], v[44:47]\n"
"ds_read_b128 v[176:179], v247 offset:64512\n"
"v_mfma_f32_16x16x32_bf16 v[44:47], v[188:191], v[124:127], v[44:47]\n"
"L_w16_%=:\n"
"s_cbranch_execz L_wd5_%=\n"
"buffer_load_dwordx4 a[0:3], v228, s[44:47], s54 offen offset:1024 sc1\n"
"v_mfma_f32_16x16x32_bf16 v[40:43], v[188:191], v[120:123], v[40:43]\n"
"ds_read_b128 v[180:183], v248 offset:6144\n"
"v_mfma_f32_16x16x32_bf16 v[40:43], v[184:187], v[112:115], v[40:43]\n"
"s_waitcnt lgkmcnt(4)\n"
"v_mfma_f32_16x16x32_bf16 v[48:51], v[160:163], v[112:115], v[48:51]\n"
"ds_read_b128 v[184:187], v248 offset:0\n"
"v_mfma_f32_16x16x32_bf16 v[48:51], v[164:167], v[120:123], v[48:51]\n"
"v_mfma_f32_16x16x32_bf16 v[52:55], v[164:167], v[124:127], v[52:55]\n"
"ds_read_b128 v[188:191], v248 offset:7168\n"
"v_mfma_f32_16x16x32_bf16 v[52:55], v[160:163], v[116:119], v[52:55]\n"
"s_waitcnt lgkmcnt(4)\n"
"v_mfma_f32_16x16x32_bf16 v[60:63], v[168:171], v[116:119], v[60:63]\n"
"ds_read_b128 v[160:163], v248 offset:1024\n"
"v_mfma_f32_16x16x32_bf16 v[60:63], v[172:175], v[124:127], v[60:63]\n"
"buffer_load_dwordx4 a[4:7], v228, s[44:47], s54 offen offset:2048 sc1\n"
"v_mfma_f32_16x16x32_bf16 v[56:59], v[172:175], v[120:123], v[56:59]\n"
"ds_read_b128 v[164:167], v248 offset:8192\n"
"v_mfma_f32_16x16x32_bf16 v[56:59], v[168:171], v[112:115], v[56:59]\n"
"s_waitcnt lgkmcnt(4)\n"
"v_mfma_f32_16x16x32_bf16 v[64:67], v[176:179], v[112:115], v[64:67]\n"
"ds_read_b128 v[168:171], v248 offset:2048\n"
"v_mfma_f32_16x16x32_bf16 v[64:67], v[180:183], v[120:123], v[64:67]\n"
"buffer_load_dwordx4 a[8:11], v228, s[44:47], s54 offen offset:3072 sc1\n"
"v_mfma_f32_16x16x32_bf16 v[68:71], v[180:183], v[124:127], v[68:71]\n"
"ds_read_b128 v[172:175], v248 offset:9216\n"
"v_mfma_f32_16x16x32_bf16 v[68:71], v[176:179], v[116:119], v[68:71]\n"
"s_add_u32 s54, s58, 53248\n"
"s_waitcnt lgkmcnt(4)\n"
"v_mfma_f32_16x16x32_bf16 v[76:79], v[184:187], v[116:119], v[76:79]\n"
"ds_read_b128 v[176:179], v248 offset:3072\n"
"v_mfma_f32_16x16x32_bf16 v[76:79], v[188:191], v[124:127], v[76:79]\n"
"buffer_load_dwordx4 a[12:15], v228, s[44:47], s54 offen offset:0 sc1\n"
"v_mfma_f32_16x16x32_bf16 v[72:75], v[188:191], v[120:123], v[72:75]\n"
"ds_read_b128 v[180:183], v248 offset:10240\n"
"v_mfma_f32_16x16x32_bf16 v[72:75], v[184:187], v[112:115], v[72:75]\n"
"s_waitcnt lgkmcnt(4)\n"
"v_mfma_f32_16x16x32_bf16 v[80:83], v[160:163], v[112:115], v[80:83]\n"
"ds_read_b128 v[184:187], v248 offset:4096\n"
"v_mfma_f32_16x16x32_bf16 v[80:83], v[164:167], v[120:123], v[80:83]\n"
"buffer_load_dwordx4 a[16:19], v228, s[44:47], s54 offen offset:1024 sc1\n"
"v_mfma_f32_16x16x32_bf16 v[84:87], v[164:167], v[124:127], v[84:87]\n"
"ds_read_b128 v[188:191], v248 offset:11264\n"
"v_mfma_f32_16x16x32_bf16 v[84:87], v[160:163], v[116:119], v[84:87]\n"
"s_waitcnt lgkmcnt(4)\n"
"v_mfma_f32_16x16x32_bf16 v[92:95], v[168:171], v[116:119], v[92:95]\n"
"ds_read_b128 v[160:163], v248 offset:12288\n"
"v_mfma_f32_16x16x32_bf16 v[92:95], v[172:175], v[124:127], v[92:95]\n"
"buffer_load_dwordx4 a[20:23], v228, s[44:47], s54 offen offset:2048 sc1\n"
"v_mfma_f32_16x16x32_bf16 v[88:91], v[172:175], v[120:123], v[88:91]\n"
"ds_read_b128 v[164:167], v248 offset:13312\n"
"v_mfma_f32_16x16x32_bf16 v[88:91], v[168:171], v[112:115], v[88:91]\n"
"s_waitcnt lgkmcnt(4)\n"
"v_mfma_f32_16x16x32_bf16 v[96:99], v[176:179], v[112:115], v[96:99]\n"
"ds_read_b128 v[168:171], v248 offset:14336\n"
"v_mfma_f32_16x16x32_bf16 v[96:99], v[180:183], v[120:123], v[96:99]\n"
"buffer_load_dwordx4 a[24:27], v228, s[44:47], s54 offen offset:3072 sc1\n"
"v_mfma_f32_16x16x32_bf16 v[100:103], v[180:183], v[124:127], v[100:103]\n"
"ds_read_b128 v[172:175], v248 offset:15360\n"
"v_mfma_f32_16x16x32_bf16 v[100:103], v[176:179], v[116:119], v[100:103]\n"
"s_waitcnt lgkmcnt(4)\n"
"v_mfma_f32_16x16x32_bf16 v[108:111], v[184:187], v[116:119], v[108:111]\n"
"ds_read_b128 v[176:179], v248 offset:16384\n"
"v_mfma_f32_16x16x32_bf16 v[108:111], v[188:191], v[124:127], v[108:111]\n"
"v_mfma_f32_16x16x32_bf16 v[104:107], v[188:191], v[120:123], v[104:107]\n"
"ds_read_b128 v[180:183], v248 offset:17408\n"
"v_mfma_f32_16x16x32_bf16 v[104:107], v[184:187], v[112:115], v[104:107]\n"
"s_add_u32 s54, s58, 57344\n"
"s_waitcnt lgkmcnt(4)\n"
"v_mfma_f32_16x16x32_bf16 v[0:3], v[160:163], v[192:195], v[0:3]\n"
"ds_read_b128 v[184:187], v248 offset:18432\n"
"v_mfma_f32_16x16x32_bf16 v[4:7], v[160:163], v[196:199], v[4:7]\n"
"buffer_load_dwordx4 a[28:31], v228, s[44:47], s54 offen offset:0 sc1\n"
"v_mfma_f32_16x16x32_bf16 v[12:15], v[164:167], v[196:199], v[12:15]\n"
"ds_read_b128 v[188:191], v248 offset:19456\n"
"v_mfma_f32_16x16x32_bf16 v[8:11], v[164:167], v[192:195], v[8:11]\n"
"s_waitcnt lgkmcnt(4)\n"
"v_mfma_f32_16x16x32_bf16 v[16:19], v[168:171], v[192:195], v[16:19]\n"
"ds_read_b128 v[160:163], v248 offset:20480\n"
"v_mfma_f32_16x16x32_bf16 v[20:23], v[168:171], v[196:199], v[20:23]\n"
"buffer_load_dwordx4 a[32:35], v228, s[44:47], s54 offen offset:1024 sc1\n"
"v_mfma_f32_16x16x32_bf16 v[28:31], v[172:175], v[196:199], v[28:31]\n"
"ds_read_b128 v[164:167], v248 offset:21504\n"
"v_mfma_f32_16x16x32_bf16 v[24:27], v[172:175], v[192:195], v[24:27]\n"
"s_waitcnt lgkmcnt(4)\n"
"v_mfma_f32_16x16x32_bf16 v[32:35], v[176:179], v[192:195], v[32:35]\n"
"ds_read_b128 v[168:171], v248 offset:22528\n"
"v_mfma_f32_16x16x32_bf16 v[36:39], v[176:179], v[196:199], v[36:39]\n"
"buffer_load_dwordx4 a[36:39], v228, s[44:47], s54 offen offset:2048 sc1\n"
"v_mfma_f32_16x16x32_bf16 v[44:47], v[180:183], v[196:199], v[44:47]\n"
"ds_read_b128 v[172:175], v248 offset:23552\n"
"v_mfma_f32_16x16x32_bf16 v[40:43], v[180:183], v[192:195], v[40:43]\n"
"s_waitcnt lgkmcnt(4)\n"
"v_mfma_f32_16x16x32_bf16 v[48:51], v[184:187], v[192:195], v[48:51]\n"
"ds_read_b128 v[176:179], v248 offset:24576\n"
"v_mfma_f32_16x16x32_bf16 v[52:55], v[184:187], v[196:199], v[52:55]\n"
"buffer_load_dwordx4 a[40:43], v228, s[44:47], s54 offen offset:3072 sc1\n"
"v_mfma_f32_16x16x32_bf16 v[60:63], v[188:191], v[196:199], v[60:63]\n"
"ds_read_b128 v[180:183], v248 offset:25600\n"
"v_mfma_f32_16x16x32_bf16 v[56:59], v[188:191], v[192:195], v[56:59]\n"
"s_add_u32 s54, s58, 61440\n"
"s_waitcnt lgkmcnt(4)\n"
"v_mfma_f32_16x16x32_bf16 v[64:67], v[160:163], v[192:195], v[64:67]\n"
"v_mfma_f32_16x16x32_bf16 v[68:71], v[160:163], v[196:199], v[68:71]\n"
"buffer_load_dwordx4 a[44:47], v228, s[44:47], s54 offen offset:0 sc1\n"
"v_mfma_f32_16x16x32_bf16 v[76:79], v[164:167], v[196:199], v[76:79]\n"
"v_mfma_f32_16x16x32_bf16 v[72:75], v[164:167], v[192:195], v[72:75]\n"
"s_waitcnt lgkmcnt(2)\n"
"v_mfma_f32_16x16x32_bf16 v[80:83], v[168:171], v[192:195], v[80:83]\n"
"v_mfma_f32_16x16x32_bf16 v[84:87], v[168:171], v[196:199], v[84:87]\n"
"buffer_load_dwordx4 a[48:51], v228, s[44:47], s54 offen offset:1024 sc1\n"
"v_mfma_f32_16x16x32_bf16 v[92:95], v[172:175], v[196:199], v[92:95]\n"
"v_mfma_f32_16x16x32_bf16 v[88:91], v[172:175], v[192:195], v[88:91]\n"
"s_waitcnt lgkmcnt(0)\n"
"v_mfma_f32_16x16x32_bf16 v[96:99], v[176:179], v[192:195], v[96:99]\n"
"v_mfma_f32_16x16x32_bf16 v[100:103], v[176:179], v[196:199], v[100:103]\n"
"v_mfma_f32_16x16x32_bf16 v[108:111], v[180:183], v[196:199], v[108:111]\n"
"v_mfma_f32_16x16x32_bf16 v[104:107], v[180:183], v[192:195], v[104:107]\n"
"s_lshr_b32 s64, s35, 2\n"
"s_mul_i32 s64, s64, 221184\n"
"s_and_b32 s65, s35, 3\n"
"s_lshl_b32 s65, s65, 10\n"
"s_add_u32 s64, s64, s65\n"
"s_add_u32 s64, s64, 50331648\n"
"s_mul_i32 s61, %14, 12288\n"
"s_add_u32 s61, s61, s64\n"
"v_max_f32 v0, 0, v0\n"
"v_max_f32 v1, 0, v1\n"
"v_max_f32 v2, 0, v2\n"
"v_max_f32 v3, 0, v3\n"
"v_pk_mul_f32 v[0:1], v[0:1], v[56:57]\n"
"v_pk_mul_f32 v[2:3], v[2:3], v[58:59]\n"
"v_cvt_pk_bf16_f32 v112, v0, v1\n"
"v_cvt_pk_bf16_f32 v113, v2, v3\n"
"v_max_f32 v8, 0, v8\n"
"v_max_f32 v9, 0, v9\n"
"v_max_f32 v10, 0, v10\n"
"v_max_f32 v11, 0, v11\n"
"v_pk_mul_f32 v[8:9], v[8:9], v[64:65]\n"
"v_pk_mul_f32 v[10:11], v[10:11], v[66:67]\n"
"v_cvt_pk_bf16_f32 v114, v8, v9\n"
"v_cvt_pk_bf16_f32 v115, v10, v11\n"
"buffer_store_dwordx4 v[112:115], v228, s[44:47], s61 offen offset:0 sc1\n"
"v_max_f32 v4, 0, v4\n"
"v_max_f32 v5, 0, v5\n"
"v_max_f32 v6, 0, v6\n"
"v_max_f32 v7, 0, v7\n"
"v_pk_mul_f32 v[4:5], v[4:5], v[60:61]\n"
"v_pk_mul_f32 v[6:7], v[6:7], v[62:63]\n"
"v_cvt_pk_bf16_f32 v116, v4, v5\n"
"v_cvt_pk_bf16_f32 v117, v6, v7\n"
"v_max_f32 v12, 0, v12\n"
"v_max_f32 v13, 0, v13\n"
"v_max_f32 v14, 0, v14\n"
"v_max_f32 v15, 0, v15\n"
"v_pk_mul_f32 v[12:13], v[12:13], v[68:69]\n"
"v_pk_mul_f32 v[14:15], v[14:15], v[70:71]\n"
"v_cvt_pk_bf16_f32 v118, v12, v13\n"
"v_cvt_pk_bf16_f32 v119, v14, v15\n"
"buffer_store_dwordx4 v[116:119], v228, s[44:47], s61 offen offset:1024 sc1\n"
"s_add_u32 s61, s61, 4096\n"
"v_max_f32 v16, 0, v16\n"
"v_max_f32 v17, 0, v17\n"
"v_max_f32 v18, 0, v18\n"
"v_max_f32 v19, 0, v19\n"
"v_pk_mul_f32 v[16:17], v[16:17], v[72:73]\n"
"v_pk_mul_f32 v[18:19], v[18:19], v[74:75]\n"
"v_cvt_pk_bf16_f32 v120, v16, v17\n"
"v_cvt_pk_bf16_f32 v121, v18, v19\n"
"v_max_f32 v24, 0, v24\n"
"v_max_f32 v25, 0, v25\n"
"v_max_f32 v26, 0, v26\n"
"v_max_f32 v27, 0, v27\n"
"v_pk_mul_f32 v[24:25], v[24:25], v[80:81]\n"
"v_pk_mul_f32 v[26:27], v[26:27], v[82:83]\n"
"v_cvt_pk_bf16_f32 v122, v24, v25\n"
"v_cvt_pk_bf16_f32 v123, v26, v27\n"
"buffer_store_dwordx4 v[120:123], v228, s[44:47], s61 offen offset:0 sc1\n"
"v_max_f32 v20, 0, v20\n"
"v_max_f32 v21, 0, v21\n"
"v_max_f32 v22, 0, v22\n"
"v_max_f32 v23, 0, v23\n"
"v_pk_mul_f32 v[20:21], v[20:21], v[76:77]\n"
"v_pk_mul_f32 v[22:23], v[22:23], v[78:79]\n"
"v_cvt_pk_bf16_f32 v124, v20, v21\n"
"v_cvt_pk_bf16_f32 v125, v22, v23\n"
"v_max_f32 v28, 0, v28\n"
"v_max_f32 v29, 0, v29\n"
"v_max_f32 v30, 0, v30\n"
"v_max_f32 v31, 0, v31\n"
"v_pk_mul_f32 v[28:29], v[28:29], v[84:85]\n"
"v_pk_mul_f32 v[30:31], v[30:31], v[86:87]\n"
"v_cvt_pk_bf16_f32 v126, v28, v29\n"
"v_cvt_pk_bf16_f32 v127, v30, v31\n"
"buffer_store_dwordx4 v[124:127], v228, s[44:47], s61 offen offset:1024 sc1\n"
"s_add_u32 s61, s61, 4096\n"
"s_lshr_b32 s65, %14, 1\n"
"s_lshl_b32 s65, s65, 12\n"
"s_add_u32 s64, s64, s65\n"
"s_and_b32 s65, %14, 1\n"
"s_lshl_b32 s65, s65, 9\n"
"s_add_u32 s64, s64, s65\n"
"s_add_u32 s64, s64, 196608\n"
"s_cmp_lt_u32 %14, 12\n"
"s_cselect_b32 s64, s64, 134217728\n"
"v_max_f32 v32, 0, v32\n"
"v_max_f32 v33, 0, v33\n"
"v_max_f32 v34, 0, v34\n"
"v_max_f32 v35, 0, v35\n"
"v_pk_mul_f32 v[32:33], v[32:33], v[88:89]\n"
"v_pk_mul_f32 v[34:35], v[34:35], v[90:91]\n"
"v_cvt_pk_bf16_f32 v128, v32, v33\n"
"v_cvt_pk_bf16_f32 v129, v34, v35\n"
"v_max_f32 v40, 0, v40\n"
"v_max_f32 v41, 0, v41\n"
"v_max_f32 v42, 0, v42\n"
"v_max_f32 v43, 0, v43\n"
"v_pk_mul_f32 v[40:41], v[40:41], v[96:97]\n"
"v_pk_mul_f32 v[42:43], v[42:43], v[98:99]\n"
"v_cvt_pk_bf16_f32 v130, v40, v41\n"
"v_cvt_pk_bf16_f32 v131, v42, v43\n"
"buffer_store_dwordx4 v[128:131], v228, s[44:47], s61 offen offset:0 sc1\n"
"v_max_f32 v36, 0, v36\n"
"v_max_f32 v37, 0, v37\n"
"v_max_f32 v38, 0, v38\n"
"v_max_f32 v39, 0, v39\n"
"v_pk_mul_f32 v[36:37], v[36:37], v[92:93]\n"
"v_pk_mul_f32 v[38:39], v[38:39], v[94:95]\n"
"v_cvt_pk_bf16_f32 v132, v36, v37\n"
"v_cvt_pk_bf16_f32 v133, v38, v39\n"
"v_max_f32 v44, 0, v44\n"
"v_max_f32 v45, 0, v45\n"
"v_max_f32 v46, 0, v46\n"
"v_max_f32 v47, 0, v47\n"
"v_pk_mul_f32 v[44:45], v[44:45], v[100:101]\n"
"v_pk_mul_f32 v[46:47], v[46:47], v[102:103]\n"
"v_cvt_pk_bf16_f32 v134, v44, v45\n"
"v_cvt_pk_bf16_f32 v135, v46, v47\n"
"buffer_store_dwordx4 v[132:135], v228, s[44:47], s61 offen offset:1024 sc1\n"
"v_max_f32 v48, 0, v48\n"
"v_max_f32 v49, 0, v49\n"
"v_max_f32 v50, 0, v50\n"
"v_max_f32 v51, 0, v51\n"
"v_pk_mul_f32 v[48:49], v[48:49], v[104:105]\n"
"v_pk_mul_f32 v[50:51], v[50:51], v[106:107]\n"
"v_cvt_pk_bf16_f32 v136, v48, v49\n"
"v_cvt_pk_bf16_f32 v137, v50, v51\n"
"buffer_store_dwordx2 v[136:137], v230, s[44:47], s64 offen offset:0 sc1\n"
"v_max_f32 v52, 0, v52\n"
"v_max_f32 v53, 0, v53\n"
"v_max_f32 v54, 0, v54\n"
"v_max_f32 v55, 0, v55\n"
"v_pk_mul_f32 v[52:53], v[52:53], v[108:109]\n"
"v_pk_mul_f32 v[54:55], v[54:55], v[110:111]\n"
"v_cvt_pk_bf16_f32 v140, v52, v53\n"
"v_cvt_pk_bf16_f32 v141, v54, v55\n"
"buffer_store_dwordx2 v[140:141], v230, s[44:47], s64 offen offset:1024 sc1\n"
"s_lshl_b32 s64, %13, 11\n"
"s_add_u32 s64, s64, 589824\n"
"v_lshl_add_u32 v16, v228, 3, s64\n"
"v_mov_b32 v17, 1\n"
"ds_read_b128 v[176:179], v252 offset:27648\n"
"ds_read_b128 v[180:183], v252 offset:27712\n"
"ds_read_b128 v[184:187], v252 offset:27776\n"
"ds_read_b128 v[188:191], v252 offset:27840\n"
"ds_read_b128 v[192:195], v252 offset:27904\n"
"s_xor_b32 s64, %17, 1\n"
"s_mul_i32 s64, s64, 15360\n"
"s_add_u32 s64, s64, 86016\n"
"v_add_u32 v246, s64, v228\n"
"s_xor_b32 s64, %17, 2\n"
"s_mul_i32 s64, s64, 15360\n"
"s_add_u32 s64, s64, 91136\n"
"v_add_u32 v247, s64, v228\n"
"s_xor_b32 s64, %17, 3\n"
"s_mul_i32 s64, s64, 15360\n"
"s_add_u32 s64, s64, 96256\n"
"v_add_u32 v248, s64, v228\n"
"s_mul_i32 s64, %17, 15360\n"
"s_add_u32 s64, s64, 86016\n"
"v_add_u32 v251, s64, v228\n"
"s_mul_i32 s61, %11, 221184\n"
"s_add_u32 s61, s61, s60\n"
"s_add_u32 s61, s61, 50331648\n"
"s_lshl_b32 s64, %11, 3\n"
"s_add_u32 s64, s64, %12\n"
"s_lshl_b32 s64, s64, 7\n"
"s_add_u32 s64, s64, 589824\n"
"v_mov_b32 v2, s64\n"
"s_lshl_b32 s35, %11, 2\n"
"s_add_u32 s35, s35, %17\n"
"s_lshl_b32 s64, s35, 10\n"
"s_add_u32 s64, s64, 65536\n"
"s_mul_i32 s35, s35, 20480\n"
"s_mul_i32 s67, %12, 160\n"
"s_add_u32 s35, s35, s67\n"
"s_add_u32 s67, s61, 16384\n"
"s_cmp_lg_u32 s60, 0\n"
"s_waitcnt vmcnt(0)\n"
"s_barrier\n"
"s_cbranch_scc1 L_hs_%=\n"
"s_mov_b64 exec, 0xffff\n"
"global_atomic_add v16, v17, %0\n"
"s_mov_b64 exec, -1\n"
"L_hs_%=:\n"
"buffer_load_dwordx4 a[52:55], v228, s[44:47], s54 offen offset:2048 sc1\n"
"buffer_load_dwordx4 a[56:59], v228, s[44:47], s54 offen offset:3072 sc1\n"
"s_add_u32 s54, s58, 65536\n"
"buffer_load_dwordx4 v[204:207], v228, s[44:47], s54 offen offset:0 sc1\n"
"s_add_u32 s54, s58, 66560\n"
"s_cmp_lt_u32 %17, 2\n"
"s_cselect_b32 s54, s54, 134217728\n"
"buffer_load_dwordx4 v[208:211], v228, s[44:47], s54 offen offset:0 sc1\n"
"buffer_load_dwordx4 v[212:215], v228, s[44:47], s54 offen offset:1024 sc1\n"
"buffer_load_dwordx4 v[216:219], v228, s[44:47], s54 offen offset:2048 sc1\n"
"buffer_load_dwordx4 v[220:223], v228, s[44:47], s54 offen offset:3072 sc1\n"
"s_add_u32 s54, s54, 4096\n"
"buffer_load_dwordx4 v[224:227], v228, s[44:47], s54 offen offset:0 sc1\n"
"s_cmp_lg_u32 s60, 0\n"
"s_cbranch_scc1 L_hb_%=\n"
"v_mov_b32 v4, 0\n"
"s_mov_b64 exec, 1\n"
"s_mov_b32 s65, 200000\n"
"L_pl7_%=:\n"
"global_atomic_add v3, v2, v4, %0 sc0\n"
"s_waitcnt vmcnt(0)\n"
"v_cmp_gt_u32 vcc, 16, v3\n"
"s_cbranch_vccz L_pd7_%=\n"
"s_sleep 1\n"
"s_sub_u32 s65, s65, 1\n"
"s_cbranch_scc0 L_pl7_%=\n"
"L_pd7_%=:\n"
"s_mov_b64 exec, -1\n"
"s_branch L_hb2_%=\n"
"L_hb_%=:\n"
"s_waitcnt vmcnt(0)\n"
"L_hb2_%=:\n"
"s_barrier\n"
"buffer_load_dwordx4 v[80:83], v232, s[44:47], s61 offen offset:0 sc1\n"
"buffer_load_dwordx4 v[96:99], v232, s[44:47], s67 offen offset:0 sc1\n"
"buffer_load_dwordx4 v[84:87], v233, s[44:47], s61 offen offset:0 sc1\n"
"buffer_load_dwordx4 v[100:103], v233, s[44:47], s67 offen offset:0 sc1\n"
"buffer_load_dwordx4 v[88:91], v234, s[44:47], s61 offen offset:0 sc1\n"
"buffer_load_dwordx4 v[104:107], v234, s[44:47], s67 offen offset:0 sc1\n"
"buffer_load_dwordx4 v[92:95], v235, s[44:47], s61 offen offset:0 sc1\n"
"buffer_load_dwordx4 v[108:111], v235, s[44:47], s67 offen offset:0 sc1\n"
"s_add_u32 s61, s61, 32768\n"
"s_add_u32 s67, s61, 16384\n"
"buffer_load_dwordx4 v[112:115], v232, s[44:47], s61 offen offset:0 sc1\n"
"buffer_load_dwordx4 v[128:131], v232, s[44:47], s67 offen offset:0 sc1\n"
"buffer_load_dwordx2 v[196:197], v231, s[44:47], s64 offen offset:0 sc1\n"
"buffer_load_dwordx2 v[198:199], v231, s[44:47], s64 offen offset:128 sc1\n"
"s_waitcnt vmcnt(10) lgkmcnt(0)\n"
"v_mfma_f32_16x16x32_bf16 v[0:3], a[212:215], v[80:83], 0\n"
"buffer_load_dwordx4 v[116:119], v233, s[44:47], s61 offen offset:0 sc1\n"
"v_mfma_f32_16x16x32_bf16 v[0:3], a[232:235], v[96:99], v[0:3]\n"
"v_mfma_f32_16x16x32_bf16 v[4:7], a[236:239], v[96:99], 0\n"
"v_mfma_f32_16x16x32_bf16 v[4:7], a[216:219], v[80:83], v[4:7]\n"
"v_mfma_f32_16x16x32_bf16 v[8:11], a[220:223], v[80:83], 0\n"
"v_mfma_f32_16x16x32_bf16 v[8:11], a[240:243], v[96:99], v[8:11]\n"
"buffer_load_dwordx4 v[132:135], v233, s[44:47], s67 offen offset:0 sc1\n"
"v_mfma_f32_16x16x32_bf16 v[12:15], a[128:131], v[96:99], 0\n"
"v_mfma_f32_16x16x32_bf16 v[12:15], a[224:227], v[80:83], v[12:15]\n"
"v_mfma_f32_16x16x32_bf16 v[16:19], a[228:231], v[80:83], 0\n"
"v_mfma_f32_16x16x32_bf16 v[16:19], a[132:135], v[96:99], v[16:19]\n"
"s_waitcnt vmcnt(10)\n"
"v_mfma_f32_16x16x32_bf16 v[36:39], a[132:135], v[100:103], 0\n"
"buffer_load_dwordx4 v[120:123], v234, s[44:47], s61 offen offset:0 sc1\n"
"v_mfma_f32_16x16x32_bf16 v[36:39], a[228:231], v[84:87], v[36:39]\n"
"v_mfma_f32_16x16x32_bf16 v[32:35], a[224:227], v[84:87], 0\n"
"v_mfma_f32_16x16x32_bf16 v[32:35], a[128:131], v[100:103], v[32:35]\n"
"v_mfma_f32_16x16x32_bf16 v[28:31], a[240:243], v[100:103], 0\n"
"v_mfma_f32_16x16x32_bf16 v[28:31], a[220:223], v[84:87], v[28:31]\n"
"buffer_load_dwordx4 v[136:139], v234, s[44:47], s67 offen offset:0 sc1\n"
"v_mfma_f32_16x16x32_bf16 v[24:27], a[216:219], v[84:87], 0\n"
"v_mfma_f32_16x16x32_bf16 v[24:27], a[236:239], v[100:103], v[24:27]\n"
"v_mfma_f32_16x16x32_bf16 v[20:23], a[232:235], v[100:103], 0\n"
"v_mfma_f32_16x16x32_bf16 v[20:23], a[212:215], v[84:87], v[20:23]\n"
"s_waitcnt vmcnt(10)\n"
"v_mfma_f32_16x16x32_bf16 v[40:43], a[212:215], v[88:91], 0\n"
"buffer_load_dwordx4 v[124:127], v235, s[44:47], s61 offen offset:0 sc1\n"
"v_mfma_f32_16x16x32_bf16 v[40:43], a[232:235], v[104:107], v[40:43]\n"
"v_mfma_f32_16x16x32_bf16 v[44:47], a[236:239], v[104:107], 0\n"
"v_mfma_f32_16x16x32_bf16 v[44:47], a[216:219], v[88:91], v[44:47]\n"
"v_mfma_f32_16x16x32_bf16 v[48:51], a[220:223], v[88:91], 0\n"
"v_mfma_f32_16x16x32_bf16 v[48:51], a[240:243], v[104:107], v[48:51]\n"
"buffer_load_dwordx4 v[140:143], v235, s[44:47], s67 offen offset:0 sc1\n"
"v_mfma_f32_16x16x32_bf16 v[52:55], a[128:131], v[104:107], 0\n"
"s_add_u32 s61, s61, 32768\n"
"s_add_u32 s67, s61, 16384\n"
"v_mfma_f32_16x16x32_bf16 v[52:55], a[224:227], v[88:91], v[52:55]\n"
"v_mfma_f32_16x16x32_bf16 v[56:59], a[228:231], v[88:91], 0\n"
"v_mfma_f32_16x16x32_bf16 v[56:59], a[132:135], v[104:107], v[56:59]\n"
"s_waitcnt vmcnt(10)\n"
"v_mfma_f32_16x16x32_bf16 v[76:79], a[132:135], v[108:111], 0\n"
"buffer_load_dwordx4 v[144:147], v232, s[44:47], s61 offen offset:0 sc1\n"
"v_mfma_f32_16x16x32_bf16 v[76:79], a[228:231], v[92:95], v[76:79]\n"
"v_mfma_f32_16x16x32_bf16 v[72:75], a[224:227], v[92:95], 0\n"
"v_mfma_f32_16x16x32_bf16 v[72:75], a[128:131], v[108:111], v[72:75]\n"
"v_mfma_f32_16x16x32_bf16 v[68:71], a[240:243], v[108:111], 0\n"
"v_mfma_f32_16x16x32_bf16 v[68:71], a[220:223], v[92:95], v[68:71]\n"
"buffer_load_dwordx4 v[160:163], v232, s[44:47], s67 offen offset:0 sc1\n"
"v_mfma_f32_16x16x32_bf16 v[64:67], a[216:219], v[92:95], 0\n"
"v_mfma_f32_16x16x32_bf16 v[64:67], a[236:239], v[108:111], v[64:67]\n"
"v_mfma_f32_16x16x32_bf16 v[60:63], a[232:235], v[108:111], 0\n"
"v_mfma_f32_16x16x32_bf16 v[60:63], a[212:215], v[92:95], v[60:63]\n"
"s_waitcnt vmcnt(10)\n"
"v_mfma_f32_16x16x32_bf16 v[0:3], a[136:139], v[112:115], v[0:3]\n"
"buffer_load_dwordx4 v[148:151], v233, s[44:47], s61 offen offset:0 sc1\n"
"v_mfma_f32_16x16x32_bf16 v[0:3], a[156:159], v[128:131], v[0:3]\n"
"v_mfma_f32_16x16x32_bf16 v[4:7], a[160:163], v[128:131], v[4:7]\n"
"v_mfma_f32_16x16x32_bf16 v[4:7], a[140:143], v[112:115], v[4:7]\n"
"v_mfma_f32_16x16x32_bf16 v[8:11], a[144:147], v[112:115], v[8:11]\n"
"v_mfma_f32_16x16x32_bf16 v[8:11], a[164:167], v[128:131], v[8:11]\n"
"buffer_load_dwordx4 v[164:167], v233, s[44:47], s67 offen offset:0 sc1\n"
"v_mfma_f32_16x16x32_bf16 v[12:15], a[168:171], v[128:131], v[12:15]\n"
"v_mfma_f32_16x16x32_bf16 v[12:15], a[148:151], v[112:115], v[12:15]\n"
"v_mfma_f32_16x16x32_bf16 v[16:19], a[152:155], v[112:115], v[16:19]\n"
"v_mfma_f32_16x16x32_bf16 v[16:19], a[172:175], v[128:131], v[16:19]\n"
"s_waitcnt vmcnt(8)\n"
"v_mfma_f32_16x16x32_bf16 v[36:39], a[172:175], v[132:135], v[36:39]\n"
"buffer_load_dwordx4 v[152:155], v234, s[44:47], s61 offen offset:0 sc1\n"
"v_mfma_f32_16x16x32_bf16 v[36:39], a[152:155], v[116:119], v[36:39]\n"
"v_mfma_f32_16x16x32_bf16 v[32:35], a[148:151], v[116:119], v[32:35]\n"
"v_mfma_f32_16x16x32_bf16 v[32:35], a[168:171], v[132:135], v[32:35]\n"
"v_mfma_f32_16x16x32_bf16 v[28:31], a[164:167], v[132:135], v[28:31]\n"
"v_mfma_f32_16x16x32_bf16 v[28:31], a[144:147], v[116:119], v[28:31]\n"
"buffer_load_dwordx4 v[168:171], v234, s[44:47], s67 offen offset:0 sc1\n"
"v_mfma_f32_16x16x32_bf16 v[24:27], a[140:143], v[116:119], v[24:27]\n"
"v_mfma_f32_16x16x32_bf16 v[24:27], a[160:163], v[132:135], v[24:27]\n"
"v_mfma_f32_16x16x32_bf16 v[20:23], a[156:159], v[132:135], v[20:23]\n"
"v_mfma_f32_16x16x32_bf16 v[20:23], a[136:139], v[116:119], v[20:23]\n"
"s_waitcnt vmcnt(8)\n"
"v_mfma_f32_16x16x32_bf16 v[40:43], a[136:139], v[120:123], v[40:43]\n"
"buffer_load_dwordx4 v[156:159], v235, s[44:47], s61 offen offset:0 sc1\n"
"v_mfma_f32_16x16x32_bf16 v[40:43], a[156:159], v[136:139], v[40:43]\n"
"v_mfma_f32_16x16x32_bf16 v[44:47], a[160:163], v[136:139], v[44:47]\n"
"v_mfma_f32_16x16x32_bf16 v[44:47], a[140:143], v[120:123], v[44:47]\n"
"v_mfma_f32_16x16x32_bf16 v[48:51], a[144:147], v[120:123], v[48:51]\n"
"v_mfma_f32_16x16x32_bf16 v[48:51], a[164:167], v[136:139], v[48:51]\n"
"buffer_load_dwordx4 v[172:175], v235, s[44:47], s67 offen offset:0 sc1\n"
"v_mfma_f32_16x16x32_bf16 v[52:55], a[168:171], v[136:139], v[52:55]\n"
"s_add_u32 s61, s61, 32768\n"
"s_add_u32 s67, s61, 16384\n"
"v_mfma_f32_16x16x32_bf16 v[52:55], a[148:151], v[120:123], v[52:55]\n"
"v_mfma_f32_16x16x32_bf16 v[56:59], a[152:155], v[120:123], v[56:59]\n"
"v_mfma_f32_16x16x32_bf16 v[56:59], a[172:175], v[136:139], v[56:59]\n"
"s_waitcnt vmcnt(8)\n"
"v_mfma_f32_16x16x32_bf16 v[76:79], a[172:175], v[140:143], v[76:79]\n"
"buffer_load_dwordx4 v[80:83], v232, s[44:47], s61 offen offset:0 sc1\n"
"v_mfma_f32_16x16x32_bf16 v[76:79], a[152:155], v[124:127], v[76:79]\n"
"v_mfma_f32_16x16x32_bf16 v[72:75], a[148:151], v[124:127], v[72:75]\n"
"v_mfma_f32_16x16x32_bf16 v[72:75], a[168:171], v[140:143], v[72:75]\n"
"v_mfma_f32_16x16x32_bf16 v[68:71], a[164:167], v[140:143], v[68:71]\n"
"v_mfma_f32_16x16x32_bf16 v[68:71], a[144:147], v[124:127], v[68:71]\n"
"buffer_load_dwordx4 v[96:99], v232, s[44:47], s67 offen offset:0 sc1\n"
"v_mfma_f32_16x16x32_bf16 v[64:67], a[140:143], v[124:127], v[64:67]\n"
"v_mfma_f32_16x16x32_bf16 v[64:67], a[160:163], v[140:143], v[64:67]\n"
"v_mfma_f32_16x16x32_bf16 v[60:63], a[156:159], v[140:143], v[60:63]\n"
"v_mfma_f32_16x16x32_bf16 v[60:63], a[136:139], v[124:127], v[60:63]\n"
"s_waitcnt vmcnt(8)\n"
"v_mfma_f32_16x16x32_bf16 v[0:3], a[176:179], v[144:147], v[0:3]\n"
"buffer_load_dwordx4 v[84:87], v233, s[44:47], s61 offen offset:0 sc1\n"
"v_mfma_f32_16x16x32_bf16 v[0:3], a[196:199], v[160:163], v[0:3]\n"
"v_add_f32 v200, v196, v198\n"
"v_mfma_f32_16x16x32_bf16 v[4:7], a[200:203], v[160:163], v[4:7]\n"
"v_mov_b32 v201, v200\n"
"v_mfma_f32_16x16x32_bf16 v[4:7], a[180:183], v[144:147], v[4:7]\n"
"s_nop 1\n"
"v_mfma_f32_16x16x32_bf16 v[8:11], a[184:187], v[144:147], v[8:11]\n"
"v_permlane16_swap_b32 v200, v201\n"
"v_mfma_f32_16x16x32_bf16 v[8:11], a[204:207], v[160:163], v[8:11]\n"
"buffer_load_dwordx4 v[100:103], v233, s[44:47], s67 offen offset:0 sc1\n"
"v_mfma_f32_16x16x32_bf16 v[12:15], a[208:211], v[160:163], v[12:15]\n"
"v_add_f32 v200, v200, v201\n"
"v_mfma_f32_16x16x32_bf16 v[12:15], a[188:191], v[144:147], v[12:15]\n"
"v_mov_b32 v201, v200\n"
"v_mfma_f32_16x16x32_bf16 v[16:19], a[192:195], v[144:147], v[16:19]\n"
"s_nop 1\n"
"v_mfma_f32_16x16x32_bf16 v[16:19], a[244:247], v[160:163], v[16:19]\n"
"v_permlane32_swap_b32 v200, v201\n"
"s_waitcnt vmcnt(8)\n"
"v_mfma_f32_16x16x32_bf16 v[36:39], a[244:247], v[164:167], v[36:39]\n"
"buffer_load_dwordx4 v[88:91], v234, s[44:47], s61 offen offset:0 sc1\n"
"v_mfma_f32_16x16x32_bf16 v[36:39], a[192:195], v[148:151], v[36:39]\n"
"v_add_f32 v200, v200, v201\n"
"v_mfma_f32_16x16x32_bf16 v[32:35], a[188:191], v[148:151], v[32:35]\n"
"v_mul_f32 v200, 0x3e000000, v200\n"
"v_mfma_f32_16x16x32_bf16 v[32:35], a[208:211], v[164:167], v[32:35]\n"
"v_sub_f32 v196, v196, v200\n"
"v_mfma_f32_16x16x32_bf16 v[28:31], a[204:207], v[164:167], v[28:31]\n"
"v_sub_f32 v198, v198, v200\n"
"v_mfma_f32_16x16x32_bf16 v[28:31], a[184:187], v[148:151], v[28:31]\n"
"buffer_load_dwordx4 v[104:107], v234, s[44:47], s67 offen offset:0 sc1\n"
"v_mfma_f32_16x16x32_bf16 v[24:27], a[180:183], v[148:151], v[24:27]\n"
"v_add_f32 v197, v197, v199\n"
"v_mfma_f32_16x16x32_bf16 v[24:27], a[200:203], v[164:167], v[24:27]\n"
"v_mul_f32 v196, v196, v196\n"
"v_mfma_f32_16x16x32_bf16 v[20:23], a[196:199], v[164:167], v[20:23]\n"
"v_fmac_f32 v196, v198, v198\n"
"v_mfma_f32_16x16x32_bf16 v[20:23], a[176:179], v[148:151], v[20:23]\n"
"v_fmac_f32 v197, 0x42a00000, v196\n"
"s_waitcnt vmcnt(8)\n"
"v_mfma_f32_16x16x32_bf16 v[40:43], a[176:179], v[152:155], v[40:43]\n"
"buffer_load_dwordx4 v[92:95], v235, s[44:47], s61 offen offset:0 sc1\n"
"v_mfma_f32_16x16x32_bf16 v[40:43], a[196:199], v[168:171], v[40:43]\n"
"v_mov_b32 v201, v197\n"
"v_mfma_f32_16x16x32_bf16 v[44:47], a[200:203], v[168:171], v[44:47]\n"
"s_nop 1\n"
"v_mfma_f32_16x16x32_bf16 v[44:47], a[180:183], v[152:155], v[44:47]\n"
"v_permlane16_swap_b32 v197, v201\n"
"v_mfma_f32_16x16x32_bf16 v[48:51], a[184:187], v[152:155], v[48:51]\n"
"v_add_f32 v197, v197, v201\n"
"v_mfma_f32_16x16x32_bf16 v[48:51], a[204:207], v[168:171], v[48:51]\n"
"buffer_load_dwordx4 v[108:111], v235, s[44:47], s67 offen offset:0 sc1\n"
"v_mfma_f32_16x16x32_bf16 v[52:55], a[208:211], v[168:171], v[52:55]\n"
"s_add_u32 s61, s61, 32768\n"
"s_add_u32 s67, s61, 16384\n"
"v_mfma_f32_16x16x32_bf16 v[52:55], a[188:191], v[152:155], v[52:55]\n"
"v_mov_b32 v201, v197\n"
"v_mfma_f32_16x16x32_bf16 v[56:59], a[192:195], v[152:155], v[56:59]\n"
"s_nop 1\n"
"v_mfma_f32_16x16x32_bf16 v[56:59], a[244:247], v[168:171], v[56:59]\n"
"v_permlane32_swap_b32 v197, v201\n"
"s_waitcnt vmcnt(8)\n"
"v_mfma_f32_16x16x32_bf16 v[76:79], a[244:247], v[172:175], v[76:79]\n"
"buffer_load_dwordx4 v[112:115], v232, s[44:47], s61 offen offset:0 sc1\n"
"v_mfma_f32_16x16x32_bf16 v[76:79], a[192:195], v[156:159], v[76:79]\n"
"v_add_f32 v197, v197, v201\n"
"v_mfma_f32_16x16x32_bf16 v[72:75], a[188:191], v[156:159], v[72:75]\n"
"v_mov_b32 v196, v200\n"
"v_mfma_f32_16x16x32_bf16 v[72:75], a[208:211], v[172:175], v[72:75]\n"
"v_mov_b32 v201, 0x3727c5ac\n"
"v_mfma_f32_16x16x32_bf16 v[68:71], a[204:207], v[172:175], v[68:71]\n"
"v_fmac_f32 v201, 0x3acccccd, v197\n"
"v_mfma_f32_16x16x32_bf16 v[68:71], a[184:187], v[156:159], v[68:71]\n"
"buffer_load_dwordx4 v[128:131], v232, s[44:47], s67 offen offset:0 sc1\n"
"v_mfma_f32_16x16x32_bf16 v[64:67], a[180:183], v[156:159], v[64:67]\n"
"v_mov_b32 v197, v201\n"
"v_mfma_f32_16x16x32_bf16 v[64:67], a[200:203], v[172:175], v[64:67]\n"
"v_rcp_f32 v197, v197\n"
"v_mfma_f32_16x16x32_bf16 v[60:63], a[196:199], v[172:175], v[60:63]\n"
"s_nop 0\n"
"v_mfma_f32_16x16x32_bf16 v[60:63], a[176:179], v[156:159], v[60:63]\n"
"v_mul_f32 v176, v197, v176\n"
"s_waitcnt vmcnt(8)\n"
"v_mfma_f32_16x16x32_bf16 v[0:3], a[248:251], v[80:83], v[0:3]\n"
"buffer_load_dwordx4 v[116:119], v233, s[44:47], s61 offen offset:0 sc1\n"
"v_mfma_f32_16x16x32_bf16 v[0:3], a[72:75], v[96:99], v[0:3]\n"
"v_mul_f32 v177, v197, v177\n"
"v_mfma_f32_16x16x32_bf16 v[4:7], a[76:79], v[96:99], v[4:7]\n"
"v_mul_f32 v178, v197, v178\n"
"v_mfma_f32_16x16x32_bf16 v[4:7], a[252:255], v[80:83], v[4:7]\n"
"v_mul_f32 v179, v197, v179\n"
"v_mfma_f32_16x16x32_bf16 v[8:11], a[60:63], v[80:83], v[8:11]\n"
"v_mul_f32 v180, v197, v180\n"
"v_mfma_f32_16x16x32_bf16 v[8:11], a[80:83], v[96:99], v[8:11]\n"
"buffer_load_dwordx4 v[132:135], v233, s[44:47], s67 offen offset:0 sc1\n"
"v_mfma_f32_16x16x32_bf16 v[12:15], a[84:87], v[96:99], v[12:15]\n"
"v_mul_f32 v181, v197, v181\n"
"v_mfma_f32_16x16x32_bf16 v[12:15], a[64:67], v[80:83], v[12:15]\n"
"v_mul_f32 v182, v197, v182\n"
"v_mfma_f32_16x16x32_bf16 v[16:19], a[68:71], v[80:83], v[16:19]\n"
"v_mul_f32 v183, v197, v183\n"
"v_mfma_f32_16x16x32_bf16 v[16:19], a[88:91], v[96:99], v[16:19]\n"
"v_mul_f32 v184, v197, v184\n"
"s_waitcnt vmcnt(8)\n"
"v_mfma_f32_16x16x32_bf16 v[36:39], a[88:91], v[100:103], v[36:39]\n"
"buffer_load_dwordx4 v[120:123], v234, s[44:47], s61 offen offset:0 sc1\n"
"v_mfma_f32_16x16x32_bf16 v[36:39], a[68:71], v[84:87], v[36:39]\n"
"v_mul_f32 v185, v197, v185\n"
"v_mfma_f32_16x16x32_bf16 v[32:35], a[64:67], v[84:87], v[32:35]\n"
"v_mul_f32 v186, v197, v186\n"
"v_mfma_f32_16x16x32_bf16 v[32:35], a[84:87], v[100:103], v[32:35]\n"
"v_mul_f32 v187, v197, v187\n"
"v_mfma_f32_16x16x32_bf16 v[28:31], a[80:83], v[100:103], v[28:31]\n"
"v_mul_f32 v188, v197, v188\n"
"v_mfma_f32_16x16x32_bf16 v[28:31], a[60:63], v[84:87], v[28:31]\n"
"buffer_load_dwordx4 v[136:139], v234, s[44:47], s67 offen offset:0 sc1\n"
"v_mfma_f32_16x16x32_bf16 v[24:27], a[252:255], v[84:87], v[24:27]\n"
"v_mul_f32 v189, v197, v189\n"
"v_mfma_f32_16x16x32_bf16 v[24:27], a[76:79], v[100:103], v[24:27]\n"
"v_mul_f32 v190, v197, v190\n"
"v_mfma_f32_16x16x32_bf16 v[20:23], a[72:75], v[100:103], v[20:23]\n"
"v_mul_f32 v191, v197, v191\n"
"v_mfma_f32_16x16x32_bf16 v[20:23], a[248:251], v[84:87], v[20:23]\n"
"v_mul_f32 v192, v197, v192\n"
"s_waitcnt vmcnt(8)\n"
"v_mfma_f32_16x16x32_bf16 v[40:43], a[248:251], v[88:91], v[40:43]\n"
"buffer_load_dwordx4 v[124:127], v235, s[44:47], s61 offen offset:0 sc1\n"
"v_mfma_f32_16x16x32_bf16 v[40:43], a[72:75], v[104:107], v[40:43]\n"
"v_mul_f32 v193, v197, v193\n"
"v_mfma_f32_16x16x32_bf16 v[44:47], a[76:79], v[104:107], v[44:47]\n"
"v_mul_f32 v194, v197, v194\n"
"v_mfma_f32_16x16x32_bf16 v[44:47], a[252:255], v[88:91], v[44:47]\n"
"v_mul_f32 v195, v197, v195\n"
"v_mfma_f32_16x16x32_bf16 v[48:51], a[60:63], v[88:91], v[48:51]\n"
"v_mfma_f32_16x16x32_bf16 v[48:51], a[80:83], v[104:107], v[48:51]\n"
"buffer_load_dwordx4 v[140:143], v235, s[44:47], s67 offen offset:0 sc1\n"
"v_mfma_f32_16x16x32_bf16 v[52:55], a[84:87], v[104:107], v[52:55]\n"
"s_add_u32 s61, s61, 32768\n"
"s_add_u32 s67, s61, 16384\n"
"v_mfma_f32_16x16x32_bf16 v[52:55], a[64:67], v[88:91], v[52:55]\n"
"v_mfma_f32_16x16x32_bf16 v[56:59], a[68:71], v[88:91], v[56:59]\n"
"v_mfma_f32_16x16x32_bf16 v[56:59], a[88:91], v[104:107], v[56:59]\n"
"s_waitcnt vmcnt(8)\n"
"v_mfma_f32_16x16x32_bf16 v[76:79], a[88:91], v[108:111], v[76:79]\n"
"buffer_load_dwordx4 v[144:147], v232, s[44:47], s61 offen offset:0 sc1\n"
"v_mfma_f32_16x16x32_bf16 v[76:79], a[68:71], v[92:95], v[76:79]\n"
"L_w17_%=:\n"
"s_cbranch_execz L_wd5_%=\n"
"v_mfma_f32_16x16x32_bf16 v[72:75], a[64:67], v[92:95], v[72:75]\n"
"v_mfma_f32_16x16x32_bf16 v[72:75], a[84:87], v[108:111], v[72:75]\n"
"v_mfma_f32_16x16x32_bf16 v[68:71], a[80:83], v[108:111], v[68:71]\n"
"v_mfma_f32_16x16x32_bf16 v[68:71], a[60:63], v[92:95], v[68:71]\n"
"buffer_load_dwordx4 v[160:163], v232, s[44:47], s67 offen offset:0 sc1\n"
"v_mfma_f32_16x16x32_bf16 v[64:67], a[252:255], v[92:95], v[64:67]\n"
"v_mfma_f32_16x16x32_bf16 v[64:67], a[76:79], v[108:111], v[64:67]\n"
"v_mfma_f32_16x16x32_bf16 v[60:63], a[72:75], v[108:111], v[60:63]\n"
"v_mfma_f32_16x16x32_bf16 v[60:63], a[248:251], v[92:95], v[60:63]\n"
"s_waitcnt vmcnt(8)\n"
"v_mfma_f32_16x16x32_bf16 v[0:3], a[92:95], v[112:115], v[0:3]\n"
"buffer_load_dwordx4 v[148:151], v233, s[44:47], s61 offen offset:0 sc1\n"
"v_mfma_f32_16x16x32_bf16 v[0:3], a[112:115], v[128:131], v[0:3]\n"
"v_mfma_f32_16x16x32_bf16 v[4:7], a[116:119], v[128:131], v[4:7]\n"
"v_mfma_f32_16x16x32_bf16 v[4:7], a[96:99], v[112:115], v[4:7]\n"
"v_mfma_f32_16x16x32_bf16 v[8:11], a[100:103], v[112:115], v[8:11]\n"
"v_mfma_f32_16x16x32_bf16 v[8:11], a[120:123], v[128:131], v[8:11]\n"
"buffer_load_dwordx4 v[164:167], v233, s[44:47], s67 offen offset:0 sc1\n"
"v_mfma_f32_16x16x32_bf16 v[12:15], a[124:127], v[128:131], v[12:15]\n"
"v_mfma_f32_16x16x32_bf16 v[12:15], a[104:107], v[112:115], v[12:15]\n"
"v_mfma_f32_16x16x32_bf16 v[16:19], a[108:111], v[112:115], v[16:19]\n"
"v_mfma_f32_16x16x32_bf16 v[16:19], a[0:3], v[128:131], v[16:19]\n"
"s_waitcnt vmcnt(8)\n"
"v_mfma_f32_16x16x32_bf16 v[36:39], a[0:3], v[132:135], v[36:39]\n"
"buffer_load_dwordx4 v[152:155], v234, s[44:47], s61 offen offset:0 sc1\n"
"v_mfma_f32_16x16x32_bf16 v[36:39], a[108:111], v[116:119], v[36:39]\n"
"v_mfma_f32_16x16x32_bf16 v[32:35], a[104:107], v[116:119], v[32:35]\n"
"v_mfma_f32_16x16x32_bf16 v[32:35], a[124:127], v[132:135], v[32:35]\n"
"v_mfma_f32_16x16x32_bf16 v[28:31], a[120:123], v[132:135], v[28:31]\n"
"v_mfma_f32_16x16x32_bf16 v[28:31], a[100:103], v[116:119], v[28:31]\n"
"buffer_load_dwordx4 v[168:171], v234, s[44:47], s67 offen offset:0 sc1\n"
"v_mfma_f32_16x16x32_bf16 v[24:27], a[96:99], v[116:119], v[24:27]\n"
"v_mfma_f32_16x16x32_bf16 v[24:27], a[116:119], v[132:135], v[24:27]\n"
"v_mfma_f32_16x16x32_bf16 v[20:23], a[112:115], v[132:135], v[20:23]\n"
"v_mfma_f32_16x16x32_bf16 v[20:23], a[92:95], v[116:119], v[20:23]\n"
"s_waitcnt vmcnt(8)\n"
"v_mfma_f32_16x16x32_bf16 v[40:43], a[92:95], v[120:123], v[40:43]\n"
"buffer_load_dwordx4 v[156:159], v235, s[44:47], s61 offen offset:0 sc1\n"
"v_mfma_f32_16x16x32_bf16 v[40:43], a[112:115], v[136:139], v[40:43]\n"
"v_mfma_f32_16x16x32_bf16 v[44:47], a[116:119], v[136:139], v[44:47]\n"
"v_mfma_f32_16x16x32_bf16 v[44:47], a[96:99], v[120:123], v[44:47]\n"
"v_mfma_f32_16x16x32_bf16 v[48:51], a[100:103], v[120:123], v[48:51]\n"
"v_mfma_f32_16x16x32_bf16 v[48:51], a[120:123], v[136:139], v[48:51]\n"
"buffer_load_dwordx4 v[172:175], v235, s[44:47], s67 offen offset:0 sc1\n"
"v_mfma_f32_16x16x32_bf16 v[52:55], a[124:127], v[136:139], v[52:55]\n"
"s_add_u32 s61, s61, 32768\n"
"s_add_u32 s67, s61, 16384\n"
"v_mfma_f32_16x16x32_bf16 v[52:55], a[104:107], v[120:123], v[52:55]\n"
"s_cmp_lt_u32 %17, 2\n"
"s_cselect_b32 s67, s67, 134217728\n"
"v_mfma_f32_16x16x32_bf16 v[56:59], a[108:111], v[120:123], v[56:59]\n"
"v_mfma_f32_16x16x32_bf16 v[56:59], a[0:3], v[136:139], v[56:59]\n"
"s_waitcnt vmcnt(8)\n"
"v_mfma_f32_16x16x32_bf16 v[76:79], a[0:3], v[140:143], v[76:79]\n"
"buffer_load_dwordx4 v[80:83], v232, s[44:47], s61 offen offset:0 sc1\n"
"v_mfma_f32_16x16x32_bf16 v[76:79], a[108:111], v[124:127], v[76:79]\n"
"v_mfma_f32_16x16x32_bf16 v[72:75], a[104:107], v[124:127], v[72:75]\n"
"v_mfma_f32_16x16x32_bf16 v[72:75], a[124:127], v[140:143], v[72:75]\n"
"v_mfma_f32_16x16x32_bf16 v[68:71], a[120:123], v[140:143], v[68:71]\n"
"v_mfma_f32_16x16x32_bf16 v[68:71], a[100:103], v[124:127], v[68:71]\n"
"buffer_load_dwordx4 v[96:99], v232, s[44:47], s67 offen offset:0 sc1\n"
"v_mfma_f32_16x16x32_bf16 v[64:67], a[96:99], v[124:127], v[64:67]\n"
"v_mfma_f32_16x16x32_bf16 v[64:67], a[116:119], v[140:143], v[64:67]\n"
"v_mfma_f32_16x16x32_bf16 v[60:63], a[112:115], v[140:143], v[60:63]\n"
"v_mfma_f32_16x16x32_bf16 v[60:63], a[92:95], v[124:127], v[60:63]\n"
"s_waitcnt vmcnt(8)\n"
"v_mfma_f32_16x16x32_bf16 v[0:3], a[4:7], v[144:147], v[0:3]\n"
"buffer_load_dwordx4 v[84:87], v233, s[44:47], s61 offen offset:0 sc1\n"
"v_mfma_f32_16x16x32_bf16 v[0:3], a[24:27], v[160:163], v[0:3]\n"
"v_mfma_f32_16x16x32_bf16 v[4:7], a[28:31], v[160:163], v[4:7]\n"
"v_mfma_f32_16x16x32_bf16 v[4:7], a[8:11], v[144:147], v[4:7]\n"
"v_mfma_f32_16x16x32_bf16 v[8:11], a[12:15], v[144:147], v[8:11]\n"
"v_mfma_f32_16x16x32_bf16 v[8:11], a[32:35], v[160:163], v[8:11]\n"
"buffer_load_dwordx4 v[100:103], v233, s[44:47], s67 offen offset:0 sc1\n"
"v_mfma_f32_16x16x32_bf16 v[12:15], a[36:39], v[160:163], v[12:15]\n"
"v_mfma_f32_16x16x32_bf16 v[12:15], a[16:19], v[144:147], v[12:15]\n"
"v_mfma_f32_16x16x32_bf16 v[16:19], a[20:23], v[144:147], v[16:19]\n"
"v_mfma_f32_16x16x32_bf16 v[16:19], a[40:43], v[160:163], v[16:19]\n"
"s_waitcnt vmcnt(8)\n"
"v_mfma_f32_16x16x32_bf16 v[36:39], a[40:43], v[164:167], v[36:39]\n"
"buffer_load_dwordx4 v[88:91], v234, s[44:47], s61 offen offset:0 sc1\n"
"v_mfma_f32_16x16x32_bf16 v[36:39], a[20:23], v[148:151], v[36:39]\n"
"v_mfma_f32_16x16x32_bf16 v[32:35], a[16:19], v[148:151], v[32:35]\n"
"v_mfma_f32_16x16x32_bf16 v[32:35], a[36:39], v[164:167], v[32:35]\n"
"v_mfma_f32_16x16x32_bf16 v[28:31], a[32:35], v[164:167], v[28:31]\n"
"v_mfma_f32_16x16x32_bf16 v[28:31], a[12:15], v[148:151], v[28:31]\n"
"buffer_load_dwordx4 v[104:107], v234, s[44:47], s67 offen offset:0 sc1\n"
"v_mfma_f32_16x16x32_bf16 v[24:27], a[8:11], v[148:151], v[24:27]\n"
"v_mfma_f32_16x16x32_bf16 v[24:27], a[28:31], v[164:167], v[24:27]\n"
"v_mfma_f32_16x16x32_bf16 v[20:23], a[24:27], v[164:167], v[20:23]\n"
"v_mfma_f32_16x16x32_bf16 v[20:23], a[4:7], v[148:151], v[20:23]\n"
"s_waitcnt vmcnt(8)\n"
"v_mfma_f32_16x16x32_bf16 v[40:43], a[4:7], v[152:155], v[40:43]\n"
"buffer_load_dwordx4 v[92:95], v235, s[44:47], s61 offen offset:0 sc1\n"
"v_mfma_f32_16x16x32_bf16 v[40:43], a[24:27], v[168:171], v[40:43]\n"
"v_mfma_f32_16x16x32_bf16 v[44:47], a[28:31], v[168:171], v[44:47]\n"
"v_mfma_f32_16x16x32_bf16 v[44:47], a[8:11], v[152:155], v[44:47]\n"
"v_mfma_f32_16x16x32_bf16 v[48:51], a[12:15], v[152:155], v[48:51]\n"
"v_mfma_f32_16x16x32_bf16 v[48:51], a[32:35], v[168:171], v[48:51]\n"
"buffer_load_dwordx4 v[108:111], v235, s[44:47], s67 offen offset:0 sc1\n"
"v_mfma_f32_16x16x32_bf16 v[52:55], a[36:39], v[168:171], v[52:55]\n"
"v_mfma_f32_16x16x32_bf16 v[52:55], a[16:19], v[152:155], v[52:55]\n"
"v_mfma_f32_16x16x32_bf16 v[56:59], a[20:23], v[152:155], v[56:59]\n"
"v_mfma_f32_16x16x32_bf16 v[56:59], a[40:43], v[168:171], v[56:59]\n"
"s_waitcnt vmcnt(8)\n"
"v_mfma_f32_16x16x32_bf16 v[76:79], a[40:43], v[172:175], v[76:79]\n"
"v_mfma_f32_16x16x32_bf16 v[76:79], a[20:23], v[156:159], v[76:79]\n"
"v_mfma_f32_16x16x32_bf16 v[72:75], a[16:19], v[156:159], v[72:75]\n"
"v_mfma_f32_16x16x32_bf16 v[72:75], a[36:39], v[172:175], v[72:75]\n"
"v_mfma_f32_16x16x32_bf16 v[68:71], a[32:35], v[172:175], v[68:71]\n"
"v_mfma_f32_16x16x32_bf16 v[68:71], a[12:15], v[156:159], v[68:71]\n"
"v_mfma_f32_16x16x32_bf16 v[64:67], a[8:11], v[156:159], v[64:67]\n"
"v_mfma_f32_16x16x32_bf16 v[64:67], a[28:31], v[172:175], v[64:67]\n"
"v_mfma_f32_16x16x32_bf16 v[60:63], a[24:27], v[172:175], v[60:63]\n"
"v_mfma_f32_16x16x32_bf16 v[60:63], a[4:7], v[156:159], v[60:63]\n"
"s_waitcnt vmcnt(4)\n"
"v_mfma_f32_16x16x32_bf16 v[20:23], a[44:47], v[84:87], v[20:23]\n"
"v_mfma_f32_16x16x32_bf16 v[20:23], v[208:211], v[100:103], v[20:23]\n"
"v_mfma_f32_16x16x32_bf16 v[24:27], v[212:215], v[100:103], v[24:27]\n"
"v_mfma_f32_16x16x32_bf16 v[24:27], a[48:51], v[84:87], v[24:27]\n"
"v_mfma_f32_16x16x32_bf16 v[28:31], a[52:55], v[84:87], v[28:31]\n"
"v_mfma_f32_16x16x32_bf16 v[28:31], v[216:219], v[100:103], v[28:31]\n"
"v_mfma_f32_16x16x32_bf16 v[32:35], v[220:223], v[100:103], v[32:35]\n"
"v_mfma_f32_16x16x32_bf16 v[32:35], a[56:59], v[84:87], v[32:35]\n"
"v_mfma_f32_16x16x32_bf16 v[36:39], v[204:207], v[84:87], v[36:39]\n"
"v_mfma_f32_16x16x32_bf16 v[36:39], v[224:227], v[100:103], v[36:39]\n"
"s_waitcnt vmcnt(2)\n"
"v_mfma_f32_16x16x32_bf16 v[56:59], v[224:227], v[104:107], v[56:59]\n"
"v_mfma_f32_16x16x32_bf16 v[56:59], v[204:207], v[88:91], v[56:59]\n"
"v_mfma_f32_16x16x32_bf16 v[52:55], a[56:59], v[88:91], v[52:55]\n"
"v_mfma_f32_16x16x32_bf16 v[52:55], v[220:223], v[104:107], v[52:55]\n"
"ds_write_b128 v246, v[20:23] offset:0\n"
"v_mfma_f32_16x16x32_bf16 v[48:51], v[216:219], v[104:107], v[48:51]\n"
"v_mfma_f32_16x16x32_bf16 v[48:51], a[52:55], v[88:91], v[48:51]\n"
"ds_write_b128 v246, v[24:27] offset:1024\n"
"v_mfma_f32_16x16x32_bf16 v[44:47], a[48:51], v[88:91], v[44:47]\n"
"v_mfma_f32_16x16x32_bf16 v[44:47], v[212:215], v[104:107], v[44:47]\n"
"ds_write_b128 v246, v[28:31] offset:2048\n"
"v_mfma_f32_16x16x32_bf16 v[40:43], v[208:211], v[104:107], v[40:43]\n"
"v_mfma_f32_16x16x32_bf16 v[40:43], a[44:47], v[88:91], v[40:43]\n"
"ds_write_b128 v246, v[32:35] offset:3072\n"
"s_waitcnt vmcnt(0)\n"
"v_mfma_f32_16x16x32_bf16 v[60:63], a[44:47], v[92:95], v[60:63]\n"
"v_mfma_f32_16x16x32_bf16 v[60:63], v[208:211], v[108:111], v[60:63]\n"
"ds_write_b128 v246, v[36:39] offset:4096\n"
"v_mfma_f32_16x16x32_bf16 v[64:67], v[212:215], v[108:111], v[64:67]\n"
"v_mfma_f32_16x16x32_bf16 v[64:67], a[48:51], v[92:95], v[64:67]\n"
"ds_write_b128 v247, v[56:59] offset:4096\n"
"v_mfma_f32_16x16x32_bf16 v[68:71], a[52:55], v[92:95], v[68:71]\n"
"v_mfma_f32_16x16x32_bf16 v[68:71], v[216:219], v[108:111], v[68:71]\n"
"ds_write_b128 v247, v[52:55] offset:3072\n"
"v_mfma_f32_16x16x32_bf16 v[72:75], v[220:223], v[108:111], v[72:75]\n"
"v_mfma_f32_16x16x32_bf16 v[72:75], a[56:59], v[92:95], v[72:75]\n"
"ds_write_b128 v247, v[48:51] offset:2048\n"
"v_mfma_f32_16x16x32_bf16 v[76:79], v[204:207], v[92:95], v[76:79]\n"
"v_mfma_f32_16x16x32_bf16 v[76:79], v[224:227], v[108:111], v[76:79]\n"
"ds_write_b128 v247, v[44:47] offset:1024\n"
"s_waitcnt vmcnt(6)\n"
"v_mfma_f32_16x16x32_bf16 v[16:19], v[224:227], v[96:99], v[16:19]\n"
"v_mfma_f32_16x16x32_bf16 v[16:19], v[204:207], v[80:83], v[16:19]\n"
"ds_write_b128 v247, v[40:43] offset:0\n"
"v_mfma_f32_16x16x32_bf16 v[12:15], a[56:59], v[80:83], v[12:15]\n"
"v_mfma_f32_16x16x32_bf16 v[12:15], v[220:223], v[96:99], v[12:15]\n"
"ds_write_b128 v248, v[60:63] offset:0\n"
"v_mfma_f32_16x16x32_bf16 v[8:11], v[216:219], v[96:99], v[8:11]\n"
"v_mfma_f32_16x16x32_bf16 v[8:11], a[52:55], v[80:83], v[8:11]\n"
"ds_write_b128 v248, v[64:67] offset:1024\n"
"v_mfma_f32_16x16x32_bf16 v[4:7], a[48:51], v[80:83], v[4:7]\n"
"v_mfma_f32_16x16x32_bf16 v[4:7], v[212:215], v[96:99], v[4:7]\n"
"ds_write_b128 v248, v[68:71] offset:2048\n"
"v_mfma_f32_16x16x32_bf16 v[0:3], v[208:211], v[96:99], v[0:3]\n"
"v_mfma_f32_16x16x32_bf16 v[0:3], a[44:47], v[80:83], v[0:3]\n"
"ds_write_b128 v248, v[72:75] offset:3072\n"
"ds_write_b128 v248, v[76:79] offset:4096\n"
"v_bfe_u32 v253, v228, 8, 1\n"
"v_mad_u32_u24 v253, v253, 24, v249\n"
"v_lshlrev_b32 v140, 16, v236\n"
"v_and_b32 v141, 0xffff0000, v236\n"
"v_lshlrev_b32 v142, 16, v237\n"
"v_and_b32 v143, 0xffff0000, v237\n"
"v_lshlrev_b32 v144, 16, v238\n"
"v_and_b32 v145, 0xffff0000, v238\n"
"v_lshlrev_b32 v146, 16, v239\n"
"v_and_b32 v147, 0xffff0000, v239\n"
"v_lshlrev_b32 v148, 16, v240\n"
"v_and_b32 v149, 0xffff0000, v240\n"
"v_lshlrev_b32 v150, 16, v241\n"
"v_and_b32 v151, 0xffff0000, v241\n"
"v_lshlrev_b32 v152, 16, v242\n"
"v_and_b32 v153, 0xffff0000, v242\n"
"v_lshlrev_b32 v154, 16, v243\n"
"v_and_b32 v155, 0xffff0000, v243\n"
"v_lshlrev_b32 v156, 16, v244\n"
"v_and_b32 v157, 0xffff0000, v244\n"
"v_lshlrev_b32 v158, 16, v245\n"
"v_and_b32 v159, 0xffff0000, v245\n"
"s_waitcnt lgkmcnt(0)\n"
"s_barrier\n"
"ds_read_b128 v[80:83], v251 offset:0\n"
"ds_read_b128 v[84:87], v251 offset:1024\n"
"ds_read_b128 v[88:91], v251 offset:2048\n"
"ds_read_b128 v[92:95], v251 offset:3072\n"
"ds_read_b128 v[96:99], v251 offset:4096\n"
"ds_read_b128 v[100:103], v251 offset:5120\n"
"ds_read_b128 v[104:107], v251 offset:6144\n"
"ds_read_b128 v[108:111], v251 offset:7168\n"
"ds_read_b128 v[112:115], v251 offset:8192\n"
"ds_read_b128 v[116:119], v251 offset:9216\n"
"ds_read_b128 v[120:123], v251 offset:10240\n"
"ds_read_b128 v[124:127], v251 offset:11264\n"
"ds_read_b128 v[128:131], v251 offset:12288\n"
"ds_read_b128 v[132:135], v251 offset:13312\n"
"ds_read_b128 v[136:139], v251 offset:14336\n"
"s_waitcnt lgkmcnt(10)\n"
"v_pk_add_f32 v[0:1], v[0:1], v[80:81]\n"
"v_pk_add_f32 v[2:3], v[2:3], v[82:83]\n"
"v_pk_add_f32 v[4:5], v[4:5], v[84:85]\n"
"v_pk_add_f32 v[6:7], v[6:7], v[86:87]\n"
"v_pk_add_f32 v[8:9], v[8:9], v[88:89]\n"
"v_pk_add_f32 v[10:11], v[10:11], v[90:91]\n"
"v_pk_add_f32 v[12:13], v[12:13], v[92:93]\n"
"v_pk_add_f32 v[14:15], v[14:15], v[94:95]\n"
"v_pk_add_f32 v[16:17], v[16:17], v[96:97]\n"
"v_pk_add_f32 v[18:19], v[18:19], v[98:99]\n"
"s_waitcnt lgkmcnt(5)\n"
"v_pk_add_f32 v[0:1], v[0:1], v[100:101]\n"
"v_pk_add_f32 v[2:3], v[2:3], v[102:103]\n"
"v_pk_add_f32 v[4:5], v[4:5], v[104:105]\n"
"v_pk_add_f32 v[6:7], v[6:7], v[106:107]\n"
"v_pk_add_f32 v[8:9], v[8:9], v[108:109]\n"
"v_pk_add_f32 v[10:11], v[10:11], v[110:111]\n"
"v_pk_add_f32 v[12:13], v[12:13], v[112:113]\n"
"v_pk_add_f32 v[14:15], v[14:15], v[114:115]\n"
"v_pk_add_f32 v[16:17], v[16:17], v[116:117]\n"
"v_pk_add_f32 v[18:19], v[18:19], v[118:119]\n"
"s_waitcnt lgkmcnt(0)\n"
"v_pk_add_f32 v[0:1], v[0:1], v[120:121]\n"
"v_pk_add_f32 v[2:3], v[2:3], v[122:123]\n"
"v_pk_add_f32 v[4:5], v[4:5], v[124:125]\n"
"v_pk_add_f32 v[6:7], v[6:7], v[126:127]\n"
"v_pk_add_f32 v[8:9], v[8:9], v[128:129]\n"
"v_pk_add_f32 v[10:11], v[10:11], v[130:131]\n"
"v_pk_add_f32 v[12:13], v[12:13], v[132:133]\n"
"v_pk_add_f32 v[14:15], v[14:15], v[134:135]\n"
"v_pk_add_f32 v[16:17], v[16:17], v[136:137]\n"
"v_pk_add_f32 v[18:19], v[18:19], v[138:139]\n"
"v_pk_fma_f32 v[140:141], v[176:177], v[0:1], v[140:141]\n"
"v_pk_fma_f32 v[142:143], v[178:179], v[2:3], v[142:143]\n"
"v_cvt_pk_bf16_f32 v20, v140, v141\n"
"v_cvt_pk_bf16_f32 v21, v142, v143\n"
"v_pk_fma_f32 v[144:145], v[180:181], v[4:5], v[144:145]\n"
"v_pk_fma_f32 v[146:147], v[182:183], v[6:7], v[146:147]\n"
"v_cvt_pk_bf16_f32 v22, v144, v145\n"
"v_cvt_pk_bf16_f32 v23, v146, v147\n"
"v_pk_fma_f32 v[148:149], v[184:185], v[8:9], v[148:149]\n"
"v_pk_fma_f32 v[150:151], v[186:187], v[10:11], v[150:151]\n"
"v_cvt_pk_bf16_f32 v24, v148, v149\n"
"v_cvt_pk_bf16_f32 v25, v150, v151\n"
"v_permlane16_swap_b32 v20, v22\n"
"v_permlane16_swap_b32 v21, v23\n"
"buffer_store_dwordx4 v[20:23], v253, s[40:43], s35 offen offset:0\n"
"v_pk_fma_f32 v[152:153], v[188:189], v[12:13], v[152:153]\n"
"v_pk_fma_f32 v[154:155], v[190:191], v[14:15], v[154:155]\n"
"v_cvt_pk_bf16_f32 v26, v152, v153\n"
"v_cvt_pk_bf16_f32 v27, v154, v155\n"
"v_pk_fma_f32 v[156:157], v[192:193], v[16:17], v[156:157]\n"
"v_pk_fma_f32 v[158:159], v[194:195], v[18:19], v[158:159]\n"
"v_cvt_pk_bf16_f32 v28, v156, v157\n"
"v_cvt_pk_bf16_f32 v29, v158, v159\n"
"buffer_store_dwordx2 v[28:29], v249, s[40:43], s35 offen offset:128\n"
"v_permlane16_swap_b32 v24, v26\n"
"v_permlane16_swap_b32 v25, v27\n"
"buffer_store_dwordx4 v[24:27], v253, s[40:43], s35 offen offset:64\n"
"s_branch L_done_%=\n"
"L_fb_%=:\n"
"s_waitcnt vmcnt(0)\n"
"s_mov_b32 s66, 1\n"
"s_mov_b32 s67, 0\n"
"s_mov_b32 s35, -1\n"
"s_mov_b32 s61, 0\n"
"s_add_u32 m0, s60, 0\n"
"v_mov_b32 v80, 0xff800000\n"
"v_mov_b32 v81, 0xff800000\n"
"v_mov_b32 v82, 0xff800000\n"
"v_mov_b32 v83, 0xff800000\n"
"L_fbl_%=:\n"
"s_barrier\n"
"buffer_load_dwordx4 v244, s[36:39], s61 offen offset:0 lds\n"
"buffer_load_dwordx4 v245, s[36:39], s61 offen offset:1024 lds\n"
"buffer_load_dwordx4 v246, s[36:39], s61 offen offset:2048 lds\n"
"buffer_load_dwordx4 v247, s[36:39], s61 offen offset:3072 lds\n"
"s_waitcnt vmcnt(0)\n"
"s_barrier\n"
"ds_read_b128 a[128:131], v232 offset:0\n"
"ds_read_b128 a[132:135], v233 offset:0\n"
"ds_read_b128 a[136:139], v234 offset:0\n"
"ds_read_b128 a[140:143], v235 offset:0\n"
"ds_read_b128 a[144:147], v232 offset:4096\n"
"ds_read_b128 a[148:151], v233 offset:4096\n"
"ds_read_b128 a[152:155], v234 offset:4096\n"
"ds_read_b128 a[156:159], v235 offset:4096\n"
"s_waitcnt lgkmcnt(0)\n"
"v_mfma_f32_16x16x32_bf16 v[0:3], a[128:131], v[128:131], 0\n"
"v_mfma_f32_16x16x32_bf16 v[0:3], a[132:135], v[132:135], v[0:3]\n"
"v_mfma_f32_16x16x32_bf16 v[0:3], a[136:139], v[136:139], v[0:3]\n"
"v_mfma_f32_16x16x32_bf16 v[0:3], a[140:143], v[140:143], v[0:3]\n"
"v_mfma_f32_16x16x32_bf16 v[4:7], a[144:147], v[128:131], 0\n"
"v_mfma_f32_16x16x32_bf16 v[4:7], a[148:151], v[132:135], v[4:7]\n"
"v_mfma_f32_16x16x32_bf16 v[4:7], a[152:155], v[136:139], v[4:7]\n"
"v_mfma_f32_16x16x32_bf16 v[4:7], a[156:159], v[140:143], v[4:7]\n"
"v_mfma_f32_16x16x32_bf16 v[8:11], a[128:131], v[144:147], 0\n"
"v_mfma_f32_16x16x32_bf16 v[8:11], a[132:135], v[148:151], v[8:11]\n"
"v_mfma_f32_16x16x32_bf16 v[8:11], a[136:139], v[152:155], v[8:11]\n"
"v_mfma_f32_16x16x32_bf16 v[8:11], a[140:143], v[156:159], v[8:11]\n"
"v_mfma_f32_16x16x32_bf16 v[12:15], a[144:147], v[144:147], 0\n"
"v_mfma_f32_16x16x32_bf16 v[12:15], a[148:151], v[148:151], v[12:15]\n"
"v_mfma_f32_16x16x32_bf16 v[12:15], a[152:155], v[152:155], v[12:15]\n"
"v_mfma_f32_16x16x32_bf16 v[12:15], a[156:159], v[156:159], v[12:15]\n"
"v_mfma_f32_16x16x32_bf16 v[16:19], a[128:131], v[160:163], 0\n"
"v_mfma_f32_16x16x32_bf16 v[16:19], a[132:135], v[164:167], v[16:19]\n"
"v_mfma_f32_16x16x32_bf16 v[16:19], a[136:139], v[168:171], v[16:19]\n"
"v_mfma_f32_16x16x32_bf16 v[16:19], a[140:143], v[172:175], v[16:19]\n"
"v_mfma_f32_16x16x32_bf16 v[20:23], a[144:147], v[160:163], 0\n"
"v_mfma_f32_16x16x32_bf16 v[20:23], a[148:151], v[164:167], v[20:23]\n"
"v_mfma_f32_16x16x32_bf16 v[20:23], a[152:155], v[168:171], v[20:23]\n"
"v_mfma_f32_16x16x32_bf16 v[20:23], a[156:159], v[172:175], v[20:23]\n"
"v_mfma_f32_16x16x32_bf16 v[24:27], a[128:131], v[176:179], 0\n"
"v_mfma_f32_16x16x32_bf16 v[24:27], a[132:135], v[180:183], v[24:27]\n"
"v_mfma_f32_16x16x32_bf16 v[24:27], a[136:139], v[184:187], v[24:27]\n"
"v_mfma_f32_16x16x32_bf16 v[24:27], a[140:143], v[188:191], v[24:27]\n"
"v_mfma_f32_16x16x32_bf16 v[28:31], a[144:147], v[176:179], 0\n"
"v_mfma_f32_16x16x32_bf16 v[28:31], a[148:151], v[180:183], v[28:31]\n"
"v_mfma_f32_16x16x32_bf16 v[28:31], a[152:155], v[184:187], v[28:31]\n"
"v_mfma_f32_16x16x32_bf16 v[28:31], a[156:159], v[188:191], v[28:31]\n"
"s_nop 7\n"
"v_max3_f32 v80, v80, v0, v1\n"
"v_max3_f32 v81, v81, v8, v9\n"
"v_max3_f32 v82, v82, v16, v17\n"
"v_max3_f32 v83, v83, v24, v25\n"
"v_max3_f32 v80, v80, v2, v3\n"
"v_max3_f32 v81, v81, v10, v11\n"
"v_max3_f32 v82, v82, v18, v19\n"
"v_max3_f32 v83, v83, v26, v27\n"
"v_max3_f32 v80, v80, v4, v5\n"
"v_max3_f32 v81, v81, v12, v13\n"
"v_max3_f32 v82, v82, v20, v21\n"
"v_max3_f32 v83, v83, v28, v29\n"
"v_max3_f32 v80, v80, v6, v7\n"
"v_max3_f32 v81, v81, v14, v15\n"
"v_max3_f32 v82, v82, v22, v23\n"
"v_max3_f32 v83, v83, v30, v31\n"
"s_nop 1\n"
"ds_read_b128 a[128:131], v232 offset:8192\n"
"ds_read_b128 a[132:135], v233 offset:8192\n"
"ds_read_b128 a[136:139], v234 offset:8192\n"
"ds_read_b128 a[140:143], v235 offset:8192\n"
"ds_read_b128 a[144:147], v232 offset:12288\n"
"ds_read_b128 a[148:151], v233 offset:12288\n"
"ds_read_b128 a[152:155], v234 offset:12288\n"
"ds_read_b128 a[156:159], v235 offset:12288\n"
"s_waitcnt lgkmcnt(0)\n"
"v_mfma_f32_16x16x32_bf16 v[0:3], a[128:131], v[128:131], 0\n"
"v_mfma_f32_16x16x32_bf16 v[0:3], a[132:135], v[132:135], v[0:3]\n"
"v_mfma_f32_16x16x32_bf16 v[0:3], a[136:139], v[136:139], v[0:3]\n"
"v_mfma_f32_16x16x32_bf16 v[0:3], a[140:143], v[140:143], v[0:3]\n"
"v_mfma_f32_16x16x32_bf16 v[4:7], a[144:147], v[128:131], 0\n"
"v_mfma_f32_16x16x32_bf16 v[4:7], a[148:151], v[132:135], v[4:7]\n"
"v_mfma_f32_16x16x32_bf16 v[4:7], a[152:155], v[136:139], v[4:7]\n"
"v_mfma_f32_16x16x32_bf16 v[4:7], a[156:159], v[140:143], v[4:7]\n"
"v_mfma_f32_16x16x32_bf16 v[8:11], a[128:131], v[144:147], 0\n"
"v_mfma_f32_16x16x32_bf16 v[8:11], a[132:135], v[148:151], v[8:11]\n"
"v_mfma_f32_16x16x32_bf16 v[8:11], a[136:139], v[152:155], v[8:11]\n"
"v_mfma_f32_16x16x32_bf16 v[8:11], a[140:143], v[156:159], v[8:11]\n"
"v_mfma_f32_16x16x32_bf16 v[12:15], a[144:147], v[144:147], 0\n"
"v_mfma_f32_16x16x32_bf16 v[12:15], a[148:151], v[148:151], v[12:15]\n"
"v_mfma_f32_16x16x32_bf16 v[12:15], a[152:155], v[152:155], v[12:15]\n"
"v_mfma_f32_16x16x32_bf16 v[12:15], a[156:159], v[156:159], v[12:15]\n"
"v_mfma_f32_16x16x32_bf16 v[16:19], a[128:131], v[160:163], 0\n"
"v_mfma_f32_16x16x32_bf16 v[16:19], a[132:135], v[164:167], v[16:19]\n"
"v_mfma_f32_16x16x32_bf16 v[16:19], a[136:139], v[168:171], v[16:19]\n"
"v_mfma_f32_16x16x32_bf16 v[16:19], a[140:143], v[172:175], v[16:19]\n"
"v_mfma_f32_16x16x32_bf16 v[20:23], a[144:147], v[160:163], 0\n"
"v_mfma_f32_16x16x32_bf16 v[20:23], a[148:151], v[164:167], v[20:23]\n"
"v_mfma_f32_16x16x32_bf16 v[20:23], a[152:155], v[168:171], v[20:23]\n"
"v_mfma_f32_16x16x32_bf16 v[20:23], a[156:159], v[172:175], v[20:23]\n"
"v_mfma_f32_16x16x32_bf16 v[24:27], a[128:131], v[176:179], 0\n"
"v_mfma_f32_16x16x32_bf16 v[24:27], a[132:135], v[180:183], v[24:27]\n"
"v_mfma_f32_16x16x32_bf16 v[24:27], a[136:139], v[184:187], v[24:27]\n"
"v_mfma_f32_16x16x32_bf16 v[24:27], a[140:143], v[188:191], v[24:27]\n"
"v_mfma_f32_16x16x32_bf16 v[28:31], a[144:147], v[176:179], 0\n"
"v_mfma_f32_16x16x32_bf16 v[28:31], a[148:151], v[180:183], v[28:31]\n"
"v_mfma_f32_16x16x32_bf16 v[28:31], a[152:155], v[184:187], v[28:31]\n"
"v_mfma_f32_16x16x32_bf16 v[28:31], a[156:159], v[188:191], v[28:31]\n"
"s_nop 7\n"
"v_max3_f32 v80, v80, v0, v1\n"
"v_max3_f32 v81, v81, v8, v9\n"
"v_max3_f32 v82, v82, v16, v17\n"
"v_max3_f32 v83, v83, v24, v25\n"
"v_max3_f32 v80, v80, v2, v3\n"
"v_max3_f32 v81, v81, v10, v11\n"
"v_max3_f32 v82, v82, v18, v19\n"
"v_max3_f32 v83, v83, v26, v27\n"
"v_max3_f32 v80, v80, v4, v5\n"
"v_max3_f32 v81, v81, v12, v13\n"
"v_max3_f32 v82, v82, v20, v21\n"
"v_max3_f32 v83, v83, v28, v29\n"
"v_max3_f32 v80, v80, v6, v7\n"
"v_max3_f32 v81, v81, v14, v15\n"
"v_max3_f32 v82, v82, v22, v23\n"
"v_max3_f32 v83, v83, v30, v31\n"
"s_nop 1\n"
"s_add_u32 s61, s61, 16384\n"
"s_cmp_lg_u32 s61, 524288\n"
"s_cbranch_scc1 L_fbl_%=\n"
"s_barrier\n"
"s_add_u32 m0, s60, 0\n"
"s_mov_b32 s61, 0\n"
"buffer_load_dwordx4 v244, s[36:39], s61 offen offset:0 lds\n"
"buffer_load_dwordx4 v245, s[36:39], s61 offen offset:1024 lds\n"
"buffer_load_dwordx4 v246, s[36:39], s61 offen offset:2048 lds\n"
"buffer_load_dwordx4 v247, s[36:39], s61 offen offset:3072 lds\n"
"s_add_u32 m0, s60, 65536\n"
"s_mov_b32 s62, 0\n"
"buffer_load_dwordx4 v248, s[40:43], s62 offen offset:0 lds\n"
"buffer_load_dwordx4 v249, s[40:43], s62 offen offset:1024 lds\n"
"buffer_load_dwordx4 v248, s[40:43], s62 offen offset:2048 lds\n"
"buffer_load_dwordx4 v249, s[40:43], s62 offen offset:3072 lds\n"
"s_add_u32 m0, s60, 16384\n"
"s_mov_b32 s61, 16384\n"
"buffer_load_dwordx4 v244, s[36:39], s61 offen offset:0 lds\n"
"buffer_load_dwordx4 v245, s[36:39], s61 offen offset:1024 lds\n"
"buffer_load_dwordx4 v246, s[36:39], s61 offen offset:2048 lds\n"
"buffer_load_dwordx4 v247, s[36:39], s61 offen offset:3072 lds\n"
"s_add_u32 m0, s60, 81920\n"
"s_mov_b32 s62, 16384\n"
"buffer_load_dwordx4 v248, s[40:43], s62 offen offset:0 lds\n"
"buffer_load_dwordx4 v249, s[40:43], s62 offen offset:1024 lds\n"
"buffer_load_dwordx4 v248, s[40:43], s62 offen offset:2048 lds\n"
"buffer_load_dwordx4 v249, s[40:43], s62 offen offset:3072 lds\n"
"s_add_u32 m0, s60, 32768\n"
"s_mov_b32 s61, 32768\n"
"buffer_load_dwordx4 v244, s[36:39], s61 offen offset:0 lds\n"
"buffer_load_dwordx4 v245, s[36:39], s61 offen offset:1024 lds\n"
"buffer_load_dwordx4 v246, s[36:39], s61 offen offset:2048 lds\n"
"buffer_load_dwordx4 v247, s[36:39], s61 offen offset:3072 lds\n"
"s_add_u32 m0, s60, 49152\n"
"s_mov_b32 s61, 49152\n"
"buffer_load_dwordx4 v244, s[36:39], s61 offen offset:0 lds\n"
"buffer_load_dwordx4 v245, s[36:39], s61 offen offset:1024 lds\n"
"buffer_load_dwordx4 v246, s[36:39], s61 offen offset:2048 lds\n"
"buffer_load_dwordx4 v247, s[36:39], s61 offen offset:3072 lds\n"
"v_mov_b32 v224, 0\n"
"v_mov_b32 v225, 0\n"
"v_mov_b32 v226, 0\n"
"v_mov_b32 v227, 0\n"
"s_nop 1\n"
"v_mfma_f32_32x32x16_bf16 a[0:15], v[224:227], v[224:227], 0\n"
"v_mfma_f32_32x32x16_bf16 a[16:31], v[224:227], v[224:227], 0\n"
"v_mfma_f32_32x32x16_bf16 a[32:47], v[224:227], v[224:227], 0\n"
"v_mfma_f32_32x32x16_bf16 a[48:63], v[224:227], v[224:227], 0\n"
"v_mfma_f32_32x32x16_bf16 a[64:79], v[224:227], v[224:227], 0\n"
"v_mfma_f32_32x32x16_bf16 a[80:95], v[224:227], v[224:227], 0\n"
"v_mfma_f32_32x32x16_bf16 a[96:111], v[224:227], v[224:227], 0\n"
"v_mfma_f32_32x32x16_bf16 a[112:127], v[224:227], v[224:227], 0\n"
"v_mfma_f32_32x32x16_bf16 a[192:207], v[224:227], v[224:227], 0\n"
"s_waitcnt vmcnt(20)\n"
"s_barrier\n"
"ds_read_b128 a[128:131], v232 offset:0\n"
"ds_read_b128 a[132:135], v233 offset:0\n"
"ds_read_b128 a[136:139], v234 offset:0\n"
"ds_read_b128 a[140:143], v235 offset:0\n"
"ds_read_b128 a[144:147], v232 offset:4096\n"
"ds_read_b128 a[148:151], v233 offset:4096\n"
"ds_read_b128 a[152:155], v234 offset:4096\n"
"ds_read_b128 a[156:159], v235 offset:4096\n"
"s_waitcnt lgkmcnt(0)\n"
"v_mfma_f32_16x16x32_bf16 v[0:3], a[128:131], v[128:131], 0\n"
"v_mfma_f32_16x16x32_bf16 v[0:3], a[132:135], v[132:135], v[0:3]\n"
"v_mfma_f32_16x16x32_bf16 v[0:3], a[136:139], v[136:139], v[0:3]\n"
"v_mfma_f32_16x16x32_bf16 v[0:3], a[140:143], v[140:143], v[0:3]\n"
"v_mfma_f32_16x16x32_bf16 v[4:7], a[144:147], v[128:131], 0\n"
"v_mfma_f32_16x16x32_bf16 v[4:7], a[148:151], v[132:135], v[4:7]\n"
"v_mfma_f32_16x16x32_bf16 v[4:7], a[152:155], v[136:139], v[4:7]\n"
"v_mfma_f32_16x16x32_bf16 v[4:7], a[156:159], v[140:143], v[4:7]\n"
"v_mfma_f32_16x16x32_bf16 v[8:11], a[128:131], v[144:147], 0\n"
"v_mfma_f32_16x16x32_bf16 v[8:11], a[132:135], v[148:151], v[8:11]\n"
"v_mfma_f32_16x16x32_bf16 v[8:11], a[136:139], v[152:155], v[8:11]\n"
"v_mfma_f32_16x16x32_bf16 v[8:11], a[140:143], v[156:159], v[8:11]\n"
"v_mfma_f32_16x16x32_bf16 v[12:15], a[144:147], v[144:147], 0\n"
"v_mfma_f32_16x16x32_bf16 v[12:15], a[148:151], v[148:151], v[12:15]\n"
"v_mfma_f32_16x16x32_bf16 v[12:15], a[152:155], v[152:155], v[12:15]\n"
"v_mfma_f32_16x16x32_bf16 v[12:15], a[156:159], v[156:159], v[12:15]\n"
"v_mfma_f32_16x16x32_bf16 v[16:19], a[128:131], v[160:163], 0\n"
"v_mfma_f32_16x16x32_bf16 v[16:19], a[132:135], v[164:167], v[16:19]\n"
"v_mfma_f32_16x16x32_bf16 v[16:19], a[136:139], v[168:171], v[16:19]\n"
"v_mfma_f32_16x16x32_bf16 v[16:19], a[140:143], v[172:175], v[16:19]\n"
"v_mfma_f32_16x16x32_bf16 v[20:23], a[144:147], v[160:163], 0\n"
"v_mfma_f32_16x16x32_bf16 v[20:23], a[148:151], v[164:167], v[20:23]\n"
"v_mfma_f32_16x16x32_bf16 v[20:23], a[152:155], v[168:171], v[20:23]\n"
"v_mfma_f32_16x16x32_bf16 v[20:23], a[156:159], v[172:175], v[20:23]\n"
"v_mfma_f32_16x16x32_bf16 v[24:27], a[128:131], v[176:179], 0\n"
"v_mfma_f32_16x16x32_bf16 v[24:27], a[132:135], v[180:183], v[24:27]\n"
"v_mfma_f32_16x16x32_bf16 v[24:27], a[136:139], v[184:187], v[24:27]\n"
"v_mfma_f32_16x16x32_bf16 v[24:27], a[140:143], v[188:191], v[24:27]\n"
"v_mfma_f32_16x16x32_bf16 v[28:31], a[144:147], v[176:179], 0\n"
"v_mfma_f32_16x16x32_bf16 v[28:31], a[148:151], v[180:183], v[28:31]\n"
"v_mfma_f32_16x16x32_bf16 v[28:31], a[152:155], v[184:187], v[28:31]\n"
"v_mfma_f32_16x16x32_bf16 v[28:31], a[156:159], v[188:191], v[28:31]\n"
"s_branch L_pipe_%=\n"
"L_ps3_%=:\n"
"s_mov_b32 s65, 200000\n"
"L_pl3_%=:\n"
"global_load_dword v225, v224, %0 sc1\n"
"s_waitcnt vmcnt(0)\n"
"v_cmp_gt_u32 vcc, 64, v225\n"
"s_cbranch_vccz L_pd3_%=\n"
"s_sleep 1\n"
"s_sub_u32 s65, s65, 1\n"
"s_cbranch_scc0 L_pl3_%=\n"
"s_branch L_pd3_%=\n"
"L_exs_%=:\n"
"s_mov_b32 s65, 200000\n"
"L_exl_%=:\n"
"buffer_load_dwordx4 v[204:207], v228, s[44:47], %9 offen offset:0 sc1\n"
"buffer_load_dwordx4 v[208:211], v228, s[44:47], %9 offen offset:1024 sc1\n"
"buffer_load_dwordx2 v[212:213], v231, s[44:47], %9 offen offset:2048 sc1\n"
"s_waitcnt vmcnt(0)\n"
"v_max3_u32 v253, v204, v205, v206\n"
"v_max3_u32 v253, v253, v207, v208\n"
"v_max3_u32 v253, v253, v209, v210\n"
"v_max3_u32 v253, v253, v211, v212\n"
"v_max3_u32 v253, v253, v213, v213\n"
"v_cmp_eq_u32 vcc, -1, v253\n"
"s_cbranch_vccz L_exd_%=\n"
"s_sleep 2\n"
"s_sub_u32 s65, s65, 1\n"
"s_cbranch_scc0 L_exl_%=\n"
"s_branch L_exd_%=\n"
"L_ps5_%=:\n"
"s_mov_b32 s65, 200000\n"
"L_pl5_%=:\n"
"global_load_dword v19, v18, %0 sc1\n"
"s_waitcnt vmcnt(0)\n"
"v_cmp_gt_u32 vcc, 64, v19\n"
"s_cbranch_vccz L_pd5_%=\n"
"s_sleep 1\n"
"s_sub_u32 s65, s65, 1\n"
"s_cbranch_scc0 L_pl5_%=\n"
"s_branch L_pd5_%=\n"
"L_done_%=:\n"
  : "=&s"(p0), "=&s"(p1), "=&s"(p2), "=&s"(p3), "=&s"(t0), "=&s"(t1), "=&s"(t2), "=&s"(t3), "=&s"(t4), "=&s"(t5), "=&s"(t6), "=&s"(t7), "=&s"(t8), "=&s"(t9), "=&s"(t10), "=&s"(t11)
  : "s"(ka), "s"(w), "s"(b), "s"((u32)(size_t)lds)
  : "v0","v1","v2","v3","v4","v5","v6","v7","v8","v9","v10","v11","v12","v13","v14","v15","v16","v17","v18","v19","v20","v21","v22","v23","v24","v25","v26","v27","v28","v29","v30","v31","v32","v33","v34","v35","v36","v37","v38","v39","v40","v41","v42","v43","v44","v45","v46","v47","v48","v49","v50","v51","v52","v53","v54","v55","v56","v57","v58","v59","v60","v61","v62","v63","v64","v65","v66","v67","v68","v69","v70","v71","v72","v73","v74","v75","v76","v77","v78","v79","v80","v81","v82","v83","v84","v85","v86","v87","v88","v89","v90","v91","v92","v93","v94","v95","v96","v97","v98","v99","v100","v101","v102","v103","v104","v105","v106","v107","v108","v109","v110","v111","v112","v113","v114","v115","v116","v117","v118","v119","v120","v121","v122","v123","v124","v125","v126","v127","v128","v129","v130","v131","v132","v133","v134","v135","v136","v137","v138","v139","v140","v141","v142","v143","v144","v145","v146","v147","v148","v149","v150","v151","v152","v153","v154","v155","v156","v157","v158","v159","v160","v161","v162","v163","v164","v165","v166","v167","v168","v169","v170","v171","v172","v173","v174","v175","v176","v177","v178","v179","v180","v181","v182","v183","v184","v185","v186","v187","v188","v189","v190","v191","v192","v193","v194","v195","v196","v197","v198","v199","v200","v201","v202","v203","v204","v205","v206","v207","v208","v209","v210","v211","v212","v213","v214","v215","v216","v217","v218","v219","v220","v221","v222","v223","v224","v225","v226","v227","v228","v229","v230","v231","v232","v233","v234","v235","v236","v237","v238","v239","v240","v241","v242","v243","v244","v245","v246","v247","v248","v249","v250","v251","v252","v253","v254","v255","a0","a1","a2","a3","a4","a5","a6","a7","a8","a9","a10","a11","a12","a13","a14","a15","a16","a17","a18","a19","a20","a21","a22","a23","a24","a25","a26","a27","a28","a29","a30","a31","a32","a33","a34","a35","a36","a37","a38","a39","a40","a41","a42","a43","a44","a45","a46","a47","a48","a49","a50","a51","a52","a53","a54","a55","a56","a57","a58","a59","a60","a61","a62","a63","a64","a65","a66","a67","a68","a69","a70","a71","a72","a73","a74","a75","a76","a77","a78","a79","a80","a81","a82","a83","a84","a85","a86","a87","a88","a89","a90","a91","a92","a93","a94","a95","a96","a97","a98","a99","a100","a101","a102","a103","a104","a105","a106","a107","a108","a109","a110","a111","a112","a113","a114","a115","a116","a117","a118","a119","a120","a121","a122","a123","a124","a125","a126","a127","a128","a129","a130","a131","a132","a133","a134","a135","a136","a137","a138","a139","a140","a141","a142","a143","a144","a145","a146","a147","a148","a149","a150","a151","a152","a153","a154","a155","a156","a157","a158","a159","a160","a161","a162","a163","a164","a165","a166","a167","a168","a169","a170","a171","a172","a173","a174","a175","a176","a177","a178","a179","a180","a181","a182","a183","a184","a185","a186","a187","a188","a189","a190","a191","a192","a193","a194","a195","a196","a197","a198","a199","a200","a201","a202","a203","a204","a205","a206","a207","a208","a209","a210","a211","a212","a213","a214","a215","a216","a217","a218","a219","a220","a221","a222","a223","a224","a225","a226","a227","a228","a229","a230","a231","a232","a233","a234","a235","a236","a237","a238","a239","a240","a241","a242","a243","a244","a245","a246","a247","a248","a249","a250","a251","a252","a253","a254","a255","s34","s35","s36","s37","s38","s39","s40","s41","s42","s43","s44","s45","s46","s47","s48","s49","s50","s51","s52","s53","s54","s55","s56","s57","s58","s59","s60","s61","s62","s63","s64","s65","s66","s67","s68","s69","s70","s71","s72","s73","s74","s75","s76","s77","s78","s79","s80","s81","s82","s83","s84","s85","s86","s87","s88","s89","s90","s91","s92","s93","s94","s95","s96","s97","s98","s99","vcc","scc","memory");
}

extern "C" void kernel_launch(void* const* d_in, const int* in_sizes, int n_in, void* d_out, int out_size, void* d_ws, size_t ws_size, hipStream_t stream) {
  const u16* x = (const u16*)d_in[0]; const u16* kc = (const u16*)d_in[1]; const u16* vc = (const u16*)d_in[2];
  const u16* wq = (const u16*)d_in[3]; const u16* wo = (const u16*)d_in[4];
  const u16* wg = (const u16*)d_in[6]; const u16* wu = (const u16*)d_in[7]; const u16* wd = (const u16*)d_in[8];
  const u16* c = (const u16*)d_in[9]; const u16* wmod = (const u16*)d_in[10];
  k_main<<<256, 256, 0, stream>>>(x, c, wmod, (char*)d_ws, kc, vc, wq, (u16*)d_out, wo, wg, wu, wd);
}
```
